```python
import torch

def _softmax(x, axis=-1):
    return torch.softmax(x, dim=axis)
def _gelu(x):
    return torch.nn.functional.gelu(x, approximate="tanh")
def _silu(x):
    return torch.nn.functional.silu(x)
def _sigmoid(x):
    return torch.sigmoid(x)
def _layernorm(x, axis=-1, eps=1e-5):
    m = x.mean(dim=axis, keepdim=True)
    v = x.var(dim=axis, keepdim=True, unbiased=False)
    return (x - m) / torch.sqrt(v + eps)
def _scatter_add(tgt, idx, src, axis=0):
    return tgt.clone().index_add_(axis, idx, src)
def _segment_sum(src, seg, n):
    out = torch.zeros((n,) + src.shape[1:], dtype=src.dtype)
    return out.index_add_(0, seg, src)
def _pad(x, pw):
    flat = tuple(v for pair in reversed(pw) for v in pair)
    return torch.nn.functional.pad(x, flat)

kernel_name = 'standalone__gqa_block_cached__near_ridge'

def setup_inputs():

    _w = (lambda a: (lambda t: t.to(torch.bfloat16) if t.is_floating_point() else t)(torch.as_tensor(a)))
    B, Sq, D = 8, 240, 640
    Hq, Hkv, Sk, Dh = 32, 32, 1024, 192
    assert Hq % Hkv == 0, (Hq, Hkv)
    return {
        "x": _w((lambda *s: torch.randn(*s, dtype=torch.bfloat16) * 0.8)(B, Sq, D)),
        "k_cache": _w((lambda *s: torch.randn(*s, dtype=torch.bfloat16) * 0.8)(B, Hkv, Sk, Dh)),
        "v_cache": _w((lambda *s: torch.randn(*s, dtype=torch.bfloat16) * 0.05)(B, Hkv, Sk, Dh)),
        "wq": _w((lambda *s: torch.randn(*s, dtype=torch.bfloat16) * 0.8)(D, Hq * Dh) * (D ** -0.5)),
        "wo": _w((lambda *s: torch.randn(*s, dtype=torch.bfloat16) * 0.05)(Hq * Dh, D) * ((Hq * Dh) ** -0.5)),
    }

def reference(x, k_cache, v_cache, wq, wo):
    B, Sq, D = x.shape
    Hq, Dh = 32, 192
    Hkv = k_cache.shape[1]
    rep = Hq // Hkv
    q = (lambda x, *d: x.permute(*d))((x @ wq).reshape(B, Sq, Hq, Dh), 0, 2, 1, 3)
    k = (lambda x, n, axis: torch.repeat_interleave(x, n, dim=axis))(k_cache, rep, axis=1)
    v = (lambda x, n, axis: torch.repeat_interleave(x, n, dim=axis))(v_cache, rep, axis=1)
    scores = torch.einsum("bhqd,bhkd->bhqk", q, k) * (Dh ** -0.5)
    p = _softmax(scores, axis=-1)
    o = torch.einsum("bhqk,bhkd->bhqd", p, v)
    return (lambda x, *d: x.permute(*d))(o, 0, 2, 1, 3).reshape(B, Sq, Hq * Dh) @ wo
```

```cpp
#include <hip/hip_runtime.h>
#include <hip/hip_bf16.h>
typedef unsigned short u16;
__device__ __forceinline__ float bf2f(u16 v) { return __uint_as_float(((unsigned)v) << 16); }
__device__ __forceinline__ u16 f2bf(float f) { unsigned u = __float_as_uint(f); return (u16)((u + 0x7FFF + ((u >> 16) & 1)) >> 16); }
constexpr int B = 8, SQ = 240, D = 640, H = 32, SK = 1024, DH = 192, HD = H * DH;
__global__ void naive_q(const u16* x, const u16* wq, u16* q) {
    int n = blockIdx.x * 256 + threadIdx.x; int m = blockIdx.y;
    float acc = 0.f;
    for (int k = 0; k < D; ++k) acc += bf2f(x[m * D + k]) * bf2f(wq[(size_t)k * HD + n]);
    q[(size_t)m * HD + n] = f2bf(acc);
}
__global__ void naive_attn(const u16* q, const u16* kc, const u16* vc, u16* o) {
    __shared__ float qs[DH]; __shared__ float p[SK]; __shared__ float red[256];
    int m = blockIdx.x; int h = blockIdx.y; int b = m / SQ; int t = threadIdx.x;
    if (t < DH) qs[t] = bf2f(q[(size_t)m * HD + h * DH + t]);
    __syncthreads();
    const u16* kb = kc + (size_t)(b * H + h) * SK * DH;
    const u16* vb = vc + (size_t)(b * H + h) * SK * DH;
    float mx = -1e30f;
    for (int k = t; k < SK; k += 256) {
        float s = 0.f;
        for (int d = 0; d < DH; ++d) s += qs[d] * bf2f(kb[(size_t)k * DH + d]);
        s *= 0.07216878364870322f; p[k] = s; mx = fmaxf(mx, s);
    }
    red[t] = mx; __syncthreads();
    for (int s = 128; s > 0; s >>= 1) { if (t < s) red[t] = fmaxf(red[t], red[t + s]); __syncthreads(); }
    mx = red[0]; __syncthreads();
    float sum = 0.f;
    for (int k = t; k < SK; k += 256) { float e = __expf(p[k] - mx); p[k] = e; sum += e; }
    red[t] = sum; __syncthreads();
    for (int s = 128; s > 0; s >>= 1) { if (t < s) red[t] += red[t + s]; __syncthreads(); }
    sum = red[0];
    if (t < DH) {
        float acc = 0.f;
        for (int k = 0; k < SK; ++k) acc += p[k] * bf2f(vb[(size_t)k * DH + t]);
        o[(size_t)m * HD + h * DH + t] = f2bf(acc / sum);
    }
}
__global__ void naive_o(const u16* o, const u16* wo, u16* out) {
    int n = blockIdx.x * 128 + threadIdx.x; int m = blockIdx.y;
    float acc = 0.f;
    for (int k = 0; k < HD; ++k) acc += bf2f(o[(size_t)m * HD + k]) * bf2f(wo[(size_t)k * D + n]);
    out[(size_t)m * D + n] = f2bf(acc);
}
extern "C" void kernel_launch(void* const* d_in, const int* in_sizes, int n_in,
                              void* d_out, int out_size, void* d_ws, size_t ws_size,
                              hipStream_t stream) {
    const u16* x = (const u16*)d_in[0]; const u16* kc = (const u16*)d_in[1]; const u16* vc = (const u16*)d_in[2];
    const u16* wq = (const u16*)d_in[3]; const u16* wo = (const u16*)d_in[4];
    u16* q = (u16*)d_ws; u16* o = q + (size_t)B * SQ * HD;
    naive_q<<<dim3(HD / 256, B * SQ), 256, 0, stream>>>(x, wq, q);
    naive_attn<<<dim3(B * SQ, H), 256, 0, stream>>>(q, kc, vc, o);
    naive_o<<<dim3(D / 128, B * SQ), 128, 0, stream>>>(o, wo, (u16*)d_out);
}
```

```cpp
#include <hip/hip_runtime.h>
#include <hip/hip_bf16.h>
typedef unsigned short u16;
extern "C" __global__ __launch_bounds__(256, 1) void k1_qattn(const void* x, const void* kc, const void* vc, const void* wq, const void* wo, void* o, void* dbg) {
  unsigned tid = threadIdx.x; unsigned bid = blockIdx.x;
  asm volatile(
"K1_CODE_BEGIN:\n"
"v_lshrrev_b32 v55, 6, %[tid]\n"
"v_and_b32 v0, 63, %[tid]\n"
"s_and_b32 s66, %[bid], 7\n"
"v_readfirstlane_b32 s56, v55\n"
"s_lshr_b32 s67, %[bid], 3\n"
"s_lshr_b32 s71, s66, 1\n"
"s_lshl_b32 s71, s71, 3\n"
"s_and_b32 s72, s67, 7\n"
"s_add_u32 s72, s72, s71\n"
"s_and_b32 s66, s66, 1\n"
"s_lshl_b32 s66, s66, 2\n"
"s_lshr_b32 s73, s67, 3\n"
"s_add_u32 s73, s73, s66\n"
"s_mul_i32 s66, s73, 307200\n"
"s_add_u32 s36, %[xlo], s66\n"
"s_addc_u32 s37, %[xhi], 0\n"
"s_mov_b32 s38, 307200\n"
"s_mov_b32 s39, 0x20000\n"
"s_mul_i32 s66, s72, 384\n"
"s_add_u32 s40, %[wlo], s66\n"
"s_addc_u32 s41, %[whi], 0\n"
"s_sub_u32 s42, 7864320, s66\n"
"s_mov_b32 s43, 0x20000\n"
"s_cmp_eq_u32 s56, 3\n"
"s_cbranch_scc1 K1B_START\n"
"s_mul_i32 s57, s56, 81920\n"
"s_mov_b32 s58, 0\n"
"s_lshl_b32 s69, s56, 13\n"
"s_lshl_b32 s70, s56, 13\n"
"s_mul_i32 s84, s56, 20480\n"
"s_add_u32 s84, s84, 245760\n"
"s_lshl_b32 s71, s56, 9\n"
"v_lshrrev_b32 v55, 3, v0\n"
"v_mul_u32_u24 v55, 1280, v55\n"
"v_and_b32 v56, 7, v0\n"
"v_lshrrev_b32 v57, 4, v0\n"
"v_xor_b32 v58, v56, v57\n"
"v_lshl_add_u32 v51, v58, 4, v55\n"
"v_xor_b32 v58, 4, v58\n"
"v_lshl_add_u32 v58, v58, 4, v55\n"
"v_add_u32 v52, 9216, v58\n"
"v_add_u32 v53, 18432, v51\n"
"v_add_u32 v54, 27648, v58\n"
"v_add_u32 v59, s71, v0\n"
"s_lshl_b32 s71, s56, 13\n"
"v_mul_u32_u24 v55, 2731, v59\n"
"v_lshrrev_b32 v55, 16, v55\n"
"v_bfe_u32 v56, v55, 1, 1\n"
"v_bfe_u32 v57, v55, 3, 1\n"
"v_lshl_add_u32 v56, v57, 1, v56\n"
"v_lshlrev_b32 v56, 1, v56\n"
"v_xor_b32 v56, v56, v0\n"
"v_lshlrev_b32 v56, 4, v56\n"
"v_mul_u32_u24 v55, 11904, v55\n"
"v_add3_u32 v43, v55, v56, s71\n"
"v_add_u32 v55, 64, v59\n"
"v_mul_u32_u24 v55, 2731, v55\n"
"v_lshrrev_b32 v55, 16, v55\n"
"v_bfe_u32 v56, v55, 1, 1\n"
"v_bfe_u32 v57, v55, 3, 1\n"
"v_lshl_add_u32 v56, v57, 1, v56\n"
"v_lshlrev_b32 v56, 1, v56\n"
"v_xor_b32 v56, v56, v0\n"
"v_lshlrev_b32 v56, 4, v56\n"
"v_mul_u32_u24 v55, 11904, v55\n"
"v_add3_u32 v44, v55, v56, s71\n"
"v_add_u32 v55, 128, v59\n"
"v_mul_u32_u24 v55, 2731, v55\n"
"v_lshrrev_b32 v55, 16, v55\n"
"v_bfe_u32 v56, v55, 1, 1\n"
"v_bfe_u32 v57, v55, 3, 1\n"
"v_lshl_add_u32 v56, v57, 1, v56\n"
"v_lshlrev_b32 v56, 1, v56\n"
"v_xor_b32 v56, v56, v0\n"
"v_lshlrev_b32 v56, 4, v56\n"
"v_mul_u32_u24 v55, 11904, v55\n"
"v_add3_u32 v45, v55, v56, s71\n"
"v_add_u32 v55, 192, v59\n"
"v_mul_u32_u24 v55, 2731, v55\n"
"v_lshrrev_b32 v55, 16, v55\n"
"v_bfe_u32 v56, v55, 1, 1\n"
"v_bfe_u32 v57, v55, 3, 1\n"
"v_lshl_add_u32 v56, v57, 1, v56\n"
"v_lshlrev_b32 v56, 1, v56\n"
"v_xor_b32 v56, v56, v0\n"
"v_lshlrev_b32 v56, 4, v56\n"
"v_mul_u32_u24 v55, 11904, v55\n"
"v_add3_u32 v46, v55, v56, s71\n"
"v_add_u32 v55, 256, v59\n"
"v_mul_u32_u24 v55, 2731, v55\n"
"v_lshrrev_b32 v55, 16, v55\n"
"v_bfe_u32 v56, v55, 1, 1\n"
"v_bfe_u32 v57, v55, 3, 1\n"
"v_lshl_add_u32 v56, v57, 1, v56\n"
"v_lshlrev_b32 v56, 1, v56\n"
"v_xor_b32 v56, v56, v0\n"
"v_lshlrev_b32 v56, 4, v56\n"
"v_mul_u32_u24 v55, 11904, v55\n"
"v_add3_u32 v47, v55, v56, s71\n"
"v_add_u32 v47, 4096, v47\n"
"v_add_u32 v55, 320, v59\n"
"v_mul_u32_u24 v55, 2731, v55\n"
"v_lshrrev_b32 v55, 16, v55\n"
"v_bfe_u32 v56, v55, 1, 1\n"
"v_bfe_u32 v57, v55, 3, 1\n"
"v_lshl_add_u32 v56, v57, 1, v56\n"
"v_lshlrev_b32 v56, 1, v56\n"
"v_xor_b32 v56, v56, v0\n"
"v_lshlrev_b32 v56, 4, v56\n"
"v_mul_u32_u24 v55, 11904, v55\n"
"v_add3_u32 v48, v55, v56, s71\n"
"v_add_u32 v48, 4096, v48\n"
"v_add_u32 v55, 384, v59\n"
"v_mul_u32_u24 v55, 2731, v55\n"
"v_lshrrev_b32 v55, 16, v55\n"
"v_bfe_u32 v56, v55, 1, 1\n"
"v_bfe_u32 v57, v55, 3, 1\n"
"v_lshl_add_u32 v56, v57, 1, v56\n"
"v_lshlrev_b32 v56, 1, v56\n"
"v_xor_b32 v56, v56, v0\n"
"v_lshlrev_b32 v56, 4, v56\n"
"v_mul_u32_u24 v55, 11904, v55\n"
"v_add3_u32 v49, v55, v56, s71\n"
"v_add_u32 v49, 4096, v49\n"
"v_add_u32 v55, 448, v59\n"
"v_mul_u32_u24 v55, 2731, v55\n"
"v_lshrrev_b32 v55, 16, v55\n"
"v_bfe_u32 v56, v55, 1, 1\n"
"v_bfe_u32 v57, v55, 3, 1\n"
"v_lshl_add_u32 v56, v57, 1, v56\n"
"v_lshlrev_b32 v56, 1, v56\n"
"v_xor_b32 v56, v56, v0\n"
"v_lshlrev_b32 v56, 4, v56\n"
"v_mul_u32_u24 v55, 11904, v55\n"
"v_add3_u32 v50, v55, v56, s71\n"
"v_add_u32 v50, 4096, v50\n"
"s_lshl_b32 s89, s56, 11\n"
"s_add_u32 m0, s69, 24576\n"
"s_nop 0\n"
"buffer_load_dwordx4 v43, s[40:43], s58 offen offset:0 lds\n"
"buffer_load_dwordx4 v44, s[40:43], s58 offen offset:1024 lds\n"
"buffer_load_dwordx4 v45, s[40:43], s58 offen offset:2048 lds\n"
"buffer_load_dwordx4 v46, s[40:43], s58 offen offset:3072 lds\n"
"s_add_u32 m0, s69, 28672\n"
"s_nop 0\n"
"buffer_load_dwordx4 v47, s[40:43], s58 offen offset:0 lds\n"
"buffer_load_dwordx4 v48, s[40:43], s58 offen offset:1024 lds\n"
"buffer_load_dwordx4 v49, s[40:43], s58 offen offset:2048 lds\n"
"buffer_load_dwordx4 v50, s[40:43], s58 offen offset:3072 lds\n"
"s_add_u32 s58, s58, 786432\n"
"s_add_u32 m0, s70, 98304\n"
"s_nop 0\n"
"buffer_load_dwordx4 v51, s[36:39], s57 offen offset:0 lds\n"
"buffer_load_dwordx4 v52, s[36:39], s57 offen offset:1024 lds\n"
"buffer_load_dwordx4 v53, s[36:39], s57 offen offset:2048 lds\n"
"buffer_load_dwordx4 v54, s[36:39], s57 offen offset:3072 lds\n"
"s_add_u32 m0, s70, 102400\n"
"s_nop 0\n"
"s_add_u32 s74, s57, 40960\n"
"buffer_load_dwordx4 v51, s[36:39], s74 offen offset:0 lds\n"
"buffer_load_dwordx4 v52, s[36:39], s74 offen offset:1024 lds\n"
"buffer_load_dwordx4 v53, s[36:39], s74 offen offset:2048 lds\n"
"buffer_load_dwordx4 v54, s[36:39], s74 offen offset:3072 lds\n"
"s_add_u32 s57, s57, 128\n"
"s_add_u32 m0, s89, 147456\n"
"s_nop 0\n"
"buffer_load_dwordx4 v51, s[36:39], s84 offen offset:0 lds\n"
"buffer_load_dwordx4 v52, s[36:39], s84 offen offset:1024 lds\n"
"s_add_u32 s84, s84, 128\n"
"s_add_u32 m0, s69, 49152\n"
"s_nop 0\n"
"buffer_load_dwordx4 v43, s[40:43], s58 offen offset:0 lds\n"
"buffer_load_dwordx4 v44, s[40:43], s58 offen offset:1024 lds\n"
"buffer_load_dwordx4 v45, s[40:43], s58 offen offset:2048 lds\n"
"buffer_load_dwordx4 v46, s[40:43], s58 offen offset:3072 lds\n"
"s_add_u32 m0, s70, 122880\n"
"s_nop 0\n"
"buffer_load_dwordx4 v51, s[36:39], s57 offen offset:0 lds\n"
"buffer_load_dwordx4 v52, s[36:39], s57 offen offset:1024 lds\n"
"buffer_load_dwordx4 v53, s[36:39], s57 offen offset:2048 lds\n"
"buffer_load_dwordx4 v54, s[36:39], s57 offen offset:3072 lds\n"
"s_add_u32 m0, s70, 126976\n"
"s_nop 0\n"
"s_add_u32 s74, s57, 40960\n"
"buffer_load_dwordx4 v51, s[36:39], s74 offen offset:0 lds\n"
"buffer_load_dwordx4 v52, s[36:39], s74 offen offset:1024 lds\n"
"buffer_load_dwordx4 v53, s[36:39], s74 offen offset:2048 lds\n"
"buffer_load_dwordx4 v54, s[36:39], s74 offen offset:3072 lds\n"
"s_add_u32 s57, s57, 128\n"
"s_lshl_b32 s66, s73, 5\n"
"s_add_u32 s66, s66, s72\n"
"s_mul_i32 s67, s66, 393216\n"
"s_add_u32 s44, %[klo], s67\n"
"s_addc_u32 s45, %[khi], 0\n"
"s_mov_b32 s46, 393216\n"
"s_mov_b32 s47, 0x20000\n"
"s_add_u32 s48, %[vlo], s67\n"
"s_addc_u32 s49, %[vhi], 0\n"
"s_mov_b32 s50, 393216\n"
"s_mov_b32 s51, 0x20000\n"
"s_mov_b32 s52, %[olo]\n"
"s_mov_b32 s53, %[ohi]\n"
"s_mov_b32 s54, 23592960\n"
"s_mov_b32 s55, 0x20000\n"
"s_mul_i32 s68, s72, 720\n"
"s_mul_i32 s66, s73, 15\n"
"s_add_u32 s68, s68, s66\n"
"s_lshl_b32 s66, s56, 2\n"
"s_add_u32 s68, s68, s66\n"
"s_lshl_b32 s68, s68, 10\n"
"s_mov_b32 s59, 0\n"
"s_mov_b32 s60, 0\n"
"s_mov_b32 s62, 0x3dd53b95\n"
"s_mov_b32 s63, 0x3dd53b95\n"
"s_mov_b32 s64, 0x41000000\n"
"v_lshlrev_b32 v13, 7, v0\n"
"s_lshl_b32 s66, s56, 13\n"
"v_add_u32 v13, s66, v13\n"
"s_mov_b32 s85, 73728\n"
"s_mov_b32 s86, 73728\n"
"v_lshrrev_b32 v55, 4, v0\n"
"v_and_b32 v56, 15, v0\n"
"v_bfe_u32 v57, v0, 2, 2\n"
"v_and_b32 v58, 3, v0\n"
"v_lshl_add_u32 v59, v55, 3, v57\n"
"v_mul_u32_u24 v59, 384, v59\n"
"v_lshl_add_u32 v59, v58, 3, v59\n"
"v_add_u32 v59, 24576, v59\n"
"v_lshrrev_b32 v60, 1, v57\n"
"v_and_b32 v61, 1, v55\n"
"v_lshl_add_u32 v60, v61, 1, v60\n"
"v_xor_b32 v61, 0, v60\n"
"v_lshl_add_u32 v37, v61, 5, v59\n"
"v_xor_b32 v61, 1, v60\n"
"v_lshl_add_u32 v38, v61, 5, v59\n"
"v_xor_b32 v61, 2, v60\n"
"v_lshl_add_u32 v39, v61, 5, v59\n"
"v_xor_b32 v61, 3, v60\n"
"v_lshl_add_u32 v40, v61, 5, v59\n"
"v_lshlrev_b32 v59, 7, v56\n"
"v_add_u32 v59, s70, v59\n"
"v_add_u32 v59, 98304, v59\n"
"v_bfe_u32 v60, v56, 1, 3\n"
"v_xor_b32 v61, v55, v60\n"
"v_lshl_add_u32 v41, v61, 4, v59\n"
"v_xor_b32 v61, 4, v61\n"
"v_lshl_add_u32 v42, v61, 4, v59\n"
"v_mul_u32_u24 v59, 384, v56\n"
"v_add_u32 v59, 73728, v59\n"
"v_xor_b32 v61, v55, v60\n"
"v_lshl_add_u32 v1, v61, 4, v59\n"
"v_xor_b32 v61, 4, v61\n"
"v_lshl_add_u32 v2, v61, 4, v59\n"
"v_add_u32 v3, 36864, v1\n"
"v_add_u32 v4, 36864, v2\n"
"v_lshl_add_u32 v59, v55, 2, v57\n"
"v_bfe_u32 v60, v59, 1, 2\n"
"v_mul_u32_u24 v59, 384, v59\n"
"v_lshl_add_u32 v59, v58, 3, v59\n"
"v_xor_b32 v61, 0, v60\n"
"v_lshl_add_u32 v5, v61, 5, v59\n"
"v_add_u32 v9, 36864, v5\n"
"v_xor_b32 v61, 1, v60\n"
"v_lshl_add_u32 v6, v61, 5, v59\n"
"v_add_u32 v10, 36864, v6\n"
"v_xor_b32 v61, 2, v60\n"
"v_lshl_add_u32 v7, v61, 5, v59\n"
"v_add_u32 v11, 36864, v7\n"
"v_xor_b32 v61, 3, v60\n"
"v_lshl_add_u32 v8, v61, 5, v59\n"
"v_add_u32 v12, 36864, v8\n"
"s_lshl_b32 s90, s56, 12\n"
"s_lshl_b32 s66, s56, 8\n"
"v_add_u32 v55, s66, v0\n"
"v_mul_u32_u24 v55, 1366, v55\n"
"v_bfe_u32 v55, v55, 16, 2\n"
"v_lshlrev_b32 v55, 1, v55\n"
"v_xor_b32 v55, v55, v0\n"
"v_lshlrev_b32 v55, 4, v55\n"
"v_add_u32 v25, s90, v55\n"
"v_add_u32 v55, s66, v0\n"
"v_add_u32 v55, 64, v55\n"
"v_mul_u32_u24 v55, 1366, v55\n"
"v_bfe_u32 v55, v55, 16, 2\n"
"v_lshlrev_b32 v55, 1, v55\n"
"v_xor_b32 v55, v55, v0\n"
"v_lshlrev_b32 v55, 4, v55\n"
"v_add_u32 v26, s90, v55\n"
"v_add_u32 v55, s66, v0\n"
"v_add_u32 v55, 128, v55\n"
"v_mul_u32_u24 v55, 1366, v55\n"
"v_bfe_u32 v55, v55, 16, 2\n"
"v_lshlrev_b32 v55, 1, v55\n"
"v_xor_b32 v55, v55, v0\n"
"v_lshlrev_b32 v55, 4, v55\n"
"v_add_u32 v27, s90, v55\n"
"v_add_u32 v55, s66, v0\n"
"v_add_u32 v55, 192, v55\n"
"v_mul_u32_u24 v55, 1366, v55\n"
"v_bfe_u32 v55, v55, 16, 2\n"
"v_lshlrev_b32 v55, 1, v55\n"
"v_xor_b32 v55, v55, v0\n"
"v_lshlrev_b32 v55, 4, v55\n"
"v_add_u32 v28, s90, v55\n"
"s_mov_b32 s60, 24576\n"
"s_mov_b32 s92, %[plo]\n"
"s_mov_b32 s93, %[phi]\n"
"s_mov_b32 s94, 7864320\n"
"s_mov_b32 s95, 0x20000\n"
"s_mul_i32 s87, %[bid], 30720\n"
"v_add_u32 v15, 24576, v13\n"
"v_cmp_gt_u32 vcc, 48, v0\n"
"s_cmp_eq_u32 s56, 0\n"
"s_cselect_b64 s[76:77], -1, 0\n"
"s_and_b64 vcc, vcc, s[76:77]\n"
"v_mov_b32 v55, 0x7fffff00\n"
"s_nop 1\n"
"v_cndmask_b32 v15, v55, v15, vcc\n"
"s_waitcnt vmcnt(12)\n"
"s_barrier\n"
"ds_read_b128 a[0:3], v41 offset:0\n"
"ds_read_b128 a[4:7], v41 offset:2048\n"
"ds_read_b128 a[8:11], v41 offset:4096\n"
"ds_read_b128 a[12:15], v41 offset:6144\n"
"ds_read_b64_tr_b16 a[64:65], v40 offset:256\n"
"ds_read_b64_tr_b16 a[66:67], v40 offset:1792\n"
"ds_read_b128 a[16:19], v42 offset:0\n"
"ds_read_b128 a[20:23], v42 offset:2048\n"
"ds_read_b128 a[24:27], v42 offset:4096\n"
"ds_read_b128 a[28:31], v42 offset:6144\n"
"ds_read_b64_tr_b16 a[68:69], v40 offset:12544\n"
"ds_read_b64_tr_b16 a[70:71], v40 offset:14080\n"
"ds_read_b64_tr_b16 a[72:73], v39 offset:256\n"
"ds_read_b64_tr_b16 a[74:75], v39 offset:1792\n"
"s_waitcnt lgkmcnt(0)\n"
"v_mfma_f32_16x16x32_bf16 v[228:231], a[64:67], a[0:3], 0\n"
"ds_read_b64_tr_b16 a[76:77], v39 offset:12544\n"
"ds_read_b64_tr_b16 a[78:79], v39 offset:14080\n"
"v_mfma_f32_16x16x32_bf16 v[236:239], a[64:67], a[4:7], 0\n"
"ds_read_b64_tr_b16 a[80:81], v38 offset:256\n"
"ds_read_b64_tr_b16 a[82:83], v38 offset:1792\n"
"v_mfma_f32_16x16x32_bf16 v[244:247], a[64:67], a[8:11], 0\n"
"ds_read_b64_tr_b16 a[84:85], v38 offset:12544\n"
"ds_read_b64_tr_b16 a[86:87], v38 offset:14080\n"
"v_mfma_f32_16x16x32_bf16 v[252:255], a[64:67], a[12:15], 0\n"
"s_add_u32 m0, s69, 53248\n"
"v_mfma_f32_16x16x32_bf16 v[228:231], a[68:71], a[16:19], v[228:231]\n"
"buffer_load_dwordx4 v47, s[40:43], s58 offen offset:0 lds\n"
"ds_read_b64_tr_b16 a[88:89], v37 offset:256\n"
"v_mfma_f32_16x16x32_bf16 v[236:239], a[68:71], a[20:23], v[236:239]\n"
"buffer_load_dwordx4 v48, s[40:43], s58 offen offset:1024 lds\n"
"ds_read_b64_tr_b16 a[90:91], v37 offset:1792\n"
"v_mfma_f32_16x16x32_bf16 v[244:247], a[68:71], a[24:27], v[244:247]\n"
"ds_read_b64_tr_b16 a[92:93], v37 offset:12544\n"
"ds_read_b64_tr_b16 a[94:95], v37 offset:14080\n"
"v_mfma_f32_16x16x32_bf16 v[252:255], a[68:71], a[28:31], v[252:255]\n"
"v_mfma_f32_16x16x32_bf16 v[224:227], a[72:75], a[0:3], 0\n"
"ds_read_b64_tr_b16 a[64:65], v40 offset:128\n"
"ds_read_b64_tr_b16 a[66:67], v40 offset:1664\n"
"v_mfma_f32_16x16x32_bf16 v[232:235], a[72:75], a[4:7], 0\n"
"v_mfma_f32_16x16x32_bf16 v[240:243], a[72:75], a[8:11], 0\n"
"buffer_load_dwordx4 v49, s[40:43], s58 offen offset:2048 lds\n"
"v_mfma_f32_16x16x32_bf16 v[248:251], a[72:75], a[12:15], 0\n"
"s_waitcnt lgkmcnt(6)\n"
"v_mfma_f32_16x16x32_bf16 v[224:227], a[76:79], a[16:19], v[224:227]\n"
"ds_read_b64_tr_b16 a[68:69], v40 offset:12416\n"
"ds_read_b64_tr_b16 a[70:71], v40 offset:13952\n"
"v_mfma_f32_16x16x32_bf16 v[232:235], a[76:79], a[20:23], v[232:235]\n"
"v_mfma_f32_16x16x32_bf16 v[240:243], a[76:79], a[24:27], v[240:243]\n"
"v_mfma_f32_16x16x32_bf16 v[248:251], a[76:79], a[28:31], v[248:251]\n"
"v_mfma_f32_16x16x32_bf16 v[196:199], a[80:83], a[0:3], 0\n"
"buffer_load_dwordx4 v50, s[40:43], s58 offen offset:3072 lds\n"
"ds_read_b64_tr_b16 a[72:73], v39 offset:128\n"
"v_mfma_f32_16x16x32_bf16 v[204:207], a[80:83], a[4:7], 0\n"
"ds_read_b64_tr_b16 a[74:75], v39 offset:1664\n"
"v_mfma_f32_16x16x32_bf16 v[212:215], a[80:83], a[8:11], 0\n"
"v_mfma_f32_16x16x32_bf16 v[220:223], a[80:83], a[12:15], 0\n"
"v_mfma_f32_16x16x32_bf16 v[196:199], a[84:87], a[16:19], v[196:199]\n"
"ds_read_b64_tr_b16 a[76:77], v39 offset:12416\n"
"ds_read_b64_tr_b16 a[78:79], v39 offset:13952\n"
"v_mfma_f32_16x16x32_bf16 v[204:207], a[84:87], a[20:23], v[204:207]\n"
"s_add_u32 s58, s58, 786432\n"
"s_add_u32 m0, s89, 153600\n"
"v_mfma_f32_16x16x32_bf16 v[212:215], a[84:87], a[24:27], v[212:215]\n"
"buffer_load_dwordx4 v51, s[36:39], s84 offen offset:0 lds\n"
"v_mfma_f32_16x16x32_bf16 v[220:223], a[84:87], a[28:31], v[220:223]\n"
"s_waitcnt lgkmcnt(6)\n"
"v_mfma_f32_16x16x32_bf16 v[192:195], a[88:91], a[0:3], 0\n"
"ds_read_b64_tr_b16 a[80:81], v38 offset:128\n"
"ds_read_b64_tr_b16 a[82:83], v38 offset:1664\n"
"v_mfma_f32_16x16x32_bf16 v[200:203], a[88:91], a[4:7], 0\n"
"v_mfma_f32_16x16x32_bf16 v[208:211], a[88:91], a[8:11], 0\n"
"buffer_load_dwordx4 v52, s[36:39], s84 offen offset:1024 lds\n"
"v_mfma_f32_16x16x32_bf16 v[216:219], a[88:91], a[12:15], 0\n"
"v_mfma_f32_16x16x32_bf16 v[192:195], a[92:95], a[16:19], v[192:195]\n"
"ds_read_b64_tr_b16 a[84:85], v38 offset:12416\n"
"ds_read_b64_tr_b16 a[86:87], v38 offset:13952\n"
"v_mfma_f32_16x16x32_bf16 v[200:203], a[92:95], a[20:23], v[200:203]\n"
"v_mfma_f32_16x16x32_bf16 v[208:211], a[92:95], a[24:27], v[208:211]\n"
"v_mfma_f32_16x16x32_bf16 v[216:219], a[92:95], a[28:31], v[216:219]\n"
"v_mfma_f32_16x16x32_bf16 v[164:167], a[64:67], a[0:3], 0\n"
"s_add_u32 s84, s84, 128\n"
"s_add_u32 m0, s70, 98304\n"
"v_mfma_f32_16x16x32_bf16 v[172:175], a[64:67], a[4:7], 0\n"
"buffer_load_dwordx4 v51, s[36:39], s57 offen offset:0 lds\n"
"ds_read_b64_tr_b16 a[88:89], v37 offset:128\n"
"v_mfma_f32_16x16x32_bf16 v[180:183], a[64:67], a[8:11], 0\n"
"ds_read_b64_tr_b16 a[90:91], v37 offset:1664\n"
"v_mfma_f32_16x16x32_bf16 v[188:191], a[64:67], a[12:15], 0\n"
"s_waitcnt lgkmcnt(6)\n"
"v_mfma_f32_16x16x32_bf16 v[164:167], a[68:71], a[16:19], v[164:167]\n"
"ds_read_b64_tr_b16 a[92:93], v37 offset:12416\n"
"ds_read_b64_tr_b16 a[94:95], v37 offset:13952\n"
"v_mfma_f32_16x16x32_bf16 v[172:175], a[68:71], a[20:23], v[172:175]\n"
"buffer_load_dwordx4 v52, s[36:39], s57 offen offset:1024 lds\n"
"v_mfma_f32_16x16x32_bf16 v[180:183], a[68:71], a[24:27], v[180:183]\n"
"v_mfma_f32_16x16x32_bf16 v[188:191], a[68:71], a[28:31], v[188:191]\n"
"v_mfma_f32_16x16x32_bf16 v[160:163], a[72:75], a[0:3], 0\n"
"ds_read_b64_tr_b16 a[64:65], v40 offset:0\n"
"ds_read_b64_tr_b16 a[66:67], v40 offset:1536\n"
"v_mfma_f32_16x16x32_bf16 v[168:171], a[72:75], a[4:7], 0\n"
"v_mfma_f32_16x16x32_bf16 v[176:179], a[72:75], a[8:11], 0\n"
"buffer_load_dwordx4 v53, s[36:39], s57 offen offset:2048 lds\n"
"v_mfma_f32_16x16x32_bf16 v[184:187], a[72:75], a[12:15], 0\n"
"v_mfma_f32_16x16x32_bf16 v[160:163], a[76:79], a[16:19], v[160:163]\n"
"ds_read_b64_tr_b16 a[68:69], v40 offset:12288\n"
"ds_read_b64_tr_b16 a[70:71], v40 offset:13824\n"
"v_mfma_f32_16x16x32_bf16 v[168:171], a[76:79], a[20:23], v[168:171]\n"
"v_mfma_f32_16x16x32_bf16 v[176:179], a[76:79], a[24:27], v[176:179]\n"
"v_mfma_f32_16x16x32_bf16 v[184:187], a[76:79], a[28:31], v[184:187]\n"
"s_waitcnt lgkmcnt(6)\n"
"v_mfma_f32_16x16x32_bf16 v[132:135], a[80:83], a[0:3], 0\n"
"buffer_load_dwordx4 v54, s[36:39], s57 offen offset:3072 lds\n"
"ds_read_b64_tr_b16 a[72:73], v39 offset:0\n"
"v_mfma_f32_16x16x32_bf16 v[140:143], a[80:83], a[4:7], 0\n"
"ds_read_b64_tr_b16 a[74:75], v39 offset:1536\n"
"v_mfma_f32_16x16x32_bf16 v[148:151], a[80:83], a[8:11], 0\n"
"v_mfma_f32_16x16x32_bf16 v[156:159], a[80:83], a[12:15], 0\n"
"v_mfma_f32_16x16x32_bf16 v[132:135], a[84:87], a[16:19], v[132:135]\n"
"ds_read_b64_tr_b16 a[76:77], v39 offset:12288\n"
"ds_read_b64_tr_b16 a[78:79], v39 offset:13824\n"
"v_mfma_f32_16x16x32_bf16 v[140:143], a[84:87], a[20:23], v[140:143]\n"
"s_add_u32 m0, s70, 102400\n"
"v_mfma_f32_16x16x32_bf16 v[148:151], a[84:87], a[24:27], v[148:151]\n"
"s_add_u32 s74, s57, 40960\n"
"buffer_load_dwordx4 v51, s[36:39], s74 offen offset:0 lds\n"
"v_mfma_f32_16x16x32_bf16 v[156:159], a[84:87], a[28:31], v[156:159]\n"
"v_mfma_f32_16x16x32_bf16 v[128:131], a[88:91], a[0:3], 0\n"
"ds_read_b64_tr_b16 a[80:81], v38 offset:0\n"
"ds_read_b64_tr_b16 a[82:83], v38 offset:1536\n"
"v_mfma_f32_16x16x32_bf16 v[136:139], a[88:91], a[4:7], 0\n"
"v_mfma_f32_16x16x32_bf16 v[144:147], a[88:91], a[8:11], 0\n"
"buffer_load_dwordx4 v52, s[36:39], s74 offen offset:1024 lds\n"
"v_mfma_f32_16x16x32_bf16 v[152:155], a[88:91], a[12:15], 0\n"
"s_waitcnt lgkmcnt(6)\n"
"v_mfma_f32_16x16x32_bf16 v[128:131], a[92:95], a[16:19], v[128:131]\n"
"ds_read_b64_tr_b16 a[84:85], v38 offset:12288\n"
"ds_read_b64_tr_b16 a[86:87], v38 offset:13824\n"
"v_mfma_f32_16x16x32_bf16 v[136:139], a[92:95], a[20:23], v[136:139]\n"
"v_mfma_f32_16x16x32_bf16 v[144:147], a[92:95], a[24:27], v[144:147]\n"
"v_mfma_f32_16x16x32_bf16 v[152:155], a[92:95], a[28:31], v[152:155]\n"
"v_mfma_f32_16x16x32_bf16 v[100:103], a[64:67], a[0:3], 0\n"
"buffer_load_dwordx4 v53, s[36:39], s74 offen offset:2048 lds\n"
"ds_read_b64_tr_b16 a[88:89], v37 offset:0\n"
"v_mfma_f32_16x16x32_bf16 v[108:111], a[64:67], a[4:7], 0\n"
"ds_read_b64_tr_b16 a[90:91], v37 offset:1536\n"
"v_mfma_f32_16x16x32_bf16 v[116:119], a[64:67], a[8:11], 0\n"
"v_mfma_f32_16x16x32_bf16 v[124:127], a[64:67], a[12:15], 0\n"
"v_mfma_f32_16x16x32_bf16 v[100:103], a[68:71], a[16:19], v[100:103]\n"
"ds_read_b64_tr_b16 a[92:93], v37 offset:12288\n"
"ds_read_b64_tr_b16 a[94:95], v37 offset:13824\n"
"v_mfma_f32_16x16x32_bf16 v[108:111], a[68:71], a[20:23], v[108:111]\n"
"buffer_load_dwordx4 v54, s[36:39], s74 offen offset:3072 lds\n"
"v_mfma_f32_16x16x32_bf16 v[116:119], a[68:71], a[24:27], v[116:119]\n"
"v_mfma_f32_16x16x32_bf16 v[124:127], a[68:71], a[28:31], v[124:127]\n"
"s_waitcnt lgkmcnt(6)\n"
"v_mfma_f32_16x16x32_bf16 v[96:99], a[72:75], a[0:3], 0\n"
"v_mfma_f32_16x16x32_bf16 v[104:107], a[72:75], a[4:7], 0\n"
"v_mfma_f32_16x16x32_bf16 v[112:115], a[72:75], a[8:11], 0\n"
"s_add_u32 s57, s57, 128\n"
"v_mfma_f32_16x16x32_bf16 v[120:123], a[72:75], a[12:15], 0\n"
"v_mfma_f32_16x16x32_bf16 v[96:99], a[76:79], a[16:19], v[96:99]\n"
"s_waitcnt vmcnt(8) lgkmcnt(0)\n"
"s_barrier\n"
"v_mfma_f32_16x16x32_bf16 v[104:107], a[76:79], a[20:23], v[104:107]\n"
"ds_read_b128 a[32:35], v41 offset:24576\n"
"ds_read_b128 a[36:39], v41 offset:26624\n"
"v_mfma_f32_16x16x32_bf16 v[112:115], a[76:79], a[24:27], v[112:115]\n"
"s_add_u32 m0, s69, 24576\n"
"ds_read_b128 a[40:43], v41 offset:28672\n"
"v_mfma_f32_16x16x32_bf16 v[120:123], a[76:79], a[28:31], v[120:123]\n"
"buffer_load_dwordx4 v43, s[40:43], s58 offen offset:0 lds\n"
"ds_read_b128 a[44:47], v41 offset:30720\n"
"v_mfma_f32_16x16x32_bf16 v[68:71], a[80:83], a[0:3], 0\n"
"ds_read_b64_tr_b16 a[64:65], v40 offset:24832\n"
"ds_read_b64_tr_b16 a[66:67], v40 offset:26368\n"
"v_mfma_f32_16x16x32_bf16 v[76:79], a[80:83], a[4:7], 0\n"
"ds_read_b128 a[48:51], v42 offset:24576\n"
"ds_read_b128 a[52:55], v42 offset:26624\n"
"v_mfma_f32_16x16x32_bf16 v[84:87], a[80:83], a[8:11], 0\n"
"buffer_load_dwordx4 v44, s[40:43], s58 offen offset:1024 lds\n"
"ds_read_b128 a[56:59], v42 offset:28672\n"
"v_mfma_f32_16x16x32_bf16 v[92:95], a[80:83], a[12:15], 0\n"
"ds_read_b128 a[60:63], v42 offset:30720\n"
"v_mfma_f32_16x16x32_bf16 v[68:71], a[84:87], a[16:19], v[68:71]\n"
"ds_read_b64_tr_b16 a[68:69], v40 offset:37120\n"
"ds_read_b64_tr_b16 a[70:71], v40 offset:38656\n"
"v_mfma_f32_16x16x32_bf16 v[76:79], a[84:87], a[20:23], v[76:79]\n"
"ds_read_b64_tr_b16 a[72:73], v39 offset:24832\n"
"ds_read_b64_tr_b16 a[74:75], v39 offset:26368\n"
"v_mfma_f32_16x16x32_bf16 v[84:87], a[84:87], a[24:27], v[84:87]\n"
"buffer_load_dwordx4 v45, s[40:43], s58 offen offset:2048 lds\n"
"v_mfma_f32_16x16x32_bf16 v[92:95], a[84:87], a[28:31], v[92:95]\n"
"v_mfma_f32_16x16x32_bf16 v[64:67], a[88:91], a[0:3], 0\n"
"v_mfma_f32_16x16x32_bf16 v[72:75], a[88:91], a[4:7], 0\n"
"v_mfma_f32_16x16x32_bf16 v[80:83], a[88:91], a[8:11], 0\n"
"buffer_load_dwordx4 v46, s[40:43], s58 offen offset:3072 lds\n"
"v_mfma_f32_16x16x32_bf16 v[88:91], a[88:91], a[12:15], 0\n"
"v_mfma_f32_16x16x32_bf16 v[64:67], a[92:95], a[16:19], v[64:67]\n"
"v_mfma_f32_16x16x32_bf16 v[72:75], a[92:95], a[20:23], v[72:75]\n"
"v_mfma_f32_16x16x32_bf16 v[80:83], a[92:95], a[24:27], v[80:83]\n"
"v_mfma_f32_16x16x32_bf16 v[88:91], a[92:95], a[28:31], v[88:91]\n"
"s_mov_b32 s61, 3\n"
"K1A_QL:\n"
"s_waitcnt lgkmcnt(0)\n"
"v_mfma_f32_16x16x32_bf16 v[228:231], a[64:67], a[32:35], v[228:231]\n"
"ds_read_b64_tr_b16 a[76:77], v39 offset:37120\n"
"ds_read_b64_tr_b16 a[78:79], v39 offset:38656\n"
"v_mfma_f32_16x16x32_bf16 v[236:239], a[64:67], a[36:39], v[236:239]\n"
"ds_read_b64_tr_b16 a[80:81], v38 offset:24832\n"
"ds_read_b64_tr_b16 a[82:83], v38 offset:26368\n"
"v_mfma_f32_16x16x32_bf16 v[244:247], a[64:67], a[40:43], v[244:247]\n"
"ds_read_b64_tr_b16 a[84:85], v38 offset:37120\n"
"ds_read_b64_tr_b16 a[86:87], v38 offset:38656\n"
"v_mfma_f32_16x16x32_bf16 v[252:255], a[64:67], a[44:47], v[252:255]\n"
"s_add_u32 m0, s69, 28672\n"
"v_mfma_f32_16x16x32_bf16 v[228:231], a[68:71], a[48:51], v[228:231]\n"
"buffer_load_dwordx4 v47, s[40:43], s58 offen offset:0 lds\n"
"ds_read_b64_tr_b16 a[88:89], v37 offset:24832\n"
"v_mfma_f32_16x16x32_bf16 v[236:239], a[68:71], a[52:55], v[236:239]\n"
"buffer_load_dwordx4 v48, s[40:43], s58 offen offset:1024 lds\n"
"ds_read_b64_tr_b16 a[90:91], v37 offset:26368\n"
"v_mfma_f32_16x16x32_bf16 v[244:247], a[68:71], a[56:59], v[244:247]\n"
"ds_read_b64_tr_b16 a[92:93], v37 offset:37120\n"
"ds_read_b64_tr_b16 a[94:95], v37 offset:38656\n"
"v_mfma_f32_16x16x32_bf16 v[252:255], a[68:71], a[60:63], v[252:255]\n"
"v_mfma_f32_16x16x32_bf16 v[224:227], a[72:75], a[32:35], v[224:227]\n"
"ds_read_b64_tr_b16 a[64:65], v40 offset:24704\n"
"ds_read_b64_tr_b16 a[66:67], v40 offset:26240\n"
"v_mfma_f32_16x16x32_bf16 v[232:235], a[72:75], a[36:39], v[232:235]\n"
"v_mfma_f32_16x16x32_bf16 v[240:243], a[72:75], a[40:43], v[240:243]\n"
"buffer_load_dwordx4 v49, s[40:43], s58 offen offset:2048 lds\n"
"v_mfma_f32_16x16x32_bf16 v[248:251], a[72:75], a[44:47], v[248:251]\n"
"s_waitcnt lgkmcnt(6)\n"
"v_mfma_f32_16x16x32_bf16 v[224:227], a[76:79], a[48:51], v[224:227]\n"
"ds_read_b64_tr_b16 a[68:69], v40 offset:36992\n"
"ds_read_b64_tr_b16 a[70:71], v40 offset:38528\n"
"v_mfma_f32_16x16x32_bf16 v[232:235], a[76:79], a[52:55], v[232:235]\n"
"v_mfma_f32_16x16x32_bf16 v[240:243], a[76:79], a[56:59], v[240:243]\n"
"v_mfma_f32_16x16x32_bf16 v[248:251], a[76:79], a[60:63], v[248:251]\n"
"v_mfma_f32_16x16x32_bf16 v[196:199], a[80:83], a[32:35], v[196:199]\n"
"buffer_load_dwordx4 v50, s[40:43], s58 offen offset:3072 lds\n"
"ds_read_b64_tr_b16 a[72:73], v39 offset:24704\n"
"v_mfma_f32_16x16x32_bf16 v[204:207], a[80:83], a[36:39], v[204:207]\n"
"ds_read_b64_tr_b16 a[74:75], v39 offset:26240\n"
"v_mfma_f32_16x16x32_bf16 v[212:215], a[80:83], a[40:43], v[212:215]\n"
"v_mfma_f32_16x16x32_bf16 v[220:223], a[80:83], a[44:47], v[220:223]\n"
"v_mfma_f32_16x16x32_bf16 v[196:199], a[84:87], a[48:51], v[196:199]\n"
"ds_read_b64_tr_b16 a[76:77], v39 offset:36992\n"
"ds_read_b64_tr_b16 a[78:79], v39 offset:38528\n"
"v_mfma_f32_16x16x32_bf16 v[204:207], a[84:87], a[52:55], v[204:207]\n"
"s_add_u32 s58, s58, 786432\n"
"s_add_u32 m0, s89, 147456\n"
"v_mfma_f32_16x16x32_bf16 v[212:215], a[84:87], a[56:59], v[212:215]\n"
"buffer_load_dwordx4 v51, s[36:39], s84 offen offset:0 lds\n"
"v_mfma_f32_16x16x32_bf16 v[220:223], a[84:87], a[60:63], v[220:223]\n"
"s_waitcnt lgkmcnt(6)\n"
"v_mfma_f32_16x16x32_bf16 v[192:195], a[88:91], a[32:35], v[192:195]\n"
"ds_read_b64_tr_b16 a[80:81], v38 offset:24704\n"
"ds_read_b64_tr_b16 a[82:83], v38 offset:26240\n"
"v_mfma_f32_16x16x32_bf16 v[200:203], a[88:91], a[36:39], v[200:203]\n"
"v_mfma_f32_16x16x32_bf16 v[208:211], a[88:91], a[40:43], v[208:211]\n"
"buffer_load_dwordx4 v52, s[36:39], s84 offen offset:1024 lds\n"
"v_mfma_f32_16x16x32_bf16 v[216:219], a[88:91], a[44:47], v[216:219]\n"
"v_mfma_f32_16x16x32_bf16 v[192:195], a[92:95], a[48:51], v[192:195]\n"
"ds_read_b64_tr_b16 a[84:85], v38 offset:36992\n"
"ds_read_b64_tr_b16 a[86:87], v38 offset:38528\n"
"v_mfma_f32_16x16x32_bf16 v[200:203], a[92:95], a[52:55], v[200:203]\n"
"v_mfma_f32_16x16x32_bf16 v[208:211], a[92:95], a[56:59], v[208:211]\n"
"v_mfma_f32_16x16x32_bf16 v[216:219], a[92:95], a[60:63], v[216:219]\n"
"v_mfma_f32_16x16x32_bf16 v[164:167], a[64:67], a[32:35], v[164:167]\n"
"s_add_u32 s84, s84, 128\n"
"s_add_u32 m0, s70, 122880\n"
"v_mfma_f32_16x16x32_bf16 v[172:175], a[64:67], a[36:39], v[172:175]\n"
"buffer_load_dwordx4 v51, s[36:39], s57 offen offset:0 lds\n"
"ds_read_b64_tr_b16 a[88:89], v37 offset:24704\n"
"v_mfma_f32_16x16x32_bf16 v[180:183], a[64:67], a[40:43], v[180:183]\n"
"ds_read_b64_tr_b16 a[90:91], v37 offset:26240\n"
"v_mfma_f32_16x16x32_bf16 v[188:191], a[64:67], a[44:47], v[188:191]\n"
"s_waitcnt lgkmcnt(6)\n"
"v_mfma_f32_16x16x32_bf16 v[164:167], a[68:71], a[48:51], v[164:167]\n"
"ds_read_b64_tr_b16 a[92:93], v37 offset:36992\n"
"ds_read_b64_tr_b16 a[94:95], v37 offset:38528\n"
"v_mfma_f32_16x16x32_bf16 v[172:175], a[68:71], a[52:55], v[172:175]\n"
"buffer_load_dwordx4 v52, s[36:39], s57 offen offset:1024 lds\n"
"v_mfma_f32_16x16x32_bf16 v[180:183], a[68:71], a[56:59], v[180:183]\n"
"v_mfma_f32_16x16x32_bf16 v[188:191], a[68:71], a[60:63], v[188:191]\n"
"v_mfma_f32_16x16x32_bf16 v[160:163], a[72:75], a[32:35], v[160:163]\n"
"ds_read_b64_tr_b16 a[64:65], v40 offset:24576\n"
"ds_read_b64_tr_b16 a[66:67], v40 offset:26112\n"
"v_mfma_f32_16x16x32_bf16 v[168:171], a[72:75], a[36:39], v[168:171]\n"
"v_mfma_f32_16x16x32_bf16 v[176:179], a[72:75], a[40:43], v[176:179]\n"
"buffer_load_dwordx4 v53, s[36:39], s57 offen offset:2048 lds\n"
"v_mfma_f32_16x16x32_bf16 v[184:187], a[72:75], a[44:47], v[184:187]\n"
"v_mfma_f32_16x16x32_bf16 v[160:163], a[76:79], a[48:51], v[160:163]\n"
"ds_read_b64_tr_b16 a[68:69], v40 offset:36864\n"
"ds_read_b64_tr_b16 a[70:71], v40 offset:38400\n"
"v_mfma_f32_16x16x32_bf16 v[168:171], a[76:79], a[52:55], v[168:171]\n"
"v_mfma_f32_16x16x32_bf16 v[176:179], a[76:79], a[56:59], v[176:179]\n"
"v_mfma_f32_16x16x32_bf16 v[184:187], a[76:79], a[60:63], v[184:187]\n"
"s_waitcnt lgkmcnt(6)\n"
"v_mfma_f32_16x16x32_bf16 v[132:135], a[80:83], a[32:35], v[132:135]\n"
"buffer_load_dwordx4 v54, s[36:39], s57 offen offset:3072 lds\n"
"ds_read_b64_tr_b16 a[72:73], v39 offset:24576\n"
"v_mfma_f32_16x16x32_bf16 v[140:143], a[80:83], a[36:39], v[140:143]\n"
"ds_read_b64_tr_b16 a[74:75], v39 offset:26112\n"
"v_mfma_f32_16x16x32_bf16 v[148:151], a[80:83], a[40:43], v[148:151]\n"
"v_mfma_f32_16x16x32_bf16 v[156:159], a[80:83], a[44:47], v[156:159]\n"
"v_mfma_f32_16x16x32_bf16 v[132:135], a[84:87], a[48:51], v[132:135]\n"
"ds_read_b64_tr_b16 a[76:77], v39 offset:36864\n"
"ds_read_b64_tr_b16 a[78:79], v39 offset:38400\n"
"v_mfma_f32_16x16x32_bf16 v[140:143], a[84:87], a[52:55], v[140:143]\n"
"s_add_u32 m0, s70, 126976\n"
"v_mfma_f32_16x16x32_bf16 v[148:151], a[84:87], a[56:59], v[148:151]\n"
"s_add_u32 s74, s57, 40960\n"
"buffer_load_dwordx4 v51, s[36:39], s74 offen offset:0 lds\n"
"v_mfma_f32_16x16x32_bf16 v[156:159], a[84:87], a[60:63], v[156:159]\n"
"v_mfma_f32_16x16x32_bf16 v[128:131], a[88:91], a[32:35], v[128:131]\n"
"ds_read_b64_tr_b16 a[80:81], v38 offset:24576\n"
"ds_read_b64_tr_b16 a[82:83], v38 offset:26112\n"
"v_mfma_f32_16x16x32_bf16 v[136:139], a[88:91], a[36:39], v[136:139]\n"
"v_mfma_f32_16x16x32_bf16 v[144:147], a[88:91], a[40:43], v[144:147]\n"
"buffer_load_dwordx4 v52, s[36:39], s74 offen offset:1024 lds\n"
"v_mfma_f32_16x16x32_bf16 v[152:155], a[88:91], a[44:47], v[152:155]\n"
"s_waitcnt lgkmcnt(6)\n"
"v_mfma_f32_16x16x32_bf16 v[128:131], a[92:95], a[48:51], v[128:131]\n"
"ds_read_b64_tr_b16 a[84:85], v38 offset:36864\n"
"ds_read_b64_tr_b16 a[86:87], v38 offset:38400\n"
"v_mfma_f32_16x16x32_bf16 v[136:139], a[92:95], a[52:55], v[136:139]\n"
"v_mfma_f32_16x16x32_bf16 v[144:147], a[92:95], a[56:59], v[144:147]\n"
"v_mfma_f32_16x16x32_bf16 v[152:155], a[92:95], a[60:63], v[152:155]\n"
"v_mfma_f32_16x16x32_bf16 v[100:103], a[64:67], a[32:35], v[100:103]\n"
"buffer_load_dwordx4 v53, s[36:39], s74 offen offset:2048 lds\n"
"ds_read_b64_tr_b16 a[88:89], v37 offset:24576\n"
"v_mfma_f32_16x16x32_bf16 v[108:111], a[64:67], a[36:39], v[108:111]\n"
"ds_read_b64_tr_b16 a[90:91], v37 offset:26112\n"
"v_mfma_f32_16x16x32_bf16 v[116:119], a[64:67], a[40:43], v[116:119]\n"
"v_mfma_f32_16x16x32_bf16 v[124:127], a[64:67], a[44:47], v[124:127]\n"
"v_mfma_f32_16x16x32_bf16 v[100:103], a[68:71], a[48:51], v[100:103]\n"
"ds_read_b64_tr_b16 a[92:93], v37 offset:36864\n"
"ds_read_b64_tr_b16 a[94:95], v37 offset:38400\n"
"v_mfma_f32_16x16x32_bf16 v[108:111], a[68:71], a[52:55], v[108:111]\n"
"buffer_load_dwordx4 v54, s[36:39], s74 offen offset:3072 lds\n"
"v_mfma_f32_16x16x32_bf16 v[116:119], a[68:71], a[56:59], v[116:119]\n"
"v_mfma_f32_16x16x32_bf16 v[124:127], a[68:71], a[60:63], v[124:127]\n"
"s_waitcnt lgkmcnt(6)\n"
"v_mfma_f32_16x16x32_bf16 v[96:99], a[72:75], a[32:35], v[96:99]\n"
"v_mfma_f32_16x16x32_bf16 v[104:107], a[72:75], a[36:39], v[104:107]\n"
"v_mfma_f32_16x16x32_bf16 v[112:115], a[72:75], a[40:43], v[112:115]\n"
"s_add_u32 s57, s57, 128\n"
"v_mfma_f32_16x16x32_bf16 v[120:123], a[72:75], a[44:47], v[120:123]\n"
"v_mfma_f32_16x16x32_bf16 v[96:99], a[76:79], a[48:51], v[96:99]\n"
"s_waitcnt vmcnt(8) lgkmcnt(0)\n"
"s_barrier\n"
"v_mfma_f32_16x16x32_bf16 v[104:107], a[76:79], a[52:55], v[104:107]\n"
"ds_read_b128 a[0:3], v41 offset:0\n"
"ds_read_b128 a[4:7], v41 offset:2048\n"
"v_mfma_f32_16x16x32_bf16 v[112:115], a[76:79], a[56:59], v[112:115]\n"
"s_add_u32 m0, s69, 49152\n"
"ds_read_b128 a[8:11], v41 offset:4096\n"
"v_mfma_f32_16x16x32_bf16 v[120:123], a[76:79], a[60:63], v[120:123]\n"
"buffer_load_dwordx4 v43, s[40:43], s58 offen offset:0 lds\n"
"ds_read_b128 a[12:15], v41 offset:6144\n"
"v_mfma_f32_16x16x32_bf16 v[68:71], a[80:83], a[32:35], v[68:71]\n"
"ds_read_b64_tr_b16 a[64:65], v40 offset:256\n"
"ds_read_b64_tr_b16 a[66:67], v40 offset:1792\n"
"v_mfma_f32_16x16x32_bf16 v[76:79], a[80:83], a[36:39], v[76:79]\n"
"ds_read_b128 a[16:19], v42 offset:0\n"
"ds_read_b128 a[20:23], v42 offset:2048\n"
"v_mfma_f32_16x16x32_bf16 v[84:87], a[80:83], a[40:43], v[84:87]\n"
"buffer_load_dwordx4 v44, s[40:43], s58 offen offset:1024 lds\n"
"ds_read_b128 a[24:27], v42 offset:4096\n"
"v_mfma_f32_16x16x32_bf16 v[92:95], a[80:83], a[44:47], v[92:95]\n"
"ds_read_b128 a[28:31], v42 offset:6144\n"
"v_mfma_f32_16x16x32_bf16 v[68:71], a[84:87], a[48:51], v[68:71]\n"
"ds_read_b64_tr_b16 a[68:69], v40 offset:12544\n"
"ds_read_b64_tr_b16 a[70:71], v40 offset:14080\n"
"v_mfma_f32_16x16x32_bf16 v[76:79], a[84:87], a[52:55], v[76:79]\n"
"ds_read_b64_tr_b16 a[72:73], v39 offset:256\n"
"ds_read_b64_tr_b16 a[74:75], v39 offset:1792\n"
"v_mfma_f32_16x16x32_bf16 v[84:87], a[84:87], a[56:59], v[84:87]\n"
"buffer_load_dwordx4 v45, s[40:43], s58 offen offset:2048 lds\n"
"v_mfma_f32_16x16x32_bf16 v[92:95], a[84:87], a[60:63], v[92:95]\n"
"v_mfma_f32_16x16x32_bf16 v[64:67], a[88:91], a[32:35], v[64:67]\n"
"v_mfma_f32_16x16x32_bf16 v[72:75], a[88:91], a[36:39], v[72:75]\n"
"v_mfma_f32_16x16x32_bf16 v[80:83], a[88:91], a[40:43], v[80:83]\n"
"buffer_load_dwordx4 v46, s[40:43], s58 offen offset:3072 lds\n"
"v_mfma_f32_16x16x32_bf16 v[88:91], a[88:91], a[44:47], v[88:91]\n"
"v_mfma_f32_16x16x32_bf16 v[64:67], a[92:95], a[48:51], v[64:67]\n"
"v_mfma_f32_16x16x32_bf16 v[72:75], a[92:95], a[52:55], v[72:75]\n"
"v_mfma_f32_16x16x32_bf16 v[80:83], a[92:95], a[56:59], v[80:83]\n"
"v_mfma_f32_16x16x32_bf16 v[88:91], a[92:95], a[60:63], v[88:91]\n"
"s_waitcnt lgkmcnt(0)\n"
"v_mfma_f32_16x16x32_bf16 v[228:231], a[64:67], a[0:3], v[228:231]\n"
"ds_read_b64_tr_b16 a[76:77], v39 offset:12544\n"
"ds_read_b64_tr_b16 a[78:79], v39 offset:14080\n"
"v_mfma_f32_16x16x32_bf16 v[236:239], a[64:67], a[4:7], v[236:239]\n"
"ds_read_b64_tr_b16 a[80:81], v38 offset:256\n"
"ds_read_b64_tr_b16 a[82:83], v38 offset:1792\n"
"v_mfma_f32_16x16x32_bf16 v[244:247], a[64:67], a[8:11], v[244:247]\n"
"ds_read_b64_tr_b16 a[84:85], v38 offset:12544\n"
"ds_read_b64_tr_b16 a[86:87], v38 offset:14080\n"
"v_mfma_f32_16x16x32_bf16 v[252:255], a[64:67], a[12:15], v[252:255]\n"
"s_add_u32 m0, s69, 53248\n"
"v_mfma_f32_16x16x32_bf16 v[228:231], a[68:71], a[16:19], v[228:231]\n"
"buffer_load_dwordx4 v47, s[40:43], s58 offen offset:0 lds\n"
"ds_read_b64_tr_b16 a[88:89], v37 offset:256\n"
"v_mfma_f32_16x16x32_bf16 v[236:239], a[68:71], a[20:23], v[236:239]\n"
"buffer_load_dwordx4 v48, s[40:43], s58 offen offset:1024 lds\n"
"ds_read_b64_tr_b16 a[90:91], v37 offset:1792\n"
"v_mfma_f32_16x16x32_bf16 v[244:247], a[68:71], a[24:27], v[244:247]\n"
"ds_read_b64_tr_b16 a[92:93], v37 offset:12544\n"
"ds_read_b64_tr_b16 a[94:95], v37 offset:14080\n"
"v_mfma_f32_16x16x32_bf16 v[252:255], a[68:71], a[28:31], v[252:255]\n"
"v_mfma_f32_16x16x32_bf16 v[224:227], a[72:75], a[0:3], v[224:227]\n"
"ds_read_b64_tr_b16 a[64:65], v40 offset:128\n"
"ds_read_b64_tr_b16 a[66:67], v40 offset:1664\n"
"v_mfma_f32_16x16x32_bf16 v[232:235], a[72:75], a[4:7], v[232:235]\n"
"v_mfma_f32_16x16x32_bf16 v[240:243], a[72:75], a[8:11], v[240:243]\n"
"buffer_load_dwordx4 v49, s[40:43], s58 offen offset:2048 lds\n"
"v_mfma_f32_16x16x32_bf16 v[248:251], a[72:75], a[12:15], v[248:251]\n"
"s_waitcnt lgkmcnt(6)\n"
"v_mfma_f32_16x16x32_bf16 v[224:227], a[76:79], a[16:19], v[224:227]\n"
"ds_read_b64_tr_b16 a[68:69], v40 offset:12416\n"
"ds_read_b64_tr_b16 a[70:71], v40 offset:13952\n"
"v_mfma_f32_16x16x32_bf16 v[232:235], a[76:79], a[20:23], v[232:235]\n"
"v_mfma_f32_16x16x32_bf16 v[240:243], a[76:79], a[24:27], v[240:243]\n"
"v_mfma_f32_16x16x32_bf16 v[248:251], a[76:79], a[28:31], v[248:251]\n"
"v_mfma_f32_16x16x32_bf16 v[196:199], a[80:83], a[0:3], v[196:199]\n"
"buffer_load_dwordx4 v50, s[40:43], s58 offen offset:3072 lds\n"
"ds_read_b64_tr_b16 a[72:73], v39 offset:128\n"
"v_mfma_f32_16x16x32_bf16 v[204:207], a[80:83], a[4:7], v[204:207]\n"
"ds_read_b64_tr_b16 a[74:75], v39 offset:1664\n"
"v_mfma_f32_16x16x32_bf16 v[212:215], a[80:83], a[8:11], v[212:215]\n"
"v_mfma_f32_16x16x32_bf16 v[220:223], a[80:83], a[12:15], v[220:223]\n"
"v_mfma_f32_16x16x32_bf16 v[196:199], a[84:87], a[16:19], v[196:199]\n"
"ds_read_b64_tr_b16 a[76:77], v39 offset:12416\n"
"ds_read_b64_tr_b16 a[78:79], v39 offset:13952\n"
"v_mfma_f32_16x16x32_bf16 v[204:207], a[84:87], a[20:23], v[204:207]\n"
"s_add_u32 s58, s58, 786432\n"
"s_add_u32 m0, s89, 153600\n"
"v_mfma_f32_16x16x32_bf16 v[212:215], a[84:87], a[24:27], v[212:215]\n"
"buffer_load_dwordx4 v51, s[36:39], s84 offen offset:0 lds\n"
"v_mfma_f32_16x16x32_bf16 v[220:223], a[84:87], a[28:31], v[220:223]\n"
"s_waitcnt lgkmcnt(6)\n"
"v_mfma_f32_16x16x32_bf16 v[192:195], a[88:91], a[0:3], v[192:195]\n"
"ds_read_b64_tr_b16 a[80:81], v38 offset:128\n"
"ds_read_b64_tr_b16 a[82:83], v38 offset:1664\n"
"v_mfma_f32_16x16x32_bf16 v[200:203], a[88:91], a[4:7], v[200:203]\n"
"v_mfma_f32_16x16x32_bf16 v[208:211], a[88:91], a[8:11], v[208:211]\n"
"buffer_load_dwordx4 v52, s[36:39], s84 offen offset:1024 lds\n"
"v_mfma_f32_16x16x32_bf16 v[216:219], a[88:91], a[12:15], v[216:219]\n"
"v_mfma_f32_16x16x32_bf16 v[192:195], a[92:95], a[16:19], v[192:195]\n"
"ds_read_b64_tr_b16 a[84:85], v38 offset:12416\n"
"ds_read_b64_tr_b16 a[86:87], v38 offset:13952\n"
"v_mfma_f32_16x16x32_bf16 v[200:203], a[92:95], a[20:23], v[200:203]\n"
"v_mfma_f32_16x16x32_bf16 v[208:211], a[92:95], a[24:27], v[208:211]\n"
"v_mfma_f32_16x16x32_bf16 v[216:219], a[92:95], a[28:31], v[216:219]\n"
"v_mfma_f32_16x16x32_bf16 v[164:167], a[64:67], a[0:3], v[164:167]\n"
"s_add_u32 s84, s84, 128\n"
"s_add_u32 m0, s70, 98304\n"
"v_mfma_f32_16x16x32_bf16 v[172:175], a[64:67], a[4:7], v[172:175]\n"
"buffer_load_dwordx4 v51, s[36:39], s57 offen offset:0 lds\n"
"ds_read_b64_tr_b16 a[88:89], v37 offset:128\n"
"v_mfma_f32_16x16x32_bf16 v[180:183], a[64:67], a[8:11], v[180:183]\n"
"ds_read_b64_tr_b16 a[90:91], v37 offset:1664\n"
"v_mfma_f32_16x16x32_bf16 v[188:191], a[64:67], a[12:15], v[188:191]\n"
"s_waitcnt lgkmcnt(6)\n"
"v_mfma_f32_16x16x32_bf16 v[164:167], a[68:71], a[16:19], v[164:167]\n"
"ds_read_b64_tr_b16 a[92:93], v37 offset:12416\n"
"ds_read_b64_tr_b16 a[94:95], v37 offset:13952\n"
"v_mfma_f32_16x16x32_bf16 v[172:175], a[68:71], a[20:23], v[172:175]\n"
"buffer_load_dwordx4 v52, s[36:39], s57 offen offset:1024 lds\n"
"v_mfma_f32_16x16x32_bf16 v[180:183], a[68:71], a[24:27], v[180:183]\n"
"v_mfma_f32_16x16x32_bf16 v[188:191], a[68:71], a[28:31], v[188:191]\n"
"v_mfma_f32_16x16x32_bf16 v[160:163], a[72:75], a[0:3], v[160:163]\n"
"ds_read_b64_tr_b16 a[64:65], v40 offset:0\n"
"ds_read_b64_tr_b16 a[66:67], v40 offset:1536\n"
"v_mfma_f32_16x16x32_bf16 v[168:171], a[72:75], a[4:7], v[168:171]\n"
"v_mfma_f32_16x16x32_bf16 v[176:179], a[72:75], a[8:11], v[176:179]\n"
"buffer_load_dwordx4 v53, s[36:39], s57 offen offset:2048 lds\n"
"v_mfma_f32_16x16x32_bf16 v[184:187], a[72:75], a[12:15], v[184:187]\n"
"v_mfma_f32_16x16x32_bf16 v[160:163], a[76:79], a[16:19], v[160:163]\n"
"ds_read_b64_tr_b16 a[68:69], v40 offset:12288\n"
"ds_read_b64_tr_b16 a[70:71], v40 offset:13824\n"
"v_mfma_f32_16x16x32_bf16 v[168:171], a[76:79], a[20:23], v[168:171]\n"
"v_mfma_f32_16x16x32_bf16 v[176:179], a[76:79], a[24:27], v[176:179]\n"
"v_mfma_f32_16x16x32_bf16 v[184:187], a[76:79], a[28:31], v[184:187]\n"
"s_waitcnt lgkmcnt(6)\n"
"v_mfma_f32_16x16x32_bf16 v[132:135], a[80:83], a[0:3], v[132:135]\n"
"buffer_load_dwordx4 v54, s[36:39], s57 offen offset:3072 lds\n"
"ds_read_b64_tr_b16 a[72:73], v39 offset:0\n"
"v_mfma_f32_16x16x32_bf16 v[140:143], a[80:83], a[4:7], v[140:143]\n"
"ds_read_b64_tr_b16 a[74:75], v39 offset:1536\n"
"v_mfma_f32_16x16x32_bf16 v[148:151], a[80:83], a[8:11], v[148:151]\n"
"v_mfma_f32_16x16x32_bf16 v[156:159], a[80:83], a[12:15], v[156:159]\n"
"v_mfma_f32_16x16x32_bf16 v[132:135], a[84:87], a[16:19], v[132:135]\n"
"ds_read_b64_tr_b16 a[76:77], v39 offset:12288\n"
"ds_read_b64_tr_b16 a[78:79], v39 offset:13824\n"
"v_mfma_f32_16x16x32_bf16 v[140:143], a[84:87], a[20:23], v[140:143]\n"
"s_add_u32 m0, s70, 102400\n"
"v_mfma_f32_16x16x32_bf16 v[148:151], a[84:87], a[24:27], v[148:151]\n"
"s_add_u32 s74, s57, 40960\n"
"buffer_load_dwordx4 v51, s[36:39], s74 offen offset:0 lds\n"
"v_mfma_f32_16x16x32_bf16 v[156:159], a[84:87], a[28:31], v[156:159]\n"
"v_mfma_f32_16x16x32_bf16 v[128:131], a[88:91], a[0:3], v[128:131]\n"
"ds_read_b64_tr_b16 a[80:81], v38 offset:0\n"
"ds_read_b64_tr_b16 a[82:83], v38 offset:1536\n"
"v_mfma_f32_16x16x32_bf16 v[136:139], a[88:91], a[4:7], v[136:139]\n"
"v_mfma_f32_16x16x32_bf16 v[144:147], a[88:91], a[8:11], v[144:147]\n"
"buffer_load_dwordx4 v52, s[36:39], s74 offen offset:1024 lds\n"
"v_mfma_f32_16x16x32_bf16 v[152:155], a[88:91], a[12:15], v[152:155]\n"
"s_waitcnt lgkmcnt(6)\n"
"v_mfma_f32_16x16x32_bf16 v[128:131], a[92:95], a[16:19], v[128:131]\n"
"ds_read_b64_tr_b16 a[84:85], v38 offset:12288\n"
"ds_read_b64_tr_b16 a[86:87], v38 offset:13824\n"
"v_mfma_f32_16x16x32_bf16 v[136:139], a[92:95], a[20:23], v[136:139]\n"
"v_mfma_f32_16x16x32_bf16 v[144:147], a[92:95], a[24:27], v[144:147]\n"
"v_mfma_f32_16x16x32_bf16 v[152:155], a[92:95], a[28:31], v[152:155]\n"
"v_mfma_f32_16x16x32_bf16 v[100:103], a[64:67], a[0:3], v[100:103]\n"
"buffer_load_dwordx4 v53, s[36:39], s74 offen offset:2048 lds\n"
"ds_read_b64_tr_b16 a[88:89], v37 offset:0\n"
"v_mfma_f32_16x16x32_bf16 v[108:111], a[64:67], a[4:7], v[108:111]\n"
"ds_read_b64_tr_b16 a[90:91], v37 offset:1536\n"
"v_mfma_f32_16x16x32_bf16 v[116:119], a[64:67], a[8:11], v[116:119]\n"
"v_mfma_f32_16x16x32_bf16 v[124:127], a[64:67], a[12:15], v[124:127]\n"
"v_mfma_f32_16x16x32_bf16 v[100:103], a[68:71], a[16:19], v[100:103]\n"
"ds_read_b64_tr_b16 a[92:93], v37 offset:12288\n"
"ds_read_b64_tr_b16 a[94:95], v37 offset:13824\n"
"v_mfma_f32_16x16x32_bf16 v[108:111], a[68:71], a[20:23], v[108:111]\n"
"buffer_load_dwordx4 v54, s[36:39], s74 offen offset:3072 lds\n"
"v_mfma_f32_16x16x32_bf16 v[116:119], a[68:71], a[24:27], v[116:119]\n"
"v_mfma_f32_16x16x32_bf16 v[124:127], a[68:71], a[28:31], v[124:127]\n"
"s_waitcnt lgkmcnt(6)\n"
"v_mfma_f32_16x16x32_bf16 v[96:99], a[72:75], a[0:3], v[96:99]\n"
"v_mfma_f32_16x16x32_bf16 v[104:107], a[72:75], a[4:7], v[104:107]\n"
"v_mfma_f32_16x16x32_bf16 v[112:115], a[72:75], a[8:11], v[112:115]\n"
"s_add_u32 s57, s57, 128\n"
"v_mfma_f32_16x16x32_bf16 v[120:123], a[72:75], a[12:15], v[120:123]\n"
"v_mfma_f32_16x16x32_bf16 v[96:99], a[76:79], a[16:19], v[96:99]\n"
"s_waitcnt vmcnt(8) lgkmcnt(0)\n"
"s_barrier\n"
"v_mfma_f32_16x16x32_bf16 v[104:107], a[76:79], a[20:23], v[104:107]\n"
"ds_read_b128 a[32:35], v41 offset:24576\n"
"ds_read_b128 a[36:39], v41 offset:26624\n"
"v_mfma_f32_16x16x32_bf16 v[112:115], a[76:79], a[24:27], v[112:115]\n"
"s_add_u32 m0, s69, 24576\n"
"ds_read_b128 a[40:43], v41 offset:28672\n"
"v_mfma_f32_16x16x32_bf16 v[120:123], a[76:79], a[28:31], v[120:123]\n"
"buffer_load_dwordx4 v43, s[40:43], s58 offen offset:0 lds\n"
"ds_read_b128 a[44:47], v41 offset:30720\n"
"v_mfma_f32_16x16x32_bf16 v[68:71], a[80:83], a[0:3], v[68:71]\n"
"ds_read_b64_tr_b16 a[64:65], v40 offset:24832\n"
"ds_read_b64_tr_b16 a[66:67], v40 offset:26368\n"
"v_mfma_f32_16x16x32_bf16 v[76:79], a[80:83], a[4:7], v[76:79]\n"
"ds_read_b128 a[48:51], v42 offset:24576\n"
"ds_read_b128 a[52:55], v42 offset:26624\n"
"v_mfma_f32_16x16x32_bf16 v[84:87], a[80:83], a[8:11], v[84:87]\n"
"buffer_load_dwordx4 v44, s[40:43], s58 offen offset:1024 lds\n"
"ds_read_b128 a[56:59], v42 offset:28672\n"
"v_mfma_f32_16x16x32_bf16 v[92:95], a[80:83], a[12:15], v[92:95]\n"
"ds_read_b128 a[60:63], v42 offset:30720\n"
"v_mfma_f32_16x16x32_bf16 v[68:71], a[84:87], a[16:19], v[68:71]\n"
"ds_read_b64_tr_b16 a[68:69], v40 offset:37120\n"
"ds_read_b64_tr_b16 a[70:71], v40 offset:38656\n"
"v_mfma_f32_16x16x32_bf16 v[76:79], a[84:87], a[20:23], v[76:79]\n"
"ds_read_b64_tr_b16 a[72:73], v39 offset:24832\n"
"ds_read_b64_tr_b16 a[74:75], v39 offset:26368\n"
"v_mfma_f32_16x16x32_bf16 v[84:87], a[84:87], a[24:27], v[84:87]\n"
"buffer_load_dwordx4 v45, s[40:43], s58 offen offset:2048 lds\n"
"v_mfma_f32_16x16x32_bf16 v[92:95], a[84:87], a[28:31], v[92:95]\n"
"v_mfma_f32_16x16x32_bf16 v[64:67], a[88:91], a[0:3], v[64:67]\n"
"v_mfma_f32_16x16x32_bf16 v[72:75], a[88:91], a[4:7], v[72:75]\n"
"v_mfma_f32_16x16x32_bf16 v[80:83], a[88:91], a[8:11], v[80:83]\n"
"buffer_load_dwordx4 v46, s[40:43], s58 offen offset:3072 lds\n"
"v_mfma_f32_16x16x32_bf16 v[88:91], a[88:91], a[12:15], v[88:91]\n"
"v_mfma_f32_16x16x32_bf16 v[64:67], a[92:95], a[16:19], v[64:67]\n"
"v_mfma_f32_16x16x32_bf16 v[72:75], a[92:95], a[20:23], v[72:75]\n"
"v_mfma_f32_16x16x32_bf16 v[80:83], a[92:95], a[24:27], v[80:83]\n"
"v_mfma_f32_16x16x32_bf16 v[88:91], a[92:95], a[28:31], v[88:91]\n"
"s_sub_u32 s61, s61, 1\n"
"s_cmp_lg_u32 s61, 0\n"
"s_cbranch_scc1 K1A_QL\n"
"s_waitcnt lgkmcnt(0)\n"
"v_mfma_f32_16x16x32_bf16 v[228:231], a[64:67], a[32:35], v[228:231]\n"
"ds_read_b64_tr_b16 a[76:77], v39 offset:37120\n"
"ds_read_b64_tr_b16 a[78:79], v39 offset:38656\n"
"v_mfma_f32_16x16x32_bf16 v[236:239], a[64:67], a[36:39], v[236:239]\n"
"ds_read_b64_tr_b16 a[80:81], v38 offset:24832\n"
"ds_read_b64_tr_b16 a[82:83], v38 offset:26368\n"
"v_mfma_f32_16x16x32_bf16 v[244:247], a[64:67], a[40:43], v[244:247]\n"
"ds_read_b64_tr_b16 a[84:85], v38 offset:37120\n"
"ds_read_b64_tr_b16 a[86:87], v38 offset:38656\n"
"v_mfma_f32_16x16x32_bf16 v[252:255], a[64:67], a[44:47], v[252:255]\n"
"s_add_u32 m0, s69, 28672\n"
"v_mfma_f32_16x16x32_bf16 v[228:231], a[68:71], a[48:51], v[228:231]\n"
"buffer_load_dwordx4 v47, s[40:43], s58 offen offset:0 lds\n"
"ds_read_b64_tr_b16 a[88:89], v37 offset:24832\n"
"v_mfma_f32_16x16x32_bf16 v[236:239], a[68:71], a[52:55], v[236:239]\n"
"buffer_load_dwordx4 v48, s[40:43], s58 offen offset:1024 lds\n"
"ds_read_b64_tr_b16 a[90:91], v37 offset:26368\n"
"v_mfma_f32_16x16x32_bf16 v[244:247], a[68:71], a[56:59], v[244:247]\n"
"ds_read_b64_tr_b16 a[92:93], v37 offset:37120\n"
"ds_read_b64_tr_b16 a[94:95], v37 offset:38656\n"
"v_mfma_f32_16x16x32_bf16 v[252:255], a[68:71], a[60:63], v[252:255]\n"
"v_mfma_f32_16x16x32_bf16 v[224:227], a[72:75], a[32:35], v[224:227]\n"
"ds_read_b64_tr_b16 a[64:65], v40 offset:24704\n"
"ds_read_b64_tr_b16 a[66:67], v40 offset:26240\n"
"v_mfma_f32_16x16x32_bf16 v[232:235], a[72:75], a[36:39], v[232:235]\n"
"v_mfma_f32_16x16x32_bf16 v[240:243], a[72:75], a[40:43], v[240:243]\n"
"buffer_load_dwordx4 v49, s[40:43], s58 offen offset:2048 lds\n"
"v_mfma_f32_16x16x32_bf16 v[248:251], a[72:75], a[44:47], v[248:251]\n"
"s_waitcnt lgkmcnt(6)\n"
"v_mfma_f32_16x16x32_bf16 v[224:227], a[76:79], a[48:51], v[224:227]\n"
"ds_read_b64_tr_b16 a[68:69], v40 offset:36992\n"
"ds_read_b64_tr_b16 a[70:71], v40 offset:38528\n"
"v_mfma_f32_16x16x32_bf16 v[232:235], a[76:79], a[52:55], v[232:235]\n"
"v_mfma_f32_16x16x32_bf16 v[240:243], a[76:79], a[56:59], v[240:243]\n"
"v_mfma_f32_16x16x32_bf16 v[248:251], a[76:79], a[60:63], v[248:251]\n"
"v_mfma_f32_16x16x32_bf16 v[196:199], a[80:83], a[32:35], v[196:199]\n"
"buffer_load_dwordx4 v50, s[40:43], s58 offen offset:3072 lds\n"
"ds_read_b64_tr_b16 a[72:73], v39 offset:24704\n"
"v_mfma_f32_16x16x32_bf16 v[204:207], a[80:83], a[36:39], v[204:207]\n"
"ds_read_b64_tr_b16 a[74:75], v39 offset:26240\n"
"v_mfma_f32_16x16x32_bf16 v[212:215], a[80:83], a[40:43], v[212:215]\n"
"v_mfma_f32_16x16x32_bf16 v[220:223], a[80:83], a[44:47], v[220:223]\n"
"v_mfma_f32_16x16x32_bf16 v[196:199], a[84:87], a[48:51], v[196:199]\n"
"ds_read_b64_tr_b16 a[76:77], v39 offset:36992\n"
"ds_read_b64_tr_b16 a[78:79], v39 offset:38528\n"
"v_mfma_f32_16x16x32_bf16 v[204:207], a[84:87], a[52:55], v[204:207]\n"
"s_add_u32 s58, s58, 786432\n"
"s_add_u32 m0, s89, 147456\n"
"v_mfma_f32_16x16x32_bf16 v[212:215], a[84:87], a[56:59], v[212:215]\n"
"buffer_load_dwordx4 v51, s[36:39], s84 offen offset:0 lds\n"
"v_mfma_f32_16x16x32_bf16 v[220:223], a[84:87], a[60:63], v[220:223]\n"
"s_waitcnt lgkmcnt(6)\n"
"v_mfma_f32_16x16x32_bf16 v[192:195], a[88:91], a[32:35], v[192:195]\n"
"ds_read_b64_tr_b16 a[80:81], v38 offset:24704\n"
"ds_read_b64_tr_b16 a[82:83], v38 offset:26240\n"
"v_mfma_f32_16x16x32_bf16 v[200:203], a[88:91], a[36:39], v[200:203]\n"
"v_mfma_f32_16x16x32_bf16 v[208:211], a[88:91], a[40:43], v[208:211]\n"
"buffer_load_dwordx4 v52, s[36:39], s84 offen offset:1024 lds\n"
"v_mfma_f32_16x16x32_bf16 v[216:219], a[88:91], a[44:47], v[216:219]\n"
"v_mfma_f32_16x16x32_bf16 v[192:195], a[92:95], a[48:51], v[192:195]\n"
"ds_read_b64_tr_b16 a[84:85], v38 offset:36992\n"
"ds_read_b64_tr_b16 a[86:87], v38 offset:38528\n"
"v_mfma_f32_16x16x32_bf16 v[200:203], a[92:95], a[52:55], v[200:203]\n"
"v_mfma_f32_16x16x32_bf16 v[208:211], a[92:95], a[56:59], v[208:211]\n"
"v_mfma_f32_16x16x32_bf16 v[216:219], a[92:95], a[60:63], v[216:219]\n"
"v_mfma_f32_16x16x32_bf16 v[164:167], a[64:67], a[32:35], v[164:167]\n"
"s_add_u32 s84, s84, 128\n"
"s_add_u32 m0, s70, 122880\n"
"v_mfma_f32_16x16x32_bf16 v[172:175], a[64:67], a[36:39], v[172:175]\n"
"buffer_load_dwordx4 v51, s[36:39], s57 offen offset:0 lds\n"
"ds_read_b64_tr_b16 a[88:89], v37 offset:24704\n"
"v_mfma_f32_16x16x32_bf16 v[180:183], a[64:67], a[40:43], v[180:183]\n"
"ds_read_b64_tr_b16 a[90:91], v37 offset:26240\n"
"v_mfma_f32_16x16x32_bf16 v[188:191], a[64:67], a[44:47], v[188:191]\n"
"s_waitcnt lgkmcnt(6)\n"
"v_mfma_f32_16x16x32_bf16 v[164:167], a[68:71], a[48:51], v[164:167]\n"
"ds_read_b64_tr_b16 a[92:93], v37 offset:36992\n"
"ds_read_b64_tr_b16 a[94:95], v37 offset:38528\n"
"v_mfma_f32_16x16x32_bf16 v[172:175], a[68:71], a[52:55], v[172:175]\n"
"buffer_load_dwordx4 v52, s[36:39], s57 offen offset:1024 lds\n"
"v_mfma_f32_16x16x32_bf16 v[180:183], a[68:71], a[56:59], v[180:183]\n"
"v_mfma_f32_16x16x32_bf16 v[188:191], a[68:71], a[60:63], v[188:191]\n"
"v_mfma_f32_16x16x32_bf16 v[160:163], a[72:75], a[32:35], v[160:163]\n"
"ds_read_b64_tr_b16 a[64:65], v40 offset:24576\n"
"ds_read_b64_tr_b16 a[66:67], v40 offset:26112\n"
"v_mfma_f32_16x16x32_bf16 v[168:171], a[72:75], a[36:39], v[168:171]\n"
"v_mfma_f32_16x16x32_bf16 v[176:179], a[72:75], a[40:43], v[176:179]\n"
"buffer_load_dwordx4 v53, s[36:39], s57 offen offset:2048 lds\n"
"v_mfma_f32_16x16x32_bf16 v[184:187], a[72:75], a[44:47], v[184:187]\n"
"v_mfma_f32_16x16x32_bf16 v[160:163], a[76:79], a[48:51], v[160:163]\n"
"ds_read_b64_tr_b16 a[68:69], v40 offset:36864\n"
"ds_read_b64_tr_b16 a[70:71], v40 offset:38400\n"
"v_mfma_f32_16x16x32_bf16 v[168:171], a[76:79], a[52:55], v[168:171]\n"
"v_mfma_f32_16x16x32_bf16 v[176:179], a[76:79], a[56:59], v[176:179]\n"
"v_mfma_f32_16x16x32_bf16 v[184:187], a[76:79], a[60:63], v[184:187]\n"
"s_waitcnt lgkmcnt(6)\n"
"v_mfma_f32_16x16x32_bf16 v[132:135], a[80:83], a[32:35], v[132:135]\n"
"buffer_load_dwordx4 v54, s[36:39], s57 offen offset:3072 lds\n"
"ds_read_b64_tr_b16 a[72:73], v39 offset:24576\n"
"v_mfma_f32_16x16x32_bf16 v[140:143], a[80:83], a[36:39], v[140:143]\n"
"ds_read_b64_tr_b16 a[74:75], v39 offset:26112\n"
"v_mfma_f32_16x16x32_bf16 v[148:151], a[80:83], a[40:43], v[148:151]\n"
"v_mfma_f32_16x16x32_bf16 v[156:159], a[80:83], a[44:47], v[156:159]\n"
"v_mfma_f32_16x16x32_bf16 v[132:135], a[84:87], a[48:51], v[132:135]\n"
"ds_read_b64_tr_b16 a[76:77], v39 offset:36864\n"
"ds_read_b64_tr_b16 a[78:79], v39 offset:38400\n"
"v_mfma_f32_16x16x32_bf16 v[140:143], a[84:87], a[52:55], v[140:143]\n"
"s_add_u32 m0, s70, 126976\n"
"v_mfma_f32_16x16x32_bf16 v[148:151], a[84:87], a[56:59], v[148:151]\n"
"s_add_u32 s74, s57, 40960\n"
"buffer_load_dwordx4 v51, s[36:39], s74 offen offset:0 lds\n"
"v_mfma_f32_16x16x32_bf16 v[156:159], a[84:87], a[60:63], v[156:159]\n"
"v_mfma_f32_16x16x32_bf16 v[128:131], a[88:91], a[32:35], v[128:131]\n"
"ds_read_b64_tr_b16 a[80:81], v38 offset:24576\n"
"ds_read_b64_tr_b16 a[82:83], v38 offset:26112\n"
"v_mfma_f32_16x16x32_bf16 v[136:139], a[88:91], a[36:39], v[136:139]\n"
"v_mfma_f32_16x16x32_bf16 v[144:147], a[88:91], a[40:43], v[144:147]\n"
"buffer_load_dwordx4 v52, s[36:39], s74 offen offset:1024 lds\n"
"v_mfma_f32_16x16x32_bf16 v[152:155], a[88:91], a[44:47], v[152:155]\n"
"s_waitcnt lgkmcnt(6)\n"
"v_mfma_f32_16x16x32_bf16 v[128:131], a[92:95], a[48:51], v[128:131]\n"
"ds_read_b64_tr_b16 a[84:85], v38 offset:36864\n"
"ds_read_b64_tr_b16 a[86:87], v38 offset:38400\n"
"v_mfma_f32_16x16x32_bf16 v[136:139], a[92:95], a[52:55], v[136:139]\n"
"v_mfma_f32_16x16x32_bf16 v[144:147], a[92:95], a[56:59], v[144:147]\n"
"v_mfma_f32_16x16x32_bf16 v[152:155], a[92:95], a[60:63], v[152:155]\n"
"v_mfma_f32_16x16x32_bf16 v[100:103], a[64:67], a[32:35], v[100:103]\n"
"buffer_load_dwordx4 v53, s[36:39], s74 offen offset:2048 lds\n"
"ds_read_b64_tr_b16 a[88:89], v37 offset:24576\n"
"v_mfma_f32_16x16x32_bf16 v[108:111], a[64:67], a[36:39], v[108:111]\n"
"ds_read_b64_tr_b16 a[90:91], v37 offset:26112\n"
"v_mfma_f32_16x16x32_bf16 v[116:119], a[64:67], a[40:43], v[116:119]\n"
"v_mfma_f32_16x16x32_bf16 v[124:127], a[64:67], a[44:47], v[124:127]\n"
"v_mfma_f32_16x16x32_bf16 v[100:103], a[68:71], a[48:51], v[100:103]\n"
"ds_read_b64_tr_b16 a[92:93], v37 offset:36864\n"
"ds_read_b64_tr_b16 a[94:95], v37 offset:38400\n"
"v_mfma_f32_16x16x32_bf16 v[108:111], a[68:71], a[52:55], v[108:111]\n"
"buffer_load_dwordx4 v54, s[36:39], s74 offen offset:3072 lds\n"
"v_mfma_f32_16x16x32_bf16 v[116:119], a[68:71], a[56:59], v[116:119]\n"
"v_mfma_f32_16x16x32_bf16 v[124:127], a[68:71], a[60:63], v[124:127]\n"
"s_waitcnt lgkmcnt(6)\n"
"v_mfma_f32_16x16x32_bf16 v[96:99], a[72:75], a[32:35], v[96:99]\n"
"v_mfma_f32_16x16x32_bf16 v[104:107], a[72:75], a[36:39], v[104:107]\n"
"v_mfma_f32_16x16x32_bf16 v[112:115], a[72:75], a[40:43], v[112:115]\n"
"s_add_u32 s57, s57, 128\n"
"v_mfma_f32_16x16x32_bf16 v[120:123], a[72:75], a[44:47], v[120:123]\n"
"v_mfma_f32_16x16x32_bf16 v[96:99], a[76:79], a[48:51], v[96:99]\n"
"s_waitcnt vmcnt(8) lgkmcnt(0)\n"
"s_barrier\n"
"v_mfma_f32_16x16x32_bf16 v[104:107], a[76:79], a[52:55], v[104:107]\n"
"ds_read_b128 a[0:3], v41 offset:0\n"
"ds_read_b128 a[4:7], v41 offset:2048\n"
"v_mfma_f32_16x16x32_bf16 v[112:115], a[76:79], a[56:59], v[112:115]\n"
"s_add_u32 m0, s69, 49152\n"
"ds_read_b128 a[8:11], v41 offset:4096\n"
"v_mfma_f32_16x16x32_bf16 v[120:123], a[76:79], a[60:63], v[120:123]\n"
"buffer_load_dwordx4 v43, s[40:43], s58 offen offset:0 lds\n"
"ds_read_b128 a[12:15], v41 offset:6144\n"
"v_mfma_f32_16x16x32_bf16 v[68:71], a[80:83], a[32:35], v[68:71]\n"
"ds_read_b64_tr_b16 a[64:65], v40 offset:256\n"
"ds_read_b64_tr_b16 a[66:67], v40 offset:1792\n"
"v_mfma_f32_16x16x32_bf16 v[76:79], a[80:83], a[36:39], v[76:79]\n"
"ds_read_b128 a[16:19], v42 offset:0\n"
"ds_read_b128 a[20:23], v42 offset:2048\n"
"v_mfma_f32_16x16x32_bf16 v[84:87], a[80:83], a[40:43], v[84:87]\n"
"buffer_load_dwordx4 v44, s[40:43], s58 offen offset:1024 lds\n"
"ds_read_b128 a[24:27], v42 offset:4096\n"
"v_mfma_f32_16x16x32_bf16 v[92:95], a[80:83], a[44:47], v[92:95]\n"
"ds_read_b128 a[28:31], v42 offset:6144\n"
"v_mfma_f32_16x16x32_bf16 v[68:71], a[84:87], a[48:51], v[68:71]\n"
"ds_read_b64_tr_b16 a[68:69], v40 offset:12544\n"
"ds_read_b64_tr_b16 a[70:71], v40 offset:14080\n"
"v_mfma_f32_16x16x32_bf16 v[76:79], a[84:87], a[52:55], v[76:79]\n"
"ds_read_b64_tr_b16 a[72:73], v39 offset:256\n"
"ds_read_b64_tr_b16 a[74:75], v39 offset:1792\n"
"v_mfma_f32_16x16x32_bf16 v[84:87], a[84:87], a[56:59], v[84:87]\n"
"buffer_load_dwordx4 v45, s[40:43], s58 offen offset:2048 lds\n"
"v_mfma_f32_16x16x32_bf16 v[92:95], a[84:87], a[60:63], v[92:95]\n"
"v_mfma_f32_16x16x32_bf16 v[64:67], a[88:91], a[32:35], v[64:67]\n"
"v_mfma_f32_16x16x32_bf16 v[72:75], a[88:91], a[36:39], v[72:75]\n"
"v_mfma_f32_16x16x32_bf16 v[80:83], a[88:91], a[40:43], v[80:83]\n"
"buffer_load_dwordx4 v46, s[40:43], s58 offen offset:3072 lds\n"
"v_mfma_f32_16x16x32_bf16 v[88:91], a[88:91], a[44:47], v[88:91]\n"
"v_mfma_f32_16x16x32_bf16 v[64:67], a[92:95], a[48:51], v[64:67]\n"
"v_mfma_f32_16x16x32_bf16 v[72:75], a[92:95], a[52:55], v[72:75]\n"
"v_mfma_f32_16x16x32_bf16 v[80:83], a[92:95], a[56:59], v[80:83]\n"
"v_mfma_f32_16x16x32_bf16 v[88:91], a[92:95], a[60:63], v[88:91]\n"
"s_waitcnt lgkmcnt(0)\n"
"v_mfma_f32_16x16x32_bf16 v[228:231], a[64:67], a[0:3], v[228:231]\n"
"ds_read_b64_tr_b16 a[76:77], v39 offset:12544\n"
"ds_read_b64_tr_b16 a[78:79], v39 offset:14080\n"
"v_mfma_f32_16x16x32_bf16 v[236:239], a[64:67], a[4:7], v[236:239]\n"
"ds_read_b64_tr_b16 a[80:81], v38 offset:256\n"
"ds_read_b64_tr_b16 a[82:83], v38 offset:1792\n"
"v_mfma_f32_16x16x32_bf16 v[244:247], a[64:67], a[8:11], v[244:247]\n"
"ds_read_b64_tr_b16 a[84:85], v38 offset:12544\n"
"ds_read_b64_tr_b16 a[86:87], v38 offset:14080\n"
"v_mfma_f32_16x16x32_bf16 v[252:255], a[64:67], a[12:15], v[252:255]\n"
"s_add_u32 m0, s69, 53248\n"
"v_mfma_f32_16x16x32_bf16 v[228:231], a[68:71], a[16:19], v[228:231]\n"
"buffer_load_dwordx4 v47, s[40:43], s58 offen offset:0 lds\n"
"ds_read_b64_tr_b16 a[88:89], v37 offset:256\n"
"v_mfma_f32_16x16x32_bf16 v[236:239], a[68:71], a[20:23], v[236:239]\n"
"buffer_load_dwordx4 v48, s[40:43], s58 offen offset:1024 lds\n"
"ds_read_b64_tr_b16 a[90:91], v37 offset:1792\n"
"v_mfma_f32_16x16x32_bf16 v[244:247], a[68:71], a[24:27], v[244:247]\n"
"ds_read_b64_tr_b16 a[92:93], v37 offset:12544\n"
"ds_read_b64_tr_b16 a[94:95], v37 offset:14080\n"
"v_mfma_f32_16x16x32_bf16 v[252:255], a[68:71], a[28:31], v[252:255]\n"
"v_mfma_f32_16x16x32_bf16 v[224:227], a[72:75], a[0:3], v[224:227]\n"
"ds_read_b64_tr_b16 a[64:65], v40 offset:128\n"
"ds_read_b64_tr_b16 a[66:67], v40 offset:1664\n"
"v_mfma_f32_16x16x32_bf16 v[232:235], a[72:75], a[4:7], v[232:235]\n"
"v_mfma_f32_16x16x32_bf16 v[240:243], a[72:75], a[8:11], v[240:243]\n"
"buffer_load_dwordx4 v49, s[40:43], s58 offen offset:2048 lds\n"
"v_mfma_f32_16x16x32_bf16 v[248:251], a[72:75], a[12:15], v[248:251]\n"
"s_waitcnt lgkmcnt(6)\n"
"v_mfma_f32_16x16x32_bf16 v[224:227], a[76:79], a[16:19], v[224:227]\n"
"ds_read_b64_tr_b16 a[68:69], v40 offset:12416\n"
"ds_read_b64_tr_b16 a[70:71], v40 offset:13952\n"
"v_mfma_f32_16x16x32_bf16 v[232:235], a[76:79], a[20:23], v[232:235]\n"
"v_mfma_f32_16x16x32_bf16 v[240:243], a[76:79], a[24:27], v[240:243]\n"
"v_mfma_f32_16x16x32_bf16 v[248:251], a[76:79], a[28:31], v[248:251]\n"
"v_mfma_f32_16x16x32_bf16 v[196:199], a[80:83], a[0:3], v[196:199]\n"
"buffer_load_dwordx4 v50, s[40:43], s58 offen offset:3072 lds\n"
"ds_read_b64_tr_b16 a[72:73], v39 offset:128\n"
"v_mfma_f32_16x16x32_bf16 v[204:207], a[80:83], a[4:7], v[204:207]\n"
"ds_read_b64_tr_b16 a[74:75], v39 offset:1664\n"
"v_mfma_f32_16x16x32_bf16 v[212:215], a[80:83], a[8:11], v[212:215]\n"
"v_mfma_f32_16x16x32_bf16 v[220:223], a[80:83], a[12:15], v[220:223]\n"
"v_mfma_f32_16x16x32_bf16 v[196:199], a[84:87], a[16:19], v[196:199]\n"
"ds_read_b64_tr_b16 a[76:77], v39 offset:12416\n"
"ds_read_b64_tr_b16 a[78:79], v39 offset:13952\n"
"v_mfma_f32_16x16x32_bf16 v[204:207], a[84:87], a[20:23], v[204:207]\n"
"s_add_u32 s58, s58, 786432\n"
"s_add_u32 m0, s89, 153600\n"
"v_mfma_f32_16x16x32_bf16 v[212:215], a[84:87], a[24:27], v[212:215]\n"
"buffer_load_dwordx4 v51, s[36:39], s84 offen offset:0 lds\n"
"v_mfma_f32_16x16x32_bf16 v[220:223], a[84:87], a[28:31], v[220:223]\n"
"s_waitcnt lgkmcnt(6)\n"
"v_mfma_f32_16x16x32_bf16 v[192:195], a[88:91], a[0:3], v[192:195]\n"
"ds_read_b64_tr_b16 a[80:81], v38 offset:128\n"
"ds_read_b64_tr_b16 a[82:83], v38 offset:1664\n"
"v_mfma_f32_16x16x32_bf16 v[200:203], a[88:91], a[4:7], v[200:203]\n"
"v_mfma_f32_16x16x32_bf16 v[208:211], a[88:91], a[8:11], v[208:211]\n"
"buffer_load_dwordx4 v52, s[36:39], s84 offen offset:1024 lds\n"
"v_mfma_f32_16x16x32_bf16 v[216:219], a[88:91], a[12:15], v[216:219]\n"
"v_mfma_f32_16x16x32_bf16 v[192:195], a[92:95], a[16:19], v[192:195]\n"
"ds_read_b64_tr_b16 a[84:85], v38 offset:12416\n"
"ds_read_b64_tr_b16 a[86:87], v38 offset:13952\n"
"v_mfma_f32_16x16x32_bf16 v[200:203], a[92:95], a[20:23], v[200:203]\n"
"v_mfma_f32_16x16x32_bf16 v[208:211], a[92:95], a[24:27], v[208:211]\n"
"v_mfma_f32_16x16x32_bf16 v[216:219], a[92:95], a[28:31], v[216:219]\n"
"v_mfma_f32_16x16x32_bf16 v[164:167], a[64:67], a[0:3], v[164:167]\n"
"s_add_u32 s84, s84, 128\n"
"ds_read_b64_tr_b16 a[88:89], v37 offset:128\n"
"v_mfma_f32_16x16x32_bf16 v[172:175], a[64:67], a[4:7], v[172:175]\n"
"ds_read_b64_tr_b16 a[90:91], v37 offset:1664\n"
"v_mfma_f32_16x16x32_bf16 v[180:183], a[64:67], a[8:11], v[180:183]\n"
"v_mfma_f32_16x16x32_bf16 v[188:191], a[64:67], a[12:15], v[188:191]\n"
"s_waitcnt lgkmcnt(6)\n"
"v_mfma_f32_16x16x32_bf16 v[164:167], a[68:71], a[16:19], v[164:167]\n"
"ds_read_b64_tr_b16 a[92:93], v37 offset:12416\n"
"ds_read_b64_tr_b16 a[94:95], v37 offset:13952\n"
"v_mfma_f32_16x16x32_bf16 v[172:175], a[68:71], a[20:23], v[172:175]\n"
"v_mfma_f32_16x16x32_bf16 v[180:183], a[68:71], a[24:27], v[180:183]\n"
"v_mfma_f32_16x16x32_bf16 v[188:191], a[68:71], a[28:31], v[188:191]\n"
"v_mfma_f32_16x16x32_bf16 v[160:163], a[72:75], a[0:3], v[160:163]\n"
"ds_read_b64_tr_b16 a[64:65], v40 offset:0\n"
"ds_read_b64_tr_b16 a[66:67], v40 offset:1536\n"
"v_mfma_f32_16x16x32_bf16 v[168:171], a[72:75], a[4:7], v[168:171]\n"
"v_mfma_f32_16x16x32_bf16 v[176:179], a[72:75], a[8:11], v[176:179]\n"
"v_mfma_f32_16x16x32_bf16 v[184:187], a[72:75], a[12:15], v[184:187]\n"
"v_mfma_f32_16x16x32_bf16 v[160:163], a[76:79], a[16:19], v[160:163]\n"
"ds_read_b64_tr_b16 a[68:69], v40 offset:12288\n"
"ds_read_b64_tr_b16 a[70:71], v40 offset:13824\n"
"v_mfma_f32_16x16x32_bf16 v[168:171], a[76:79], a[20:23], v[168:171]\n"
"v_mfma_f32_16x16x32_bf16 v[176:179], a[76:79], a[24:27], v[176:179]\n"
"v_mfma_f32_16x16x32_bf16 v[184:187], a[76:79], a[28:31], v[184:187]\n"
"s_waitcnt lgkmcnt(6)\n"
"v_mfma_f32_16x16x32_bf16 v[132:135], a[80:83], a[0:3], v[132:135]\n"
"ds_read_b64_tr_b16 a[72:73], v39 offset:0\n"
"ds_read_b64_tr_b16 a[74:75], v39 offset:1536\n"
"v_mfma_f32_16x16x32_bf16 v[140:143], a[80:83], a[4:7], v[140:143]\n"
"v_mfma_f32_16x16x32_bf16 v[148:151], a[80:83], a[8:11], v[148:151]\n"
"v_mfma_f32_16x16x32_bf16 v[156:159], a[80:83], a[12:15], v[156:159]\n"
"v_mfma_f32_16x16x32_bf16 v[132:135], a[84:87], a[16:19], v[132:135]\n"
"ds_read_b64_tr_b16 a[76:77], v39 offset:12288\n"
"ds_read_b64_tr_b16 a[78:79], v39 offset:13824\n"
"v_mfma_f32_16x16x32_bf16 v[140:143], a[84:87], a[20:23], v[140:143]\n"
"v_mfma_f32_16x16x32_bf16 v[148:151], a[84:87], a[24:27], v[148:151]\n"
"v_mfma_f32_16x16x32_bf16 v[156:159], a[84:87], a[28:31], v[156:159]\n"
"v_mfma_f32_16x16x32_bf16 v[128:131], a[88:91], a[0:3], v[128:131]\n"
"ds_read_b64_tr_b16 a[80:81], v38 offset:0\n"
"ds_read_b64_tr_b16 a[82:83], v38 offset:1536\n"
"v_mfma_f32_16x16x32_bf16 v[136:139], a[88:91], a[4:7], v[136:139]\n"
"v_mfma_f32_16x16x32_bf16 v[144:147], a[88:91], a[8:11], v[144:147]\n"
"v_mfma_f32_16x16x32_bf16 v[152:155], a[88:91], a[12:15], v[152:155]\n"
"s_waitcnt lgkmcnt(6)\n"
"v_mfma_f32_16x16x32_bf16 v[128:131], a[92:95], a[16:19], v[128:131]\n"
"ds_read_b64_tr_b16 a[84:85], v38 offset:12288\n"
"ds_read_b64_tr_b16 a[86:87], v38 offset:13824\n"
"v_mfma_f32_16x16x32_bf16 v[136:139], a[92:95], a[20:23], v[136:139]\n"
"v_mfma_f32_16x16x32_bf16 v[144:147], a[92:95], a[24:27], v[144:147]\n"
"v_mfma_f32_16x16x32_bf16 v[152:155], a[92:95], a[28:31], v[152:155]\n"
"v_mfma_f32_16x16x32_bf16 v[100:103], a[64:67], a[0:3], v[100:103]\n"
"ds_read_b64_tr_b16 a[88:89], v37 offset:0\n"
"ds_read_b64_tr_b16 a[90:91], v37 offset:1536\n"
"v_mfma_f32_16x16x32_bf16 v[108:111], a[64:67], a[4:7], v[108:111]\n"
"v_mfma_f32_16x16x32_bf16 v[116:119], a[64:67], a[8:11], v[116:119]\n"
"v_mfma_f32_16x16x32_bf16 v[124:127], a[64:67], a[12:15], v[124:127]\n"
"v_mfma_f32_16x16x32_bf16 v[100:103], a[68:71], a[16:19], v[100:103]\n"
"ds_read_b64_tr_b16 a[92:93], v37 offset:12288\n"
"ds_read_b64_tr_b16 a[94:95], v37 offset:13824\n"
"v_mfma_f32_16x16x32_bf16 v[108:111], a[68:71], a[20:23], v[108:111]\n"
"v_mfma_f32_16x16x32_bf16 v[116:119], a[68:71], a[24:27], v[116:119]\n"
"v_mfma_f32_16x16x32_bf16 v[124:127], a[68:71], a[28:31], v[124:127]\n"
"s_waitcnt lgkmcnt(6)\n"
"v_mfma_f32_16x16x32_bf16 v[96:99], a[72:75], a[0:3], v[96:99]\n"
"v_mfma_f32_16x16x32_bf16 v[104:107], a[72:75], a[4:7], v[104:107]\n"
"v_mfma_f32_16x16x32_bf16 v[112:115], a[72:75], a[8:11], v[112:115]\n"
"v_mfma_f32_16x16x32_bf16 v[120:123], a[72:75], a[12:15], v[120:123]\n"
"v_mfma_f32_16x16x32_bf16 v[96:99], a[76:79], a[16:19], v[96:99]\n"
"s_waitcnt vmcnt(0) lgkmcnt(0)\n"
"s_barrier\n"
"v_mfma_f32_16x16x32_bf16 v[104:107], a[76:79], a[20:23], v[104:107]\n"
"ds_read_b128 a[32:35], v41 offset:24576\n"
"ds_read_b128 a[36:39], v41 offset:26624\n"
"v_mfma_f32_16x16x32_bf16 v[112:115], a[76:79], a[24:27], v[112:115]\n"
"ds_read_b128 a[40:43], v41 offset:28672\n"
"ds_read_b128 a[44:47], v41 offset:30720\n"
"v_mfma_f32_16x16x32_bf16 v[120:123], a[76:79], a[28:31], v[120:123]\n"
"ds_read_b64_tr_b16 a[64:65], v40 offset:24832\n"
"ds_read_b64_tr_b16 a[66:67], v40 offset:26368\n"
"v_mfma_f32_16x16x32_bf16 v[68:71], a[80:83], a[0:3], v[68:71]\n"
"ds_read_b128 a[48:51], v42 offset:24576\n"
"ds_read_b128 a[52:55], v42 offset:26624\n"
"v_mfma_f32_16x16x32_bf16 v[76:79], a[80:83], a[4:7], v[76:79]\n"
"ds_read_b128 a[56:59], v42 offset:28672\n"
"ds_read_b128 a[60:63], v42 offset:30720\n"
"v_mfma_f32_16x16x32_bf16 v[84:87], a[80:83], a[8:11], v[84:87]\n"
"ds_read_b64_tr_b16 a[68:69], v40 offset:37120\n"
"ds_read_b64_tr_b16 a[70:71], v40 offset:38656\n"
"v_mfma_f32_16x16x32_bf16 v[92:95], a[80:83], a[12:15], v[92:95]\n"
"ds_read_b64_tr_b16 a[72:73], v39 offset:24832\n"
"v_mfma_f32_16x16x32_bf16 v[68:71], a[84:87], a[16:19], v[68:71]\n"
"ds_read_b64_tr_b16 a[74:75], v39 offset:26368\n"
"v_mfma_f32_16x16x32_bf16 v[76:79], a[84:87], a[20:23], v[76:79]\n"
"v_mfma_f32_16x16x32_bf16 v[84:87], a[84:87], a[24:27], v[84:87]\n"
"v_mfma_f32_16x16x32_bf16 v[92:95], a[84:87], a[28:31], v[92:95]\n"
"v_mfma_f32_16x16x32_bf16 v[64:67], a[88:91], a[0:3], v[64:67]\n"
"v_mfma_f32_16x16x32_bf16 v[72:75], a[88:91], a[4:7], v[72:75]\n"
"v_mfma_f32_16x16x32_bf16 v[80:83], a[88:91], a[8:11], v[80:83]\n"
"v_mfma_f32_16x16x32_bf16 v[88:91], a[88:91], a[12:15], v[88:91]\n"
"v_mfma_f32_16x16x32_bf16 v[64:67], a[92:95], a[16:19], v[64:67]\n"
"v_mfma_f32_16x16x32_bf16 v[72:75], a[92:95], a[20:23], v[72:75]\n"
"v_mfma_f32_16x16x32_bf16 v[80:83], a[92:95], a[24:27], v[80:83]\n"
"v_mfma_f32_16x16x32_bf16 v[88:91], a[92:95], a[28:31], v[88:91]\n"
"s_waitcnt lgkmcnt(0)\n"
"v_mfma_f32_16x16x32_bf16 v[228:231], a[64:67], a[32:35], v[228:231]\n"
"ds_read_b64_tr_b16 a[76:77], v39 offset:37120\n"
"ds_read_b64_tr_b16 a[78:79], v39 offset:38656\n"
"v_mfma_f32_16x16x32_bf16 v[236:239], a[64:67], a[36:39], v[236:239]\n"
"ds_read_b64_tr_b16 a[80:81], v38 offset:24832\n"
"ds_read_b64_tr_b16 a[82:83], v38 offset:26368\n"
"v_mfma_f32_16x16x32_bf16 v[244:247], a[64:67], a[40:43], v[244:247]\n"
"ds_read_b64_tr_b16 a[84:85], v38 offset:37120\n"
"ds_read_b64_tr_b16 a[86:87], v38 offset:38656\n"
"v_mfma_f32_16x16x32_bf16 v[252:255], a[64:67], a[44:47], v[252:255]\n"
"ds_read_b64_tr_b16 a[88:89], v37 offset:24832\n"
"v_mfma_f32_16x16x32_bf16 v[228:231], a[68:71], a[48:51], v[228:231]\n"
"ds_read_b64_tr_b16 a[90:91], v37 offset:26368\n"
"ds_read_b64_tr_b16 a[92:93], v37 offset:37120\n"
"v_mfma_f32_16x16x32_bf16 v[236:239], a[68:71], a[52:55], v[236:239]\n"
"ds_read_b64_tr_b16 a[94:95], v37 offset:38656\n"
"v_mfma_f32_16x16x32_bf16 v[244:247], a[68:71], a[56:59], v[244:247]\n"
"v_mfma_f32_16x16x32_bf16 v[252:255], a[68:71], a[60:63], v[252:255]\n"
"v_mfma_f32_16x16x32_bf16 v[224:227], a[72:75], a[32:35], v[224:227]\n"
"ds_read_b64_tr_b16 a[64:65], v40 offset:24704\n"
"ds_read_b64_tr_b16 a[66:67], v40 offset:26240\n"
"v_mfma_f32_16x16x32_bf16 v[232:235], a[72:75], a[36:39], v[232:235]\n"
"v_mfma_f32_16x16x32_bf16 v[240:243], a[72:75], a[40:43], v[240:243]\n"
"v_mfma_f32_16x16x32_bf16 v[248:251], a[72:75], a[44:47], v[248:251]\n"
"s_waitcnt lgkmcnt(6)\n"
"v_mfma_f32_16x16x32_bf16 v[224:227], a[76:79], a[48:51], v[224:227]\n"
"ds_read_b64_tr_b16 a[68:69], v40 offset:36992\n"
"ds_read_b64_tr_b16 a[70:71], v40 offset:38528\n"
"v_mfma_f32_16x16x32_bf16 v[232:235], a[76:79], a[52:55], v[232:235]\n"
"v_mfma_f32_16x16x32_bf16 v[240:243], a[76:79], a[56:59], v[240:243]\n"
"v_mfma_f32_16x16x32_bf16 v[248:251], a[76:79], a[60:63], v[248:251]\n"
"v_mfma_f32_16x16x32_bf16 v[196:199], a[80:83], a[32:35], v[196:199]\n"
"ds_read_b64_tr_b16 a[72:73], v39 offset:24704\n"
"ds_read_b64_tr_b16 a[74:75], v39 offset:26240\n"
"v_mfma_f32_16x16x32_bf16 v[204:207], a[80:83], a[36:39], v[204:207]\n"
"v_mfma_f32_16x16x32_bf16 v[212:215], a[80:83], a[40:43], v[212:215]\n"
"v_mfma_f32_16x16x32_bf16 v[220:223], a[80:83], a[44:47], v[220:223]\n"
"v_mfma_f32_16x16x32_bf16 v[196:199], a[84:87], a[48:51], v[196:199]\n"
"ds_read_b64_tr_b16 a[76:77], v39 offset:36992\n"
"ds_read_b64_tr_b16 a[78:79], v39 offset:38528\n"
"v_mfma_f32_16x16x32_bf16 v[204:207], a[84:87], a[52:55], v[204:207]\n"
"v_pk_mul_f32 v[248:249], v[248:249], s[62:63]\n"
"v_pk_mul_f32 v[250:251], v[250:251], s[62:63]\n"
"v_mfma_f32_16x16x32_bf16 v[212:215], a[84:87], a[56:59], v[212:215]\n"
"v_pk_mul_f32 v[252:253], v[252:253], s[62:63]\n"
"v_pk_mul_f32 v[254:255], v[254:255], s[62:63]\n"
"v_mfma_f32_16x16x32_bf16 v[220:223], a[84:87], a[60:63], v[220:223]\n"
"v_cvt_pk_bf16_f32 v255, v254, v255\n"
"s_waitcnt lgkmcnt(6)\n"
"v_mfma_f32_16x16x32_bf16 v[192:195], a[88:91], a[32:35], v[192:195]\n"
"ds_read_b64_tr_b16 a[80:81], v38 offset:24704\n"
"ds_read_b64_tr_b16 a[82:83], v38 offset:26240\n"
"v_mfma_f32_16x16x32_bf16 v[200:203], a[88:91], a[36:39], v[200:203]\n"
"v_cvt_pk_bf16_f32 v254, v252, v253\n"
"v_cvt_pk_bf16_f32 v253, v250, v251\n"
"v_mfma_f32_16x16x32_bf16 v[208:211], a[88:91], a[40:43], v[208:211]\n"
"v_cvt_pk_bf16_f32 v252, v248, v249\n"
"v_mfma_f32_16x16x32_bf16 v[216:219], a[88:91], a[44:47], v[216:219]\n"
"v_mfma_f32_16x16x32_bf16 v[192:195], a[92:95], a[48:51], v[192:195]\n"
"ds_read_b64_tr_b16 a[84:85], v38 offset:36992\n"
"ds_read_b64_tr_b16 a[86:87], v38 offset:38528\n"
"v_mfma_f32_16x16x32_bf16 v[200:203], a[92:95], a[52:55], v[200:203]\n"
"v_permlane32_swap_b32 v252, v254\n"
"v_permlane32_swap_b32 v253, v255\n"
"v_mfma_f32_16x16x32_bf16 v[208:211], a[92:95], a[56:59], v[208:211]\n"
"v_permlane16_swap_b32 v252, v254\n"
"v_permlane16_swap_b32 v253, v255\n"
"v_mfma_f32_16x16x32_bf16 v[216:219], a[92:95], a[60:63], v[216:219]\n"
"v_pk_mul_f32 v[240:241], v[240:241], s[62:63]\n"
"v_pk_mul_f32 v[242:243], v[242:243], s[62:63]\n"
"v_mfma_f32_16x16x32_bf16 v[164:167], a[64:67], a[32:35], v[164:167]\n"
"ds_read_b64_tr_b16 a[88:89], v37 offset:24704\n"
"ds_read_b64_tr_b16 a[90:91], v37 offset:26240\n"
"v_mfma_f32_16x16x32_bf16 v[172:175], a[64:67], a[36:39], v[172:175]\n"
"v_pk_mul_f32 v[244:245], v[244:245], s[62:63]\n"
"v_pk_mul_f32 v[246:247], v[246:247], s[62:63]\n"
"v_mfma_f32_16x16x32_bf16 v[180:183], a[64:67], a[40:43], v[180:183]\n"
"v_cvt_pk_bf16_f32 v251, v246, v247\n"
"v_cvt_pk_bf16_f32 v250, v244, v245\n"
"v_mfma_f32_16x16x32_bf16 v[188:191], a[64:67], a[44:47], v[188:191]\n"
"v_cvt_pk_bf16_f32 v249, v242, v243\n"
"s_waitcnt lgkmcnt(6)\n"
"v_mfma_f32_16x16x32_bf16 v[164:167], a[68:71], a[48:51], v[164:167]\n"
"ds_read_b64_tr_b16 a[92:93], v37 offset:36992\n"
"ds_read_b64_tr_b16 a[94:95], v37 offset:38528\n"
"v_mfma_f32_16x16x32_bf16 v[172:175], a[68:71], a[52:55], v[172:175]\n"
"v_cvt_pk_bf16_f32 v248, v240, v241\n"
"v_mfma_f32_16x16x32_bf16 v[180:183], a[68:71], a[56:59], v[180:183]\n"
"v_mfma_f32_16x16x32_bf16 v[188:191], a[68:71], a[60:63], v[188:191]\n"
"v_permlane32_swap_b32 v248, v250\n"
"v_permlane32_swap_b32 v249, v251\n"
"v_mfma_f32_16x16x32_bf16 v[160:163], a[72:75], a[32:35], v[160:163]\n"
"ds_read_b64_tr_b16 a[64:65], v40 offset:24576\n"
"ds_read_b64_tr_b16 a[66:67], v40 offset:26112\n"
"v_mfma_f32_16x16x32_bf16 v[168:171], a[72:75], a[36:39], v[168:171]\n"
"v_permlane16_swap_b32 v248, v250\n"
"v_permlane16_swap_b32 v249, v251\n"
"v_mfma_f32_16x16x32_bf16 v[176:179], a[72:75], a[40:43], v[176:179]\n"
"v_pk_mul_f32 v[232:233], v[232:233], s[62:63]\n"
"v_pk_mul_f32 v[234:235], v[234:235], s[62:63]\n"
"v_mfma_f32_16x16x32_bf16 v[184:187], a[72:75], a[44:47], v[184:187]\n"
"v_pk_mul_f32 v[236:237], v[236:237], s[62:63]\n"
"v_pk_mul_f32 v[238:239], v[238:239], s[62:63]\n"
"v_mfma_f32_16x16x32_bf16 v[160:163], a[76:79], a[48:51], v[160:163]\n"
"ds_read_b64_tr_b16 a[68:69], v40 offset:36864\n"
"ds_read_b64_tr_b16 a[70:71], v40 offset:38400\n"
"v_mfma_f32_16x16x32_bf16 v[168:171], a[76:79], a[52:55], v[168:171]\n"
"v_cvt_pk_bf16_f32 v247, v238, v239\n"
"v_cvt_pk_bf16_f32 v246, v236, v237\n"
"v_mfma_f32_16x16x32_bf16 v[176:179], a[76:79], a[56:59], v[176:179]\n"
"v_cvt_pk_bf16_f32 v245, v234, v235\n"
"v_cvt_pk_bf16_f32 v244, v232, v233\n"
"v_mfma_f32_16x16x32_bf16 v[184:187], a[76:79], a[60:63], v[184:187]\n"
"s_waitcnt lgkmcnt(6)\n"
"v_mfma_f32_16x16x32_bf16 v[132:135], a[80:83], a[32:35], v[132:135]\n"
"ds_read_b64_tr_b16 a[72:73], v39 offset:24576\n"
"ds_read_b64_tr_b16 a[74:75], v39 offset:26112\n"
"v_mfma_f32_16x16x32_bf16 v[140:143], a[80:83], a[36:39], v[140:143]\n"
"v_permlane32_swap_b32 v244, v246\n"
"v_permlane32_swap_b32 v245, v247\n"
"v_mfma_f32_16x16x32_bf16 v[148:151], a[80:83], a[40:43], v[148:151]\n"
"v_permlane16_swap_b32 v244, v246\n"
"v_permlane16_swap_b32 v245, v247\n"
"v_mfma_f32_16x16x32_bf16 v[156:159], a[80:83], a[44:47], v[156:159]\n"
"v_pk_mul_f32 v[224:225], v[224:225], s[62:63]\n"
"v_pk_mul_f32 v[226:227], v[226:227], s[62:63]\n"
"v_mfma_f32_16x16x32_bf16 v[132:135], a[84:87], a[48:51], v[132:135]\n"
"ds_read_b64_tr_b16 a[76:77], v39 offset:36864\n"
"ds_read_b64_tr_b16 a[78:79], v39 offset:38400\n"
"v_mfma_f32_16x16x32_bf16 v[140:143], a[84:87], a[52:55], v[140:143]\n"
"v_pk_mul_f32 v[228:229], v[228:229], s[62:63]\n"
"v_pk_mul_f32 v[230:231], v[230:231], s[62:63]\n"
"v_mfma_f32_16x16x32_bf16 v[148:151], a[84:87], a[56:59], v[148:151]\n"
"v_cvt_pk_bf16_f32 v243, v230, v231\n"
"v_cvt_pk_bf16_f32 v242, v228, v229\n"
"v_mfma_f32_16x16x32_bf16 v[156:159], a[84:87], a[60:63], v[156:159]\n"
"v_cvt_pk_bf16_f32 v241, v226, v227\n"
"v_cvt_pk_bf16_f32 v240, v224, v225\n"
"v_mfma_f32_16x16x32_bf16 v[128:131], a[88:91], a[32:35], v[128:131]\n"
"ds_read_b64_tr_b16 a[80:81], v38 offset:24576\n"
"ds_read_b64_tr_b16 a[82:83], v38 offset:26112\n"
"v_mfma_f32_16x16x32_bf16 v[136:139], a[88:91], a[36:39], v[136:139]\n"
"v_permlane32_swap_b32 v240, v242\n"
"v_permlane32_swap_b32 v241, v243\n"
"v_mfma_f32_16x16x32_bf16 v[144:147], a[88:91], a[40:43], v[144:147]\n"
"v_permlane16_swap_b32 v240, v242\n"
"v_permlane16_swap_b32 v241, v243\n"
"v_mfma_f32_16x16x32_bf16 v[152:155], a[88:91], a[44:47], v[152:155]\n"
"v_pk_mul_f32 v[216:217], v[216:217], s[62:63]\n"
"s_waitcnt lgkmcnt(6)\n"
"v_mfma_f32_16x16x32_bf16 v[128:131], a[92:95], a[48:51], v[128:131]\n"
"ds_read_b64_tr_b16 a[84:85], v38 offset:36864\n"
"ds_read_b64_tr_b16 a[86:87], v38 offset:38400\n"
"v_mfma_f32_16x16x32_bf16 v[136:139], a[92:95], a[52:55], v[136:139]\n"
"v_pk_mul_f32 v[218:219], v[218:219], s[62:63]\n"
"v_pk_mul_f32 v[220:221], v[220:221], s[62:63]\n"
"v_mfma_f32_16x16x32_bf16 v[144:147], a[92:95], a[56:59], v[144:147]\n"
"v_pk_mul_f32 v[222:223], v[222:223], s[62:63]\n"
"v_cvt_pk_bf16_f32 v239, v222, v223\n"
"v_mfma_f32_16x16x32_bf16 v[152:155], a[92:95], a[60:63], v[152:155]\n"
"v_cvt_pk_bf16_f32 v238, v220, v221\n"
"v_cvt_pk_bf16_f32 v237, v218, v219\n"
"v_mfma_f32_16x16x32_bf16 v[100:103], a[64:67], a[32:35], v[100:103]\n"
"ds_read_b64_tr_b16 a[88:89], v37 offset:24576\n"
"ds_read_b64_tr_b16 a[90:91], v37 offset:26112\n"
"v_mfma_f32_16x16x32_bf16 v[108:111], a[64:67], a[36:39], v[108:111]\n"
"v_cvt_pk_bf16_f32 v236, v216, v217\n"
"v_mfma_f32_16x16x32_bf16 v[116:119], a[64:67], a[40:43], v[116:119]\n"
"v_mfma_f32_16x16x32_bf16 v[124:127], a[64:67], a[44:47], v[124:127]\n"
"v_permlane32_swap_b32 v236, v238\n"
"v_permlane32_swap_b32 v237, v239\n"
"v_mfma_f32_16x16x32_bf16 v[100:103], a[68:71], a[48:51], v[100:103]\n"
"ds_read_b64_tr_b16 a[92:93], v37 offset:36864\n"
"ds_read_b64_tr_b16 a[94:95], v37 offset:38400\n"
"v_mfma_f32_16x16x32_bf16 v[108:111], a[68:71], a[52:55], v[108:111]\n"
"v_permlane16_swap_b32 v236, v238\n"
"v_permlane16_swap_b32 v237, v239\n"
"v_mfma_f32_16x16x32_bf16 v[116:119], a[68:71], a[56:59], v[116:119]\n"
"v_pk_mul_f32 v[208:209], v[208:209], s[62:63]\n"
"v_pk_mul_f32 v[210:211], v[210:211], s[62:63]\n"
"v_mfma_f32_16x16x32_bf16 v[124:127], a[68:71], a[60:63], v[124:127]\n"
"v_pk_mul_f32 v[212:213], v[212:213], s[62:63]\n"
"s_waitcnt lgkmcnt(6)\n"
"v_mfma_f32_16x16x32_bf16 v[96:99], a[72:75], a[32:35], v[96:99]\n"
"v_pk_mul_f32 v[214:215], v[214:215], s[62:63]\n"
"v_cvt_pk_bf16_f32 v235, v214, v215\n"
"v_mfma_f32_16x16x32_bf16 v[104:107], a[72:75], a[36:39], v[104:107]\n"
"v_cvt_pk_bf16_f32 v234, v212, v213\n"
"v_cvt_pk_bf16_f32 v233, v210, v211\n"
"v_mfma_f32_16x16x32_bf16 v[112:115], a[72:75], a[40:43], v[112:115]\n"
"v_cvt_pk_bf16_f32 v232, v208, v209\n"
"v_mfma_f32_16x16x32_bf16 v[120:123], a[72:75], a[44:47], v[120:123]\n"
"v_mfma_f32_16x16x32_bf16 v[96:99], a[76:79], a[48:51], v[96:99]\n"
"s_waitcnt lgkmcnt(0)\n"
"s_barrier\n"
"v_mfma_f32_16x16x32_bf16 v[104:107], a[76:79], a[52:55], v[104:107]\n"
"v_permlane32_swap_b32 v232, v234\n"
"v_permlane32_swap_b32 v233, v235\n"
"v_mfma_f32_16x16x32_bf16 v[112:115], a[76:79], a[56:59], v[112:115]\n"
"v_permlane16_swap_b32 v232, v234\n"
"v_permlane16_swap_b32 v233, v235\n"
"v_mfma_f32_16x16x32_bf16 v[120:123], a[76:79], a[60:63], v[120:123]\n"
"v_pk_mul_f32 v[200:201], v[200:201], s[62:63]\n"
"v_pk_mul_f32 v[202:203], v[202:203], s[62:63]\n"
"v_mfma_f32_16x16x32_bf16 v[68:71], a[80:83], a[32:35], v[68:71]\n"
"v_pk_mul_f32 v[204:205], v[204:205], s[62:63]\n"
"v_pk_mul_f32 v[206:207], v[206:207], s[62:63]\n"
"v_mfma_f32_16x16x32_bf16 v[76:79], a[80:83], a[36:39], v[76:79]\n"
"v_cvt_pk_bf16_f32 v231, v206, v207\n"
"v_cvt_pk_bf16_f32 v230, v204, v205\n"
"v_mfma_f32_16x16x32_bf16 v[84:87], a[80:83], a[40:43], v[84:87]\n"
"v_cvt_pk_bf16_f32 v229, v202, v203\n"
"v_cvt_pk_bf16_f32 v228, v200, v201\n"
"v_mfma_f32_16x16x32_bf16 v[92:95], a[80:83], a[44:47], v[92:95]\n"
"ds_read_b128 a[192:195], v1 offset:0\n"
"v_mfma_f32_16x16x32_bf16 v[68:71], a[84:87], a[48:51], v[68:71]\n"
"v_permlane32_swap_b32 v228, v230\n"
"v_permlane32_swap_b32 v229, v231\n"
"v_mfma_f32_16x16x32_bf16 v[76:79], a[84:87], a[52:55], v[76:79]\n"
"v_permlane16_swap_b32 v228, v230\n"
"v_permlane16_swap_b32 v229, v231\n"
"v_mfma_f32_16x16x32_bf16 v[84:87], a[84:87], a[56:59], v[84:87]\n"
"v_pk_mul_f32 v[192:193], v[192:193], s[62:63]\n"
"v_pk_mul_f32 v[194:195], v[194:195], s[62:63]\n"
"v_mfma_f32_16x16x32_bf16 v[92:95], a[84:87], a[60:63], v[92:95]\n"
"v_pk_mul_f32 v[196:197], v[196:197], s[62:63]\n"
"v_pk_mul_f32 v[198:199], v[198:199], s[62:63]\n"
"v_mfma_f32_16x16x32_bf16 v[64:67], a[88:91], a[32:35], v[64:67]\n"
"v_cvt_pk_bf16_f32 v227, v198, v199\n"
"v_cvt_pk_bf16_f32 v226, v196, v197\n"
"v_mfma_f32_16x16x32_bf16 v[72:75], a[88:91], a[36:39], v[72:75]\n"
"v_cvt_pk_bf16_f32 v225, v194, v195\n"
"v_cvt_pk_bf16_f32 v224, v192, v193\n"
"v_mfma_f32_16x16x32_bf16 v[80:83], a[88:91], a[40:43], v[80:83]\n"
"ds_read_b128 a[196:199], v1 offset:6144\n"
"ds_read_b128 a[200:203], v2 offset:0\n"
"v_mfma_f32_16x16x32_bf16 v[88:91], a[88:91], a[44:47], v[88:91]\n"
"v_permlane32_swap_b32 v224, v226\n"
"v_permlane32_swap_b32 v225, v227\n"
"v_mfma_f32_16x16x32_bf16 v[64:67], a[92:95], a[48:51], v[64:67]\n"
"v_permlane16_swap_b32 v224, v226\n"
"v_permlane16_swap_b32 v225, v227\n"
"v_mfma_f32_16x16x32_bf16 v[72:75], a[92:95], a[52:55], v[72:75]\n"
"ds_read_b128 a[204:207], v2 offset:6144\n"
"ds_read_b128 a[208:211], v1 offset:128\n"
"v_mfma_f32_16x16x32_bf16 v[80:83], a[92:95], a[56:59], v[80:83]\n"
"v_mfma_f32_16x16x32_bf16 v[88:91], a[92:95], a[60:63], v[88:91]\n"
"s_nop 7\n"
"v_pk_mul_f32 v[184:185], v[184:185], s[62:63]\n"
"v_pk_mul_f32 v[186:187], v[186:187], s[62:63]\n"
"v_pk_mul_f32 v[188:189], v[188:189], s[62:63]\n"
"v_pk_mul_f32 v[190:191], v[190:191], s[62:63]\n"
"v_cvt_pk_bf16_f32 v223, v190, v191\n"
"v_cvt_pk_bf16_f32 v222, v188, v189\n"
"v_cvt_pk_bf16_f32 v221, v186, v187\n"
"v_cvt_pk_bf16_f32 v220, v184, v185\n"
"v_pk_mul_f32 v[176:177], v[176:177], s[62:63]\n"
"v_pk_mul_f32 v[178:179], v[178:179], s[62:63]\n"
"v_pk_mul_f32 v[180:181], v[180:181], s[62:63]\n"
"v_pk_mul_f32 v[182:183], v[182:183], s[62:63]\n"
"v_permlane32_swap_b32 v220, v222\n"
"v_permlane32_swap_b32 v221, v223\n"
"v_cvt_pk_bf16_f32 v219, v182, v183\n"
"v_cvt_pk_bf16_f32 v218, v180, v181\n"
"v_cvt_pk_bf16_f32 v217, v178, v179\n"
"v_cvt_pk_bf16_f32 v216, v176, v177\n"
"v_permlane16_swap_b32 v220, v222\n"
"v_permlane16_swap_b32 v221, v223\n"
"v_pk_mul_f32 v[168:169], v[168:169], s[62:63]\n"
"v_pk_mul_f32 v[170:171], v[170:171], s[62:63]\n"
"v_pk_mul_f32 v[172:173], v[172:173], s[62:63]\n"
"v_pk_mul_f32 v[174:175], v[174:175], s[62:63]\n"
"v_permlane32_swap_b32 v216, v218\n"
"v_permlane32_swap_b32 v217, v219\n"
"v_cvt_pk_bf16_f32 v215, v174, v175\n"
"v_cvt_pk_bf16_f32 v214, v172, v173\n"
"v_cvt_pk_bf16_f32 v213, v170, v171\n"
"v_cvt_pk_bf16_f32 v212, v168, v169\n"
"v_permlane16_swap_b32 v216, v218\n"
"v_permlane16_swap_b32 v217, v219\n"
"v_pk_mul_f32 v[160:161], v[160:161], s[62:63]\n"
"v_pk_mul_f32 v[162:163], v[162:163], s[62:63]\n"
"v_pk_mul_f32 v[164:165], v[164:165], s[62:63]\n"
"v_pk_mul_f32 v[166:167], v[166:167], s[62:63]\n"
"v_permlane32_swap_b32 v212, v214\n"
"v_permlane32_swap_b32 v213, v215\n"
"v_cvt_pk_bf16_f32 v211, v166, v167\n"
"v_cvt_pk_bf16_f32 v210, v164, v165\n"
"v_cvt_pk_bf16_f32 v209, v162, v163\n"
"v_cvt_pk_bf16_f32 v208, v160, v161\n"
"v_permlane16_swap_b32 v212, v214\n"
"v_permlane16_swap_b32 v213, v215\n"
"v_pk_mul_f32 v[152:153], v[152:153], s[62:63]\n"
"v_pk_mul_f32 v[154:155], v[154:155], s[62:63]\n"
"v_pk_mul_f32 v[156:157], v[156:157], s[62:63]\n"
"v_pk_mul_f32 v[158:159], v[158:159], s[62:63]\n"
"v_permlane32_swap_b32 v208, v210\n"
"v_permlane32_swap_b32 v209, v211\n"
"v_cvt_pk_bf16_f32 v207, v158, v159\n"
"v_cvt_pk_bf16_f32 v206, v156, v157\n"
"v_cvt_pk_bf16_f32 v205, v154, v155\n"
"v_cvt_pk_bf16_f32 v204, v152, v153\n"
"v_permlane16_swap_b32 v208, v210\n"
"v_permlane16_swap_b32 v209, v211\n"
"v_pk_mul_f32 v[144:145], v[144:145], s[62:63]\n"
"v_pk_mul_f32 v[146:147], v[146:147], s[62:63]\n"
"v_pk_mul_f32 v[148:149], v[148:149], s[62:63]\n"
"v_pk_mul_f32 v[150:151], v[150:151], s[62:63]\n"
"v_permlane32_swap_b32 v204, v206\n"
"v_permlane32_swap_b32 v205, v207\n"
"v_cvt_pk_bf16_f32 v203, v150, v151\n"
"v_cvt_pk_bf16_f32 v202, v148, v149\n"
"v_cvt_pk_bf16_f32 v201, v146, v147\n"
"v_cvt_pk_bf16_f32 v200, v144, v145\n"
"v_permlane16_swap_b32 v204, v206\n"
"v_permlane16_swap_b32 v205, v207\n"
"v_pk_mul_f32 v[136:137], v[136:137], s[62:63]\n"
"v_pk_mul_f32 v[138:139], v[138:139], s[62:63]\n"
"v_pk_mul_f32 v[140:141], v[140:141], s[62:63]\n"
"v_pk_mul_f32 v[142:143], v[142:143], s[62:63]\n"
"v_permlane32_swap_b32 v200, v202\n"
"v_permlane32_swap_b32 v201, v203\n"
"v_cvt_pk_bf16_f32 v199, v142, v143\n"
"v_cvt_pk_bf16_f32 v198, v140, v141\n"
"v_cvt_pk_bf16_f32 v197, v138, v139\n"
"v_cvt_pk_bf16_f32 v196, v136, v137\n"
"v_permlane16_swap_b32 v200, v202\n"
"v_permlane16_swap_b32 v201, v203\n"
"v_pk_mul_f32 v[128:129], v[128:129], s[62:63]\n"
"v_pk_mul_f32 v[130:131], v[130:131], s[62:63]\n"
"v_pk_mul_f32 v[132:133], v[132:133], s[62:63]\n"
"v_pk_mul_f32 v[134:135], v[134:135], s[62:63]\n"
"v_permlane32_swap_b32 v196, v198\n"
"v_permlane32_swap_b32 v197, v199\n"
"v_cvt_pk_bf16_f32 v195, v134, v135\n"
"v_cvt_pk_bf16_f32 v194, v132, v133\n"
"v_cvt_pk_bf16_f32 v193, v130, v131\n"
"v_cvt_pk_bf16_f32 v192, v128, v129\n"
"v_permlane16_swap_b32 v196, v198\n"
"v_permlane16_swap_b32 v197, v199\n"
"v_pk_mul_f32 v[120:121], v[120:121], s[62:63]\n"
"v_pk_mul_f32 v[122:123], v[122:123], s[62:63]\n"
"v_pk_mul_f32 v[124:125], v[124:125], s[62:63]\n"
"v_pk_mul_f32 v[126:127], v[126:127], s[62:63]\n"
"v_permlane32_swap_b32 v192, v194\n"
"v_permlane32_swap_b32 v193, v195\n"
"v_cvt_pk_bf16_f32 v191, v126, v127\n"
"v_cvt_pk_bf16_f32 v190, v124, v125\n"
"v_cvt_pk_bf16_f32 v189, v122, v123\n"
"v_cvt_pk_bf16_f32 v188, v120, v121\n"
"v_permlane16_swap_b32 v192, v194\n"
"v_permlane16_swap_b32 v193, v195\n"
"v_pk_mul_f32 v[112:113], v[112:113], s[62:63]\n"
"v_pk_mul_f32 v[114:115], v[114:115], s[62:63]\n"
"v_pk_mul_f32 v[116:117], v[116:117], s[62:63]\n"
"v_pk_mul_f32 v[118:119], v[118:119], s[62:63]\n"
"v_permlane32_swap_b32 v188, v190\n"
"v_permlane32_swap_b32 v189, v191\n"
"v_cvt_pk_bf16_f32 v187, v118, v119\n"
"v_cvt_pk_bf16_f32 v186, v116, v117\n"
"v_cvt_pk_bf16_f32 v185, v114, v115\n"
"v_cvt_pk_bf16_f32 v184, v112, v113\n"
"v_permlane16_swap_b32 v188, v190\n"
"v_permlane16_swap_b32 v189, v191\n"
"v_pk_mul_f32 v[104:105], v[104:105], s[62:63]\n"
"v_pk_mul_f32 v[106:107], v[106:107], s[62:63]\n"
"v_pk_mul_f32 v[108:109], v[108:109], s[62:63]\n"
"v_pk_mul_f32 v[110:111], v[110:111], s[62:63]\n"
"v_permlane32_swap_b32 v184, v186\n"
"v_permlane32_swap_b32 v185, v187\n"
"v_cvt_pk_bf16_f32 v183, v110, v111\n"
"v_cvt_pk_bf16_f32 v182, v108, v109\n"
"v_cvt_pk_bf16_f32 v181, v106, v107\n"
"v_cvt_pk_bf16_f32 v180, v104, v105\n"
"v_permlane16_swap_b32 v184, v186\n"
"v_permlane16_swap_b32 v185, v187\n"
"v_pk_mul_f32 v[96:97], v[96:97], s[62:63]\n"
"v_pk_mul_f32 v[98:99], v[98:99], s[62:63]\n"
"v_pk_mul_f32 v[100:101], v[100:101], s[62:63]\n"
"v_pk_mul_f32 v[102:103], v[102:103], s[62:63]\n"
"v_permlane32_swap_b32 v180, v182\n"
"v_permlane32_swap_b32 v181, v183\n"
"v_cvt_pk_bf16_f32 v179, v102, v103\n"
"v_cvt_pk_bf16_f32 v178, v100, v101\n"
"v_cvt_pk_bf16_f32 v177, v98, v99\n"
"v_cvt_pk_bf16_f32 v176, v96, v97\n"
"v_permlane16_swap_b32 v180, v182\n"
"v_permlane16_swap_b32 v181, v183\n"
"v_pk_mul_f32 v[88:89], v[88:89], s[62:63]\n"
"v_pk_mul_f32 v[90:91], v[90:91], s[62:63]\n"
"v_pk_mul_f32 v[92:93], v[92:93], s[62:63]\n"
"v_pk_mul_f32 v[94:95], v[94:95], s[62:63]\n"
"v_permlane32_swap_b32 v176, v178\n"
"v_permlane32_swap_b32 v177, v179\n"
"v_cvt_pk_bf16_f32 v175, v94, v95\n"
"v_cvt_pk_bf16_f32 v174, v92, v93\n"
"v_cvt_pk_bf16_f32 v173, v90, v91\n"
"v_cvt_pk_bf16_f32 v172, v88, v89\n"
"v_permlane16_swap_b32 v176, v178\n"
"v_permlane16_swap_b32 v177, v179\n"
"v_pk_mul_f32 v[80:81], v[80:81], s[62:63]\n"
"v_pk_mul_f32 v[82:83], v[82:83], s[62:63]\n"
"v_pk_mul_f32 v[84:85], v[84:85], s[62:63]\n"
"v_pk_mul_f32 v[86:87], v[86:87], s[62:63]\n"
"v_permlane32_swap_b32 v172, v174\n"
"v_permlane32_swap_b32 v173, v175\n"
"v_cvt_pk_bf16_f32 v171, v86, v87\n"
"v_cvt_pk_bf16_f32 v170, v84, v85\n"
"v_cvt_pk_bf16_f32 v169, v82, v83\n"
"v_cvt_pk_bf16_f32 v168, v80, v81\n"
"v_permlane16_swap_b32 v172, v174\n"
"v_permlane16_swap_b32 v173, v175\n"
"v_pk_mul_f32 v[72:73], v[72:73], s[62:63]\n"
"v_pk_mul_f32 v[74:75], v[74:75], s[62:63]\n"
"v_pk_mul_f32 v[76:77], v[76:77], s[62:63]\n"
"v_pk_mul_f32 v[78:79], v[78:79], s[62:63]\n"
"v_permlane32_swap_b32 v168, v170\n"
"v_permlane32_swap_b32 v169, v171\n"
"v_cvt_pk_bf16_f32 v167, v78, v79\n"
"v_cvt_pk_bf16_f32 v166, v76, v77\n"
"v_cvt_pk_bf16_f32 v165, v74, v75\n"
"v_cvt_pk_bf16_f32 v164, v72, v73\n"
"v_permlane16_swap_b32 v168, v170\n"
"v_permlane16_swap_b32 v169, v171\n"
"v_pk_mul_f32 v[64:65], v[64:65], s[62:63]\n"
"v_pk_mul_f32 v[66:67], v[66:67], s[62:63]\n"
"v_pk_mul_f32 v[68:69], v[68:69], s[62:63]\n"
"v_pk_mul_f32 v[70:71], v[70:71], s[62:63]\n"
"v_permlane32_swap_b32 v164, v166\n"
"v_permlane32_swap_b32 v165, v167\n"
"v_cvt_pk_bf16_f32 v163, v70, v71\n"
"v_cvt_pk_bf16_f32 v162, v68, v69\n"
"v_cvt_pk_bf16_f32 v161, v66, v67\n"
"v_cvt_pk_bf16_f32 v160, v64, v65\n"
"v_permlane16_swap_b32 v164, v166\n"
"v_permlane16_swap_b32 v165, v167\n"
"s_nop 1\n"
"v_permlane32_swap_b32 v160, v162\n"
"v_permlane32_swap_b32 v161, v163\n"
"s_nop 1\n"
"v_permlane16_swap_b32 v160, v162\n"
"v_permlane16_swap_b32 v161, v163\n"
"s_nop 1\n"
"s_waitcnt lgkmcnt(2)\n"
"v_mfma_f32_16x16x32_bf16 v[64:67], a[192:195], v[160:163], 0\n"
"ds_read_b128 a[212:215], v1 offset:6272\n"
"v_mfma_f32_16x16x32_bf16 v[72:75], a[192:195], v[164:167], 0\n"
"v_mfma_f32_16x16x32_bf16 v[80:83], a[192:195], v[168:171], 0\n"
"v_mfma_f32_16x16x32_bf16 v[88:91], a[192:195], v[172:175], 0\n"
"v_mfma_f32_16x16x32_bf16 v[68:71], a[196:199], v[160:163], 0\n"
"ds_read_b128 a[216:219], v2 offset:128\n"
"v_mfma_f32_16x16x32_bf16 v[76:79], a[196:199], v[164:167], 0\n"
"v_mfma_f32_16x16x32_bf16 v[84:87], a[196:199], v[168:171], 0\n"
"v_mfma_f32_16x16x32_bf16 v[92:95], a[196:199], v[172:175], 0\n"
"v_mfma_f32_16x16x32_bf16 v[64:67], a[200:203], v[176:179], v[64:67]\n"
"ds_read_b128 a[220:223], v2 offset:6272\n"
"v_mfma_f32_16x16x32_bf16 v[72:75], a[200:203], v[180:183], v[72:75]\n"
"v_mfma_f32_16x16x32_bf16 v[80:83], a[200:203], v[184:187], v[80:83]\n"
"v_mfma_f32_16x16x32_bf16 v[88:91], a[200:203], v[188:191], v[88:91]\n"
"s_waitcnt lgkmcnt(2)\n"
"v_mfma_f32_16x16x32_bf16 v[68:71], a[204:207], v[176:179], v[68:71]\n"
"ds_read_b128 a[192:195], v1 offset:256\n"
"v_mfma_f32_16x16x32_bf16 v[76:79], a[204:207], v[180:183], v[76:79]\n"
"v_mfma_f32_16x16x32_bf16 v[84:87], a[204:207], v[184:187], v[84:87]\n"
"v_mfma_f32_16x16x32_bf16 v[92:95], a[204:207], v[188:191], v[92:95]\n"
"v_mfma_f32_16x16x32_bf16 v[64:67], a[208:211], v[192:195], v[64:67]\n"
"ds_read_b128 a[196:199], v1 offset:6400\n"
"v_mfma_f32_16x16x32_bf16 v[72:75], a[208:211], v[196:199], v[72:75]\n"
"v_mfma_f32_16x16x32_bf16 v[80:83], a[208:211], v[200:203], v[80:83]\n"
"v_mfma_f32_16x16x32_bf16 v[88:91], a[208:211], v[204:207], v[88:91]\n"
"v_mfma_f32_16x16x32_bf16 v[68:71], a[212:215], v[192:195], v[68:71]\n"
"ds_read_b128 a[200:203], v2 offset:256\n"
"v_mfma_f32_16x16x32_bf16 v[76:79], a[212:215], v[196:199], v[76:79]\n"
"v_mfma_f32_16x16x32_bf16 v[84:87], a[212:215], v[200:203], v[84:87]\n"
"v_mfma_f32_16x16x32_bf16 v[92:95], a[212:215], v[204:207], v[92:95]\n"
"s_waitcnt lgkmcnt(2)\n"
"v_mfma_f32_16x16x32_bf16 v[64:67], a[216:219], v[208:211], v[64:67]\n"
"ds_read_b128 a[204:207], v2 offset:6400\n"
"v_mfma_f32_16x16x32_bf16 v[72:75], a[216:219], v[212:215], v[72:75]\n"
"v_mfma_f32_16x16x32_bf16 v[80:83], a[216:219], v[216:219], v[80:83]\n"
"v_mfma_f32_16x16x32_bf16 v[88:91], a[216:219], v[220:223], v[88:91]\n"
"v_mfma_f32_16x16x32_bf16 v[68:71], a[220:223], v[208:211], v[68:71]\n"
"v_mfma_f32_16x16x32_bf16 v[76:79], a[220:223], v[212:215], v[76:79]\n"
"v_mfma_f32_16x16x32_bf16 v[84:87], a[220:223], v[216:219], v[84:87]\n"
"v_mfma_f32_16x16x32_bf16 v[92:95], a[220:223], v[220:223], v[92:95]\n"
"v_mfma_f32_16x16x32_bf16 v[64:67], a[192:195], v[224:227], v[64:67]\n"
"v_mfma_f32_16x16x32_bf16 v[72:75], a[192:195], v[228:231], v[72:75]\n"
"v_mfma_f32_16x16x32_bf16 v[80:83], a[192:195], v[232:235], v[80:83]\n"
"v_mfma_f32_16x16x32_bf16 v[88:91], a[192:195], v[236:239], v[88:91]\n"
"s_waitcnt lgkmcnt(0)\n"
"v_mfma_f32_16x16x32_bf16 v[68:71], a[196:199], v[224:227], v[68:71]\n"
"v_mfma_f32_16x16x32_bf16 v[76:79], a[196:199], v[228:231], v[76:79]\n"
"v_mfma_f32_16x16x32_bf16 v[84:87], a[196:199], v[232:235], v[84:87]\n"
"v_mfma_f32_16x16x32_bf16 v[92:95], a[196:199], v[236:239], v[92:95]\n"
"v_mfma_f32_16x16x32_bf16 v[64:67], a[200:203], v[240:243], v[64:67]\n"
"v_mfma_f32_16x16x32_bf16 v[72:75], a[200:203], v[244:247], v[72:75]\n"
"v_mfma_f32_16x16x32_bf16 v[80:83], a[200:203], v[248:251], v[80:83]\n"
"v_mfma_f32_16x16x32_bf16 v[88:91], a[200:203], v[252:255], v[88:91]\n"
"v_mfma_f32_16x16x32_bf16 v[68:71], a[204:207], v[240:243], v[68:71]\n"
"v_mfma_f32_16x16x32_bf16 v[76:79], a[204:207], v[244:247], v[76:79]\n"
"v_mfma_f32_16x16x32_bf16 v[84:87], a[204:207], v[248:251], v[84:87]\n"
"v_mfma_f32_16x16x32_bf16 v[92:95], a[204:207], v[252:255], v[92:95]\n"
"ds_read_b128 a[208:211], v1 offset:12288\n"
"ds_read_b128 a[212:215], v1 offset:18432\n"
"ds_read_b128 a[216:219], v2 offset:12288\n"
"ds_read_b128 a[220:223], v2 offset:18432\n"
"ds_read_b128 a[192:195], v1 offset:12416\n"
"s_nop 7\n"
"v_max3_f32 v55, v64, v65, v66\n"
"v_max3_f32 v56, v72, v73, v74\n"
"v_max3_f32 v57, v80, v81, v82\n"
"v_max3_f32 v58, v88, v89, v90\n"
"v_max3_f32 v55, v55, v67, v68\n"
"v_max3_f32 v56, v56, v75, v76\n"
"v_max3_f32 v57, v57, v83, v84\n"
"v_max3_f32 v58, v58, v91, v92\n"
"v_max3_f32 v55, v55, v69, v70\n"
"v_max3_f32 v56, v56, v77, v78\n"
"v_max3_f32 v57, v57, v85, v86\n"
"v_max3_f32 v58, v58, v93, v94\n"
"v_max_f32 v55, v55, v71\n"
"v_max_f32 v56, v56, v79\n"
"v_max_f32 v57, v57, v87\n"
"v_max_f32 v58, v58, v95\n"
"v_mov_b32 v49, v55\n"
"v_mov_b32 v50, v56\n"
"v_mov_b32 v51, v57\n"
"v_mov_b32 v52, v58\n"
"v_permlane32_swap_b32 v49, v55\n"
"v_permlane32_swap_b32 v50, v56\n"
"v_permlane32_swap_b32 v51, v57\n"
"v_permlane32_swap_b32 v52, v58\n"
"v_max_f32 v55, v55, v49\n"
"v_max_f32 v56, v56, v50\n"
"v_max_f32 v57, v57, v51\n"
"v_max_f32 v58, v58, v52\n"
"v_mov_b32 v49, v55\n"
"v_mov_b32 v50, v56\n"
"v_mov_b32 v51, v57\n"
"v_mov_b32 v52, v58\n"
"v_permlane16_swap_b32 v49, v55\n"
"v_permlane16_swap_b32 v50, v56\n"
"v_permlane16_swap_b32 v51, v57\n"
"v_permlane16_swap_b32 v52, v58\n"
"v_max_f32 v55, v55, v49\n"
"v_max_f32 v56, v56, v50\n"
"v_max_f32 v57, v57, v51\n"
"v_max_f32 v58, v58, v52\n"
"v_sub_f32 v144, 0, v55\n"
"v_sub_f32 v145, 0, v55\n"
"v_sub_f32 v146, 0, v55\n"
"v_sub_f32 v147, 0, v55\n"
"v_sub_f32 v64, v64, v55\n"
"v_sub_f32 v65, v65, v55\n"
"v_sub_f32 v66, v66, v55\n"
"v_sub_f32 v67, v67, v55\n"
"v_sub_f32 v68, v68, v55\n"
"v_sub_f32 v69, v69, v55\n"
"v_sub_f32 v70, v70, v55\n"
"v_sub_f32 v71, v71, v55\n"
"v_mov_b32 v37, 0\n"
"v_mov_b32 v38, 0\n"
"v_sub_f32 v148, 0, v56\n"
"v_sub_f32 v149, 0, v56\n"
"v_sub_f32 v150, 0, v56\n"
"v_sub_f32 v151, 0, v56\n"
"v_sub_f32 v72, v72, v56\n"
"v_sub_f32 v73, v73, v56\n"
"v_sub_f32 v74, v74, v56\n"
"v_sub_f32 v75, v75, v56\n"
"v_sub_f32 v76, v76, v56\n"
"v_sub_f32 v77, v77, v56\n"
"v_sub_f32 v78, v78, v56\n"
"v_sub_f32 v79, v79, v56\n"
"v_mov_b32 v39, 0\n"
"v_mov_b32 v40, 0\n"
"v_sub_f32 v152, 0, v57\n"
"v_sub_f32 v153, 0, v57\n"
"v_sub_f32 v154, 0, v57\n"
"v_sub_f32 v155, 0, v57\n"
"v_sub_f32 v80, v80, v57\n"
"v_sub_f32 v81, v81, v57\n"
"v_sub_f32 v82, v82, v57\n"
"v_sub_f32 v83, v83, v57\n"
"v_sub_f32 v84, v84, v57\n"
"v_sub_f32 v85, v85, v57\n"
"v_sub_f32 v86, v86, v57\n"
"v_sub_f32 v87, v87, v57\n"
"v_mov_b32 v41, 0\n"
"v_mov_b32 v42, 0\n"
"v_sub_f32 v156, 0, v58\n"
"v_sub_f32 v157, 0, v58\n"
"v_sub_f32 v158, 0, v58\n"
"v_sub_f32 v159, 0, v58\n"
"v_sub_f32 v88, v88, v58\n"
"v_sub_f32 v89, v89, v58\n"
"v_sub_f32 v90, v90, v58\n"
"v_sub_f32 v91, v91, v58\n"
"v_sub_f32 v92, v92, v58\n"
"v_sub_f32 v93, v93, v58\n"
"v_sub_f32 v94, v94, v58\n"
"v_sub_f32 v95, v95, v58\n"
"v_mov_b32 v43, 0\n"
"v_mov_b32 v44, 0\n"
"v_exp_f32 v64, v64\n"
"v_exp_f32 v65, v65\n"
"v_exp_f32 v66, v66\n"
"v_exp_f32 v67, v67\n"
"v_exp_f32 v68, v68\n"
"v_exp_f32 v69, v69\n"
"v_exp_f32 v70, v70\n"
"v_exp_f32 v71, v71\n"
"v_exp_f32 v72, v72\n"
"v_exp_f32 v73, v73\n"
"v_exp_f32 v74, v74\n"
"v_exp_f32 v75, v75\n"
"v_exp_f32 v76, v76\n"
"v_exp_f32 v77, v77\n"
"v_exp_f32 v78, v78\n"
"v_exp_f32 v79, v79\n"
"s_nop 0\n"
"v_add_f32 v37, v37, v64\n"
"v_add_f32 v38, v38, v65\n"
"v_add_f32 v37, v37, v66\n"
"v_add_f32 v38, v38, v67\n"
"v_add_f32 v37, v37, v68\n"
"v_add_f32 v38, v38, v69\n"
"v_add_f32 v37, v37, v70\n"
"v_add_f32 v38, v38, v71\n"
"v_add_f32 v39, v39, v72\n"
"v_add_f32 v40, v40, v73\n"
"v_add_f32 v39, v39, v74\n"
"v_add_f32 v40, v40, v75\n"
"v_add_f32 v39, v39, v76\n"
"v_add_f32 v40, v40, v77\n"
"v_add_f32 v39, v39, v78\n"
"v_add_f32 v40, v40, v79\n"
"s_waitcnt lgkmcnt(2)\n"
"v_mfma_f32_16x16x32_bf16 v[96:99], a[208:211], v[160:163], v[144:147]\n"
"ds_read_b128 a[196:199], v1 offset:18560\n"
"v_cvt_pk_bf16_f32 v128, v64, v65\n"
"v_mfma_f32_16x16x32_bf16 v[104:107], a[208:211], v[164:167], v[148:151]\n"
"v_exp_f32 v80, v80\n"
"v_mfma_f32_16x16x32_bf16 v[112:115], a[208:211], v[168:171], v[152:155]\n"
"v_exp_f32 v81, v81\n"
"v_mfma_f32_16x16x32_bf16 v[120:123], a[208:211], v[172:175], v[156:159]\n"
"v_exp_f32 v82, v82\n"
"v_mfma_f32_16x16x32_bf16 v[100:103], a[212:215], v[160:163], v[144:147]\n"
"ds_read_b128 a[200:203], v2 offset:12416\n"
"v_cvt_pk_bf16_f32 v129, v66, v67\n"
"v_mfma_f32_16x16x32_bf16 v[108:111], a[212:215], v[164:167], v[148:151]\n"
"v_exp_f32 v83, v83\n"
"v_mfma_f32_16x16x32_bf16 v[116:119], a[212:215], v[168:171], v[152:155]\n"
"v_exp_f32 v84, v84\n"
"v_mfma_f32_16x16x32_bf16 v[124:127], a[212:215], v[172:175], v[156:159]\n"
"v_exp_f32 v85, v85\n"
"v_mfma_f32_16x16x32_bf16 v[96:99], a[216:219], v[176:179], v[96:99]\n"
"ds_read_b128 a[204:207], v2 offset:18560\n"
"v_cvt_pk_bf16_f32 v130, v68, v69\n"
"v_mfma_f32_16x16x32_bf16 v[104:107], a[216:219], v[180:183], v[104:107]\n"
"v_exp_f32 v86, v86\n"
"v_mfma_f32_16x16x32_bf16 v[112:115], a[216:219], v[184:187], v[112:115]\n"
"v_exp_f32 v87, v87\n"
"v_mfma_f32_16x16x32_bf16 v[120:123], a[216:219], v[188:191], v[120:123]\n"
"v_cvt_pk_bf16_f32 v131, v70, v71\n"
"s_waitcnt lgkmcnt(2)\n"
"v_mfma_f32_16x16x32_bf16 v[100:103], a[220:223], v[176:179], v[100:103]\n"
"ds_read_b128 a[208:211], v1 offset:12544\n"
"v_cvt_pk_bf16_f32 v132, v72, v73\n"
"v_mfma_f32_16x16x32_bf16 v[108:111], a[220:223], v[180:183], v[108:111]\n"
"v_exp_f32 v88, v88\n"
"v_mfma_f32_16x16x32_bf16 v[116:119], a[220:223], v[184:187], v[116:119]\n"
"v_exp_f32 v89, v89\n"
"v_mfma_f32_16x16x32_bf16 v[124:127], a[220:223], v[188:191], v[124:127]\n"
"v_exp_f32 v90, v90\n"
"v_mfma_f32_16x16x32_bf16 v[96:99], a[192:195], v[192:195], v[96:99]\n"
"ds_read_b128 a[212:215], v1 offset:18688\n"
"v_cvt_pk_bf16_f32 v133, v74, v75\n"
"v_mfma_f32_16x16x32_bf16 v[104:107], a[192:195], v[196:199], v[104:107]\n"
"v_exp_f32 v91, v91\n"
"v_mfma_f32_16x16x32_bf16 v[112:115], a[192:195], v[200:203], v[112:115]\n"
"v_exp_f32 v92, v92\n"
"v_mfma_f32_16x16x32_bf16 v[120:123], a[192:195], v[204:207], v[120:123]\n"
"v_exp_f32 v93, v93\n"
"v_mfma_f32_16x16x32_bf16 v[100:103], a[196:199], v[192:195], v[100:103]\n"
"ds_read_b128 a[216:219], v2 offset:12544\n"
"v_cvt_pk_bf16_f32 v134, v76, v77\n"
"v_mfma_f32_16x16x32_bf16 v[108:111], a[196:199], v[196:199], v[108:111]\n"
"v_exp_f32 v94, v94\n"
"v_mfma_f32_16x16x32_bf16 v[116:119], a[196:199], v[200:203], v[116:119]\n"
"v_exp_f32 v95, v95\n"
"v_mfma_f32_16x16x32_bf16 v[124:127], a[196:199], v[204:207], v[124:127]\n"
"v_cvt_pk_bf16_f32 v135, v78, v79\n"
"s_waitcnt lgkmcnt(2)\n"
"v_mfma_f32_16x16x32_bf16 v[96:99], a[200:203], v[208:211], v[96:99]\n"
"ds_read_b128 a[220:223], v2 offset:18688\n"
"v_cvt_pk_bf16_f32 v136, v80, v81\n"
"v_mfma_f32_16x16x32_bf16 v[104:107], a[200:203], v[212:215], v[104:107]\n"
"v_cvt_pk_bf16_f32 v137, v82, v83\n"
"v_cvt_pk_bf16_f32 v138, v84, v85\n"
"v_mfma_f32_16x16x32_bf16 v[112:115], a[200:203], v[216:219], v[112:115]\n"
"v_cvt_pk_bf16_f32 v139, v86, v87\n"
"v_cvt_pk_bf16_f32 v140, v88, v89\n"
"v_mfma_f32_16x16x32_bf16 v[120:123], a[200:203], v[220:223], v[120:123]\n"
"v_cvt_pk_bf16_f32 v141, v90, v91\n"
"v_cvt_pk_bf16_f32 v142, v92, v93\n"
"v_mfma_f32_16x16x32_bf16 v[100:103], a[204:207], v[208:211], v[100:103]\n"
"ds_read_b64_tr_b16 a[224:225], v5 offset:0\n"
"ds_read_b64_tr_b16 a[226:227], v5 offset:6144\n"
"v_mfma_f32_16x16x32_bf16 v[108:111], a[204:207], v[212:215], v[108:111]\n"
"v_cvt_pk_bf16_f32 v143, v94, v95\n"
"v_add_f32 v41, v41, v80\n"
"v_mfma_f32_16x16x32_bf16 v[116:119], a[204:207], v[216:219], v[116:119]\n"
"v_add_f32 v42, v42, v81\n"
"v_add_f32 v41, v41, v82\n"
"v_mfma_f32_16x16x32_bf16 v[124:127], a[204:207], v[220:223], v[124:127]\n"
"v_add_f32 v42, v42, v83\n"
"v_add_f32 v41, v41, v84\n"
"v_mfma_f32_16x16x32_bf16 v[96:99], a[208:211], v[224:227], v[96:99]\n"
"ds_read_b64_tr_b16 a[228:229], v6 offset:0\n"
"ds_read_b64_tr_b16 a[230:231], v6 offset:6144\n"
"v_mfma_f32_16x16x32_bf16 v[104:107], a[208:211], v[228:231], v[104:107]\n"
"v_add_f32 v42, v42, v85\n"
"v_add_f32 v41, v41, v86\n"
"v_mfma_f32_16x16x32_bf16 v[112:115], a[208:211], v[232:235], v[112:115]\n"
"v_add_f32 v42, v42, v87\n"
"v_add_f32 v43, v43, v88\n"
"v_mfma_f32_16x16x32_bf16 v[120:123], a[208:211], v[236:239], v[120:123]\n"
"v_add_f32 v44, v44, v89\n"
"s_waitcnt lgkmcnt(4)\n"
"v_mfma_f32_16x16x32_bf16 v[100:103], a[212:215], v[224:227], v[100:103]\n"
"ds_read_b64_tr_b16 a[232:233], v7 offset:0\n"
"ds_read_b64_tr_b16 a[234:235], v7 offset:6144\n"
"v_mfma_f32_16x16x32_bf16 v[108:111], a[212:215], v[228:231], v[108:111]\n"
"v_add_f32 v43, v43, v90\n"
"v_add_f32 v44, v44, v91\n"
"v_mfma_f32_16x16x32_bf16 v[116:119], a[212:215], v[232:235], v[116:119]\n"
"v_add_f32 v43, v43, v92\n"
"v_add_f32 v44, v44, v93\n"
"v_mfma_f32_16x16x32_bf16 v[124:127], a[212:215], v[236:239], v[124:127]\n"
"v_add_f32 v43, v43, v94\n"
"v_add_f32 v44, v44, v95\n"
"v_mfma_f32_16x16x32_bf16 v[96:99], a[216:219], v[240:243], v[96:99]\n"
"ds_read_b64_tr_b16 a[236:237], v8 offset:0\n"
"ds_read_b64_tr_b16 a[238:239], v8 offset:6144\n"
"v_mfma_f32_16x16x32_bf16 v[104:107], a[216:219], v[244:247], v[104:107]\n"
"v_mfma_f32_16x16x32_bf16 v[112:115], a[216:219], v[248:251], v[112:115]\n"
"v_mfma_f32_16x16x32_bf16 v[120:123], a[216:219], v[252:255], v[120:123]\n"
"v_mfma_f32_16x16x32_bf16 v[100:103], a[220:223], v[240:243], v[100:103]\n"
"ds_read_b64_tr_b16 a[240:241], v5 offset:128\n"
"ds_read_b64_tr_b16 a[242:243], v5 offset:6272\n"
"v_mfma_f32_16x16x32_bf16 v[108:111], a[220:223], v[244:247], v[108:111]\n"
"v_max3_f32 v53, v96, v97, v98\n"
"v_mfma_f32_16x16x32_bf16 v[116:119], a[220:223], v[248:251], v[116:119]\n"
"v_max3_f32 v54, v99, v104, v105\n"
"v_max3_f32 v53, v53, v106, v107\n"
"v_mfma_f32_16x16x32_bf16 v[124:127], a[220:223], v[252:255], v[124:127]\n"
"v_max3_f32 v54, v54, v112, v113\n"
"s_waitcnt lgkmcnt(4)\n"
"v_mfma_f32_16x16x32_bf16 a[0:3], a[224:227], v[128:131], 0\n"
"ds_read_b64_tr_b16 a[244:245], v6 offset:128\n"
"ds_read_b64_tr_b16 a[246:247], v6 offset:6272\n"
"v_mfma_f32_16x16x32_bf16 a[4:7], a[224:227], v[132:135], 0\n"
"v_max3_f32 v53, v53, v114, v115\n"
"v_max3_f32 v54, v54, v120, v121\n"
"v_mfma_f32_16x16x32_bf16 a[8:11], a[224:227], v[136:139], 0\n"
"v_max3_f32 v53, v53, v122, v123\n"
"v_max3_f32 v54, v54, v100, v101\n"
"v_mfma_f32_16x16x32_bf16 a[12:15], a[224:227], v[140:143], 0\n"
"v_max3_f32 v53, v53, v102, v103\n"
"v_max3_f32 v54, v54, v108, v109\n"
"v_mfma_f32_16x16x32_bf16 a[16:19], a[228:231], v[128:131], 0\n"
"v_max3_f32 v53, v53, v110, v111\n"
"v_max3_f32 v54, v54, v116, v117\n"
"v_mfma_f32_16x16x32_bf16 a[20:23], a[228:231], v[132:135], 0\n"
"v_max3_f32 v53, v53, v118, v119\n"
"v_max3_f32 v54, v54, v124, v125\n"
"v_mfma_f32_16x16x32_bf16 a[24:27], a[228:231], v[136:139], 0\n"
"v_max3_f32 v53, v53, v126, v127\n"
"v_max_f32 v53, v53, v54\n"
"v_mfma_f32_16x16x32_bf16 a[28:31], a[228:231], v[140:143], 0\n"
"v_cmp_lt_f32 vcc, s64, v53\n"
"ds_read_b64_tr_b16 a[248:249], v7 offset:128\n"
"v_mfma_f32_16x16x32_bf16 a[32:35], a[232:235], v[128:131], 0\n"
"ds_read_b64_tr_b16 a[250:251], v7 offset:6272\n"
"ds_read_b64_tr_b16 a[252:253], v8 offset:128\n"
"v_mfma_f32_16x16x32_bf16 a[36:39], a[232:235], v[132:135], 0\n"
"ds_read_b64_tr_b16 a[254:255], v8 offset:6272\n"
"v_mfma_f32_16x16x32_bf16 a[40:43], a[232:235], v[136:139], 0\n"
"v_mfma_f32_16x16x32_bf16 a[44:47], a[232:235], v[140:143], 0\n"
"s_waitcnt lgkmcnt(4)\n"
"v_mfma_f32_16x16x32_bf16 a[48:51], a[236:239], v[128:131], 0\n"
"ds_read_b64_tr_b16 a[224:225], v5 offset:256\n"
"ds_read_b64_tr_b16 a[226:227], v5 offset:6400\n"
"v_mfma_f32_16x16x32_bf16 a[52:55], a[236:239], v[132:135], 0\n"
"s_cbranch_vccnz K1A_RS0\n"
"K1A_RSb0:\n"
"v_mfma_f32_16x16x32_bf16 a[56:59], a[236:239], v[136:139], 0\n"
"v_exp_f32 v96, v96\n"
"v_mfma_f32_16x16x32_bf16 a[60:63], a[236:239], v[140:143], 0\n"
"v_exp_f32 v97, v97\n"
"v_mfma_f32_16x16x32_bf16 a[64:67], a[240:243], v[128:131], 0\n"
"ds_read_b64_tr_b16 a[228:229], v6 offset:256\n"
"ds_read_b64_tr_b16 a[230:231], v6 offset:6400\n"
"v_mfma_f32_16x16x32_bf16 a[68:71], a[240:243], v[132:135], 0\n"
"v_exp_f32 v98, v98\n"
"v_mfma_f32_16x16x32_bf16 a[72:75], a[240:243], v[136:139], 0\n"
"v_exp_f32 v99, v99\n"
"v_mfma_f32_16x16x32_bf16 a[76:79], a[240:243], v[140:143], 0\n"
"v_exp_f32 v100, v100\n"
"v_mfma_f32_16x16x32_bf16 a[80:83], a[244:247], v[128:131], 0\n"
"ds_read_b64_tr_b16 a[232:233], v7 offset:256\n"
"ds_read_b64_tr_b16 a[234:235], v7 offset:6400\n"
"v_mfma_f32_16x16x32_bf16 a[84:87], a[244:247], v[132:135], 0\n"
"v_exp_f32 v101, v101\n"
"v_mfma_f32_16x16x32_bf16 a[88:91], a[244:247], v[136:139], 0\n"
"v_exp_f32 v102, v102\n"
"v_mfma_f32_16x16x32_bf16 a[92:95], a[244:247], v[140:143], 0\n"
"v_add_f32 v37, v37, v96\n"
"s_waitcnt lgkmcnt(4)\n"
"v_mfma_f32_16x16x32_bf16 a[96:99], a[248:251], v[128:131], 0\n"
"ds_read_b64_tr_b16 a[236:237], v8 offset:256\n"
"ds_read_b64_tr_b16 a[238:239], v8 offset:6400\n"
"v_mfma_f32_16x16x32_bf16 a[100:103], a[248:251], v[132:135], 0\n"
"v_exp_f32 v103, v103\n"
"v_mfma_f32_16x16x32_bf16 a[104:107], a[248:251], v[136:139], 0\n"
"s_barrier\n"
"v_add_f32 v38, v38, v97\n"
"v_mfma_f32_16x16x32_bf16 a[108:111], a[248:251], v[140:143], 0\n"
"v_exp_f32 v104, v104\n"
"v_mfma_f32_16x16x32_bf16 a[112:115], a[252:255], v[128:131], 0\n"
"ds_read_b128 a[192:195], v1 offset:24576\n"
"v_add_f32 v37, v37, v98\n"
"v_mfma_f32_16x16x32_bf16 a[116:119], a[252:255], v[132:135], 0\n"
"v_exp_f32 v105, v105\n"
"v_mfma_f32_16x16x32_bf16 a[120:123], a[252:255], v[136:139], 0\n"
"v_exp_f32 v106, v106\n"
"v_mfma_f32_16x16x32_bf16 a[124:127], a[252:255], v[140:143], 0\n"
"v_exp_f32 v107, v107\n"
"v_mfma_f32_16x16x32_bf16 a[128:131], a[224:227], v[128:131], 0\n"
"ds_read_b128 a[196:199], v1 offset:30720\n"
"v_add_f32 v38, v38, v99\n"
"v_mfma_f32_16x16x32_bf16 a[132:135], a[224:227], v[132:135], 0\n"
"v_exp_f32 v108, v108\n"
"v_mfma_f32_16x16x32_bf16 a[136:139], a[224:227], v[136:139], 0\n"
"v_exp_f32 v109, v109\n"
"v_mfma_f32_16x16x32_bf16 a[140:143], a[224:227], v[140:143], 0\n"
"v_add_f32 v37, v37, v100\n"
"s_waitcnt lgkmcnt(2)\n"
"v_mfma_f32_16x16x32_bf16 a[144:147], a[228:231], v[128:131], 0\n"
"ds_read_b128 a[200:203], v2 offset:24576\n"
"v_add_f32 v38, v38, v101\n"
"v_mfma_f32_16x16x32_bf16 a[148:151], a[228:231], v[132:135], 0\n"
"v_exp_f32 v110, v110\n"
"v_mfma_f32_16x16x32_bf16 a[152:155], a[228:231], v[136:139], 0\n"
"v_exp_f32 v111, v111\n"
"v_mfma_f32_16x16x32_bf16 a[156:159], a[228:231], v[140:143], 0\n"
"v_add_f32 v37, v37, v102\n"
"v_add_f32 v38, v38, v103\n"
"v_mfma_f32_16x16x32_bf16 a[160:163], a[232:235], v[128:131], 0\n"
"ds_read_b128 a[204:207], v2 offset:30720\n"
"v_add_f32 v39, v39, v104\n"
"v_mfma_f32_16x16x32_bf16 a[164:167], a[232:235], v[132:135], 0\n"
"v_add_f32 v40, v40, v105\n"
"v_add_f32 v39, v39, v106\n"
"v_mfma_f32_16x16x32_bf16 a[168:171], a[232:235], v[136:139], 0\n"
"v_add_f32 v40, v40, v107\n"
"v_add_f32 v39, v39, v108\n"
"v_mfma_f32_16x16x32_bf16 a[172:175], a[232:235], v[140:143], 0\n"
"v_add_f32 v40, v40, v109\n"
"v_add_f32 v39, v39, v110\n"
"v_mfma_f32_16x16x32_bf16 a[176:179], a[236:239], v[128:131], 0\n"
"ds_read_b128 a[208:211], v1 offset:24704\n"
"v_add_f32 v40, v40, v111\n"
"v_mfma_f32_16x16x32_bf16 a[180:183], a[236:239], v[132:135], 0\n"
"v_mfma_f32_16x16x32_bf16 a[184:187], a[236:239], v[136:139], 0\n"
"v_mfma_f32_16x16x32_bf16 a[188:191], a[236:239], v[140:143], 0\n"
"s_cbranch_vccnz K1A_RO0\n"
"K1A_ROb0:\n"
"s_mov_b32 s61, 5\n"
"K1A_AL:\n"
"s_waitcnt lgkmcnt(2)\n"
"v_mfma_f32_16x16x32_bf16 v[64:67], a[192:195], v[160:163], v[144:147]\n"
"ds_read_b128 a[212:215], v1 offset:30848\n"
"v_cvt_pk_bf16_f32 v128, v96, v97\n"
"v_mfma_f32_16x16x32_bf16 v[72:75], a[192:195], v[164:167], v[148:151]\n"
"v_exp_f32 v112, v112\n"
"v_mfma_f32_16x16x32_bf16 v[80:83], a[192:195], v[168:171], v[152:155]\n"
"v_exp_f32 v113, v113\n"
"v_mfma_f32_16x16x32_bf16 v[88:91], a[192:195], v[172:175], v[156:159]\n"
"v_exp_f32 v114, v114\n"
"v_mfma_f32_16x16x32_bf16 v[68:71], a[196:199], v[160:163], v[144:147]\n"
"ds_read_b128 a[216:219], v2 offset:24704\n"
"v_cvt_pk_bf16_f32 v129, v98, v99\n"
"v_mfma_f32_16x16x32_bf16 v[76:79], a[196:199], v[164:167], v[148:151]\n"
"v_exp_f32 v115, v115\n"
"v_mfma_f32_16x16x32_bf16 v[84:87], a[196:199], v[168:171], v[152:155]\n"
"v_exp_f32 v116, v116\n"
"v_mfma_f32_16x16x32_bf16 v[92:95], a[196:199], v[172:175], v[156:159]\n"
"v_exp_f32 v117, v117\n"
"v_mfma_f32_16x16x32_bf16 v[64:67], a[200:203], v[176:179], v[64:67]\n"
"ds_read_b128 a[220:223], v2 offset:30848\n"
"v_cvt_pk_bf16_f32 v130, v100, v101\n"
"v_mfma_f32_16x16x32_bf16 v[72:75], a[200:203], v[180:183], v[72:75]\n"
"v_exp_f32 v118, v118\n"
"v_mfma_f32_16x16x32_bf16 v[80:83], a[200:203], v[184:187], v[80:83]\n"
"v_exp_f32 v119, v119\n"
"v_mfma_f32_16x16x32_bf16 v[88:91], a[200:203], v[188:191], v[88:91]\n"
"v_cvt_pk_bf16_f32 v131, v102, v103\n"
"s_waitcnt lgkmcnt(2)\n"
"v_mfma_f32_16x16x32_bf16 v[68:71], a[204:207], v[176:179], v[68:71]\n"
"ds_read_b128 a[192:195], v1 offset:24832\n"
"v_cvt_pk_bf16_f32 v132, v104, v105\n"
"v_mfma_f32_16x16x32_bf16 v[76:79], a[204:207], v[180:183], v[76:79]\n"
"v_exp_f32 v120, v120\n"
"v_mfma_f32_16x16x32_bf16 v[84:87], a[204:207], v[184:187], v[84:87]\n"
"v_exp_f32 v121, v121\n"
"v_mfma_f32_16x16x32_bf16 v[92:95], a[204:207], v[188:191], v[92:95]\n"
"v_exp_f32 v122, v122\n"
"v_mfma_f32_16x16x32_bf16 v[64:67], a[208:211], v[192:195], v[64:67]\n"
"ds_read_b128 a[196:199], v1 offset:30976\n"
"v_cvt_pk_bf16_f32 v133, v106, v107\n"
"v_mfma_f32_16x16x32_bf16 v[72:75], a[208:211], v[196:199], v[72:75]\n"
"v_exp_f32 v123, v123\n"
"v_mfma_f32_16x16x32_bf16 v[80:83], a[208:211], v[200:203], v[80:83]\n"
"v_exp_f32 v124, v124\n"
"v_mfma_f32_16x16x32_bf16 v[88:91], a[208:211], v[204:207], v[88:91]\n"
"v_exp_f32 v125, v125\n"
"v_mfma_f32_16x16x32_bf16 v[68:71], a[212:215], v[192:195], v[68:71]\n"
"ds_read_b128 a[200:203], v2 offset:24832\n"
"v_cvt_pk_bf16_f32 v134, v108, v109\n"
"v_mfma_f32_16x16x32_bf16 v[76:79], a[212:215], v[196:199], v[76:79]\n"
"v_exp_f32 v126, v126\n"
"v_mfma_f32_16x16x32_bf16 v[84:87], a[212:215], v[200:203], v[84:87]\n"
"v_exp_f32 v127, v127\n"
"v_mfma_f32_16x16x32_bf16 v[92:95], a[212:215], v[204:207], v[92:95]\n"
"v_cvt_pk_bf16_f32 v135, v110, v111\n"
"s_waitcnt lgkmcnt(2)\n"
"v_mfma_f32_16x16x32_bf16 v[64:67], a[216:219], v[208:211], v[64:67]\n"
"ds_read_b128 a[204:207], v2 offset:30976\n"
"v_cvt_pk_bf16_f32 v136, v112, v113\n"
"v_mfma_f32_16x16x32_bf16 v[72:75], a[216:219], v[212:215], v[72:75]\n"
"v_cvt_pk_bf16_f32 v137, v114, v115\n"
"v_cvt_pk_bf16_f32 v138, v116, v117\n"
"v_mfma_f32_16x16x32_bf16 v[80:83], a[216:219], v[216:219], v[80:83]\n"
"v_cvt_pk_bf16_f32 v139, v118, v119\n"
"v_cvt_pk_bf16_f32 v140, v120, v121\n"
"v_mfma_f32_16x16x32_bf16 v[88:91], a[216:219], v[220:223], v[88:91]\n"
"v_cvt_pk_bf16_f32 v141, v122, v123\n"
"v_cvt_pk_bf16_f32 v142, v124, v125\n"
"v_mfma_f32_16x16x32_bf16 v[68:71], a[220:223], v[208:211], v[68:71]\n"
"ds_read_b64_tr_b16 a[240:241], v5 offset:12288\n"
"ds_read_b64_tr_b16 a[242:243], v5 offset:18432\n"
"v_mfma_f32_16x16x32_bf16 v[76:79], a[220:223], v[212:215], v[76:79]\n"
"v_cvt_pk_bf16_f32 v143, v126, v127\n"
"v_add_f32 v41, v41, v112\n"
"v_mfma_f32_16x16x32_bf16 v[84:87], a[220:223], v[216:219], v[84:87]\n"
"v_add_f32 v42, v42, v113\n"
"v_add_f32 v41, v41, v114\n"
"v_mfma_f32_16x16x32_bf16 v[92:95], a[220:223], v[220:223], v[92:95]\n"
"v_add_f32 v42, v42, v115\n"
"v_add_f32 v41, v41, v116\n"
"v_mfma_f32_16x16x32_bf16 v[64:67], a[192:195], v[224:227], v[64:67]\n"
"ds_read_b64_tr_b16 a[244:245], v6 offset:12288\n"
"ds_read_b64_tr_b16 a[246:247], v6 offset:18432\n"
"v_mfma_f32_16x16x32_bf16 v[72:75], a[192:195], v[228:231], v[72:75]\n"
"v_add_f32 v42, v42, v117\n"
"v_add_f32 v41, v41, v118\n"
"v_mfma_f32_16x16x32_bf16 v[80:83], a[192:195], v[232:235], v[80:83]\n"
"v_add_f32 v42, v42, v119\n"
"v_add_f32 v43, v43, v120\n"
"v_mfma_f32_16x16x32_bf16 v[88:91], a[192:195], v[236:239], v[88:91]\n"
"v_add_f32 v44, v44, v121\n"
"s_waitcnt lgkmcnt(4)\n"
"v_mfma_f32_16x16x32_bf16 v[68:71], a[196:199], v[224:227], v[68:71]\n"
"ds_read_b64_tr_b16 a[248:249], v7 offset:12288\n"
"ds_read_b64_tr_b16 a[250:251], v7 offset:18432\n"
"v_mfma_f32_16x16x32_bf16 v[76:79], a[196:199], v[228:231], v[76:79]\n"
"v_add_f32 v43, v43, v122\n"
"v_add_f32 v44, v44, v123\n"
"v_mfma_f32_16x16x32_bf16 v[84:87], a[196:199], v[232:235], v[84:87]\n"
"v_add_f32 v43, v43, v124\n"
"v_add_f32 v44, v44, v125\n"
"v_mfma_f32_16x16x32_bf16 v[92:95], a[196:199], v[236:239], v[92:95]\n"
"v_add_f32 v43, v43, v126\n"
"v_add_f32 v44, v44, v127\n"
"v_mfma_f32_16x16x32_bf16 v[64:67], a[200:203], v[240:243], v[64:67]\n"
"ds_read_b64_tr_b16 a[252:253], v8 offset:12288\n"
"ds_read_b64_tr_b16 a[254:255], v8 offset:18432\n"
"v_mfma_f32_16x16x32_bf16 v[72:75], a[200:203], v[244:247], v[72:75]\n"
"v_mfma_f32_16x16x32_bf16 v[80:83], a[200:203], v[248:251], v[80:83]\n"
"v_mfma_f32_16x16x32_bf16 v[88:91], a[200:203], v[252:255], v[88:91]\n"
"v_mfma_f32_16x16x32_bf16 v[68:71], a[204:207], v[240:243], v[68:71]\n"
"ds_read_b64_tr_b16 a[224:225], v5 offset:12416\n"
"ds_read_b64_tr_b16 a[226:227], v5 offset:18560\n"
"v_mfma_f32_16x16x32_bf16 v[76:79], a[204:207], v[244:247], v[76:79]\n"
"v_max3_f32 v53, v64, v65, v66\n"
"v_mfma_f32_16x16x32_bf16 v[84:87], a[204:207], v[248:251], v[84:87]\n"
"v_max3_f32 v54, v67, v72, v73\n"
"v_max3_f32 v53, v53, v74, v75\n"
"v_mfma_f32_16x16x32_bf16 v[92:95], a[204:207], v[252:255], v[92:95]\n"
"v_max3_f32 v54, v54, v80, v81\n"
"s_waitcnt lgkmcnt(4)\n"
"v_mfma_f32_16x16x32_bf16 a[0:3], a[240:243], v[128:131], a[0:3]\n"
"ds_read_b64_tr_b16 a[228:229], v6 offset:12416\n"
"ds_read_b64_tr_b16 a[230:231], v6 offset:18560\n"
"v_mfma_f32_16x16x32_bf16 a[4:7], a[240:243], v[132:135], a[4:7]\n"
"v_max3_f32 v53, v53, v82, v83\n"
"v_max3_f32 v54, v54, v88, v89\n"
"v_mfma_f32_16x16x32_bf16 a[8:11], a[240:243], v[136:139], a[8:11]\n"
"v_max3_f32 v53, v53, v90, v91\n"
"v_max3_f32 v54, v54, v68, v69\n"
"v_mfma_f32_16x16x32_bf16 a[12:15], a[240:243], v[140:143], a[12:15]\n"
"v_max3_f32 v53, v53, v70, v71\n"
"v_max3_f32 v54, v54, v76, v77\n"
"v_mfma_f32_16x16x32_bf16 a[16:19], a[244:247], v[128:131], a[16:19]\n"
"v_max3_f32 v53, v53, v78, v79\n"
"v_max3_f32 v54, v54, v84, v85\n"
"v_mfma_f32_16x16x32_bf16 a[20:23], a[244:247], v[132:135], a[20:23]\n"
"v_max3_f32 v53, v53, v86, v87\n"
"v_max3_f32 v54, v54, v92, v93\n"
"v_mfma_f32_16x16x32_bf16 a[24:27], a[244:247], v[136:139], a[24:27]\n"
"v_max3_f32 v53, v53, v94, v95\n"
"v_max_f32 v53, v53, v54\n"
"v_mfma_f32_16x16x32_bf16 a[28:31], a[244:247], v[140:143], a[28:31]\n"
"v_cmp_lt_f32 vcc, s64, v53\n"
"ds_read_b64_tr_b16 a[232:233], v7 offset:12416\n"
"v_mfma_f32_16x16x32_bf16 a[32:35], a[248:251], v[128:131], a[32:35]\n"
"ds_read_b64_tr_b16 a[234:235], v7 offset:18560\n"
"ds_read_b64_tr_b16 a[236:237], v8 offset:12416\n"
"v_mfma_f32_16x16x32_bf16 a[36:39], a[248:251], v[132:135], a[36:39]\n"
"ds_read_b64_tr_b16 a[238:239], v8 offset:18560\n"
"v_mfma_f32_16x16x32_bf16 a[40:43], a[248:251], v[136:139], a[40:43]\n"
"v_mfma_f32_16x16x32_bf16 a[44:47], a[248:251], v[140:143], a[44:47]\n"
"s_waitcnt lgkmcnt(4)\n"
"v_mfma_f32_16x16x32_bf16 a[48:51], a[252:255], v[128:131], a[48:51]\n"
"ds_read_b64_tr_b16 a[240:241], v5 offset:12544\n"
"ds_read_b64_tr_b16 a[242:243], v5 offset:18688\n"
"v_mfma_f32_16x16x32_bf16 a[52:55], a[252:255], v[132:135], a[52:55]\n"
"s_cbranch_vccnz K1A_RS1\n"
"K1A_RSb1:\n"
"v_mfma_f32_16x16x32_bf16 a[56:59], a[252:255], v[136:139], a[56:59]\n"
"v_exp_f32 v64, v64\n"
"v_mfma_f32_16x16x32_bf16 a[60:63], a[252:255], v[140:143], a[60:63]\n"
"v_exp_f32 v65, v65\n"
"v_mfma_f32_16x16x32_bf16 a[64:67], a[224:227], v[128:131], a[64:67]\n"
"ds_read_b64_tr_b16 a[244:245], v6 offset:12544\n"
"ds_read_b64_tr_b16 a[246:247], v6 offset:18688\n"
"v_mfma_f32_16x16x32_bf16 a[68:71], a[224:227], v[132:135], a[68:71]\n"
"v_exp_f32 v66, v66\n"
"v_mfma_f32_16x16x32_bf16 a[72:75], a[224:227], v[136:139], a[72:75]\n"
"v_exp_f32 v67, v67\n"
"v_mfma_f32_16x16x32_bf16 a[76:79], a[224:227], v[140:143], a[76:79]\n"
"v_exp_f32 v68, v68\n"
"v_mfma_f32_16x16x32_bf16 a[80:83], a[228:231], v[128:131], a[80:83]\n"
"ds_read_b64_tr_b16 a[248:249], v7 offset:12544\n"
"ds_read_b64_tr_b16 a[250:251], v7 offset:18688\n"
"v_mfma_f32_16x16x32_bf16 a[84:87], a[228:231], v[132:135], a[84:87]\n"
"v_exp_f32 v69, v69\n"
"v_mfma_f32_16x16x32_bf16 a[88:91], a[228:231], v[136:139], a[88:91]\n"
"v_exp_f32 v70, v70\n"
"v_mfma_f32_16x16x32_bf16 a[92:95], a[228:231], v[140:143], a[92:95]\n"
"v_add_f32 v37, v37, v64\n"
"s_waitcnt lgkmcnt(4)\n"
"v_mfma_f32_16x16x32_bf16 a[96:99], a[232:235], v[128:131], a[96:99]\n"
"ds_read_b64_tr_b16 a[252:253], v8 offset:12544\n"
"ds_read_b64_tr_b16 a[254:255], v8 offset:18688\n"
"v_mfma_f32_16x16x32_bf16 a[100:103], a[232:235], v[132:135], a[100:103]\n"
"v_exp_f32 v71, v71\n"
"v_mfma_f32_16x16x32_bf16 a[104:107], a[232:235], v[136:139], a[104:107]\n"
"s_barrier\n"
"v_add_f32 v38, v38, v65\n"
"v_mfma_f32_16x16x32_bf16 a[108:111], a[232:235], v[140:143], a[108:111]\n"
"v_exp_f32 v72, v72\n"
"v_mfma_f32_16x16x32_bf16 a[112:115], a[236:239], v[128:131], a[112:115]\n"
"ds_read_b128 a[208:211], v3 offset:0\n"
"v_add_f32 v37, v37, v66\n"
"v_mfma_f32_16x16x32_bf16 a[116:119], a[236:239], v[132:135], a[116:119]\n"
"v_exp_f32 v73, v73\n"
"v_mfma_f32_16x16x32_bf16 a[120:123], a[236:239], v[136:139], a[120:123]\n"
"v_exp_f32 v74, v74\n"
"v_mfma_f32_16x16x32_bf16 a[124:127], a[236:239], v[140:143], a[124:127]\n"
"v_exp_f32 v75, v75\n"
"v_mfma_f32_16x16x32_bf16 a[128:131], a[240:243], v[128:131], a[128:131]\n"
"ds_read_b128 a[212:215], v3 offset:6144\n"
"v_add_f32 v38, v38, v67\n"
"v_mfma_f32_16x16x32_bf16 a[132:135], a[240:243], v[132:135], a[132:135]\n"
"v_exp_f32 v76, v76\n"
"v_mfma_f32_16x16x32_bf16 a[136:139], a[240:243], v[136:139], a[136:139]\n"
"v_exp_f32 v77, v77\n"
"v_mfma_f32_16x16x32_bf16 a[140:143], a[240:243], v[140:143], a[140:143]\n"
"v_add_f32 v37, v37, v68\n"
"s_waitcnt lgkmcnt(2)\n"
"v_mfma_f32_16x16x32_bf16 a[144:147], a[244:247], v[128:131], a[144:147]\n"
"ds_read_b128 a[216:219], v4 offset:0\n"
"v_add_f32 v38, v38, v69\n"
"v_mfma_f32_16x16x32_bf16 a[148:151], a[244:247], v[132:135], a[148:151]\n"
"v_exp_f32 v78, v78\n"
"v_mfma_f32_16x16x32_bf16 a[152:155], a[244:247], v[136:139], a[152:155]\n"
"v_exp_f32 v79, v79\n"
"v_mfma_f32_16x16x32_bf16 a[156:159], a[244:247], v[140:143], a[156:159]\n"
"v_add_f32 v37, v37, v70\n"
"v_add_f32 v38, v38, v71\n"
"v_mfma_f32_16x16x32_bf16 a[160:163], a[248:251], v[128:131], a[160:163]\n"
"ds_read_b128 a[220:223], v4 offset:6144\n"
"v_add_f32 v39, v39, v72\n"
"v_mfma_f32_16x16x32_bf16 a[164:167], a[248:251], v[132:135], a[164:167]\n"
"v_add_f32 v40, v40, v73\n"
"v_add_f32 v39, v39, v74\n"
"v_mfma_f32_16x16x32_bf16 a[168:171], a[248:251], v[136:139], a[168:171]\n"
"v_add_f32 v40, v40, v75\n"
"v_add_f32 v39, v39, v76\n"
"v_mfma_f32_16x16x32_bf16 a[172:175], a[248:251], v[140:143], a[172:175]\n"
"v_add_f32 v40, v40, v77\n"
"v_add_f32 v39, v39, v78\n"
"v_mfma_f32_16x16x32_bf16 a[176:179], a[252:255], v[128:131], a[176:179]\n"
"ds_read_b128 a[192:195], v3 offset:128\n"
"v_add_f32 v40, v40, v79\n"
"v_mfma_f32_16x16x32_bf16 a[180:183], a[252:255], v[132:135], a[180:183]\n"
"v_mfma_f32_16x16x32_bf16 a[184:187], a[252:255], v[136:139], a[184:187]\n"
"v_mfma_f32_16x16x32_bf16 a[188:191], a[252:255], v[140:143], a[188:191]\n"
"s_cbranch_vccnz K1A_RO1\n"
"K1A_ROb1:\n"
"s_waitcnt lgkmcnt(2)\n"
"v_mfma_f32_16x16x32_bf16 v[96:99], a[208:211], v[160:163], v[144:147]\n"
"ds_read_b128 a[196:199], v3 offset:6272\n"
"v_cvt_pk_bf16_f32 v128, v64, v65\n"
"v_mfma_f32_16x16x32_bf16 v[104:107], a[208:211], v[164:167], v[148:151]\n"
"v_exp_f32 v80, v80\n"
"v_mfma_f32_16x16x32_bf16 v[112:115], a[208:211], v[168:171], v[152:155]\n"
"v_exp_f32 v81, v81\n"
"v_mfma_f32_16x16x32_bf16 v[120:123], a[208:211], v[172:175], v[156:159]\n"
"v_exp_f32 v82, v82\n"
"v_mfma_f32_16x16x32_bf16 v[100:103], a[212:215], v[160:163], v[144:147]\n"
"ds_read_b128 a[200:203], v4 offset:128\n"
"v_cvt_pk_bf16_f32 v129, v66, v67\n"
"v_mfma_f32_16x16x32_bf16 v[108:111], a[212:215], v[164:167], v[148:151]\n"
"v_exp_f32 v83, v83\n"
"v_mfma_f32_16x16x32_bf16 v[116:119], a[212:215], v[168:171], v[152:155]\n"
"v_exp_f32 v84, v84\n"
"v_mfma_f32_16x16x32_bf16 v[124:127], a[212:215], v[172:175], v[156:159]\n"
"v_exp_f32 v85, v85\n"
"v_mfma_f32_16x16x32_bf16 v[96:99], a[216:219], v[176:179], v[96:99]\n"
"ds_read_b128 a[204:207], v4 offset:6272\n"
"v_cvt_pk_bf16_f32 v130, v68, v69\n"
"v_mfma_f32_16x16x32_bf16 v[104:107], a[216:219], v[180:183], v[104:107]\n"
"v_exp_f32 v86, v86\n"
"v_mfma_f32_16x16x32_bf16 v[112:115], a[216:219], v[184:187], v[112:115]\n"
"v_exp_f32 v87, v87\n"
"v_mfma_f32_16x16x32_bf16 v[120:123], a[216:219], v[188:191], v[120:123]\n"
"v_cvt_pk_bf16_f32 v131, v70, v71\n"
"s_waitcnt lgkmcnt(2)\n"
"v_mfma_f32_16x16x32_bf16 v[100:103], a[220:223], v[176:179], v[100:103]\n"
"ds_read_b128 a[208:211], v3 offset:256\n"
"v_cvt_pk_bf16_f32 v132, v72, v73\n"
"v_mfma_f32_16x16x32_bf16 v[108:111], a[220:223], v[180:183], v[108:111]\n"
"v_exp_f32 v88, v88\n"
"v_mfma_f32_16x16x32_bf16 v[116:119], a[220:223], v[184:187], v[116:119]\n"
"v_exp_f32 v89, v89\n"
"v_mfma_f32_16x16x32_bf16 v[124:127], a[220:223], v[188:191], v[124:127]\n"
"v_exp_f32 v90, v90\n"
"v_mfma_f32_16x16x32_bf16 v[96:99], a[192:195], v[192:195], v[96:99]\n"
"ds_read_b128 a[212:215], v3 offset:6400\n"
"v_cvt_pk_bf16_f32 v133, v74, v75\n"
"v_mfma_f32_16x16x32_bf16 v[104:107], a[192:195], v[196:199], v[104:107]\n"
"v_exp_f32 v91, v91\n"
"v_mfma_f32_16x16x32_bf16 v[112:115], a[192:195], v[200:203], v[112:115]\n"
"v_exp_f32 v92, v92\n"
"v_mfma_f32_16x16x32_bf16 v[120:123], a[192:195], v[204:207], v[120:123]\n"
"v_exp_f32 v93, v93\n"
"v_mfma_f32_16x16x32_bf16 v[100:103], a[196:199], v[192:195], v[100:103]\n"
"ds_read_b128 a[216:219], v4 offset:256\n"
"v_cvt_pk_bf16_f32 v134, v76, v77\n"
"v_mfma_f32_16x16x32_bf16 v[108:111], a[196:199], v[196:199], v[108:111]\n"
"v_exp_f32 v94, v94\n"
"v_mfma_f32_16x16x32_bf16 v[116:119], a[196:199], v[200:203], v[116:119]\n"
"v_exp_f32 v95, v95\n"
"v_mfma_f32_16x16x32_bf16 v[124:127], a[196:199], v[204:207], v[124:127]\n"
"v_cvt_pk_bf16_f32 v135, v78, v79\n"
"s_waitcnt lgkmcnt(2)\n"
"v_mfma_f32_16x16x32_bf16 v[96:99], a[200:203], v[208:211], v[96:99]\n"
"ds_read_b128 a[220:223], v4 offset:6400\n"
"v_cvt_pk_bf16_f32 v136, v80, v81\n"
"v_mfma_f32_16x16x32_bf16 v[104:107], a[200:203], v[212:215], v[104:107]\n"
"v_cvt_pk_bf16_f32 v137, v82, v83\n"
"v_cvt_pk_bf16_f32 v138, v84, v85\n"
"v_mfma_f32_16x16x32_bf16 v[112:115], a[200:203], v[216:219], v[112:115]\n"
"v_cvt_pk_bf16_f32 v139, v86, v87\n"
"v_cvt_pk_bf16_f32 v140, v88, v89\n"
"v_mfma_f32_16x16x32_bf16 v[120:123], a[200:203], v[220:223], v[120:123]\n"
"v_cvt_pk_bf16_f32 v141, v90, v91\n"
"v_cvt_pk_bf16_f32 v142, v92, v93\n"
"v_mfma_f32_16x16x32_bf16 v[100:103], a[204:207], v[208:211], v[100:103]\n"
"ds_read_b64_tr_b16 a[224:225], v5 offset:24576\n"
"ds_read_b64_tr_b16 a[226:227], v5 offset:30720\n"
"v_mfma_f32_16x16x32_bf16 v[108:111], a[204:207], v[212:215], v[108:111]\n"
"v_cvt_pk_bf16_f32 v143, v94, v95\n"
"v_add_f32 v41, v41, v80\n"
"v_mfma_f32_16x16x32_bf16 v[116:119], a[204:207], v[216:219], v[116:119]\n"
"v_add_f32 v42, v42, v81\n"
"v_add_f32 v41, v41, v82\n"
"v_mfma_f32_16x16x32_bf16 v[124:127], a[204:207], v[220:223], v[124:127]\n"
"v_add_f32 v42, v42, v83\n"
"v_add_f32 v41, v41, v84\n"
"v_mfma_f32_16x16x32_bf16 v[96:99], a[208:211], v[224:227], v[96:99]\n"
"ds_read_b64_tr_b16 a[228:229], v6 offset:24576\n"
"ds_read_b64_tr_b16 a[230:231], v6 offset:30720\n"
"v_mfma_f32_16x16x32_bf16 v[104:107], a[208:211], v[228:231], v[104:107]\n"
"v_add_f32 v42, v42, v85\n"
"v_add_f32 v41, v41, v86\n"
"v_mfma_f32_16x16x32_bf16 v[112:115], a[208:211], v[232:235], v[112:115]\n"
"v_add_f32 v42, v42, v87\n"
"v_add_f32 v43, v43, v88\n"
"v_mfma_f32_16x16x32_bf16 v[120:123], a[208:211], v[236:239], v[120:123]\n"
"v_add_f32 v44, v44, v89\n"
"s_waitcnt lgkmcnt(4)\n"
"v_mfma_f32_16x16x32_bf16 v[100:103], a[212:215], v[224:227], v[100:103]\n"
"ds_read_b64_tr_b16 a[232:233], v7 offset:24576\n"
"ds_read_b64_tr_b16 a[234:235], v7 offset:30720\n"
"v_mfma_f32_16x16x32_bf16 v[108:111], a[212:215], v[228:231], v[108:111]\n"
"v_add_f32 v43, v43, v90\n"
"v_add_f32 v44, v44, v91\n"
"v_mfma_f32_16x16x32_bf16 v[116:119], a[212:215], v[232:235], v[116:119]\n"
"v_add_f32 v43, v43, v92\n"
"v_add_f32 v44, v44, v93\n"
"v_mfma_f32_16x16x32_bf16 v[124:127], a[212:215], v[236:239], v[124:127]\n"
"v_add_f32 v43, v43, v94\n"
"v_add_f32 v44, v44, v95\n"
"v_mfma_f32_16x16x32_bf16 v[96:99], a[216:219], v[240:243], v[96:99]\n"
"ds_read_b64_tr_b16 a[236:237], v8 offset:24576\n"
"ds_read_b64_tr_b16 a[238:239], v8 offset:30720\n"
"v_mfma_f32_16x16x32_bf16 v[104:107], a[216:219], v[244:247], v[104:107]\n"
"v_mfma_f32_16x16x32_bf16 v[112:115], a[216:219], v[248:251], v[112:115]\n"
"v_mfma_f32_16x16x32_bf16 v[120:123], a[216:219], v[252:255], v[120:123]\n"
"v_mfma_f32_16x16x32_bf16 v[100:103], a[220:223], v[240:243], v[100:103]\n"
"ds_read_b64_tr_b16 a[240:241], v5 offset:24704\n"
"ds_read_b64_tr_b16 a[242:243], v5 offset:30848\n"
"v_mfma_f32_16x16x32_bf16 v[108:111], a[220:223], v[244:247], v[108:111]\n"
"v_max3_f32 v53, v96, v97, v98\n"
"v_mfma_f32_16x16x32_bf16 v[116:119], a[220:223], v[248:251], v[116:119]\n"
"v_max3_f32 v54, v99, v104, v105\n"
"v_max3_f32 v53, v53, v106, v107\n"
"v_mfma_f32_16x16x32_bf16 v[124:127], a[220:223], v[252:255], v[124:127]\n"
"v_max3_f32 v54, v54, v112, v113\n"
"s_waitcnt lgkmcnt(4)\n"
"v_mfma_f32_16x16x32_bf16 a[0:3], a[224:227], v[128:131], a[0:3]\n"
"ds_read_b64_tr_b16 a[244:245], v6 offset:24704\n"
"ds_read_b64_tr_b16 a[246:247], v6 offset:30848\n"
"v_mfma_f32_16x16x32_bf16 a[4:7], a[224:227], v[132:135], a[4:7]\n"
"v_max3_f32 v53, v53, v114, v115\n"
"v_max3_f32 v54, v54, v120, v121\n"
"v_mfma_f32_16x16x32_bf16 a[8:11], a[224:227], v[136:139], a[8:11]\n"
"v_max3_f32 v53, v53, v122, v123\n"
"v_max3_f32 v54, v54, v100, v101\n"
"v_mfma_f32_16x16x32_bf16 a[12:15], a[224:227], v[140:143], a[12:15]\n"
"v_max3_f32 v53, v53, v102, v103\n"
"v_max3_f32 v54, v54, v108, v109\n"
"v_mfma_f32_16x16x32_bf16 a[16:19], a[228:231], v[128:131], a[16:19]\n"
"v_max3_f32 v53, v53, v110, v111\n"
"v_max3_f32 v54, v54, v116, v117\n"
"v_mfma_f32_16x16x32_bf16 a[20:23], a[228:231], v[132:135], a[20:23]\n"
"v_max3_f32 v53, v53, v118, v119\n"
"v_max3_f32 v54, v54, v124, v125\n"
"v_mfma_f32_16x16x32_bf16 a[24:27], a[228:231], v[136:139], a[24:27]\n"
"v_max3_f32 v53, v53, v126, v127\n"
"v_max_f32 v53, v53, v54\n"
"v_mfma_f32_16x16x32_bf16 a[28:31], a[228:231], v[140:143], a[28:31]\n"
"v_cmp_lt_f32 vcc, s64, v53\n"
"ds_read_b64_tr_b16 a[248:249], v7 offset:24704\n"
"v_mfma_f32_16x16x32_bf16 a[32:35], a[232:235], v[128:131], a[32:35]\n"
"ds_read_b64_tr_b16 a[250:251], v7 offset:30848\n"
"ds_read_b64_tr_b16 a[252:253], v8 offset:24704\n"
"v_mfma_f32_16x16x32_bf16 a[36:39], a[232:235], v[132:135], a[36:39]\n"
"ds_read_b64_tr_b16 a[254:255], v8 offset:30848\n"
"v_mfma_f32_16x16x32_bf16 a[40:43], a[232:235], v[136:139], a[40:43]\n"
"v_mfma_f32_16x16x32_bf16 a[44:47], a[232:235], v[140:143], a[44:47]\n"
"s_waitcnt lgkmcnt(4)\n"
"v_mfma_f32_16x16x32_bf16 a[48:51], a[236:239], v[128:131], a[48:51]\n"
"ds_read_b64_tr_b16 a[224:225], v5 offset:24832\n"
"ds_read_b64_tr_b16 a[226:227], v5 offset:30976\n"
"v_mfma_f32_16x16x32_bf16 a[52:55], a[236:239], v[132:135], a[52:55]\n"
"s_cbranch_vccnz K1A_RS2\n"
"K1A_RSb2:\n"
"v_mfma_f32_16x16x32_bf16 a[56:59], a[236:239], v[136:139], a[56:59]\n"
"v_exp_f32 v96, v96\n"
"v_mfma_f32_16x16x32_bf16 a[60:63], a[236:239], v[140:143], a[60:63]\n"
"v_exp_f32 v97, v97\n"
"v_mfma_f32_16x16x32_bf16 a[64:67], a[240:243], v[128:131], a[64:67]\n"
"ds_read_b64_tr_b16 a[228:229], v6 offset:24832\n"
"ds_read_b64_tr_b16 a[230:231], v6 offset:30976\n"
"v_mfma_f32_16x16x32_bf16 a[68:71], a[240:243], v[132:135], a[68:71]\n"
"v_exp_f32 v98, v98\n"
"v_mfma_f32_16x16x32_bf16 a[72:75], a[240:243], v[136:139], a[72:75]\n"
"v_exp_f32 v99, v99\n"
"v_mfma_f32_16x16x32_bf16 a[76:79], a[240:243], v[140:143], a[76:79]\n"
"v_exp_f32 v100, v100\n"
"v_mfma_f32_16x16x32_bf16 a[80:83], a[244:247], v[128:131], a[80:83]\n"
"ds_read_b64_tr_b16 a[232:233], v7 offset:24832\n"
"ds_read_b64_tr_b16 a[234:235], v7 offset:30976\n"
"v_mfma_f32_16x16x32_bf16 a[84:87], a[244:247], v[132:135], a[84:87]\n"
"v_exp_f32 v101, v101\n"
"v_mfma_f32_16x16x32_bf16 a[88:91], a[244:247], v[136:139], a[88:91]\n"
"v_exp_f32 v102, v102\n"
"v_mfma_f32_16x16x32_bf16 a[92:95], a[244:247], v[140:143], a[92:95]\n"
"v_add_f32 v37, v37, v96\n"
"s_waitcnt lgkmcnt(4)\n"
"v_mfma_f32_16x16x32_bf16 a[96:99], a[248:251], v[128:131], a[96:99]\n"
"ds_read_b64_tr_b16 a[236:237], v8 offset:24832\n"
"ds_read_b64_tr_b16 a[238:239], v8 offset:30976\n"
"v_mfma_f32_16x16x32_bf16 a[100:103], a[248:251], v[132:135], a[100:103]\n"
"v_exp_f32 v103, v103\n"
"v_mfma_f32_16x16x32_bf16 a[104:107], a[248:251], v[136:139], a[104:107]\n"
"s_barrier\n"
"v_add_f32 v38, v38, v97\n"
"v_mfma_f32_16x16x32_bf16 a[108:111], a[248:251], v[140:143], a[108:111]\n"
"v_exp_f32 v104, v104\n"
"v_mfma_f32_16x16x32_bf16 a[112:115], a[252:255], v[128:131], a[112:115]\n"
"ds_read_b128 a[192:195], v3 offset:12288\n"
"v_add_f32 v37, v37, v98\n"
"v_mfma_f32_16x16x32_bf16 a[116:119], a[252:255], v[132:135], a[116:119]\n"
"v_exp_f32 v105, v105\n"
"v_mfma_f32_16x16x32_bf16 a[120:123], a[252:255], v[136:139], a[120:123]\n"
"v_exp_f32 v106, v106\n"
"v_mfma_f32_16x16x32_bf16 a[124:127], a[252:255], v[140:143], a[124:127]\n"
"v_exp_f32 v107, v107\n"
"v_mfma_f32_16x16x32_bf16 a[128:131], a[224:227], v[128:131], a[128:131]\n"
"ds_read_b128 a[196:199], v3 offset:18432\n"
"v_add_f32 v38, v38, v99\n"
"v_mfma_f32_16x16x32_bf16 a[132:135], a[224:227], v[132:135], a[132:135]\n"
"v_exp_f32 v108, v108\n"
"v_mfma_f32_16x16x32_bf16 a[136:139], a[224:227], v[136:139], a[136:139]\n"
"v_exp_f32 v109, v109\n"
"v_mfma_f32_16x16x32_bf16 a[140:143], a[224:227], v[140:143], a[140:143]\n"
"v_add_f32 v37, v37, v100\n"
"s_waitcnt lgkmcnt(2)\n"
"v_mfma_f32_16x16x32_bf16 a[144:147], a[228:231], v[128:131], a[144:147]\n"
"ds_read_b128 a[200:203], v4 offset:12288\n"
"v_add_f32 v38, v38, v101\n"
"v_mfma_f32_16x16x32_bf16 a[148:151], a[228:231], v[132:135], a[148:151]\n"
"v_exp_f32 v110, v110\n"
"v_mfma_f32_16x16x32_bf16 a[152:155], a[228:231], v[136:139], a[152:155]\n"
"v_exp_f32 v111, v111\n"
"v_mfma_f32_16x16x32_bf16 a[156:159], a[228:231], v[140:143], a[156:159]\n"
"v_add_f32 v37, v37, v102\n"
"v_add_f32 v38, v38, v103\n"
"v_mfma_f32_16x16x32_bf16 a[160:163], a[232:235], v[128:131], a[160:163]\n"
"ds_read_b128 a[204:207], v4 offset:18432\n"
"v_add_f32 v39, v39, v104\n"
"v_mfma_f32_16x16x32_bf16 a[164:167], a[232:235], v[132:135], a[164:167]\n"
"v_add_f32 v40, v40, v105\n"
"v_add_f32 v39, v39, v106\n"
"v_mfma_f32_16x16x32_bf16 a[168:171], a[232:235], v[136:139], a[168:171]\n"
"v_add_f32 v40, v40, v107\n"
"v_add_f32 v39, v39, v108\n"
"v_mfma_f32_16x16x32_bf16 a[172:175], a[232:235], v[140:143], a[172:175]\n"
"v_add_f32 v40, v40, v109\n"
"v_add_f32 v39, v39, v110\n"
"v_mfma_f32_16x16x32_bf16 a[176:179], a[236:239], v[128:131], a[176:179]\n"
"ds_read_b128 a[208:211], v3 offset:12416\n"
"v_add_f32 v40, v40, v111\n"
"v_mfma_f32_16x16x32_bf16 a[180:183], a[236:239], v[132:135], a[180:183]\n"
"v_mfma_f32_16x16x32_bf16 a[184:187], a[236:239], v[136:139], a[184:187]\n"
"v_mfma_f32_16x16x32_bf16 a[188:191], a[236:239], v[140:143], a[188:191]\n"
"s_cbranch_vccnz K1A_RO2\n"
"K1A_ROb2:\n"
"s_waitcnt lgkmcnt(2)\n"
"v_mfma_f32_16x16x32_bf16 v[64:67], a[192:195], v[160:163], v[144:147]\n"
"ds_read_b128 a[212:215], v3 offset:18560\n"
"v_cvt_pk_bf16_f32 v128, v96, v97\n"
"v_mfma_f32_16x16x32_bf16 v[72:75], a[192:195], v[164:167], v[148:151]\n"
"v_exp_f32 v112, v112\n"
"v_mfma_f32_16x16x32_bf16 v[80:83], a[192:195], v[168:171], v[152:155]\n"
"v_exp_f32 v113, v113\n"
"v_mfma_f32_16x16x32_bf16 v[88:91], a[192:195], v[172:175], v[156:159]\n"
"v_exp_f32 v114, v114\n"
"v_mfma_f32_16x16x32_bf16 v[68:71], a[196:199], v[160:163], v[144:147]\n"
"ds_read_b128 a[216:219], v4 offset:12416\n"
"v_cvt_pk_bf16_f32 v129, v98, v99\n"
"v_mfma_f32_16x16x32_bf16 v[76:79], a[196:199], v[164:167], v[148:151]\n"
"v_exp_f32 v115, v115\n"
"v_mfma_f32_16x16x32_bf16 v[84:87], a[196:199], v[168:171], v[152:155]\n"
"v_exp_f32 v116, v116\n"
"v_mfma_f32_16x16x32_bf16 v[92:95], a[196:199], v[172:175], v[156:159]\n"
"v_exp_f32 v117, v117\n"
"v_mfma_f32_16x16x32_bf16 v[64:67], a[200:203], v[176:179], v[64:67]\n"
"ds_read_b128 a[220:223], v4 offset:18560\n"
"v_cvt_pk_bf16_f32 v130, v100, v101\n"
"v_mfma_f32_16x16x32_bf16 v[72:75], a[200:203], v[180:183], v[72:75]\n"
"v_exp_f32 v118, v118\n"
"v_mfma_f32_16x16x32_bf16 v[80:83], a[200:203], v[184:187], v[80:83]\n"
"v_exp_f32 v119, v119\n"
"v_mfma_f32_16x16x32_bf16 v[88:91], a[200:203], v[188:191], v[88:91]\n"
"v_cvt_pk_bf16_f32 v131, v102, v103\n"
"s_waitcnt lgkmcnt(2)\n"
"v_mfma_f32_16x16x32_bf16 v[68:71], a[204:207], v[176:179], v[68:71]\n"
"ds_read_b128 a[192:195], v3 offset:12544\n"
"v_cvt_pk_bf16_f32 v132, v104, v105\n"
"v_mfma_f32_16x16x32_bf16 v[76:79], a[204:207], v[180:183], v[76:79]\n"
"v_exp_f32 v120, v120\n"
"v_mfma_f32_16x16x32_bf16 v[84:87], a[204:207], v[184:187], v[84:87]\n"
"v_exp_f32 v121, v121\n"
"v_mfma_f32_16x16x32_bf16 v[92:95], a[204:207], v[188:191], v[92:95]\n"
"v_exp_f32 v122, v122\n"
"v_mfma_f32_16x16x32_bf16 v[64:67], a[208:211], v[192:195], v[64:67]\n"
"ds_read_b128 a[196:199], v3 offset:18688\n"
"v_cvt_pk_bf16_f32 v133, v106, v107\n"
"v_mfma_f32_16x16x32_bf16 v[72:75], a[208:211], v[196:199], v[72:75]\n"
"v_exp_f32 v123, v123\n"
"v_mfma_f32_16x16x32_bf16 v[80:83], a[208:211], v[200:203], v[80:83]\n"
"v_exp_f32 v124, v124\n"
"v_mfma_f32_16x16x32_bf16 v[88:91], a[208:211], v[204:207], v[88:91]\n"
"v_exp_f32 v125, v125\n"
"v_mfma_f32_16x16x32_bf16 v[68:71], a[212:215], v[192:195], v[68:71]\n"
"ds_read_b128 a[200:203], v4 offset:12544\n"
"v_cvt_pk_bf16_f32 v134, v108, v109\n"
"v_mfma_f32_16x16x32_bf16 v[76:79], a[212:215], v[196:199], v[76:79]\n"
"v_exp_f32 v126, v126\n"
"v_mfma_f32_16x16x32_bf16 v[84:87], a[212:215], v[200:203], v[84:87]\n"
"v_exp_f32 v127, v127\n"
"v_mfma_f32_16x16x32_bf16 v[92:95], a[212:215], v[204:207], v[92:95]\n"
"v_cvt_pk_bf16_f32 v135, v110, v111\n"
"s_waitcnt lgkmcnt(2)\n"
"v_mfma_f32_16x16x32_bf16 v[64:67], a[216:219], v[208:211], v[64:67]\n"
"ds_read_b128 a[204:207], v4 offset:18688\n"
"v_cvt_pk_bf16_f32 v136, v112, v113\n"
"v_mfma_f32_16x16x32_bf16 v[72:75], a[216:219], v[212:215], v[72:75]\n"
"v_cvt_pk_bf16_f32 v137, v114, v115\n"
"v_cvt_pk_bf16_f32 v138, v116, v117\n"
"v_mfma_f32_16x16x32_bf16 v[80:83], a[216:219], v[216:219], v[80:83]\n"
"v_cvt_pk_bf16_f32 v139, v118, v119\n"
"v_cvt_pk_bf16_f32 v140, v120, v121\n"
"v_mfma_f32_16x16x32_bf16 v[88:91], a[216:219], v[220:223], v[88:91]\n"
"v_cvt_pk_bf16_f32 v141, v122, v123\n"
"v_cvt_pk_bf16_f32 v142, v124, v125\n"
"v_mfma_f32_16x16x32_bf16 v[68:71], a[220:223], v[208:211], v[68:71]\n"
"ds_read_b64_tr_b16 a[240:241], v9 offset:0\n"
"ds_read_b64_tr_b16 a[242:243], v9 offset:6144\n"
"v_mfma_f32_16x16x32_bf16 v[76:79], a[220:223], v[212:215], v[76:79]\n"
"v_cvt_pk_bf16_f32 v143, v126, v127\n"
"v_add_f32 v41, v41, v112\n"
"v_mfma_f32_16x16x32_bf16 v[84:87], a[220:223], v[216:219], v[84:87]\n"
"v_add_f32 v42, v42, v113\n"
"v_add_f32 v41, v41, v114\n"
"v_mfma_f32_16x16x32_bf16 v[92:95], a[220:223], v[220:223], v[92:95]\n"
"v_add_f32 v42, v42, v115\n"
"v_add_f32 v41, v41, v116\n"
"v_mfma_f32_16x16x32_bf16 v[64:67], a[192:195], v[224:227], v[64:67]\n"
"ds_read_b64_tr_b16 a[244:245], v10 offset:0\n"
"ds_read_b64_tr_b16 a[246:247], v10 offset:6144\n"
"v_mfma_f32_16x16x32_bf16 v[72:75], a[192:195], v[228:231], v[72:75]\n"
"v_add_f32 v42, v42, v117\n"
"v_add_f32 v41, v41, v118\n"
"v_mfma_f32_16x16x32_bf16 v[80:83], a[192:195], v[232:235], v[80:83]\n"
"v_add_f32 v42, v42, v119\n"
"v_add_f32 v43, v43, v120\n"
"v_mfma_f32_16x16x32_bf16 v[88:91], a[192:195], v[236:239], v[88:91]\n"
"v_add_f32 v44, v44, v121\n"
"s_waitcnt lgkmcnt(4)\n"
"v_mfma_f32_16x16x32_bf16 v[68:71], a[196:199], v[224:227], v[68:71]\n"
"ds_read_b64_tr_b16 a[248:249], v11 offset:0\n"
"ds_read_b64_tr_b16 a[250:251], v11 offset:6144\n"
"v_mfma_f32_16x16x32_bf16 v[76:79], a[196:199], v[228:231], v[76:79]\n"
"v_add_f32 v43, v43, v122\n"
"v_add_f32 v44, v44, v123\n"
"v_mfma_f32_16x16x32_bf16 v[84:87], a[196:199], v[232:235], v[84:87]\n"
"v_add_f32 v43, v43, v124\n"
"v_add_f32 v44, v44, v125\n"
"v_mfma_f32_16x16x32_bf16 v[92:95], a[196:199], v[236:239], v[92:95]\n"
"v_add_f32 v43, v43, v126\n"
"v_add_f32 v44, v44, v127\n"
"v_mfma_f32_16x16x32_bf16 v[64:67], a[200:203], v[240:243], v[64:67]\n"
"ds_read_b64_tr_b16 a[252:253], v12 offset:0\n"
"ds_read_b64_tr_b16 a[254:255], v12 offset:6144\n"
"v_mfma_f32_16x16x32_bf16 v[72:75], a[200:203], v[244:247], v[72:75]\n"
"v_mfma_f32_16x16x32_bf16 v[80:83], a[200:203], v[248:251], v[80:83]\n"
"v_mfma_f32_16x16x32_bf16 v[88:91], a[200:203], v[252:255], v[88:91]\n"
"v_mfma_f32_16x16x32_bf16 v[68:71], a[204:207], v[240:243], v[68:71]\n"
"s_cmp_eq_u32 s61, 1\n"
"s_cselect_b32 s94, 7864320, 0\n"
"buffer_load_dword v14, v13, s[92:95], s87 offen\n"
"v_mfma_f32_16x16x32_bf16 v[76:79], a[204:207], v[244:247], v[76:79]\n"
"ds_read_b64_tr_b16 a[224:225], v9 offset:128\n"
"ds_read_b64_tr_b16 a[226:227], v9 offset:6272\n"
"v_mfma_f32_16x16x32_bf16 v[84:87], a[204:207], v[248:251], v[84:87]\n"
"v_max3_f32 v53, v64, v65, v66\n"
"v_max3_f32 v54, v67, v72, v73\n"
"v_mfma_f32_16x16x32_bf16 v[92:95], a[204:207], v[252:255], v[92:95]\n"
"v_max3_f32 v53, v53, v74, v75\n"
"s_waitcnt lgkmcnt(4)\n"
"v_mfma_f32_16x16x32_bf16 a[0:3], a[240:243], v[128:131], a[0:3]\n"
"ds_read_b64_tr_b16 a[228:229], v10 offset:128\n"
"ds_read_b64_tr_b16 a[230:231], v10 offset:6272\n"
"v_mfma_f32_16x16x32_bf16 a[4:7], a[240:243], v[132:135], a[4:7]\n"
"v_max3_f32 v54, v54, v80, v81\n"
"v_max3_f32 v53, v53, v82, v83\n"
"v_mfma_f32_16x16x32_bf16 a[8:11], a[240:243], v[136:139], a[8:11]\n"
"v_max3_f32 v54, v54, v88, v89\n"
"v_max3_f32 v53, v53, v90, v91\n"
"v_mfma_f32_16x16x32_bf16 a[12:15], a[240:243], v[140:143], a[12:15]\n"
"v_max3_f32 v54, v54, v68, v69\n"
"v_max3_f32 v53, v53, v70, v71\n"
"v_mfma_f32_16x16x32_bf16 a[16:19], a[244:247], v[128:131], a[16:19]\n"
"v_max3_f32 v54, v54, v76, v77\n"
"v_max3_f32 v53, v53, v78, v79\n"
"v_mfma_f32_16x16x32_bf16 a[20:23], a[244:247], v[132:135], a[20:23]\n"
"v_max3_f32 v54, v54, v84, v85\n"
"v_max3_f32 v53, v53, v86, v87\n"
"v_mfma_f32_16x16x32_bf16 a[24:27], a[244:247], v[136:139], a[24:27]\n"
"v_max3_f32 v54, v54, v92, v93\n"
"v_max3_f32 v53, v53, v94, v95\n"
"v_mfma_f32_16x16x32_bf16 a[28:31], a[244:247], v[140:143], a[28:31]\n"
"v_max_f32 v53, v53, v54\n"
"v_cmp_lt_f32 vcc, s64, v53\n"
"v_mfma_f32_16x16x32_bf16 a[32:35], a[248:251], v[128:131], a[32:35]\n"
"ds_read_b64_tr_b16 a[232:233], v11 offset:128\n"
"ds_read_b64_tr_b16 a[234:235], v11 offset:6272\n"
"v_mfma_f32_16x16x32_bf16 a[36:39], a[248:251], v[132:135], a[36:39]\n"
"ds_read_b64_tr_b16 a[236:237], v12 offset:128\n"
"ds_read_b64_tr_b16 a[238:239], v12 offset:6272\n"
"v_mfma_f32_16x16x32_bf16 a[40:43], a[248:251], v[136:139], a[40:43]\n"
"v_mfma_f32_16x16x32_bf16 a[44:47], a[248:251], v[140:143], a[44:47]\n"
"s_waitcnt lgkmcnt(4)\n"
"v_mfma_f32_16x16x32_bf16 a[48:51], a[252:255], v[128:131], a[48:51]\n"
"ds_read_b64_tr_b16 a[240:241], v9 offset:256\n"
"ds_read_b64_tr_b16 a[242:243], v9 offset:6400\n"
"v_mfma_f32_16x16x32_bf16 a[52:55], a[252:255], v[132:135], a[52:55]\n"
"s_cbranch_vccnz K1A_RS3\n"
"K1A_RSb3:\n"
"v_mfma_f32_16x16x32_bf16 a[56:59], a[252:255], v[136:139], a[56:59]\n"
"v_exp_f32 v64, v64\n"
"v_mfma_f32_16x16x32_bf16 a[60:63], a[252:255], v[140:143], a[60:63]\n"
"v_exp_f32 v65, v65\n"
"v_mfma_f32_16x16x32_bf16 a[64:67], a[224:227], v[128:131], a[64:67]\n"
"ds_read_b64_tr_b16 a[244:245], v10 offset:256\n"
"ds_read_b64_tr_b16 a[246:247], v10 offset:6400\n"
"v_mfma_f32_16x16x32_bf16 a[68:71], a[224:227], v[132:135], a[68:71]\n"
"v_exp_f32 v66, v66\n"
"v_mfma_f32_16x16x32_bf16 a[72:75], a[224:227], v[136:139], a[72:75]\n"
"v_exp_f32 v67, v67\n"
"v_mfma_f32_16x16x32_bf16 a[76:79], a[224:227], v[140:143], a[76:79]\n"
"v_exp_f32 v68, v68\n"
"v_mfma_f32_16x16x32_bf16 a[80:83], a[228:231], v[128:131], a[80:83]\n"
"ds_read_b64_tr_b16 a[248:249], v11 offset:256\n"
"ds_read_b64_tr_b16 a[250:251], v11 offset:6400\n"
"v_mfma_f32_16x16x32_bf16 a[84:87], a[228:231], v[132:135], a[84:87]\n"
"v_exp_f32 v69, v69\n"
"v_mfma_f32_16x16x32_bf16 a[88:91], a[228:231], v[136:139], a[88:91]\n"
"v_exp_f32 v70, v70\n"
"v_mfma_f32_16x16x32_bf16 a[92:95], a[228:231], v[140:143], a[92:95]\n"
"v_add_f32 v37, v37, v64\n"
"s_waitcnt lgkmcnt(4)\n"
"v_mfma_f32_16x16x32_bf16 a[96:99], a[232:235], v[128:131], a[96:99]\n"
"ds_read_b64_tr_b16 a[252:253], v12 offset:256\n"
"ds_read_b64_tr_b16 a[254:255], v12 offset:6400\n"
"v_mfma_f32_16x16x32_bf16 a[100:103], a[232:235], v[132:135], a[100:103]\n"
"v_exp_f32 v71, v71\n"
"v_mfma_f32_16x16x32_bf16 a[104:107], a[232:235], v[136:139], a[104:107]\n"
"s_barrier\n"
"v_add_f32 v38, v38, v65\n"
"v_mfma_f32_16x16x32_bf16 a[108:111], a[232:235], v[140:143], a[108:111]\n"
"v_exp_f32 v72, v72\n"
"v_mfma_f32_16x16x32_bf16 a[112:115], a[236:239], v[128:131], a[112:115]\n"
"ds_read_b128 a[208:211], v3 offset:24576\n"
"v_add_f32 v37, v37, v66\n"
"v_mfma_f32_16x16x32_bf16 a[116:119], a[236:239], v[132:135], a[116:119]\n"
"v_exp_f32 v73, v73\n"
"v_mfma_f32_16x16x32_bf16 a[120:123], a[236:239], v[136:139], a[120:123]\n"
"v_exp_f32 v74, v74\n"
"v_mfma_f32_16x16x32_bf16 a[124:127], a[236:239], v[140:143], a[124:127]\n"
"v_exp_f32 v75, v75\n"
"v_mfma_f32_16x16x32_bf16 a[128:131], a[240:243], v[128:131], a[128:131]\n"
"ds_read_b128 a[212:215], v3 offset:30720\n"
"v_add_f32 v38, v38, v67\n"
"v_mfma_f32_16x16x32_bf16 a[132:135], a[240:243], v[132:135], a[132:135]\n"
"v_exp_f32 v76, v76\n"
"v_mfma_f32_16x16x32_bf16 a[136:139], a[240:243], v[136:139], a[136:139]\n"
"v_exp_f32 v77, v77\n"
"v_mfma_f32_16x16x32_bf16 a[140:143], a[240:243], v[140:143], a[140:143]\n"
"v_add_f32 v37, v37, v68\n"
"s_waitcnt lgkmcnt(2)\n"
"v_mfma_f32_16x16x32_bf16 a[144:147], a[244:247], v[128:131], a[144:147]\n"
"ds_read_b128 a[216:219], v4 offset:24576\n"
"v_add_f32 v38, v38, v69\n"
"v_mfma_f32_16x16x32_bf16 a[148:151], a[244:247], v[132:135], a[148:151]\n"
"v_exp_f32 v78, v78\n"
"v_mfma_f32_16x16x32_bf16 a[152:155], a[244:247], v[136:139], a[152:155]\n"
"v_exp_f32 v79, v79\n"
"v_mfma_f32_16x16x32_bf16 a[156:159], a[244:247], v[140:143], a[156:159]\n"
"v_add_f32 v37, v37, v70\n"
"v_add_f32 v38, v38, v71\n"
"v_mfma_f32_16x16x32_bf16 a[160:163], a[248:251], v[128:131], a[160:163]\n"
"ds_read_b128 a[220:223], v4 offset:30720\n"
"v_add_f32 v39, v39, v72\n"
"v_mfma_f32_16x16x32_bf16 a[164:167], a[248:251], v[132:135], a[164:167]\n"
"v_add_f32 v40, v40, v73\n"
"v_add_f32 v39, v39, v74\n"
"v_mfma_f32_16x16x32_bf16 a[168:171], a[248:251], v[136:139], a[168:171]\n"
"v_add_f32 v40, v40, v75\n"
"v_add_f32 v39, v39, v76\n"
"v_mfma_f32_16x16x32_bf16 a[172:175], a[248:251], v[140:143], a[172:175]\n"
"v_add_f32 v40, v40, v77\n"
"v_add_f32 v39, v39, v78\n"
"v_mfma_f32_16x16x32_bf16 a[176:179], a[252:255], v[128:131], a[176:179]\n"
"ds_read_b128 a[192:195], v3 offset:24704\n"
"v_add_f32 v40, v40, v79\n"
"v_mfma_f32_16x16x32_bf16 a[180:183], a[252:255], v[132:135], a[180:183]\n"
"v_mfma_f32_16x16x32_bf16 a[184:187], a[252:255], v[136:139], a[184:187]\n"
"v_mfma_f32_16x16x32_bf16 a[188:191], a[252:255], v[140:143], a[188:191]\n"
"s_cbranch_vccnz K1A_RO3\n"
"K1A_ROb3:\n"
"s_waitcnt lgkmcnt(2)\n"
"v_mfma_f32_16x16x32_bf16 v[96:99], a[208:211], v[160:163], v[144:147]\n"
"ds_read_b128 a[196:199], v3 offset:30848\n"
"v_cvt_pk_bf16_f32 v128, v64, v65\n"
"v_mfma_f32_16x16x32_bf16 v[104:107], a[208:211], v[164:167], v[148:151]\n"
"v_exp_f32 v80, v80\n"
"v_mfma_f32_16x16x32_bf16 v[112:115], a[208:211], v[168:171], v[152:155]\n"
"v_exp_f32 v81, v81\n"
"v_mfma_f32_16x16x32_bf16 v[120:123], a[208:211], v[172:175], v[156:159]\n"
"v_exp_f32 v82, v82\n"
"v_mfma_f32_16x16x32_bf16 v[100:103], a[212:215], v[160:163], v[144:147]\n"
"ds_read_b128 a[200:203], v4 offset:24704\n"
"v_cvt_pk_bf16_f32 v129, v66, v67\n"
"v_mfma_f32_16x16x32_bf16 v[108:111], a[212:215], v[164:167], v[148:151]\n"
"v_exp_f32 v83, v83\n"
"v_mfma_f32_16x16x32_bf16 v[116:119], a[212:215], v[168:171], v[152:155]\n"
"v_exp_f32 v84, v84\n"
"v_mfma_f32_16x16x32_bf16 v[124:127], a[212:215], v[172:175], v[156:159]\n"
"v_exp_f32 v85, v85\n"
"v_mfma_f32_16x16x32_bf16 v[96:99], a[216:219], v[176:179], v[96:99]\n"
"ds_read_b128 a[204:207], v4 offset:30848\n"
"v_cvt_pk_bf16_f32 v130, v68, v69\n"
"v_mfma_f32_16x16x32_bf16 v[104:107], a[216:219], v[180:183], v[104:107]\n"
"v_exp_f32 v86, v86\n"
"v_mfma_f32_16x16x32_bf16 v[112:115], a[216:219], v[184:187], v[112:115]\n"
"v_exp_f32 v87, v87\n"
"v_mfma_f32_16x16x32_bf16 v[120:123], a[216:219], v[188:191], v[120:123]\n"
"v_cvt_pk_bf16_f32 v131, v70, v71\n"
"s_waitcnt lgkmcnt(2)\n"
"v_mfma_f32_16x16x32_bf16 v[100:103], a[220:223], v[176:179], v[100:103]\n"
"ds_read_b128 a[208:211], v3 offset:24832\n"
"v_cvt_pk_bf16_f32 v132, v72, v73\n"
"v_mfma_f32_16x16x32_bf16 v[108:111], a[220:223], v[180:183], v[108:111]\n"
"v_exp_f32 v88, v88\n"
"v_mfma_f32_16x16x32_bf16 v[116:119], a[220:223], v[184:187], v[116:119]\n"
"v_exp_f32 v89, v89\n"
"v_mfma_f32_16x16x32_bf16 v[124:127], a[220:223], v[188:191], v[124:127]\n"
"v_exp_f32 v90, v90\n"
"v_mfma_f32_16x16x32_bf16 v[96:99], a[192:195], v[192:195], v[96:99]\n"
"ds_read_b128 a[212:215], v3 offset:30976\n"
"v_cvt_pk_bf16_f32 v133, v74, v75\n"
"v_mfma_f32_16x16x32_bf16 v[104:107], a[192:195], v[196:199], v[104:107]\n"
"v_exp_f32 v91, v91\n"
"v_mfma_f32_16x16x32_bf16 v[112:115], a[192:195], v[200:203], v[112:115]\n"
"v_exp_f32 v92, v92\n"
"v_mfma_f32_16x16x32_bf16 v[120:123], a[192:195], v[204:207], v[120:123]\n"
"v_exp_f32 v93, v93\n"
"v_mfma_f32_16x16x32_bf16 v[100:103], a[196:199], v[192:195], v[100:103]\n"
"ds_read_b128 a[216:219], v4 offset:24832\n"
"v_cvt_pk_bf16_f32 v134, v76, v77\n"
"v_mfma_f32_16x16x32_bf16 v[108:111], a[196:199], v[196:199], v[108:111]\n"
"v_exp_f32 v94, v94\n"
"v_mfma_f32_16x16x32_bf16 v[116:119], a[196:199], v[200:203], v[116:119]\n"
"v_exp_f32 v95, v95\n"
"v_mfma_f32_16x16x32_bf16 v[124:127], a[196:199], v[204:207], v[124:127]\n"
"v_cvt_pk_bf16_f32 v135, v78, v79\n"
"s_waitcnt lgkmcnt(2)\n"
"v_mfma_f32_16x16x32_bf16 v[96:99], a[200:203], v[208:211], v[96:99]\n"
"ds_read_b128 a[220:223], v4 offset:30976\n"
"v_cvt_pk_bf16_f32 v136, v80, v81\n"
"v_mfma_f32_16x16x32_bf16 v[104:107], a[200:203], v[212:215], v[104:107]\n"
"v_cvt_pk_bf16_f32 v137, v82, v83\n"
"v_cvt_pk_bf16_f32 v138, v84, v85\n"
"v_mfma_f32_16x16x32_bf16 v[112:115], a[200:203], v[216:219], v[112:115]\n"
"v_cvt_pk_bf16_f32 v139, v86, v87\n"
"v_cvt_pk_bf16_f32 v140, v88, v89\n"
"v_mfma_f32_16x16x32_bf16 v[120:123], a[200:203], v[220:223], v[120:123]\n"
"v_cvt_pk_bf16_f32 v141, v90, v91\n"
"v_cvt_pk_bf16_f32 v142, v92, v93\n"
"v_mfma_f32_16x16x32_bf16 v[100:103], a[204:207], v[208:211], v[100:103]\n"
"ds_read_b64_tr_b16 a[224:225], v9 offset:12288\n"
"ds_read_b64_tr_b16 a[226:227], v9 offset:18432\n"
"v_mfma_f32_16x16x32_bf16 v[108:111], a[204:207], v[212:215], v[108:111]\n"
"v_cvt_pk_bf16_f32 v143, v94, v95\n"
"v_add_f32 v41, v41, v80\n"
"v_mfma_f32_16x16x32_bf16 v[116:119], a[204:207], v[216:219], v[116:119]\n"
"v_add_f32 v42, v42, v81\n"
"v_add_f32 v41, v41, v82\n"
"v_mfma_f32_16x16x32_bf16 v[124:127], a[204:207], v[220:223], v[124:127]\n"
"v_add_f32 v42, v42, v83\n"
"v_add_f32 v41, v41, v84\n"
"v_mfma_f32_16x16x32_bf16 v[96:99], a[208:211], v[224:227], v[96:99]\n"
"ds_read_b64_tr_b16 a[228:229], v10 offset:12288\n"
"ds_read_b64_tr_b16 a[230:231], v10 offset:18432\n"
"v_mfma_f32_16x16x32_bf16 v[104:107], a[208:211], v[228:231], v[104:107]\n"
"v_add_f32 v42, v42, v85\n"
"v_add_f32 v41, v41, v86\n"
"v_mfma_f32_16x16x32_bf16 v[112:115], a[208:211], v[232:235], v[112:115]\n"
"v_add_f32 v42, v42, v87\n"
"v_add_f32 v43, v43, v88\n"
"v_mfma_f32_16x16x32_bf16 v[120:123], a[208:211], v[236:239], v[120:123]\n"
"v_add_f32 v44, v44, v89\n"
"s_waitcnt lgkmcnt(4)\n"
"v_mfma_f32_16x16x32_bf16 v[100:103], a[212:215], v[224:227], v[100:103]\n"
"ds_read_b64_tr_b16 a[232:233], v11 offset:12288\n"
"ds_read_b64_tr_b16 a[234:235], v11 offset:18432\n"
"v_mfma_f32_16x16x32_bf16 v[108:111], a[212:215], v[228:231], v[108:111]\n"
"v_add_f32 v43, v43, v90\n"
"v_add_f32 v44, v44, v91\n"
"v_mfma_f32_16x16x32_bf16 v[116:119], a[212:215], v[232:235], v[116:119]\n"
"v_add_f32 v43, v43, v92\n"
"v_add_f32 v44, v44, v93\n"
"v_mfma_f32_16x16x32_bf16 v[124:127], a[212:215], v[236:239], v[124:127]\n"
"v_add_f32 v43, v43, v94\n"
"v_add_f32 v44, v44, v95\n"
"v_mfma_f32_16x16x32_bf16 v[96:99], a[216:219], v[240:243], v[96:99]\n"
"ds_read_b64_tr_b16 a[236:237], v12 offset:12288\n"
"ds_read_b64_tr_b16 a[238:239], v12 offset:18432\n"
"v_mfma_f32_16x16x32_bf16 v[104:107], a[216:219], v[244:247], v[104:107]\n"
"buffer_load_dword v14, v15, s[92:95], s87 offen\n"
"v_mfma_f32_16x16x32_bf16 v[112:115], a[216:219], v[248:251], v[112:115]\n"
"v_mfma_f32_16x16x32_bf16 v[120:123], a[216:219], v[252:255], v[120:123]\n"
"v_mfma_f32_16x16x32_bf16 v[100:103], a[220:223], v[240:243], v[100:103]\n"
"ds_read_b64_tr_b16 a[240:241], v9 offset:12416\n"
"ds_read_b64_tr_b16 a[242:243], v9 offset:18560\n"
"v_mfma_f32_16x16x32_bf16 v[108:111], a[220:223], v[244:247], v[108:111]\n"
"v_max3_f32 v53, v96, v97, v98\n"
"v_mfma_f32_16x16x32_bf16 v[116:119], a[220:223], v[248:251], v[116:119]\n"
"v_max3_f32 v54, v99, v104, v105\n"
"v_max3_f32 v53, v53, v106, v107\n"
"v_mfma_f32_16x16x32_bf16 v[124:127], a[220:223], v[252:255], v[124:127]\n"
"v_max3_f32 v54, v54, v112, v113\n"
"s_waitcnt lgkmcnt(4)\n"
"v_mfma_f32_16x16x32_bf16 a[0:3], a[224:227], v[128:131], a[0:3]\n"
"ds_read_b64_tr_b16 a[244:245], v10 offset:12416\n"
"ds_read_b64_tr_b16 a[246:247], v10 offset:18560\n"
"v_mfma_f32_16x16x32_bf16 a[4:7], a[224:227], v[132:135], a[4:7]\n"
"v_max3_f32 v53, v53, v114, v115\n"
"v_max3_f32 v54, v54, v120, v121\n"
"v_mfma_f32_16x16x32_bf16 a[8:11], a[224:227], v[136:139], a[8:11]\n"
"v_max3_f32 v53, v53, v122, v123\n"
"v_max3_f32 v54, v54, v100, v101\n"
"v_mfma_f32_16x16x32_bf16 a[12:15], a[224:227], v[140:143], a[12:15]\n"
"v_max3_f32 v53, v53, v102, v103\n"
"v_max3_f32 v54, v54, v108, v109\n"
"v_mfma_f32_16x16x32_bf16 a[16:19], a[228:231], v[128:131], a[16:19]\n"
"v_max3_f32 v53, v53, v110, v111\n"
"v_max3_f32 v54, v54, v116, v117\n"
"v_mfma_f32_16x16x32_bf16 a[20:23], a[228:231], v[132:135], a[20:23]\n"
"v_max3_f32 v53, v53, v118, v119\n"
"v_max3_f32 v54, v54, v124, v125\n"
"v_mfma_f32_16x16x32_bf16 a[24:27], a[228:231], v[136:139], a[24:27]\n"
"v_max3_f32 v53, v53, v126, v127\n"
"v_max_f32 v53, v53, v54\n"
"v_mfma_f32_16x16x32_bf16 a[28:31], a[228:231], v[140:143], a[28:31]\n"
"v_cmp_lt_f32 vcc, s64, v53\n"
"ds_read_b64_tr_b16 a[248:249], v11 offset:12416\n"
"v_mfma_f32_16x16x32_bf16 a[32:35], a[232:235], v[128:131], a[32:35]\n"
"ds_read_b64_tr_b16 a[250:251], v11 offset:18560\n"
"ds_read_b64_tr_b16 a[252:253], v12 offset:12416\n"
"v_mfma_f32_16x16x32_bf16 a[36:39], a[232:235], v[132:135], a[36:39]\n"
"ds_read_b64_tr_b16 a[254:255], v12 offset:18560\n"
"v_mfma_f32_16x16x32_bf16 a[40:43], a[232:235], v[136:139], a[40:43]\n"
"v_mfma_f32_16x16x32_bf16 a[44:47], a[232:235], v[140:143], a[44:47]\n"
"s_waitcnt lgkmcnt(4)\n"
"v_mfma_f32_16x16x32_bf16 a[48:51], a[236:239], v[128:131], a[48:51]\n"
"ds_read_b64_tr_b16 a[224:225], v9 offset:12544\n"
"ds_read_b64_tr_b16 a[226:227], v9 offset:18688\n"
"v_mfma_f32_16x16x32_bf16 a[52:55], a[236:239], v[132:135], a[52:55]\n"
"s_cbranch_vccnz K1A_RS4\n"
"K1A_RSb4:\n"
"v_mfma_f32_16x16x32_bf16 a[56:59], a[236:239], v[136:139], a[56:59]\n"
"v_exp_f32 v96, v96\n"
"v_mfma_f32_16x16x32_bf16 a[60:63], a[236:239], v[140:143], a[60:63]\n"
"v_exp_f32 v97, v97\n"
"v_mfma_f32_16x16x32_bf16 a[64:67], a[240:243], v[128:131], a[64:67]\n"
"ds_read_b64_tr_b16 a[228:229], v10 offset:12544\n"
"ds_read_b64_tr_b16 a[230:231], v10 offset:18688\n"
"v_mfma_f32_16x16x32_bf16 a[68:71], a[240:243], v[132:135], a[68:71]\n"
"v_exp_f32 v98, v98\n"
"v_mfma_f32_16x16x32_bf16 a[72:75], a[240:243], v[136:139], a[72:75]\n"
"v_exp_f32 v99, v99\n"
"v_mfma_f32_16x16x32_bf16 a[76:79], a[240:243], v[140:143], a[76:79]\n"
"v_exp_f32 v100, v100\n"
"v_mfma_f32_16x16x32_bf16 a[80:83], a[244:247], v[128:131], a[80:83]\n"
"ds_read_b64_tr_b16 a[232:233], v11 offset:12544\n"
"ds_read_b64_tr_b16 a[234:235], v11 offset:18688\n"
"v_mfma_f32_16x16x32_bf16 a[84:87], a[244:247], v[132:135], a[84:87]\n"
"v_exp_f32 v101, v101\n"
"v_mfma_f32_16x16x32_bf16 a[88:91], a[244:247], v[136:139], a[88:91]\n"
"v_exp_f32 v102, v102\n"
"v_mfma_f32_16x16x32_bf16 a[92:95], a[244:247], v[140:143], a[92:95]\n"
"v_add_f32 v37, v37, v96\n"
"s_waitcnt lgkmcnt(4)\n"
"v_mfma_f32_16x16x32_bf16 a[96:99], a[248:251], v[128:131], a[96:99]\n"
"ds_read_b64_tr_b16 a[236:237], v12 offset:12544\n"
"ds_read_b64_tr_b16 a[238:239], v12 offset:18688\n"
"v_mfma_f32_16x16x32_bf16 a[100:103], a[248:251], v[132:135], a[100:103]\n"
"v_exp_f32 v103, v103\n"
"v_mfma_f32_16x16x32_bf16 a[104:107], a[248:251], v[136:139], a[104:107]\n"
"s_barrier\n"
"v_add_f32 v38, v38, v97\n"
"v_mfma_f32_16x16x32_bf16 a[108:111], a[248:251], v[140:143], a[108:111]\n"
"v_exp_f32 v104, v104\n"
"v_mfma_f32_16x16x32_bf16 a[112:115], a[252:255], v[128:131], a[112:115]\n"
"ds_read_b128 a[192:195], v1 offset:0\n"
"v_add_f32 v37, v37, v98\n"
"v_mfma_f32_16x16x32_bf16 a[116:119], a[252:255], v[132:135], a[116:119]\n"
"v_exp_f32 v105, v105\n"
"v_mfma_f32_16x16x32_bf16 a[120:123], a[252:255], v[136:139], a[120:123]\n"
"v_exp_f32 v106, v106\n"
"v_mfma_f32_16x16x32_bf16 a[124:127], a[252:255], v[140:143], a[124:127]\n"
"v_exp_f32 v107, v107\n"
"v_mfma_f32_16x16x32_bf16 a[128:131], a[224:227], v[128:131], a[128:131]\n"
"ds_read_b128 a[196:199], v1 offset:6144\n"
"v_add_f32 v38, v38, v99\n"
"v_mfma_f32_16x16x32_bf16 a[132:135], a[224:227], v[132:135], a[132:135]\n"
"v_exp_f32 v108, v108\n"
"v_mfma_f32_16x16x32_bf16 a[136:139], a[224:227], v[136:139], a[136:139]\n"
"v_exp_f32 v109, v109\n"
"v_mfma_f32_16x16x32_bf16 a[140:143], a[224:227], v[140:143], a[140:143]\n"
"v_add_f32 v37, v37, v100\n"
"s_waitcnt lgkmcnt(2)\n"
"v_mfma_f32_16x16x32_bf16 a[144:147], a[228:231], v[128:131], a[144:147]\n"
"ds_read_b128 a[200:203], v2 offset:0\n"
"v_add_f32 v38, v38, v101\n"
"v_mfma_f32_16x16x32_bf16 a[148:151], a[228:231], v[132:135], a[148:151]\n"
"v_exp_f32 v110, v110\n"
"v_mfma_f32_16x16x32_bf16 a[152:155], a[228:231], v[136:139], a[152:155]\n"
"v_exp_f32 v111, v111\n"
"v_mfma_f32_16x16x32_bf16 a[156:159], a[228:231], v[140:143], a[156:159]\n"
"v_add_f32 v37, v37, v102\n"
"v_add_f32 v38, v38, v103\n"
"v_mfma_f32_16x16x32_bf16 a[160:163], a[232:235], v[128:131], a[160:163]\n"
"ds_read_b128 a[204:207], v2 offset:6144\n"
"v_add_f32 v39, v39, v104\n"
"v_mfma_f32_16x16x32_bf16 a[164:167], a[232:235], v[132:135], a[164:167]\n"
"v_add_f32 v40, v40, v105\n"
"v_add_f32 v39, v39, v106\n"
"v_mfma_f32_16x16x32_bf16 a[168:171], a[232:235], v[136:139], a[168:171]\n"
"v_add_f32 v40, v40, v107\n"
"v_add_f32 v39, v39, v108\n"
"v_mfma_f32_16x16x32_bf16 a[172:175], a[232:235], v[140:143], a[172:175]\n"
"v_add_f32 v40, v40, v109\n"
"v_add_f32 v39, v39, v110\n"
"v_mfma_f32_16x16x32_bf16 a[176:179], a[236:239], v[128:131], a[176:179]\n"
"ds_read_b128 a[208:211], v1 offset:128\n"
"v_add_f32 v40, v40, v111\n"
"v_mfma_f32_16x16x32_bf16 a[180:183], a[236:239], v[132:135], a[180:183]\n"
"v_mfma_f32_16x16x32_bf16 a[184:187], a[236:239], v[136:139], a[184:187]\n"
"v_mfma_f32_16x16x32_bf16 a[188:191], a[236:239], v[140:143], a[188:191]\n"
"s_cbranch_vccnz K1A_RO4\n"
"K1A_ROb4:\n"
"s_waitcnt lgkmcnt(2)\n"
"v_mfma_f32_16x16x32_bf16 v[64:67], a[192:195], v[160:163], v[144:147]\n"
"ds_read_b128 a[212:215], v1 offset:6272\n"
"v_cvt_pk_bf16_f32 v128, v96, v97\n"
"v_mfma_f32_16x16x32_bf16 v[72:75], a[192:195], v[164:167], v[148:151]\n"
"v_exp_f32 v112, v112\n"
"v_mfma_f32_16x16x32_bf16 v[80:83], a[192:195], v[168:171], v[152:155]\n"
"v_exp_f32 v113, v113\n"
"v_mfma_f32_16x16x32_bf16 v[88:91], a[192:195], v[172:175], v[156:159]\n"
"v_exp_f32 v114, v114\n"
"v_mfma_f32_16x16x32_bf16 v[68:71], a[196:199], v[160:163], v[144:147]\n"
"ds_read_b128 a[216:219], v2 offset:128\n"
"v_cvt_pk_bf16_f32 v129, v98, v99\n"
"v_mfma_f32_16x16x32_bf16 v[76:79], a[196:199], v[164:167], v[148:151]\n"
"v_exp_f32 v115, v115\n"
"v_mfma_f32_16x16x32_bf16 v[84:87], a[196:199], v[168:171], v[152:155]\n"
"v_exp_f32 v116, v116\n"
"v_mfma_f32_16x16x32_bf16 v[92:95], a[196:199], v[172:175], v[156:159]\n"
"v_exp_f32 v117, v117\n"
"v_mfma_f32_16x16x32_bf16 v[64:67], a[200:203], v[176:179], v[64:67]\n"
"ds_read_b128 a[220:223], v2 offset:6272\n"
"v_cvt_pk_bf16_f32 v130, v100, v101\n"
"v_mfma_f32_16x16x32_bf16 v[72:75], a[200:203], v[180:183], v[72:75]\n"
"v_exp_f32 v118, v118\n"
"v_mfma_f32_16x16x32_bf16 v[80:83], a[200:203], v[184:187], v[80:83]\n"
"v_exp_f32 v119, v119\n"
"v_mfma_f32_16x16x32_bf16 v[88:91], a[200:203], v[188:191], v[88:91]\n"
"v_cvt_pk_bf16_f32 v131, v102, v103\n"
"s_waitcnt lgkmcnt(2)\n"
"v_mfma_f32_16x16x32_bf16 v[68:71], a[204:207], v[176:179], v[68:71]\n"
"ds_read_b128 a[192:195], v1 offset:256\n"
"v_cvt_pk_bf16_f32 v132, v104, v105\n"
"v_mfma_f32_16x16x32_bf16 v[76:79], a[204:207], v[180:183], v[76:79]\n"
"v_exp_f32 v120, v120\n"
"v_mfma_f32_16x16x32_bf16 v[84:87], a[204:207], v[184:187], v[84:87]\n"
"v_exp_f32 v121, v121\n"
"v_mfma_f32_16x16x32_bf16 v[92:95], a[204:207], v[188:191], v[92:95]\n"
"v_exp_f32 v122, v122\n"
"v_mfma_f32_16x16x32_bf16 v[64:67], a[208:211], v[192:195], v[64:67]\n"
"ds_read_b128 a[196:199], v1 offset:6400\n"
"v_cvt_pk_bf16_f32 v133, v106, v107\n"
"v_mfma_f32_16x16x32_bf16 v[72:75], a[208:211], v[196:199], v[72:75]\n"
"v_exp_f32 v123, v123\n"
"v_mfma_f32_16x16x32_bf16 v[80:83], a[208:211], v[200:203], v[80:83]\n"
"v_exp_f32 v124, v124\n"
"v_mfma_f32_16x16x32_bf16 v[88:91], a[208:211], v[204:207], v[88:91]\n"
"v_exp_f32 v125, v125\n"
"v_mfma_f32_16x16x32_bf16 v[68:71], a[212:215], v[192:195], v[68:71]\n"
"ds_read_b128 a[200:203], v2 offset:256\n"
"v_cvt_pk_bf16_f32 v134, v108, v109\n"
"v_mfma_f32_16x16x32_bf16 v[76:79], a[212:215], v[196:199], v[76:79]\n"
"v_exp_f32 v126, v126\n"
"v_mfma_f32_16x16x32_bf16 v[84:87], a[212:215], v[200:203], v[84:87]\n"
"v_exp_f32 v127, v127\n"
"v_mfma_f32_16x16x32_bf16 v[92:95], a[212:215], v[204:207], v[92:95]\n"
"v_cvt_pk_bf16_f32 v135, v110, v111\n"
"s_waitcnt lgkmcnt(2)\n"
"v_mfma_f32_16x16x32_bf16 v[64:67], a[216:219], v[208:211], v[64:67]\n"
"ds_read_b128 a[204:207], v2 offset:6400\n"
"v_cvt_pk_bf16_f32 v136, v112, v113\n"
"v_mfma_f32_16x16x32_bf16 v[72:75], a[216:219], v[212:215], v[72:75]\n"
"v_cvt_pk_bf16_f32 v137, v114, v115\n"
"v_cvt_pk_bf16_f32 v138, v116, v117\n"
"v_mfma_f32_16x16x32_bf16 v[80:83], a[216:219], v[216:219], v[80:83]\n"
"v_cvt_pk_bf16_f32 v139, v118, v119\n"
"v_cvt_pk_bf16_f32 v140, v120, v121\n"
"v_mfma_f32_16x16x32_bf16 v[88:91], a[216:219], v[220:223], v[88:91]\n"
"v_cvt_pk_bf16_f32 v141, v122, v123\n"
"v_cvt_pk_bf16_f32 v142, v124, v125\n"
"v_mfma_f32_16x16x32_bf16 v[68:71], a[220:223], v[208:211], v[68:71]\n"
"ds_read_b64_tr_b16 a[240:241], v9 offset:24576\n"
"ds_read_b64_tr_b16 a[242:243], v9 offset:30720\n"
"v_mfma_f32_16x16x32_bf16 v[76:79], a[220:223], v[212:215], v[76:79]\n"
"v_cvt_pk_bf16_f32 v143, v126, v127\n"
"v_add_f32 v41, v41, v112\n"
"v_mfma_f32_16x16x32_bf16 v[84:87], a[220:223], v[216:219], v[84:87]\n"
"v_add_f32 v42, v42, v113\n"
"v_add_f32 v41, v41, v114\n"
"v_mfma_f32_16x16x32_bf16 v[92:95], a[220:223], v[220:223], v[92:95]\n"
"v_add_f32 v42, v42, v115\n"
"v_add_f32 v41, v41, v116\n"
"v_mfma_f32_16x16x32_bf16 v[64:67], a[192:195], v[224:227], v[64:67]\n"
"ds_read_b64_tr_b16 a[244:245], v10 offset:24576\n"
"ds_read_b64_tr_b16 a[246:247], v10 offset:30720\n"
"v_mfma_f32_16x16x32_bf16 v[72:75], a[192:195], v[228:231], v[72:75]\n"
"v_add_f32 v42, v42, v117\n"
"v_add_f32 v41, v41, v118\n"
"v_mfma_f32_16x16x32_bf16 v[80:83], a[192:195], v[232:235], v[80:83]\n"
"v_add_f32 v42, v42, v119\n"
"v_add_f32 v43, v43, v120\n"
"v_mfma_f32_16x16x32_bf16 v[88:91], a[192:195], v[236:239], v[88:91]\n"
"v_add_f32 v44, v44, v121\n"
"s_waitcnt lgkmcnt(4)\n"
"v_mfma_f32_16x16x32_bf16 v[68:71], a[196:199], v[224:227], v[68:71]\n"
"ds_read_b64_tr_b16 a[248:249], v11 offset:24576\n"
"ds_read_b64_tr_b16 a[250:251], v11 offset:30720\n"
"v_mfma_f32_16x16x32_bf16 v[76:79], a[196:199], v[228:231], v[76:79]\n"
"v_add_f32 v43, v43, v122\n"
"v_add_f32 v44, v44, v123\n"
"v_mfma_f32_16x16x32_bf16 v[84:87], a[196:199], v[232:235], v[84:87]\n"
"v_add_f32 v43, v43, v124\n"
"v_add_f32 v44, v44, v125\n"
"v_mfma_f32_16x16x32_bf16 v[92:95], a[196:199], v[236:239], v[92:95]\n"
"v_add_f32 v43, v43, v126\n"
"v_add_f32 v44, v44, v127\n"
"v_mfma_f32_16x16x32_bf16 v[64:67], a[200:203], v[240:243], v[64:67]\n"
"ds_read_b64_tr_b16 a[252:253], v12 offset:24576\n"
"ds_read_b64_tr_b16 a[254:255], v12 offset:30720\n"
"v_mfma_f32_16x16x32_bf16 v[72:75], a[200:203], v[244:247], v[72:75]\n"
"v_mfma_f32_16x16x32_bf16 v[80:83], a[200:203], v[248:251], v[80:83]\n"
"v_mfma_f32_16x16x32_bf16 v[88:91], a[200:203], v[252:255], v[88:91]\n"
"v_mfma_f32_16x16x32_bf16 v[68:71], a[204:207], v[240:243], v[68:71]\n"
"ds_read_b64_tr_b16 a[224:225], v9 offset:24704\n"
"ds_read_b64_tr_b16 a[226:227], v9 offset:30848\n"
"v_mfma_f32_16x16x32_bf16 v[76:79], a[204:207], v[244:247], v[76:79]\n"
"v_max3_f32 v53, v64, v65, v66\n"
"v_mfma_f32_16x16x32_bf16 v[84:87], a[204:207], v[248:251], v[84:87]\n"
"v_max3_f32 v54, v67, v72, v73\n"
"v_max3_f32 v53, v53, v74, v75\n"
"v_mfma_f32_16x16x32_bf16 v[92:95], a[204:207], v[252:255], v[92:95]\n"
"v_max3_f32 v54, v54, v80, v81\n"
"s_waitcnt lgkmcnt(4)\n"
"v_mfma_f32_16x16x32_bf16 a[0:3], a[240:243], v[128:131], a[0:3]\n"
"ds_read_b64_tr_b16 a[228:229], v10 offset:24704\n"
"ds_read_b64_tr_b16 a[230:231], v10 offset:30848\n"
"v_mfma_f32_16x16x32_bf16 a[4:7], a[240:243], v[132:135], a[4:7]\n"
"v_max3_f32 v53, v53, v82, v83\n"
"v_max3_f32 v54, v54, v88, v89\n"
"v_mfma_f32_16x16x32_bf16 a[8:11], a[240:243], v[136:139], a[8:11]\n"
"v_max3_f32 v53, v53, v90, v91\n"
"v_max3_f32 v54, v54, v68, v69\n"
"v_mfma_f32_16x16x32_bf16 a[12:15], a[240:243], v[140:143], a[12:15]\n"
"v_max3_f32 v53, v53, v70, v71\n"
"v_max3_f32 v54, v54, v76, v77\n"
"v_mfma_f32_16x16x32_bf16 a[16:19], a[244:247], v[128:131], a[16:19]\n"
"v_max3_f32 v53, v53, v78, v79\n"
"v_max3_f32 v54, v54, v84, v85\n"
"v_mfma_f32_16x16x32_bf16 a[20:23], a[244:247], v[132:135], a[20:23]\n"
"v_max3_f32 v53, v53, v86, v87\n"
"v_max3_f32 v54, v54, v92, v93\n"
"v_mfma_f32_16x16x32_bf16 a[24:27], a[244:247], v[136:139], a[24:27]\n"
"v_max3_f32 v53, v53, v94, v95\n"
"v_max_f32 v53, v53, v54\n"
"v_mfma_f32_16x16x32_bf16 a[28:31], a[244:247], v[140:143], a[28:31]\n"
"v_cmp_lt_f32 vcc, s64, v53\n"
"ds_read_b64_tr_b16 a[232:233], v11 offset:24704\n"
"v_mfma_f32_16x16x32_bf16 a[32:35], a[248:251], v[128:131], a[32:35]\n"
"ds_read_b64_tr_b16 a[234:235], v11 offset:30848\n"
"ds_read_b64_tr_b16 a[236:237], v12 offset:24704\n"
"v_mfma_f32_16x16x32_bf16 a[36:39], a[248:251], v[132:135], a[36:39]\n"
"ds_read_b64_tr_b16 a[238:239], v12 offset:30848\n"
"v_mfma_f32_16x16x32_bf16 a[40:43], a[248:251], v[136:139], a[40:43]\n"
"v_mfma_f32_16x16x32_bf16 a[44:47], a[248:251], v[140:143], a[44:47]\n"
"s_waitcnt lgkmcnt(4)\n"
"v_mfma_f32_16x16x32_bf16 a[48:51], a[252:255], v[128:131], a[48:51]\n"
"ds_read_b64_tr_b16 a[240:241], v9 offset:24832\n"
"ds_read_b64_tr_b16 a[242:243], v9 offset:30976\n"
"v_mfma_f32_16x16x32_bf16 a[52:55], a[252:255], v[132:135], a[52:55]\n"
"s_cbranch_vccnz K1A_RS5\n"
"K1A_RSb5:\n"
"v_mfma_f32_16x16x32_bf16 a[56:59], a[252:255], v[136:139], a[56:59]\n"
"v_exp_f32 v64, v64\n"
"v_mfma_f32_16x16x32_bf16 a[60:63], a[252:255], v[140:143], a[60:63]\n"
"v_exp_f32 v65, v65\n"
"v_mfma_f32_16x16x32_bf16 a[64:67], a[224:227], v[128:131], a[64:67]\n"
"ds_read_b64_tr_b16 a[244:245], v10 offset:24832\n"
"ds_read_b64_tr_b16 a[246:247], v10 offset:30976\n"
"v_mfma_f32_16x16x32_bf16 a[68:71], a[224:227], v[132:135], a[68:71]\n"
"v_exp_f32 v66, v66\n"
"v_mfma_f32_16x16x32_bf16 a[72:75], a[224:227], v[136:139], a[72:75]\n"
"v_exp_f32 v67, v67\n"
"v_mfma_f32_16x16x32_bf16 a[76:79], a[224:227], v[140:143], a[76:79]\n"
"v_exp_f32 v68, v68\n"
"v_mfma_f32_16x16x32_bf16 a[80:83], a[228:231], v[128:131], a[80:83]\n"
"ds_read_b64_tr_b16 a[248:249], v11 offset:24832\n"
"ds_read_b64_tr_b16 a[250:251], v11 offset:30976\n"
"v_mfma_f32_16x16x32_bf16 a[84:87], a[228:231], v[132:135], a[84:87]\n"
"v_exp_f32 v69, v69\n"
"v_mfma_f32_16x16x32_bf16 a[88:91], a[228:231], v[136:139], a[88:91]\n"
"v_exp_f32 v70, v70\n"
"v_mfma_f32_16x16x32_bf16 a[92:95], a[228:231], v[140:143], a[92:95]\n"
"v_add_f32 v37, v37, v64\n"
"s_waitcnt lgkmcnt(4)\n"
"v_mfma_f32_16x16x32_bf16 a[96:99], a[232:235], v[128:131], a[96:99]\n"
"ds_read_b64_tr_b16 a[252:253], v12 offset:24832\n"
"ds_read_b64_tr_b16 a[254:255], v12 offset:30976\n"
"v_mfma_f32_16x16x32_bf16 a[100:103], a[232:235], v[132:135], a[100:103]\n"
"v_exp_f32 v71, v71\n"
"v_mfma_f32_16x16x32_bf16 a[104:107], a[232:235], v[136:139], a[104:107]\n"
"s_barrier\n"
"v_add_f32 v38, v38, v65\n"
"v_mfma_f32_16x16x32_bf16 a[108:111], a[232:235], v[140:143], a[108:111]\n"
"v_exp_f32 v72, v72\n"
"v_mfma_f32_16x16x32_bf16 a[112:115], a[236:239], v[128:131], a[112:115]\n"
"ds_read_b128 a[208:211], v1 offset:12288\n"
"v_add_f32 v37, v37, v66\n"
"v_mfma_f32_16x16x32_bf16 a[116:119], a[236:239], v[132:135], a[116:119]\n"
"v_exp_f32 v73, v73\n"
"v_mfma_f32_16x16x32_bf16 a[120:123], a[236:239], v[136:139], a[120:123]\n"
"v_exp_f32 v74, v74\n"
"v_mfma_f32_16x16x32_bf16 a[124:127], a[236:239], v[140:143], a[124:127]\n"
"v_exp_f32 v75, v75\n"
"v_mfma_f32_16x16x32_bf16 a[128:131], a[240:243], v[128:131], a[128:131]\n"
"ds_read_b128 a[212:215], v1 offset:18432\n"
"v_add_f32 v38, v38, v67\n"
"v_mfma_f32_16x16x32_bf16 a[132:135], a[240:243], v[132:135], a[132:135]\n"
"v_exp_f32 v76, v76\n"
"v_mfma_f32_16x16x32_bf16 a[136:139], a[240:243], v[136:139], a[136:139]\n"
"v_exp_f32 v77, v77\n"
"v_mfma_f32_16x16x32_bf16 a[140:143], a[240:243], v[140:143], a[140:143]\n"
"v_add_f32 v37, v37, v68\n"
"s_waitcnt lgkmcnt(2)\n"
"v_mfma_f32_16x16x32_bf16 a[144:147], a[244:247], v[128:131], a[144:147]\n"
"ds_read_b128 a[216:219], v2 offset:12288\n"
"v_add_f32 v38, v38, v69\n"
"v_mfma_f32_16x16x32_bf16 a[148:151], a[244:247], v[132:135], a[148:151]\n"
"v_exp_f32 v78, v78\n"
"v_mfma_f32_16x16x32_bf16 a[152:155], a[244:247], v[136:139], a[152:155]\n"
"v_exp_f32 v79, v79\n"
"v_mfma_f32_16x16x32_bf16 a[156:159], a[244:247], v[140:143], a[156:159]\n"
"v_add_f32 v37, v37, v70\n"
"v_add_f32 v38, v38, v71\n"
"v_mfma_f32_16x16x32_bf16 a[160:163], a[248:251], v[128:131], a[160:163]\n"
"ds_read_b128 a[220:223], v2 offset:18432\n"
"v_add_f32 v39, v39, v72\n"
"v_mfma_f32_16x16x32_bf16 a[164:167], a[248:251], v[132:135], a[164:167]\n"
"v_add_f32 v40, v40, v73\n"
"v_add_f32 v39, v39, v74\n"
"v_mfma_f32_16x16x32_bf16 a[168:171], a[248:251], v[136:139], a[168:171]\n"
"v_add_f32 v40, v40, v75\n"
"v_add_f32 v39, v39, v76\n"
"v_mfma_f32_16x16x32_bf16 a[172:175], a[248:251], v[140:143], a[172:175]\n"
"v_add_f32 v40, v40, v77\n"
"v_add_f32 v39, v39, v78\n"
"v_mfma_f32_16x16x32_bf16 a[176:179], a[252:255], v[128:131], a[176:179]\n"
"ds_read_b128 a[192:195], v1 offset:12416\n"
"v_add_f32 v40, v40, v79\n"
"v_mfma_f32_16x16x32_bf16 a[180:183], a[252:255], v[132:135], a[180:183]\n"
"v_mfma_f32_16x16x32_bf16 a[184:187], a[252:255], v[136:139], a[184:187]\n"
"v_mfma_f32_16x16x32_bf16 a[188:191], a[252:255], v[140:143], a[188:191]\n"
"s_cbranch_vccnz K1A_RO5\n"
"K1A_ROb5:\n"
"s_waitcnt lgkmcnt(2)\n"
"v_mfma_f32_16x16x32_bf16 v[96:99], a[208:211], v[160:163], v[144:147]\n"
"ds_read_b128 a[196:199], v1 offset:18560\n"
"v_cvt_pk_bf16_f32 v128, v64, v65\n"
"v_mfma_f32_16x16x32_bf16 v[104:107], a[208:211], v[164:167], v[148:151]\n"
"v_exp_f32 v80, v80\n"
"v_mfma_f32_16x16x32_bf16 v[112:115], a[208:211], v[168:171], v[152:155]\n"
"v_exp_f32 v81, v81\n"
"v_mfma_f32_16x16x32_bf16 v[120:123], a[208:211], v[172:175], v[156:159]\n"
"v_exp_f32 v82, v82\n"
"v_mfma_f32_16x16x32_bf16 v[100:103], a[212:215], v[160:163], v[144:147]\n"
"ds_read_b128 a[200:203], v2 offset:12416\n"
"v_cvt_pk_bf16_f32 v129, v66, v67\n"
"v_mfma_f32_16x16x32_bf16 v[108:111], a[212:215], v[164:167], v[148:151]\n"
"v_exp_f32 v83, v83\n"
"v_mfma_f32_16x16x32_bf16 v[116:119], a[212:215], v[168:171], v[152:155]\n"
"v_exp_f32 v84, v84\n"
"v_mfma_f32_16x16x32_bf16 v[124:127], a[212:215], v[172:175], v[156:159]\n"
"v_exp_f32 v85, v85\n"
"v_mfma_f32_16x16x32_bf16 v[96:99], a[216:219], v[176:179], v[96:99]\n"
"ds_read_b128 a[204:207], v2 offset:18560\n"
"v_cvt_pk_bf16_f32 v130, v68, v69\n"
"v_mfma_f32_16x16x32_bf16 v[104:107], a[216:219], v[180:183], v[104:107]\n"
"v_exp_f32 v86, v86\n"
"v_mfma_f32_16x16x32_bf16 v[112:115], a[216:219], v[184:187], v[112:115]\n"
"v_exp_f32 v87, v87\n"
"v_mfma_f32_16x16x32_bf16 v[120:123], a[216:219], v[188:191], v[120:123]\n"
"v_cvt_pk_bf16_f32 v131, v70, v71\n"
"s_waitcnt lgkmcnt(2)\n"
"v_mfma_f32_16x16x32_bf16 v[100:103], a[220:223], v[176:179], v[100:103]\n"
"ds_read_b128 a[208:211], v1 offset:12544\n"
"v_cvt_pk_bf16_f32 v132, v72, v73\n"
"v_mfma_f32_16x16x32_bf16 v[108:111], a[220:223], v[180:183], v[108:111]\n"
"v_exp_f32 v88, v88\n"
"v_mfma_f32_16x16x32_bf16 v[116:119], a[220:223], v[184:187], v[116:119]\n"
"v_exp_f32 v89, v89\n"
"v_mfma_f32_16x16x32_bf16 v[124:127], a[220:223], v[188:191], v[124:127]\n"
"v_exp_f32 v90, v90\n"
"v_mfma_f32_16x16x32_bf16 v[96:99], a[192:195], v[192:195], v[96:99]\n"
"ds_read_b128 a[212:215], v1 offset:18688\n"
"v_cvt_pk_bf16_f32 v133, v74, v75\n"
"v_mfma_f32_16x16x32_bf16 v[104:107], a[192:195], v[196:199], v[104:107]\n"
"v_exp_f32 v91, v91\n"
"v_mfma_f32_16x16x32_bf16 v[112:115], a[192:195], v[200:203], v[112:115]\n"
"v_exp_f32 v92, v92\n"
"v_mfma_f32_16x16x32_bf16 v[120:123], a[192:195], v[204:207], v[120:123]\n"
"v_exp_f32 v93, v93\n"
"v_mfma_f32_16x16x32_bf16 v[100:103], a[196:199], v[192:195], v[100:103]\n"
"ds_read_b128 a[216:219], v2 offset:12544\n"
"v_cvt_pk_bf16_f32 v134, v76, v77\n"
"v_mfma_f32_16x16x32_bf16 v[108:111], a[196:199], v[196:199], v[108:111]\n"
"v_exp_f32 v94, v94\n"
"v_mfma_f32_16x16x32_bf16 v[116:119], a[196:199], v[200:203], v[116:119]\n"
"v_exp_f32 v95, v95\n"
"v_mfma_f32_16x16x32_bf16 v[124:127], a[196:199], v[204:207], v[124:127]\n"
"v_cvt_pk_bf16_f32 v135, v78, v79\n"
"s_waitcnt lgkmcnt(2)\n"
"v_mfma_f32_16x16x32_bf16 v[96:99], a[200:203], v[208:211], v[96:99]\n"
"ds_read_b128 a[220:223], v2 offset:18688\n"
"v_cvt_pk_bf16_f32 v136, v80, v81\n"
"v_mfma_f32_16x16x32_bf16 v[104:107], a[200:203], v[212:215], v[104:107]\n"
"v_cvt_pk_bf16_f32 v137, v82, v83\n"
"v_cvt_pk_bf16_f32 v138, v84, v85\n"
"v_mfma_f32_16x16x32_bf16 v[112:115], a[200:203], v[216:219], v[112:115]\n"
"v_cvt_pk_bf16_f32 v139, v86, v87\n"
"v_cvt_pk_bf16_f32 v140, v88, v89\n"
"v_mfma_f32_16x16x32_bf16 v[120:123], a[200:203], v[220:223], v[120:123]\n"
"v_cvt_pk_bf16_f32 v141, v90, v91\n"
"v_cvt_pk_bf16_f32 v142, v92, v93\n"
"v_mfma_f32_16x16x32_bf16 v[100:103], a[204:207], v[208:211], v[100:103]\n"
"ds_read_b64_tr_b16 a[224:225], v5 offset:0\n"
"ds_read_b64_tr_b16 a[226:227], v5 offset:6144\n"
"v_mfma_f32_16x16x32_bf16 v[108:111], a[204:207], v[212:215], v[108:111]\n"
"v_cvt_pk_bf16_f32 v143, v94, v95\n"
"v_add_f32 v41, v41, v80\n"
"v_mfma_f32_16x16x32_bf16 v[116:119], a[204:207], v[216:219], v[116:119]\n"
"v_add_f32 v42, v42, v81\n"
"v_add_f32 v41, v41, v82\n"
"v_mfma_f32_16x16x32_bf16 v[124:127], a[204:207], v[220:223], v[124:127]\n"
"v_add_f32 v42, v42, v83\n"
"v_add_f32 v41, v41, v84\n"
"v_mfma_f32_16x16x32_bf16 v[96:99], a[208:211], v[224:227], v[96:99]\n"
"ds_read_b64_tr_b16 a[228:229], v6 offset:0\n"
"ds_read_b64_tr_b16 a[230:231], v6 offset:6144\n"
"v_mfma_f32_16x16x32_bf16 v[104:107], a[208:211], v[228:231], v[104:107]\n"
"v_add_f32 v42, v42, v85\n"
"v_add_f32 v41, v41, v86\n"
"v_mfma_f32_16x16x32_bf16 v[112:115], a[208:211], v[232:235], v[112:115]\n"
"v_add_f32 v42, v42, v87\n"
"v_add_f32 v43, v43, v88\n"
"v_mfma_f32_16x16x32_bf16 v[120:123], a[208:211], v[236:239], v[120:123]\n"
"v_add_f32 v44, v44, v89\n"
"s_waitcnt lgkmcnt(4)\n"
"v_mfma_f32_16x16x32_bf16 v[100:103], a[212:215], v[224:227], v[100:103]\n"
"ds_read_b64_tr_b16 a[232:233], v7 offset:0\n"
"ds_read_b64_tr_b16 a[234:235], v7 offset:6144\n"
"v_mfma_f32_16x16x32_bf16 v[108:111], a[212:215], v[228:231], v[108:111]\n"
"v_add_f32 v43, v43, v90\n"
"v_add_f32 v44, v44, v91\n"
"v_mfma_f32_16x16x32_bf16 v[116:119], a[212:215], v[232:235], v[116:119]\n"
"v_add_f32 v43, v43, v92\n"
"v_add_f32 v44, v44, v93\n"
"v_mfma_f32_16x16x32_bf16 v[124:127], a[212:215], v[236:239], v[124:127]\n"
"v_add_f32 v43, v43, v94\n"
"v_add_f32 v44, v44, v95\n"
"v_mfma_f32_16x16x32_bf16 v[96:99], a[216:219], v[240:243], v[96:99]\n"
"ds_read_b64_tr_b16 a[236:237], v8 offset:0\n"
"ds_read_b64_tr_b16 a[238:239], v8 offset:6144\n"
"v_mfma_f32_16x16x32_bf16 v[104:107], a[216:219], v[244:247], v[104:107]\n"
"v_mfma_f32_16x16x32_bf16 v[112:115], a[216:219], v[248:251], v[112:115]\n"
"v_mfma_f32_16x16x32_bf16 v[120:123], a[216:219], v[252:255], v[120:123]\n"
"v_mfma_f32_16x16x32_bf16 v[100:103], a[220:223], v[240:243], v[100:103]\n"
"ds_read_b64_tr_b16 a[240:241], v5 offset:128\n"
"ds_read_b64_tr_b16 a[242:243], v5 offset:6272\n"
"v_mfma_f32_16x16x32_bf16 v[108:111], a[220:223], v[244:247], v[108:111]\n"
"v_max3_f32 v53, v96, v97, v98\n"
"v_mfma_f32_16x16x32_bf16 v[116:119], a[220:223], v[248:251], v[116:119]\n"
"v_max3_f32 v54, v99, v104, v105\n"
"v_max3_f32 v53, v53, v106, v107\n"
"v_mfma_f32_16x16x32_bf16 v[124:127], a[220:223], v[252:255], v[124:127]\n"
"v_max3_f32 v54, v54, v112, v113\n"
"s_waitcnt lgkmcnt(4)\n"
"v_mfma_f32_16x16x32_bf16 a[0:3], a[224:227], v[128:131], a[0:3]\n"
"ds_read_b64_tr_b16 a[244:245], v6 offset:128\n"
"ds_read_b64_tr_b16 a[246:247], v6 offset:6272\n"
"v_mfma_f32_16x16x32_bf16 a[4:7], a[224:227], v[132:135], a[4:7]\n"
"v_max3_f32 v53, v53, v114, v115\n"
"v_max3_f32 v54, v54, v120, v121\n"
"v_mfma_f32_16x16x32_bf16 a[8:11], a[224:227], v[136:139], a[8:11]\n"
"v_max3_f32 v53, v53, v122, v123\n"
"v_max3_f32 v54, v54, v100, v101\n"
"v_mfma_f32_16x16x32_bf16 a[12:15], a[224:227], v[140:143], a[12:15]\n"
"v_max3_f32 v53, v53, v102, v103\n"
"v_max3_f32 v54, v54, v108, v109\n"
"v_mfma_f32_16x16x32_bf16 a[16:19], a[228:231], v[128:131], a[16:19]\n"
"v_max3_f32 v53, v53, v110, v111\n"
"v_max3_f32 v54, v54, v116, v117\n"
"v_mfma_f32_16x16x32_bf16 a[20:23], a[228:231], v[132:135], a[20:23]\n"
"v_max3_f32 v53, v53, v118, v119\n"
"v_max3_f32 v54, v54, v124, v125\n"
"v_mfma_f32_16x16x32_bf16 a[24:27], a[228:231], v[136:139], a[24:27]\n"
"v_max3_f32 v53, v53, v126, v127\n"
"v_max_f32 v53, v53, v54\n"
"v_mfma_f32_16x16x32_bf16 a[28:31], a[228:231], v[140:143], a[28:31]\n"
"v_cmp_lt_f32 vcc, s64, v53\n"
"ds_read_b64_tr_b16 a[248:249], v7 offset:128\n"
"v_mfma_f32_16x16x32_bf16 a[32:35], a[232:235], v[128:131], a[32:35]\n"
"ds_read_b64_tr_b16 a[250:251], v7 offset:6272\n"
"ds_read_b64_tr_b16 a[252:253], v8 offset:128\n"
"v_mfma_f32_16x16x32_bf16 a[36:39], a[232:235], v[132:135], a[36:39]\n"
"ds_read_b64_tr_b16 a[254:255], v8 offset:6272\n"
"v_mfma_f32_16x16x32_bf16 a[40:43], a[232:235], v[136:139], a[40:43]\n"
"v_mfma_f32_16x16x32_bf16 a[44:47], a[232:235], v[140:143], a[44:47]\n"
"s_waitcnt lgkmcnt(4)\n"
"v_mfma_f32_16x16x32_bf16 a[48:51], a[236:239], v[128:131], a[48:51]\n"
"ds_read_b64_tr_b16 a[224:225], v5 offset:256\n"
"ds_read_b64_tr_b16 a[226:227], v5 offset:6400\n"
"v_mfma_f32_16x16x32_bf16 a[52:55], a[236:239], v[132:135], a[52:55]\n"
"s_cbranch_vccnz K1A_RS6\n"
"K1A_RSb6:\n"
"v_mfma_f32_16x16x32_bf16 a[56:59], a[236:239], v[136:139], a[56:59]\n"
"v_exp_f32 v96, v96\n"
"v_mfma_f32_16x16x32_bf16 a[60:63], a[236:239], v[140:143], a[60:63]\n"
"v_exp_f32 v97, v97\n"
"v_mfma_f32_16x16x32_bf16 a[64:67], a[240:243], v[128:131], a[64:67]\n"
"ds_read_b64_tr_b16 a[228:229], v6 offset:256\n"
"ds_read_b64_tr_b16 a[230:231], v6 offset:6400\n"
"v_mfma_f32_16x16x32_bf16 a[68:71], a[240:243], v[132:135], a[68:71]\n"
"v_exp_f32 v98, v98\n"
"v_mfma_f32_16x16x32_bf16 a[72:75], a[240:243], v[136:139], a[72:75]\n"
"v_exp_f32 v99, v99\n"
"v_mfma_f32_16x16x32_bf16 a[76:79], a[240:243], v[140:143], a[76:79]\n"
"v_exp_f32 v100, v100\n"
"v_mfma_f32_16x16x32_bf16 a[80:83], a[244:247], v[128:131], a[80:83]\n"
"ds_read_b64_tr_b16 a[232:233], v7 offset:256\n"
"ds_read_b64_tr_b16 a[234:235], v7 offset:6400\n"
"v_mfma_f32_16x16x32_bf16 a[84:87], a[244:247], v[132:135], a[84:87]\n"
"v_exp_f32 v101, v101\n"
"v_mfma_f32_16x16x32_bf16 a[88:91], a[244:247], v[136:139], a[88:91]\n"
"v_exp_f32 v102, v102\n"
"v_mfma_f32_16x16x32_bf16 a[92:95], a[244:247], v[140:143], a[92:95]\n"
"v_add_f32 v37, v37, v96\n"
"s_waitcnt lgkmcnt(4)\n"
"v_mfma_f32_16x16x32_bf16 a[96:99], a[248:251], v[128:131], a[96:99]\n"
"ds_read_b64_tr_b16 a[236:237], v8 offset:256\n"
"ds_read_b64_tr_b16 a[238:239], v8 offset:6400\n"
"v_mfma_f32_16x16x32_bf16 a[100:103], a[248:251], v[132:135], a[100:103]\n"
"v_exp_f32 v103, v103\n"
"v_mfma_f32_16x16x32_bf16 a[104:107], a[248:251], v[136:139], a[104:107]\n"
"s_barrier\n"
"v_add_f32 v38, v38, v97\n"
"v_mfma_f32_16x16x32_bf16 a[108:111], a[248:251], v[140:143], a[108:111]\n"
"v_exp_f32 v104, v104\n"
"v_mfma_f32_16x16x32_bf16 a[112:115], a[252:255], v[128:131], a[112:115]\n"
"ds_read_b128 a[192:195], v1 offset:24576\n"
"v_add_f32 v37, v37, v98\n"
"v_mfma_f32_16x16x32_bf16 a[116:119], a[252:255], v[132:135], a[116:119]\n"
"v_exp_f32 v105, v105\n"
"v_mfma_f32_16x16x32_bf16 a[120:123], a[252:255], v[136:139], a[120:123]\n"
"v_exp_f32 v106, v106\n"
"v_mfma_f32_16x16x32_bf16 a[124:127], a[252:255], v[140:143], a[124:127]\n"
"v_exp_f32 v107, v107\n"
"v_mfma_f32_16x16x32_bf16 a[128:131], a[224:227], v[128:131], a[128:131]\n"
"ds_read_b128 a[196:199], v1 offset:30720\n"
"v_add_f32 v38, v38, v99\n"
"v_mfma_f32_16x16x32_bf16 a[132:135], a[224:227], v[132:135], a[132:135]\n"
"v_exp_f32 v108, v108\n"
"v_mfma_f32_16x16x32_bf16 a[136:139], a[224:227], v[136:139], a[136:139]\n"
"v_exp_f32 v109, v109\n"
"v_mfma_f32_16x16x32_bf16 a[140:143], a[224:227], v[140:143], a[140:143]\n"
"v_add_f32 v37, v37, v100\n"
"s_waitcnt lgkmcnt(2)\n"
"v_mfma_f32_16x16x32_bf16 a[144:147], a[228:231], v[128:131], a[144:147]\n"
"ds_read_b128 a[200:203], v2 offset:24576\n"
"v_add_f32 v38, v38, v101\n"
"v_mfma_f32_16x16x32_bf16 a[148:151], a[228:231], v[132:135], a[148:151]\n"
"v_exp_f32 v110, v110\n"
"v_mfma_f32_16x16x32_bf16 a[152:155], a[228:231], v[136:139], a[152:155]\n"
"v_exp_f32 v111, v111\n"
"v_mfma_f32_16x16x32_bf16 a[156:159], a[228:231], v[140:143], a[156:159]\n"
"v_add_f32 v37, v37, v102\n"
"v_add_f32 v38, v38, v103\n"
"v_mfma_f32_16x16x32_bf16 a[160:163], a[232:235], v[128:131], a[160:163]\n"
"ds_read_b128 a[204:207], v2 offset:30720\n"
"v_add_f32 v39, v39, v104\n"
"v_mfma_f32_16x16x32_bf16 a[164:167], a[232:235], v[132:135], a[164:167]\n"
"v_add_f32 v40, v40, v105\n"
"v_add_f32 v39, v39, v106\n"
"v_mfma_f32_16x16x32_bf16 a[168:171], a[232:235], v[136:139], a[168:171]\n"
"v_add_f32 v40, v40, v107\n"
"v_add_f32 v39, v39, v108\n"
"v_mfma_f32_16x16x32_bf16 a[172:175], a[232:235], v[140:143], a[172:175]\n"
"v_add_f32 v40, v40, v109\n"
"v_add_f32 v39, v39, v110\n"
"v_mfma_f32_16x16x32_bf16 a[176:179], a[236:239], v[128:131], a[176:179]\n"
"ds_read_b128 a[208:211], v1 offset:24704\n"
"v_add_f32 v40, v40, v111\n"
"v_mfma_f32_16x16x32_bf16 a[180:183], a[236:239], v[132:135], a[180:183]\n"
"v_mfma_f32_16x16x32_bf16 a[184:187], a[236:239], v[136:139], a[184:187]\n"
"v_mfma_f32_16x16x32_bf16 a[188:191], a[236:239], v[140:143], a[188:191]\n"
"s_cbranch_vccnz K1A_RO6\n"
"K1A_ROb6:\n"
"s_sub_u32 s61, s61, 1\n"
"s_cmp_lg_u32 s61, 0\n"
"s_cbranch_scc1 K1A_AL\n"
"s_waitcnt vmcnt(0)\n"
"ds_read_b64_tr_b16 a[240:241], v5 offset:12288\n"
"ds_read_b64_tr_b16 a[242:243], v5 offset:18432\n"
"ds_read_b64_tr_b16 a[244:245], v6 offset:12288\n"
"ds_read_b64_tr_b16 a[246:247], v6 offset:18432\n"
"ds_read_b64_tr_b16 a[248:249], v7 offset:12288\n"
"ds_read_b64_tr_b16 a[250:251], v7 offset:18432\n"
"ds_read_b64_tr_b16 a[252:253], v8 offset:12288\n"
"ds_read_b64_tr_b16 a[254:255], v8 offset:18432\n"
"ds_read_b64_tr_b16 a[224:225], v5 offset:12416\n"
"ds_read_b64_tr_b16 a[226:227], v5 offset:18560\n"
"v_exp_f32 v112, v112\n"
"v_exp_f32 v113, v113\n"
"v_exp_f32 v114, v114\n"
"v_exp_f32 v115, v115\n"
"v_exp_f32 v116, v116\n"
"v_exp_f32 v117, v117\n"
"v_exp_f32 v118, v118\n"
"v_exp_f32 v119, v119\n"
"v_exp_f32 v120, v120\n"
"v_exp_f32 v121, v121\n"
"v_exp_f32 v122, v122\n"
"v_exp_f32 v123, v123\n"
"v_exp_f32 v124, v124\n"
"v_exp_f32 v125, v125\n"
"v_exp_f32 v126, v126\n"
"v_exp_f32 v127, v127\n"
"s_nop 0\n"
"v_cvt_pk_bf16_f32 v128, v96, v97\n"
"v_cvt_pk_bf16_f32 v129, v98, v99\n"
"v_cvt_pk_bf16_f32 v130, v100, v101\n"
"v_cvt_pk_bf16_f32 v131, v102, v103\n"
"v_cvt_pk_bf16_f32 v132, v104, v105\n"
"v_cvt_pk_bf16_f32 v133, v106, v107\n"
"v_cvt_pk_bf16_f32 v134, v108, v109\n"
"v_cvt_pk_bf16_f32 v135, v110, v111\n"
"v_cvt_pk_bf16_f32 v136, v112, v113\n"
"v_cvt_pk_bf16_f32 v137, v114, v115\n"
"v_cvt_pk_bf16_f32 v138, v116, v117\n"
"v_cvt_pk_bf16_f32 v139, v118, v119\n"
"v_cvt_pk_bf16_f32 v140, v120, v121\n"
"v_cvt_pk_bf16_f32 v141, v122, v123\n"
"v_cvt_pk_bf16_f32 v142, v124, v125\n"
"v_cvt_pk_bf16_f32 v143, v126, v127\n"
"s_waitcnt lgkmcnt(4)\n"
"v_mfma_f32_16x16x32_bf16 a[0:3], a[240:243], v[128:131], a[0:3]\n"
"ds_read_b64_tr_b16 a[228:229], v6 offset:12416\n"
"ds_read_b64_tr_b16 a[230:231], v6 offset:18560\n"
"v_mfma_f32_16x16x32_bf16 a[4:7], a[240:243], v[132:135], a[4:7]\n"
"v_add_f32 v41, v41, v112\n"
"v_add_f32 v42, v42, v113\n"
"v_mfma_f32_16x16x32_bf16 a[8:11], a[240:243], v[136:139], a[8:11]\n"
"v_add_f32 v41, v41, v114\n"
"v_add_f32 v42, v42, v115\n"
"v_mfma_f32_16x16x32_bf16 a[12:15], a[240:243], v[140:143], a[12:15]\n"
"v_add_f32 v41, v41, v116\n"
"v_add_f32 v42, v42, v117\n"
"v_mfma_f32_16x16x32_bf16 a[16:19], a[244:247], v[128:131], a[16:19]\n"
"ds_read_b64_tr_b16 a[232:233], v7 offset:12416\n"
"ds_read_b64_tr_b16 a[234:235], v7 offset:18560\n"
"v_mfma_f32_16x16x32_bf16 a[20:23], a[244:247], v[132:135], a[20:23]\n"
"v_add_f32 v41, v41, v118\n"
"v_add_f32 v42, v42, v119\n"
"v_mfma_f32_16x16x32_bf16 a[24:27], a[244:247], v[136:139], a[24:27]\n"
"v_add_f32 v43, v43, v120\n"
"v_add_f32 v44, v44, v121\n"
"v_mfma_f32_16x16x32_bf16 a[28:31], a[244:247], v[140:143], a[28:31]\n"
"v_add_f32 v43, v43, v122\n"
"v_add_f32 v44, v44, v123\n"
"v_mfma_f32_16x16x32_bf16 a[32:35], a[248:251], v[128:131], a[32:35]\n"
"ds_read_b64_tr_b16 a[236:237], v8 offset:12416\n"
"ds_read_b64_tr_b16 a[238:239], v8 offset:18560\n"
"v_mfma_f32_16x16x32_bf16 a[36:39], a[248:251], v[132:135], a[36:39]\n"
"v_add_f32 v43, v43, v124\n"
"v_add_f32 v44, v44, v125\n"
"v_mfma_f32_16x16x32_bf16 a[40:43], a[248:251], v[136:139], a[40:43]\n"
"v_add_f32 v43, v43, v126\n"
"v_add_f32 v44, v44, v127\n"
"v_mfma_f32_16x16x32_bf16 a[44:47], a[248:251], v[140:143], a[44:47]\n"
"v_add_f32 v37, v37, v38\n"
"s_waitcnt lgkmcnt(4)\n"
"v_mfma_f32_16x16x32_bf16 a[48:51], a[252:255], v[128:131], a[48:51]\n"
"ds_read_b64_tr_b16 a[240:241], v5 offset:12544\n"
"ds_read_b64_tr_b16 a[242:243], v5 offset:18688\n"
"v_mfma_f32_16x16x32_bf16 a[52:55], a[252:255], v[132:135], a[52:55]\n"
"v_mov_b32 v55, v37\n"
"v_add_f32 v39, v39, v40\n"
"v_mfma_f32_16x16x32_bf16 a[56:59], a[252:255], v[136:139], a[56:59]\n"
"v_mov_b32 v56, v39\n"
"v_add_f32 v41, v41, v42\n"
"v_mfma_f32_16x16x32_bf16 a[60:63], a[252:255], v[140:143], a[60:63]\n"
"v_permlane32_swap_b32 v55, v37\n"
"v_mov_b32 v57, v41\n"
"v_mfma_f32_16x16x32_bf16 a[64:67], a[224:227], v[128:131], a[64:67]\n"
"ds_read_b64_tr_b16 a[244:245], v6 offset:12544\n"
"ds_read_b64_tr_b16 a[246:247], v6 offset:18688\n"
"v_mfma_f32_16x16x32_bf16 a[68:71], a[224:227], v[132:135], a[68:71]\n"
"v_add_f32 v37, v37, v55\n"
"v_mov_b32 v55, v37\n"
"v_mfma_f32_16x16x32_bf16 a[72:75], a[224:227], v[136:139], a[72:75]\n"
"v_permlane32_swap_b32 v56, v39\n"
"v_permlane32_swap_b32 v57, v41\n"
"v_mfma_f32_16x16x32_bf16 a[76:79], a[224:227], v[140:143], a[76:79]\n"
"v_permlane16_swap_b32 v55, v37\n"
"v_add_f32 v39, v39, v56\n"
"v_mfma_f32_16x16x32_bf16 a[80:83], a[228:231], v[128:131], a[80:83]\n"
"ds_read_b64_tr_b16 a[248:249], v7 offset:12544\n"
"ds_read_b64_tr_b16 a[250:251], v7 offset:18688\n"
"v_mfma_f32_16x16x32_bf16 a[84:87], a[228:231], v[132:135], a[84:87]\n"
"v_add_f32 v37, v37, v55\n"
"v_mov_b32 v56, v39\n"
"v_mfma_f32_16x16x32_bf16 a[88:91], a[228:231], v[136:139], a[88:91]\n"
"v_rcp_f32 v49, v37\n"
"v_mfma_f32_16x16x32_bf16 a[92:95], a[228:231], v[140:143], a[92:95]\n"
"v_permlane16_swap_b32 v56, v39\n"
"s_waitcnt lgkmcnt(4)\n"
"v_mfma_f32_16x16x32_bf16 a[96:99], a[232:235], v[128:131], a[96:99]\n"
"ds_read_b64_tr_b16 a[252:253], v8 offset:12544\n"
"ds_read_b64_tr_b16 a[254:255], v8 offset:18688\n"
"v_mfma_f32_16x16x32_bf16 a[100:103], a[232:235], v[132:135], a[100:103]\n"
"v_add_f32 v39, v39, v56\n"
"v_add_f32 v41, v41, v57\n"
"v_mfma_f32_16x16x32_bf16 a[104:107], a[232:235], v[136:139], a[104:107]\n"
"v_rcp_f32 v50, v39\n"
"v_mfma_f32_16x16x32_bf16 a[108:111], a[232:235], v[140:143], a[108:111]\n"
"v_mov_b32 v57, v41\n"
"v_add_f32 v43, v43, v44\n"
"v_mfma_f32_16x16x32_bf16 a[112:115], a[236:239], v[128:131], a[112:115]\n"
"v_mov_b32 v58, v43\n"
"v_mfma_f32_16x16x32_bf16 a[116:119], a[236:239], v[132:135], a[116:119]\n"
"v_permlane16_swap_b32 v57, v41\n"
"v_mfma_f32_16x16x32_bf16 a[120:123], a[236:239], v[136:139], a[120:123]\n"
"v_add_f32 v41, v41, v57\n"
"v_permlane32_swap_b32 v58, v43\n"
"v_mfma_f32_16x16x32_bf16 a[124:127], a[236:239], v[140:143], a[124:127]\n"
"v_rcp_f32 v51, v41\n"
"v_mfma_f32_16x16x32_bf16 a[128:131], a[240:243], v[128:131], a[128:131]\n"
"v_add_f32 v43, v43, v58\n"
"v_mov_b32 v58, v43\n"
"v_mfma_f32_16x16x32_bf16 a[132:135], a[240:243], v[132:135], a[132:135]\n"
"v_mfma_f32_16x16x32_bf16 a[136:139], a[240:243], v[136:139], a[136:139]\n"
"v_permlane16_swap_b32 v58, v43\n"
"v_mfma_f32_16x16x32_bf16 a[140:143], a[240:243], v[140:143], a[140:143]\n"
"v_add_f32 v43, v43, v58\n"
"s_waitcnt lgkmcnt(0)\n"
"v_mfma_f32_16x16x32_bf16 a[144:147], a[244:247], v[128:131], a[144:147]\n"
"v_rcp_f32 v52, v43\n"
"v_mfma_f32_16x16x32_bf16 a[148:151], a[244:247], v[132:135], a[148:151]\n"
"v_mfma_f32_16x16x32_bf16 a[152:155], a[244:247], v[136:139], a[152:155]\n"
"v_mfma_f32_16x16x32_bf16 a[156:159], a[244:247], v[140:143], a[156:159]\n"
"v_mfma_f32_16x16x32_bf16 a[160:163], a[248:251], v[128:131], a[160:163]\n"
"v_mfma_f32_16x16x32_bf16 a[164:167], a[248:251], v[132:135], a[164:167]\n"
"v_mfma_f32_16x16x32_bf16 a[168:171], a[248:251], v[136:139], a[168:171]\n"
"v_mfma_f32_16x16x32_bf16 a[172:175], a[248:251], v[140:143], a[172:175]\n"
"v_mfma_f32_16x16x32_bf16 a[176:179], a[252:255], v[128:131], a[176:179]\n"
"v_mfma_f32_16x16x32_bf16 a[180:183], a[252:255], v[132:135], a[180:183]\n"
"v_mfma_f32_16x16x32_bf16 a[184:187], a[252:255], v[136:139], a[184:187]\n"
"v_mfma_f32_16x16x32_bf16 a[188:191], a[252:255], v[140:143], a[188:191]\n"
"v_lshlrev_b32 v1, 4, v0\n"
"s_nop 7\n"
"v_accvgpr_read_b32 v68, a0\n"
"v_accvgpr_read_b32 v69, a1\n"
"v_accvgpr_read_b32 v70, a2\n"
"v_accvgpr_read_b32 v71, a3\n"
"v_accvgpr_read_b32 v72, a16\n"
"v_accvgpr_read_b32 v73, a17\n"
"v_accvgpr_read_b32 v74, a18\n"
"v_accvgpr_read_b32 v75, a19\n"
"v_pk_mul_f32 v[68:69], v[68:69], v[48:49] op_sel:[0,1] op_sel_hi:[1,1]\n"
"v_pk_mul_f32 v[70:71], v[70:71], v[48:49] op_sel:[0,1] op_sel_hi:[1,1]\n"
"v_pk_mul_f32 v[72:73], v[72:73], v[48:49] op_sel:[0,1] op_sel_hi:[1,1]\n"
"v_pk_mul_f32 v[74:75], v[74:75], v[48:49] op_sel:[0,1] op_sel_hi:[1,1]\n"
"v_cvt_pk_bf16_f32 v64, v68, v69\n"
"v_cvt_pk_bf16_f32 v65, v70, v71\n"
"v_cvt_pk_bf16_f32 v66, v72, v73\n"
"v_cvt_pk_bf16_f32 v67, v74, v75\n"
"buffer_store_dwordx4 v[64:67], v1, s[52:55], s68 offen offset:0\n"
"v_accvgpr_read_b32 v84, a4\n"
"v_accvgpr_read_b32 v85, a5\n"
"v_accvgpr_read_b32 v86, a6\n"
"v_accvgpr_read_b32 v87, a7\n"
"v_accvgpr_read_b32 v88, a20\n"
"v_accvgpr_read_b32 v89, a21\n"
"v_accvgpr_read_b32 v90, a22\n"
"v_accvgpr_read_b32 v91, a23\n"
"v_pk_mul_f32 v[84:85], v[84:85], v[50:51] op_sel:[0,0] op_sel_hi:[1,0]\n"
"v_pk_mul_f32 v[86:87], v[86:87], v[50:51] op_sel:[0,0] op_sel_hi:[1,0]\n"
"v_pk_mul_f32 v[88:89], v[88:89], v[50:51] op_sel:[0,0] op_sel_hi:[1,0]\n"
"v_pk_mul_f32 v[90:91], v[90:91], v[50:51] op_sel:[0,0] op_sel_hi:[1,0]\n"
"v_cvt_pk_bf16_f32 v80, v84, v85\n"
"v_cvt_pk_bf16_f32 v81, v86, v87\n"
"v_cvt_pk_bf16_f32 v82, v88, v89\n"
"v_cvt_pk_bf16_f32 v83, v90, v91\n"
"buffer_store_dwordx4 v[80:83], v1, s[52:55], s68 offen offset:1024\n"
"v_accvgpr_read_b32 v100, a8\n"
"v_accvgpr_read_b32 v101, a9\n"
"v_accvgpr_read_b32 v102, a10\n"
"v_accvgpr_read_b32 v103, a11\n"
"v_accvgpr_read_b32 v104, a24\n"
"v_accvgpr_read_b32 v105, a25\n"
"v_accvgpr_read_b32 v106, a26\n"
"v_accvgpr_read_b32 v107, a27\n"
"v_pk_mul_f32 v[100:101], v[100:101], v[50:51] op_sel:[0,1] op_sel_hi:[1,1]\n"
"v_pk_mul_f32 v[102:103], v[102:103], v[50:51] op_sel:[0,1] op_sel_hi:[1,1]\n"
"v_pk_mul_f32 v[104:105], v[104:105], v[50:51] op_sel:[0,1] op_sel_hi:[1,1]\n"
"v_pk_mul_f32 v[106:107], v[106:107], v[50:51] op_sel:[0,1] op_sel_hi:[1,1]\n"
"v_cvt_pk_bf16_f32 v96, v100, v101\n"
"v_cvt_pk_bf16_f32 v97, v102, v103\n"
"v_cvt_pk_bf16_f32 v98, v104, v105\n"
"v_cvt_pk_bf16_f32 v99, v106, v107\n"
"buffer_store_dwordx4 v[96:99], v1, s[52:55], s68 offen offset:2048\n"
"v_accvgpr_read_b32 v116, a12\n"
"v_accvgpr_read_b32 v117, a13\n"
"v_accvgpr_read_b32 v118, a14\n"
"v_accvgpr_read_b32 v119, a15\n"
"v_accvgpr_read_b32 v120, a28\n"
"v_accvgpr_read_b32 v121, a29\n"
"v_accvgpr_read_b32 v122, a30\n"
"v_accvgpr_read_b32 v123, a31\n"
"v_pk_mul_f32 v[116:117], v[116:117], v[52:53] op_sel:[0,0] op_sel_hi:[1,0]\n"
"v_pk_mul_f32 v[118:119], v[118:119], v[52:53] op_sel:[0,0] op_sel_hi:[1,0]\n"
"v_pk_mul_f32 v[120:121], v[120:121], v[52:53] op_sel:[0,0] op_sel_hi:[1,0]\n"
"v_pk_mul_f32 v[122:123], v[122:123], v[52:53] op_sel:[0,0] op_sel_hi:[1,0]\n"
"v_cvt_pk_bf16_f32 v112, v116, v117\n"
"v_cvt_pk_bf16_f32 v113, v118, v119\n"
"v_cvt_pk_bf16_f32 v114, v120, v121\n"
"v_cvt_pk_bf16_f32 v115, v122, v123\n"
"buffer_store_dwordx4 v[112:115], v1, s[52:55], s68 offen offset:3072\n"
"s_add_u32 s68, s68, 122880\n"
"v_accvgpr_read_b32 v68, a32\n"
"v_accvgpr_read_b32 v69, a33\n"
"v_accvgpr_read_b32 v70, a34\n"
"v_accvgpr_read_b32 v71, a35\n"
"v_accvgpr_read_b32 v72, a48\n"
"v_accvgpr_read_b32 v73, a49\n"
"v_accvgpr_read_b32 v74, a50\n"
"v_accvgpr_read_b32 v75, a51\n"
"v_pk_mul_f32 v[68:69], v[68:69], v[48:49] op_sel:[0,1] op_sel_hi:[1,1]\n"
"v_pk_mul_f32 v[70:71], v[70:71], v[48:49] op_sel:[0,1] op_sel_hi:[1,1]\n"
"v_pk_mul_f32 v[72:73], v[72:73], v[48:49] op_sel:[0,1] op_sel_hi:[1,1]\n"
"v_pk_mul_f32 v[74:75], v[74:75], v[48:49] op_sel:[0,1] op_sel_hi:[1,1]\n"
"v_cvt_pk_bf16_f32 v64, v68, v69\n"
"v_cvt_pk_bf16_f32 v65, v70, v71\n"
"v_cvt_pk_bf16_f32 v66, v72, v73\n"
"v_cvt_pk_bf16_f32 v67, v74, v75\n"
"buffer_store_dwordx4 v[64:67], v1, s[52:55], s68 offen offset:0\n"
"v_accvgpr_read_b32 v84, a36\n"
"v_accvgpr_read_b32 v85, a37\n"
"v_accvgpr_read_b32 v86, a38\n"
"v_accvgpr_read_b32 v87, a39\n"
"v_accvgpr_read_b32 v88, a52\n"
"v_accvgpr_read_b32 v89, a53\n"
"v_accvgpr_read_b32 v90, a54\n"
"v_accvgpr_read_b32 v91, a55\n"
"v_pk_mul_f32 v[84:85], v[84:85], v[50:51] op_sel:[0,0] op_sel_hi:[1,0]\n"
"v_pk_mul_f32 v[86:87], v[86:87], v[50:51] op_sel:[0,0] op_sel_hi:[1,0]\n"
"v_pk_mul_f32 v[88:89], v[88:89], v[50:51] op_sel:[0,0] op_sel_hi:[1,0]\n"
"v_pk_mul_f32 v[90:91], v[90:91], v[50:51] op_sel:[0,0] op_sel_hi:[1,0]\n"
"v_cvt_pk_bf16_f32 v80, v84, v85\n"
"v_cvt_pk_bf16_f32 v81, v86, v87\n"
"v_cvt_pk_bf16_f32 v82, v88, v89\n"
"v_cvt_pk_bf16_f32 v83, v90, v91\n"
"buffer_store_dwordx4 v[80:83], v1, s[52:55], s68 offen offset:1024\n"
"v_accvgpr_read_b32 v100, a40\n"
"v_accvgpr_read_b32 v101, a41\n"
"v_accvgpr_read_b32 v102, a42\n"
"v_accvgpr_read_b32 v103, a43\n"
"v_accvgpr_read_b32 v104, a56\n"
"v_accvgpr_read_b32 v105, a57\n"
"v_accvgpr_read_b32 v106, a58\n"
"v_accvgpr_read_b32 v107, a59\n"
"v_pk_mul_f32 v[100:101], v[100:101], v[50:51] op_sel:[0,1] op_sel_hi:[1,1]\n"
"v_pk_mul_f32 v[102:103], v[102:103], v[50:51] op_sel:[0,1] op_sel_hi:[1,1]\n"
"v_pk_mul_f32 v[104:105], v[104:105], v[50:51] op_sel:[0,1] op_sel_hi:[1,1]\n"
"v_pk_mul_f32 v[106:107], v[106:107], v[50:51] op_sel:[0,1] op_sel_hi:[1,1]\n"
"v_cvt_pk_bf16_f32 v96, v100, v101\n"
"v_cvt_pk_bf16_f32 v97, v102, v103\n"
"v_cvt_pk_bf16_f32 v98, v104, v105\n"
"v_cvt_pk_bf16_f32 v99, v106, v107\n"
"buffer_store_dwordx4 v[96:99], v1, s[52:55], s68 offen offset:2048\n"
"v_accvgpr_read_b32 v116, a44\n"
"v_accvgpr_read_b32 v117, a45\n"
"v_accvgpr_read_b32 v118, a46\n"
"v_accvgpr_read_b32 v119, a47\n"
"v_accvgpr_read_b32 v120, a60\n"
"v_accvgpr_read_b32 v121, a61\n"
"v_accvgpr_read_b32 v122, a62\n"
"v_accvgpr_read_b32 v123, a63\n"
"v_pk_mul_f32 v[116:117], v[116:117], v[52:53] op_sel:[0,0] op_sel_hi:[1,0]\n"
"v_pk_mul_f32 v[118:119], v[118:119], v[52:53] op_sel:[0,0] op_sel_hi:[1,0]\n"
"v_pk_mul_f32 v[120:121], v[120:121], v[52:53] op_sel:[0,0] op_sel_hi:[1,0]\n"
"v_pk_mul_f32 v[122:123], v[122:123], v[52:53] op_sel:[0,0] op_sel_hi:[1,0]\n"
"v_cvt_pk_bf16_f32 v112, v116, v117\n"
"v_cvt_pk_bf16_f32 v113, v118, v119\n"
"v_cvt_pk_bf16_f32 v114, v120, v121\n"
"v_cvt_pk_bf16_f32 v115, v122, v123\n"
"buffer_store_dwordx4 v[112:115], v1, s[52:55], s68 offen offset:3072\n"
"s_add_u32 s68, s68, 122880\n"
"v_accvgpr_read_b32 v68, a64\n"
"v_accvgpr_read_b32 v69, a65\n"
"v_accvgpr_read_b32 v70, a66\n"
"v_accvgpr_read_b32 v71, a67\n"
"v_accvgpr_read_b32 v72, a80\n"
"v_accvgpr_read_b32 v73, a81\n"
"v_accvgpr_read_b32 v74, a82\n"
"v_accvgpr_read_b32 v75, a83\n"
"v_pk_mul_f32 v[68:69], v[68:69], v[48:49] op_sel:[0,1] op_sel_hi:[1,1]\n"
"v_pk_mul_f32 v[70:71], v[70:71], v[48:49] op_sel:[0,1] op_sel_hi:[1,1]\n"
"v_pk_mul_f32 v[72:73], v[72:73], v[48:49] op_sel:[0,1] op_sel_hi:[1,1]\n"
"v_pk_mul_f32 v[74:75], v[74:75], v[48:49] op_sel:[0,1] op_sel_hi:[1,1]\n"
"v_cvt_pk_bf16_f32 v64, v68, v69\n"
"v_cvt_pk_bf16_f32 v65, v70, v71\n"
"v_cvt_pk_bf16_f32 v66, v72, v73\n"
"v_cvt_pk_bf16_f32 v67, v74, v75\n"
"buffer_store_dwordx4 v[64:67], v1, s[52:55], s68 offen offset:0\n"
"v_accvgpr_read_b32 v84, a68\n"
"v_accvgpr_read_b32 v85, a69\n"
"v_accvgpr_read_b32 v86, a70\n"
"v_accvgpr_read_b32 v87, a71\n"
"v_accvgpr_read_b32 v88, a84\n"
"v_accvgpr_read_b32 v89, a85\n"
"v_accvgpr_read_b32 v90, a86\n"
"v_accvgpr_read_b32 v91, a87\n"
"v_pk_mul_f32 v[84:85], v[84:85], v[50:51] op_sel:[0,0] op_sel_hi:[1,0]\n"
"v_pk_mul_f32 v[86:87], v[86:87], v[50:51] op_sel:[0,0] op_sel_hi:[1,0]\n"
"v_pk_mul_f32 v[88:89], v[88:89], v[50:51] op_sel:[0,0] op_sel_hi:[1,0]\n"
"v_pk_mul_f32 v[90:91], v[90:91], v[50:51] op_sel:[0,0] op_sel_hi:[1,0]\n"
"v_cvt_pk_bf16_f32 v80, v84, v85\n"
"v_cvt_pk_bf16_f32 v81, v86, v87\n"
"v_cvt_pk_bf16_f32 v82, v88, v89\n"
"v_cvt_pk_bf16_f32 v83, v90, v91\n"
"buffer_store_dwordx4 v[80:83], v1, s[52:55], s68 offen offset:1024\n"
"v_accvgpr_read_b32 v100, a72\n"
"v_accvgpr_read_b32 v101, a73\n"
"v_accvgpr_read_b32 v102, a74\n"
"v_accvgpr_read_b32 v103, a75\n"
"v_accvgpr_read_b32 v104, a88\n"
"v_accvgpr_read_b32 v105, a89\n"
"v_accvgpr_read_b32 v106, a90\n"
"v_accvgpr_read_b32 v107, a91\n"
"v_pk_mul_f32 v[100:101], v[100:101], v[50:51] op_sel:[0,1] op_sel_hi:[1,1]\n"
"v_pk_mul_f32 v[102:103], v[102:103], v[50:51] op_sel:[0,1] op_sel_hi:[1,1]\n"
"v_pk_mul_f32 v[104:105], v[104:105], v[50:51] op_sel:[0,1] op_sel_hi:[1,1]\n"
"v_pk_mul_f32 v[106:107], v[106:107], v[50:51] op_sel:[0,1] op_sel_hi:[1,1]\n"
"v_cvt_pk_bf16_f32 v96, v100, v101\n"
"v_cvt_pk_bf16_f32 v97, v102, v103\n"
"v_cvt_pk_bf16_f32 v98, v104, v105\n"
"v_cvt_pk_bf16_f32 v99, v106, v107\n"
"buffer_store_dwordx4 v[96:99], v1, s[52:55], s68 offen offset:2048\n"
"v_accvgpr_read_b32 v116, a76\n"
"v_accvgpr_read_b32 v117, a77\n"
"v_accvgpr_read_b32 v118, a78\n"
"v_accvgpr_read_b32 v119, a79\n"
"v_accvgpr_read_b32 v120, a92\n"
"v_accvgpr_read_b32 v121, a93\n"
"v_accvgpr_read_b32 v122, a94\n"
"v_accvgpr_read_b32 v123, a95\n"
"v_pk_mul_f32 v[116:117], v[116:117], v[52:53] op_sel:[0,0] op_sel_hi:[1,0]\n"
"v_pk_mul_f32 v[118:119], v[118:119], v[52:53] op_sel:[0,0] op_sel_hi:[1,0]\n"
"v_pk_mul_f32 v[120:121], v[120:121], v[52:53] op_sel:[0,0] op_sel_hi:[1,0]\n"
"v_pk_mul_f32 v[122:123], v[122:123], v[52:53] op_sel:[0,0] op_sel_hi:[1,0]\n"
"v_cvt_pk_bf16_f32 v112, v116, v117\n"
"v_cvt_pk_bf16_f32 v113, v118, v119\n"
"v_cvt_pk_bf16_f32 v114, v120, v121\n"
"v_cvt_pk_bf16_f32 v115, v122, v123\n"
"buffer_store_dwordx4 v[112:115], v1, s[52:55], s68 offen offset:3072\n"
"s_add_u32 s68, s68, 122880\n"
"v_accvgpr_read_b32 v68, a96\n"
"v_accvgpr_read_b32 v69, a97\n"
"v_accvgpr_read_b32 v70, a98\n"
"v_accvgpr_read_b32 v71, a99\n"
"v_accvgpr_read_b32 v72, a112\n"
"v_accvgpr_read_b32 v73, a113\n"
"v_accvgpr_read_b32 v74, a114\n"
"v_accvgpr_read_b32 v75, a115\n"
"v_pk_mul_f32 v[68:69], v[68:69], v[48:49] op_sel:[0,1] op_sel_hi:[1,1]\n"
"v_pk_mul_f32 v[70:71], v[70:71], v[48:49] op_sel:[0,1] op_sel_hi:[1,1]\n"
"v_pk_mul_f32 v[72:73], v[72:73], v[48:49] op_sel:[0,1] op_sel_hi:[1,1]\n"
"v_pk_mul_f32 v[74:75], v[74:75], v[48:49] op_sel:[0,1] op_sel_hi:[1,1]\n"
"v_cvt_pk_bf16_f32 v64, v68, v69\n"
"v_cvt_pk_bf16_f32 v65, v70, v71\n"
"v_cvt_pk_bf16_f32 v66, v72, v73\n"
"v_cvt_pk_bf16_f32 v67, v74, v75\n"
"buffer_store_dwordx4 v[64:67], v1, s[52:55], s68 offen offset:0\n"
"v_accvgpr_read_b32 v84, a100\n"
"v_accvgpr_read_b32 v85, a101\n"
"v_accvgpr_read_b32 v86, a102\n"
"v_accvgpr_read_b32 v87, a103\n"
"v_accvgpr_read_b32 v88, a116\n"
"v_accvgpr_read_b32 v89, a117\n"
"v_accvgpr_read_b32 v90, a118\n"
"v_accvgpr_read_b32 v91, a119\n"
"v_pk_mul_f32 v[84:85], v[84:85], v[50:51] op_sel:[0,0] op_sel_hi:[1,0]\n"
"v_pk_mul_f32 v[86:87], v[86:87], v[50:51] op_sel:[0,0] op_sel_hi:[1,0]\n"
"v_pk_mul_f32 v[88:89], v[88:89], v[50:51] op_sel:[0,0] op_sel_hi:[1,0]\n"
"v_pk_mul_f32 v[90:91], v[90:91], v[50:51] op_sel:[0,0] op_sel_hi:[1,0]\n"
"v_cvt_pk_bf16_f32 v80, v84, v85\n"
"v_cvt_pk_bf16_f32 v81, v86, v87\n"
"v_cvt_pk_bf16_f32 v82, v88, v89\n"
"v_cvt_pk_bf16_f32 v83, v90, v91\n"
"buffer_store_dwordx4 v[80:83], v1, s[52:55], s68 offen offset:1024\n"
"v_accvgpr_read_b32 v100, a104\n"
"v_accvgpr_read_b32 v101, a105\n"
"v_accvgpr_read_b32 v102, a106\n"
"v_accvgpr_read_b32 v103, a107\n"
"v_accvgpr_read_b32 v104, a120\n"
"v_accvgpr_read_b32 v105, a121\n"
"v_accvgpr_read_b32 v106, a122\n"
"v_accvgpr_read_b32 v107, a123\n"
"v_pk_mul_f32 v[100:101], v[100:101], v[50:51] op_sel:[0,1] op_sel_hi:[1,1]\n"
"v_pk_mul_f32 v[102:103], v[102:103], v[50:51] op_sel:[0,1] op_sel_hi:[1,1]\n"
"v_pk_mul_f32 v[104:105], v[104:105], v[50:51] op_sel:[0,1] op_sel_hi:[1,1]\n"
"v_pk_mul_f32 v[106:107], v[106:107], v[50:51] op_sel:[0,1] op_sel_hi:[1,1]\n"
"v_cvt_pk_bf16_f32 v96, v100, v101\n"
"v_cvt_pk_bf16_f32 v97, v102, v103\n"
"v_cvt_pk_bf16_f32 v98, v104, v105\n"
"v_cvt_pk_bf16_f32 v99, v106, v107\n"
"buffer_store_dwordx4 v[96:99], v1, s[52:55], s68 offen offset:2048\n"
"v_accvgpr_read_b32 v116, a108\n"
"v_accvgpr_read_b32 v117, a109\n"
"v_accvgpr_read_b32 v118, a110\n"
"v_accvgpr_read_b32 v119, a111\n"
"v_accvgpr_read_b32 v120, a124\n"
"v_accvgpr_read_b32 v121, a125\n"
"v_accvgpr_read_b32 v122, a126\n"
"v_accvgpr_read_b32 v123, a127\n"
"v_pk_mul_f32 v[116:117], v[116:117], v[52:53] op_sel:[0,0] op_sel_hi:[1,0]\n"
"v_pk_mul_f32 v[118:119], v[118:119], v[52:53] op_sel:[0,0] op_sel_hi:[1,0]\n"
"v_pk_mul_f32 v[120:121], v[120:121], v[52:53] op_sel:[0,0] op_sel_hi:[1,0]\n"
"v_pk_mul_f32 v[122:123], v[122:123], v[52:53] op_sel:[0,0] op_sel_hi:[1,0]\n"
"v_cvt_pk_bf16_f32 v112, v116, v117\n"
"v_cvt_pk_bf16_f32 v113, v118, v119\n"
"v_cvt_pk_bf16_f32 v114, v120, v121\n"
"v_cvt_pk_bf16_f32 v115, v122, v123\n"
"buffer_store_dwordx4 v[112:115], v1, s[52:55], s68 offen offset:3072\n"
"s_add_u32 s68, s68, 122880\n"
"v_accvgpr_read_b32 v68, a128\n"
"v_accvgpr_read_b32 v69, a129\n"
"v_accvgpr_read_b32 v70, a130\n"
"v_accvgpr_read_b32 v71, a131\n"
"v_accvgpr_read_b32 v72, a144\n"
"v_accvgpr_read_b32 v73, a145\n"
"v_accvgpr_read_b32 v74, a146\n"
"v_accvgpr_read_b32 v75, a147\n"
"v_pk_mul_f32 v[68:69], v[68:69], v[48:49] op_sel:[0,1] op_sel_hi:[1,1]\n"
"v_pk_mul_f32 v[70:71], v[70:71], v[48:49] op_sel:[0,1] op_sel_hi:[1,1]\n"
"v_pk_mul_f32 v[72:73], v[72:73], v[48:49] op_sel:[0,1] op_sel_hi:[1,1]\n"
"v_pk_mul_f32 v[74:75], v[74:75], v[48:49] op_sel:[0,1] op_sel_hi:[1,1]\n"
"v_cvt_pk_bf16_f32 v64, v68, v69\n"
"v_cvt_pk_bf16_f32 v65, v70, v71\n"
"v_cvt_pk_bf16_f32 v66, v72, v73\n"
"v_cvt_pk_bf16_f32 v67, v74, v75\n"
"buffer_store_dwordx4 v[64:67], v1, s[52:55], s68 offen offset:0\n"
"v_accvgpr_read_b32 v84, a132\n"
"v_accvgpr_read_b32 v85, a133\n"
"v_accvgpr_read_b32 v86, a134\n"
"v_accvgpr_read_b32 v87, a135\n"
"v_accvgpr_read_b32 v88, a148\n"
"v_accvgpr_read_b32 v89, a149\n"
"v_accvgpr_read_b32 v90, a150\n"
"v_accvgpr_read_b32 v91, a151\n"
"v_pk_mul_f32 v[84:85], v[84:85], v[50:51] op_sel:[0,0] op_sel_hi:[1,0]\n"
"v_pk_mul_f32 v[86:87], v[86:87], v[50:51] op_sel:[0,0] op_sel_hi:[1,0]\n"
"v_pk_mul_f32 v[88:89], v[88:89], v[50:51] op_sel:[0,0] op_sel_hi:[1,0]\n"
"v_pk_mul_f32 v[90:91], v[90:91], v[50:51] op_sel:[0,0] op_sel_hi:[1,0]\n"
"v_cvt_pk_bf16_f32 v80, v84, v85\n"
"v_cvt_pk_bf16_f32 v81, v86, v87\n"
"v_cvt_pk_bf16_f32 v82, v88, v89\n"
"v_cvt_pk_bf16_f32 v83, v90, v91\n"
"buffer_store_dwordx4 v[80:83], v1, s[52:55], s68 offen offset:1024\n"
"v_accvgpr_read_b32 v100, a136\n"
"v_accvgpr_read_b32 v101, a137\n"
"v_accvgpr_read_b32 v102, a138\n"
"v_accvgpr_read_b32 v103, a139\n"
"v_accvgpr_read_b32 v104, a152\n"
"v_accvgpr_read_b32 v105, a153\n"
"v_accvgpr_read_b32 v106, a154\n"
"v_accvgpr_read_b32 v107, a155\n"
"v_pk_mul_f32 v[100:101], v[100:101], v[50:51] op_sel:[0,1] op_sel_hi:[1,1]\n"
"v_pk_mul_f32 v[102:103], v[102:103], v[50:51] op_sel:[0,1] op_sel_hi:[1,1]\n"
"v_pk_mul_f32 v[104:105], v[104:105], v[50:51] op_sel:[0,1] op_sel_hi:[1,1]\n"
"v_pk_mul_f32 v[106:107], v[106:107], v[50:51] op_sel:[0,1] op_sel_hi:[1,1]\n"
"v_cvt_pk_bf16_f32 v96, v100, v101\n"
"v_cvt_pk_bf16_f32 v97, v102, v103\n"
"v_cvt_pk_bf16_f32 v98, v104, v105\n"
"v_cvt_pk_bf16_f32 v99, v106, v107\n"
"buffer_store_dwordx4 v[96:99], v1, s[52:55], s68 offen offset:2048\n"
"v_accvgpr_read_b32 v116, a140\n"
"v_accvgpr_read_b32 v117, a141\n"
"v_accvgpr_read_b32 v118, a142\n"
"v_accvgpr_read_b32 v119, a143\n"
"v_accvgpr_read_b32 v120, a156\n"
"v_accvgpr_read_b32 v121, a157\n"
"v_accvgpr_read_b32 v122, a158\n"
"v_accvgpr_read_b32 v123, a159\n"
"v_pk_mul_f32 v[116:117], v[116:117], v[52:53] op_sel:[0,0] op_sel_hi:[1,0]\n"
"v_pk_mul_f32 v[118:119], v[118:119], v[52:53] op_sel:[0,0] op_sel_hi:[1,0]\n"
"v_pk_mul_f32 v[120:121], v[120:121], v[52:53] op_sel:[0,0] op_sel_hi:[1,0]\n"
"v_pk_mul_f32 v[122:123], v[122:123], v[52:53] op_sel:[0,0] op_sel_hi:[1,0]\n"
"v_cvt_pk_bf16_f32 v112, v116, v117\n"
"v_cvt_pk_bf16_f32 v113, v118, v119\n"
"v_cvt_pk_bf16_f32 v114, v120, v121\n"
"v_cvt_pk_bf16_f32 v115, v122, v123\n"
"buffer_store_dwordx4 v[112:115], v1, s[52:55], s68 offen offset:3072\n"
"s_add_u32 s68, s68, 122880\n"
"v_accvgpr_read_b32 v68, a160\n"
"v_accvgpr_read_b32 v69, a161\n"
"v_accvgpr_read_b32 v70, a162\n"
"v_accvgpr_read_b32 v71, a163\n"
"v_accvgpr_read_b32 v72, a176\n"
"v_accvgpr_read_b32 v73, a177\n"
"v_accvgpr_read_b32 v74, a178\n"
"v_accvgpr_read_b32 v75, a179\n"
"v_pk_mul_f32 v[68:69], v[68:69], v[48:49] op_sel:[0,1] op_sel_hi:[1,1]\n"
"v_pk_mul_f32 v[70:71], v[70:71], v[48:49] op_sel:[0,1] op_sel_hi:[1,1]\n"
"v_pk_mul_f32 v[72:73], v[72:73], v[48:49] op_sel:[0,1] op_sel_hi:[1,1]\n"
"v_pk_mul_f32 v[74:75], v[74:75], v[48:49] op_sel:[0,1] op_sel_hi:[1,1]\n"
"v_cvt_pk_bf16_f32 v64, v68, v69\n"
"v_cvt_pk_bf16_f32 v65, v70, v71\n"
"v_cvt_pk_bf16_f32 v66, v72, v73\n"
"v_cvt_pk_bf16_f32 v67, v74, v75\n"
"buffer_store_dwordx4 v[64:67], v1, s[52:55], s68 offen offset:0\n"
"v_accvgpr_read_b32 v84, a164\n"
"v_accvgpr_read_b32 v85, a165\n"
"v_accvgpr_read_b32 v86, a166\n"
"v_accvgpr_read_b32 v87, a167\n"
"v_accvgpr_read_b32 v88, a180\n"
"v_accvgpr_read_b32 v89, a181\n"
"v_accvgpr_read_b32 v90, a182\n"
"v_accvgpr_read_b32 v91, a183\n"
"v_pk_mul_f32 v[84:85], v[84:85], v[50:51] op_sel:[0,0] op_sel_hi:[1,0]\n"
"v_pk_mul_f32 v[86:87], v[86:87], v[50:51] op_sel:[0,0] op_sel_hi:[1,0]\n"
"v_pk_mul_f32 v[88:89], v[88:89], v[50:51] op_sel:[0,0] op_sel_hi:[1,0]\n"
"v_pk_mul_f32 v[90:91], v[90:91], v[50:51] op_sel:[0,0] op_sel_hi:[1,0]\n"
"v_cvt_pk_bf16_f32 v80, v84, v85\n"
"v_cvt_pk_bf16_f32 v81, v86, v87\n"
"v_cvt_pk_bf16_f32 v82, v88, v89\n"
"v_cvt_pk_bf16_f32 v83, v90, v91\n"
"buffer_store_dwordx4 v[80:83], v1, s[52:55], s68 offen offset:1024\n"
"v_accvgpr_read_b32 v100, a168\n"
"v_accvgpr_read_b32 v101, a169\n"
"v_accvgpr_read_b32 v102, a170\n"
"v_accvgpr_read_b32 v103, a171\n"
"v_accvgpr_read_b32 v104, a184\n"
"v_accvgpr_read_b32 v105, a185\n"
"v_accvgpr_read_b32 v106, a186\n"
"v_accvgpr_read_b32 v107, a187\n"
"v_pk_mul_f32 v[100:101], v[100:101], v[50:51] op_sel:[0,1] op_sel_hi:[1,1]\n"
"v_pk_mul_f32 v[102:103], v[102:103], v[50:51] op_sel:[0,1] op_sel_hi:[1,1]\n"
"v_pk_mul_f32 v[104:105], v[104:105], v[50:51] op_sel:[0,1] op_sel_hi:[1,1]\n"
"v_pk_mul_f32 v[106:107], v[106:107], v[50:51] op_sel:[0,1] op_sel_hi:[1,1]\n"
"v_cvt_pk_bf16_f32 v96, v100, v101\n"
"v_cvt_pk_bf16_f32 v97, v102, v103\n"
"v_cvt_pk_bf16_f32 v98, v104, v105\n"
"v_cvt_pk_bf16_f32 v99, v106, v107\n"
"buffer_store_dwordx4 v[96:99], v1, s[52:55], s68 offen offset:2048\n"
"v_accvgpr_read_b32 v116, a172\n"
"v_accvgpr_read_b32 v117, a173\n"
"v_accvgpr_read_b32 v118, a174\n"
"v_accvgpr_read_b32 v119, a175\n"
"v_accvgpr_read_b32 v120, a188\n"
"v_accvgpr_read_b32 v121, a189\n"
"v_accvgpr_read_b32 v122, a190\n"
"v_accvgpr_read_b32 v123, a191\n"
"v_pk_mul_f32 v[116:117], v[116:117], v[52:53] op_sel:[0,0] op_sel_hi:[1,0]\n"
"v_pk_mul_f32 v[118:119], v[118:119], v[52:53] op_sel:[0,0] op_sel_hi:[1,0]\n"
"v_pk_mul_f32 v[120:121], v[120:121], v[52:53] op_sel:[0,0] op_sel_hi:[1,0]\n"
"v_pk_mul_f32 v[122:123], v[122:123], v[52:53] op_sel:[0,0] op_sel_hi:[1,0]\n"
"v_cvt_pk_bf16_f32 v112, v116, v117\n"
"v_cvt_pk_bf16_f32 v113, v118, v119\n"
"v_cvt_pk_bf16_f32 v114, v120, v121\n"
"v_cvt_pk_bf16_f32 v115, v122, v123\n"
"buffer_store_dwordx4 v[112:115], v1, s[52:55], s68 offen offset:3072\n"
"s_branch K1_END\n"
"K1A_RS0:\n"
"s_mov_b32 s65, 0\n"
"s_branch K1A_RSX1\n"
"K1A_RO0:\n"
"s_mov_b32 s65, 0\n"
"s_branch K1A_ROX\n"
"K1A_RS1:\n"
"s_mov_b32 s65, 1\n"
"s_branch K1A_RSX0\n"
"K1A_RO1:\n"
"s_mov_b32 s65, 1\n"
"s_branch K1A_ROX\n"
"K1A_RS2:\n"
"s_mov_b32 s65, 2\n"
"s_branch K1A_RSX1\n"
"K1A_RO2:\n"
"s_mov_b32 s65, 2\n"
"s_branch K1A_ROX\n"
"K1A_RS3:\n"
"s_mov_b32 s65, 3\n"
"s_branch K1A_RSX0\n"
"K1A_RO3:\n"
"s_mov_b32 s65, 3\n"
"s_branch K1A_ROX\n"
"K1A_RS4:\n"
"s_mov_b32 s65, 4\n"
"s_branch K1A_RSX1\n"
"K1A_RO4:\n"
"s_mov_b32 s65, 4\n"
"s_branch K1A_ROX\n"
"K1A_RS5:\n"
"s_mov_b32 s65, 5\n"
"s_branch K1A_RSX0\n"
"K1A_RO5:\n"
"s_mov_b32 s65, 5\n"
"s_branch K1A_ROX\n"
"K1A_RS6:\n"
"s_mov_b32 s65, 6\n"
"s_branch K1A_RSX1\n"
"K1A_RO6:\n"
"s_mov_b32 s65, 6\n"
"s_branch K1A_ROX\n"
"K1A_RSX0:\n"
"v_max3_f32 v55, v64, v65, v66\n"
"v_max3_f32 v55, v55, v67, v68\n"
"v_max3_f32 v55, v55, v69, v70\n"
"v_max_f32 v55, v55, v71\n"
"v_mov_b32 v59, v55\n"
"s_nop 1\n"
"v_permlane32_swap_b32 v59, v55\n"
"s_nop 0\n"
"v_max_f32 v55, v55, v59\n"
"v_mov_b32 v59, v55\n"
"s_nop 1\n"
"v_permlane16_swap_b32 v59, v55\n"
"s_nop 0\n"
"v_max_f32 v55, v55, v59\n"
"v_max_f32 v55, 0, v55\n"
"v_sub_f32 v64, v64, v55\n"
"v_sub_f32 v65, v65, v55\n"
"v_sub_f32 v66, v66, v55\n"
"v_sub_f32 v67, v67, v55\n"
"v_sub_f32 v68, v68, v55\n"
"v_sub_f32 v69, v69, v55\n"
"v_sub_f32 v70, v70, v55\n"
"v_sub_f32 v71, v71, v55\n"
"v_sub_f32 v144, v144, v55\n"
"v_sub_f32 v145, v145, v55\n"
"v_sub_f32 v146, v146, v55\n"
"v_sub_f32 v147, v147, v55\n"
"v_sub_f32 v55, 0, v55\n"
"v_exp_f32 v49, v55\n"
"s_nop 0\n"
"v_mul_f32 v37, v37, v49\n"
"v_mul_f32 v38, v38, v49\n"
"v_max3_f32 v56, v72, v73, v74\n"
"v_max3_f32 v56, v56, v75, v76\n"
"v_max3_f32 v56, v56, v77, v78\n"
"v_max_f32 v56, v56, v79\n"
"v_mov_b32 v59, v56\n"
"s_nop 1\n"
"v_permlane32_swap_b32 v59, v56\n"
"s_nop 0\n"
"v_max_f32 v56, v56, v59\n"
"v_mov_b32 v59, v56\n"
"s_nop 1\n"
"v_permlane16_swap_b32 v59, v56\n"
"s_nop 0\n"
"v_max_f32 v56, v56, v59\n"
"v_max_f32 v56, 0, v56\n"
"v_sub_f32 v72, v72, v56\n"
"v_sub_f32 v73, v73, v56\n"
"v_sub_f32 v74, v74, v56\n"
"v_sub_f32 v75, v75, v56\n"
"v_sub_f32 v76, v76, v56\n"
"v_sub_f32 v77, v77, v56\n"
"v_sub_f32 v78, v78, v56\n"
"v_sub_f32 v79, v79, v56\n"
"v_sub_f32 v148, v148, v56\n"
"v_sub_f32 v149, v149, v56\n"
"v_sub_f32 v150, v150, v56\n"
"v_sub_f32 v151, v151, v56\n"
"v_sub_f32 v56, 0, v56\n"
"v_exp_f32 v50, v56\n"
"s_nop 0\n"
"v_mul_f32 v39, v39, v50\n"
"v_mul_f32 v40, v40, v50\n"
"v_max3_f32 v57, v80, v81, v82\n"
"v_max3_f32 v57, v57, v83, v84\n"
"v_max3_f32 v57, v57, v85, v86\n"
"v_max_f32 v57, v57, v87\n"
"v_mov_b32 v59, v57\n"
"s_nop 1\n"
"v_permlane32_swap_b32 v59, v57\n"
"s_nop 0\n"
"v_max_f32 v57, v57, v59\n"
"v_mov_b32 v59, v57\n"
"s_nop 1\n"
"v_permlane16_swap_b32 v59, v57\n"
"s_nop 0\n"
"v_max_f32 v57, v57, v59\n"
"v_max_f32 v57, 0, v57\n"
"v_sub_f32 v80, v80, v57\n"
"v_sub_f32 v81, v81, v57\n"
"v_sub_f32 v82, v82, v57\n"
"v_sub_f32 v83, v83, v57\n"
"v_sub_f32 v84, v84, v57\n"
"v_sub_f32 v85, v85, v57\n"
"v_sub_f32 v86, v86, v57\n"
"v_sub_f32 v87, v87, v57\n"
"v_sub_f32 v152, v152, v57\n"
"v_sub_f32 v153, v153, v57\n"
"v_sub_f32 v154, v154, v57\n"
"v_sub_f32 v155, v155, v57\n"
"v_sub_f32 v57, 0, v57\n"
"v_exp_f32 v51, v57\n"
"s_nop 0\n"
"v_mul_f32 v41, v41, v51\n"
"v_mul_f32 v42, v42, v51\n"
"v_max3_f32 v58, v88, v89, v90\n"
"v_max3_f32 v58, v58, v91, v92\n"
"v_max3_f32 v58, v58, v93, v94\n"
"v_max_f32 v58, v58, v95\n"
"v_mov_b32 v59, v58\n"
"s_nop 1\n"
"v_permlane32_swap_b32 v59, v58\n"
"s_nop 0\n"
"v_max_f32 v58, v58, v59\n"
"v_mov_b32 v59, v58\n"
"s_nop 1\n"
"v_permlane16_swap_b32 v59, v58\n"
"s_nop 0\n"
"v_max_f32 v58, v58, v59\n"
"v_max_f32 v58, 0, v58\n"
"v_sub_f32 v88, v88, v58\n"
"v_sub_f32 v89, v89, v58\n"
"v_sub_f32 v90, v90, v58\n"
"v_sub_f32 v91, v91, v58\n"
"v_sub_f32 v92, v92, v58\n"
"v_sub_f32 v93, v93, v58\n"
"v_sub_f32 v94, v94, v58\n"
"v_sub_f32 v95, v95, v58\n"
"v_sub_f32 v156, v156, v58\n"
"v_sub_f32 v157, v157, v58\n"
"v_sub_f32 v158, v158, v58\n"
"v_sub_f32 v159, v159, v58\n"
"v_sub_f32 v58, 0, v58\n"
"v_exp_f32 v52, v58\n"
"s_nop 0\n"
"v_mul_f32 v43, v43, v52\n"
"v_mul_f32 v44, v44, v52\n"
"s_mov_b64 vcc, -1\n"
"s_nop 1\n"
"s_cmp_eq_u32 s65, 1\n"
"s_cbranch_scc1 K1A_RSb1\n"
"s_cmp_eq_u32 s65, 3\n"
"s_cbranch_scc1 K1A_RSb3\n"
"s_branch K1A_RSb5\n"
"K1A_RSX1:\n"
"v_max3_f32 v55, v96, v97, v98\n"
"v_max3_f32 v55, v55, v99, v100\n"
"v_max3_f32 v55, v55, v101, v102\n"
"v_max_f32 v55, v55, v103\n"
"v_mov_b32 v59, v55\n"
"s_nop 1\n"
"v_permlane32_swap_b32 v59, v55\n"
"s_nop 0\n"
"v_max_f32 v55, v55, v59\n"
"v_mov_b32 v59, v55\n"
"s_nop 1\n"
"v_permlane16_swap_b32 v59, v55\n"
"s_nop 0\n"
"v_max_f32 v55, v55, v59\n"
"v_max_f32 v55, 0, v55\n"
"v_sub_f32 v96, v96, v55\n"
"v_sub_f32 v97, v97, v55\n"
"v_sub_f32 v98, v98, v55\n"
"v_sub_f32 v99, v99, v55\n"
"v_sub_f32 v100, v100, v55\n"
"v_sub_f32 v101, v101, v55\n"
"v_sub_f32 v102, v102, v55\n"
"v_sub_f32 v103, v103, v55\n"
"v_sub_f32 v144, v144, v55\n"
"v_sub_f32 v145, v145, v55\n"
"v_sub_f32 v146, v146, v55\n"
"v_sub_f32 v147, v147, v55\n"
"v_sub_f32 v55, 0, v55\n"
"v_exp_f32 v49, v55\n"
"s_nop 0\n"
"v_mul_f32 v37, v37, v49\n"
"v_mul_f32 v38, v38, v49\n"
"v_max3_f32 v56, v104, v105, v106\n"
"v_max3_f32 v56, v56, v107, v108\n"
"v_max3_f32 v56, v56, v109, v110\n"
"v_max_f32 v56, v56, v111\n"
"v_mov_b32 v59, v56\n"
"s_nop 1\n"
"v_permlane32_swap_b32 v59, v56\n"
"s_nop 0\n"
"v_max_f32 v56, v56, v59\n"
"v_mov_b32 v59, v56\n"
"s_nop 1\n"
"v_permlane16_swap_b32 v59, v56\n"
"s_nop 0\n"
"v_max_f32 v56, v56, v59\n"
"v_max_f32 v56, 0, v56\n"
"v_sub_f32 v104, v104, v56\n"
"v_sub_f32 v105, v105, v56\n"
"v_sub_f32 v106, v106, v56\n"
"v_sub_f32 v107, v107, v56\n"
"v_sub_f32 v108, v108, v56\n"
"v_sub_f32 v109, v109, v56\n"
"v_sub_f32 v110, v110, v56\n"
"v_sub_f32 v111, v111, v56\n"
"v_sub_f32 v148, v148, v56\n"
"v_sub_f32 v149, v149, v56\n"
"v_sub_f32 v150, v150, v56\n"
"v_sub_f32 v151, v151, v56\n"
"v_sub_f32 v56, 0, v56\n"
"v_exp_f32 v50, v56\n"
"s_nop 0\n"
"v_mul_f32 v39, v39, v50\n"
"v_mul_f32 v40, v40, v50\n"
"v_max3_f32 v57, v112, v113, v114\n"
"v_max3_f32 v57, v57, v115, v116\n"
"v_max3_f32 v57, v57, v117, v118\n"
"v_max_f32 v57, v57, v119\n"
"v_mov_b32 v59, v57\n"
"s_nop 1\n"
"v_permlane32_swap_b32 v59, v57\n"
"s_nop 0\n"
"v_max_f32 v57, v57, v59\n"
"v_mov_b32 v59, v57\n"
"s_nop 1\n"
"v_permlane16_swap_b32 v59, v57\n"
"s_nop 0\n"
"v_max_f32 v57, v57, v59\n"
"v_max_f32 v57, 0, v57\n"
"v_sub_f32 v112, v112, v57\n"
"v_sub_f32 v113, v113, v57\n"
"v_sub_f32 v114, v114, v57\n"
"v_sub_f32 v115, v115, v57\n"
"v_sub_f32 v116, v116, v57\n"
"v_sub_f32 v117, v117, v57\n"
"v_sub_f32 v118, v118, v57\n"
"v_sub_f32 v119, v119, v57\n"
"v_sub_f32 v152, v152, v57\n"
"v_sub_f32 v153, v153, v57\n"
"v_sub_f32 v154, v154, v57\n"
"v_sub_f32 v155, v155, v57\n"
"v_sub_f32 v57, 0, v57\n"
"v_exp_f32 v51, v57\n"
"s_nop 0\n"
"v_mul_f32 v41, v41, v51\n"
"v_mul_f32 v42, v42, v51\n"
"v_max3_f32 v58, v120, v121, v122\n"
"v_max3_f32 v58, v58, v123, v124\n"
"v_max3_f32 v58, v58, v125, v126\n"
"v_max_f32 v58, v58, v127\n"
"v_mov_b32 v59, v58\n"
"s_nop 1\n"
"v_permlane32_swap_b32 v59, v58\n"
"s_nop 0\n"
"v_max_f32 v58, v58, v59\n"
"v_mov_b32 v59, v58\n"
"s_nop 1\n"
"v_permlane16_swap_b32 v59, v58\n"
"s_nop 0\n"
"v_max_f32 v58, v58, v59\n"
"v_max_f32 v58, 0, v58\n"
"v_sub_f32 v120, v120, v58\n"
"v_sub_f32 v121, v121, v58\n"
"v_sub_f32 v122, v122, v58\n"
"v_sub_f32 v123, v123, v58\n"
"v_sub_f32 v124, v124, v58\n"
"v_sub_f32 v125, v125, v58\n"
"v_sub_f32 v126, v126, v58\n"
"v_sub_f32 v127, v127, v58\n"
"v_sub_f32 v156, v156, v58\n"
"v_sub_f32 v157, v157, v58\n"
"v_sub_f32 v158, v158, v58\n"
"v_sub_f32 v159, v159, v58\n"
"v_sub_f32 v58, 0, v58\n"
"v_exp_f32 v52, v58\n"
"s_nop 0\n"
"v_mul_f32 v43, v43, v52\n"
"v_mul_f32 v44, v44, v52\n"
"s_mov_b64 vcc, -1\n"
"s_nop 1\n"
"s_cmp_eq_u32 s65, 0\n"
"s_cbranch_scc1 K1A_RSb0\n"
"s_cmp_eq_u32 s65, 2\n"
"s_cbranch_scc1 K1A_RSb2\n"
"s_cmp_eq_u32 s65, 4\n"
"s_cbranch_scc1 K1A_RSb4\n"
"s_branch K1A_RSb6\n"
"K1A_ROX:\n"
"s_nop 7\n"
"v_accvgpr_read_b32 v55, a0\n"
"v_accvgpr_read_b32 v56, a1\n"
"v_accvgpr_read_b32 v57, a2\n"
"v_accvgpr_read_b32 v58, a3\n"
"v_mul_f32 v55, v55, v49\n"
"v_mul_f32 v56, v56, v49\n"
"v_mul_f32 v57, v57, v49\n"
"v_mul_f32 v58, v58, v49\n"
"v_accvgpr_write_b32 a0, v55\n"
"v_accvgpr_write_b32 a1, v56\n"
"v_accvgpr_write_b32 a2, v57\n"
"v_accvgpr_write_b32 a3, v58\n"
"v_accvgpr_read_b32 v55, a4\n"
"v_accvgpr_read_b32 v56, a5\n"
"v_accvgpr_read_b32 v57, a6\n"
"v_accvgpr_read_b32 v58, a7\n"
"v_mul_f32 v55, v55, v50\n"
"v_mul_f32 v56, v56, v50\n"
"v_mul_f32 v57, v57, v50\n"
"v_mul_f32 v58, v58, v50\n"
"v_accvgpr_write_b32 a4, v55\n"
"v_accvgpr_write_b32 a5, v56\n"
"v_accvgpr_write_b32 a6, v57\n"
"v_accvgpr_write_b32 a7, v58\n"
"v_accvgpr_read_b32 v55, a8\n"
"v_accvgpr_read_b32 v56, a9\n"
"v_accvgpr_read_b32 v57, a10\n"
"v_accvgpr_read_b32 v58, a11\n"
"v_mul_f32 v55, v55, v51\n"
"v_mul_f32 v56, v56, v51\n"
"v_mul_f32 v57, v57, v51\n"
"v_mul_f32 v58, v58, v51\n"
"v_accvgpr_write_b32 a8, v55\n"
"v_accvgpr_write_b32 a9, v56\n"
"v_accvgpr_write_b32 a10, v57\n"
"v_accvgpr_write_b32 a11, v58\n"
"v_accvgpr_read_b32 v55, a12\n"
"v_accvgpr_read_b32 v56, a13\n"
"v_accvgpr_read_b32 v57, a14\n"
"v_accvgpr_read_b32 v58, a15\n"
"v_mul_f32 v55, v55, v52\n"
"v_mul_f32 v56, v56, v52\n"
"v_mul_f32 v57, v57, v52\n"
"v_mul_f32 v58, v58, v52\n"
"v_accvgpr_write_b32 a12, v55\n"
"v_accvgpr_write_b32 a13, v56\n"
"v_accvgpr_write_b32 a14, v57\n"
"v_accvgpr_write_b32 a15, v58\n"
"v_accvgpr_read_b32 v55, a16\n"
"v_accvgpr_read_b32 v56, a17\n"
"v_accvgpr_read_b32 v57, a18\n"
"v_accvgpr_read_b32 v58, a19\n"
"v_mul_f32 v55, v55, v49\n"
"v_mul_f32 v56, v56, v49\n"
"v_mul_f32 v57, v57, v49\n"
"v_mul_f32 v58, v58, v49\n"
"v_accvgpr_write_b32 a16, v55\n"
"v_accvgpr_write_b32 a17, v56\n"
"v_accvgpr_write_b32 a18, v57\n"
"v_accvgpr_write_b32 a19, v58\n"
"v_accvgpr_read_b32 v55, a20\n"
"v_accvgpr_read_b32 v56, a21\n"
"v_accvgpr_read_b32 v57, a22\n"
"v_accvgpr_read_b32 v58, a23\n"
"v_mul_f32 v55, v55, v50\n"
"v_mul_f32 v56, v56, v50\n"
"v_mul_f32 v57, v57, v50\n"
"v_mul_f32 v58, v58, v50\n"
"v_accvgpr_write_b32 a20, v55\n"
"v_accvgpr_write_b32 a21, v56\n"
"v_accvgpr_write_b32 a22, v57\n"
"v_accvgpr_write_b32 a23, v58\n"
"v_accvgpr_read_b32 v55, a24\n"
"v_accvgpr_read_b32 v56, a25\n"
"v_accvgpr_read_b32 v57, a26\n"
"v_accvgpr_read_b32 v58, a27\n"
"v_mul_f32 v55, v55, v51\n"
"v_mul_f32 v56, v56, v51\n"
"v_mul_f32 v57, v57, v51\n"
"v_mul_f32 v58, v58, v51\n"
"v_accvgpr_write_b32 a24, v55\n"
"v_accvgpr_write_b32 a25, v56\n"
"v_accvgpr_write_b32 a26, v57\n"
"v_accvgpr_write_b32 a27, v58\n"
"v_accvgpr_read_b32 v55, a28\n"
"v_accvgpr_read_b32 v56, a29\n"
"v_accvgpr_read_b32 v57, a30\n"
"v_accvgpr_read_b32 v58, a31\n"
"v_mul_f32 v55, v55, v52\n"
"v_mul_f32 v56, v56, v52\n"
"v_mul_f32 v57, v57, v52\n"
"v_mul_f32 v58, v58, v52\n"
"v_accvgpr_write_b32 a28, v55\n"
"v_accvgpr_write_b32 a29, v56\n"
"v_accvgpr_write_b32 a30, v57\n"
"v_accvgpr_write_b32 a31, v58\n"
"v_accvgpr_read_b32 v55, a32\n"
"v_accvgpr_read_b32 v56, a33\n"
"v_accvgpr_read_b32 v57, a34\n"
"v_accvgpr_read_b32 v58, a35\n"
"v_mul_f32 v55, v55, v49\n"
"v_mul_f32 v56, v56, v49\n"
"v_mul_f32 v57, v57, v49\n"
"v_mul_f32 v58, v58, v49\n"
"v_accvgpr_write_b32 a32, v55\n"
"v_accvgpr_write_b32 a33, v56\n"
"v_accvgpr_write_b32 a34, v57\n"
"v_accvgpr_write_b32 a35, v58\n"
"v_accvgpr_read_b32 v55, a36\n"
"v_accvgpr_read_b32 v56, a37\n"
"v_accvgpr_read_b32 v57, a38\n"
"v_accvgpr_read_b32 v58, a39\n"
"v_mul_f32 v55, v55, v50\n"
"v_mul_f32 v56, v56, v50\n"
"v_mul_f32 v57, v57, v50\n"
"v_mul_f32 v58, v58, v50\n"
"v_accvgpr_write_b32 a36, v55\n"
"v_accvgpr_write_b32 a37, v56\n"
"v_accvgpr_write_b32 a38, v57\n"
"v_accvgpr_write_b32 a39, v58\n"
"v_accvgpr_read_b32 v55, a40\n"
"v_accvgpr_read_b32 v56, a41\n"
"v_accvgpr_read_b32 v57, a42\n"
"v_accvgpr_read_b32 v58, a43\n"
"v_mul_f32 v55, v55, v51\n"
"v_mul_f32 v56, v56, v51\n"
"v_mul_f32 v57, v57, v51\n"
"v_mul_f32 v58, v58, v51\n"
"v_accvgpr_write_b32 a40, v55\n"
"v_accvgpr_write_b32 a41, v56\n"
"v_accvgpr_write_b32 a42, v57\n"
"v_accvgpr_write_b32 a43, v58\n"
"v_accvgpr_read_b32 v55, a44\n"
"v_accvgpr_read_b32 v56, a45\n"
"v_accvgpr_read_b32 v57, a46\n"
"v_accvgpr_read_b32 v58, a47\n"
"v_mul_f32 v55, v55, v52\n"
"v_mul_f32 v56, v56, v52\n"
"v_mul_f32 v57, v57, v52\n"
"v_mul_f32 v58, v58, v52\n"
"v_accvgpr_write_b32 a44, v55\n"
"v_accvgpr_write_b32 a45, v56\n"
"v_accvgpr_write_b32 a46, v57\n"
"v_accvgpr_write_b32 a47, v58\n"
"v_accvgpr_read_b32 v55, a48\n"
"v_accvgpr_read_b32 v56, a49\n"
"v_accvgpr_read_b32 v57, a50\n"
"v_accvgpr_read_b32 v58, a51\n"
"v_mul_f32 v55, v55, v49\n"
"v_mul_f32 v56, v56, v49\n"
"v_mul_f32 v57, v57, v49\n"
"v_mul_f32 v58, v58, v49\n"
"v_accvgpr_write_b32 a48, v55\n"
"v_accvgpr_write_b32 a49, v56\n"
"v_accvgpr_write_b32 a50, v57\n"
"v_accvgpr_write_b32 a51, v58\n"
"v_accvgpr_read_b32 v55, a52\n"
"v_accvgpr_read_b32 v56, a53\n"
"v_accvgpr_read_b32 v57, a54\n"
"v_accvgpr_read_b32 v58, a55\n"
"v_mul_f32 v55, v55, v50\n"
"v_mul_f32 v56, v56, v50\n"
"v_mul_f32 v57, v57, v50\n"
"v_mul_f32 v58, v58, v50\n"
"v_accvgpr_write_b32 a52, v55\n"
"v_accvgpr_write_b32 a53, v56\n"
"v_accvgpr_write_b32 a54, v57\n"
"v_accvgpr_write_b32 a55, v58\n"
"v_accvgpr_read_b32 v55, a56\n"
"v_accvgpr_read_b32 v56, a57\n"
"v_accvgpr_read_b32 v57, a58\n"
"v_accvgpr_read_b32 v58, a59\n"
"v_mul_f32 v55, v55, v51\n"
"v_mul_f32 v56, v56, v51\n"
"v_mul_f32 v57, v57, v51\n"
"v_mul_f32 v58, v58, v51\n"
"v_accvgpr_write_b32 a56, v55\n"
"v_accvgpr_write_b32 a57, v56\n"
"v_accvgpr_write_b32 a58, v57\n"
"v_accvgpr_write_b32 a59, v58\n"
"v_accvgpr_read_b32 v55, a60\n"
"v_accvgpr_read_b32 v56, a61\n"
"v_accvgpr_read_b32 v57, a62\n"
"v_accvgpr_read_b32 v58, a63\n"
"v_mul_f32 v55, v55, v52\n"
"v_mul_f32 v56, v56, v52\n"
"v_mul_f32 v57, v57, v52\n"
"v_mul_f32 v58, v58, v52\n"
"v_accvgpr_write_b32 a60, v55\n"
"v_accvgpr_write_b32 a61, v56\n"
"v_accvgpr_write_b32 a62, v57\n"
"v_accvgpr_write_b32 a63, v58\n"
"v_accvgpr_read_b32 v55, a64\n"
"v_accvgpr_read_b32 v56, a65\n"
"v_accvgpr_read_b32 v57, a66\n"
"v_accvgpr_read_b32 v58, a67\n"
"v_mul_f32 v55, v55, v49\n"
"v_mul_f32 v56, v56, v49\n"
"v_mul_f32 v57, v57, v49\n"
"v_mul_f32 v58, v58, v49\n"
"v_accvgpr_write_b32 a64, v55\n"
"v_accvgpr_write_b32 a65, v56\n"
"v_accvgpr_write_b32 a66, v57\n"
"v_accvgpr_write_b32 a67, v58\n"
"v_accvgpr_read_b32 v55, a68\n"
"v_accvgpr_read_b32 v56, a69\n"
"v_accvgpr_read_b32 v57, a70\n"
"v_accvgpr_read_b32 v58, a71\n"
"v_mul_f32 v55, v55, v50\n"
"v_mul_f32 v56, v56, v50\n"
"v_mul_f32 v57, v57, v50\n"
"v_mul_f32 v58, v58, v50\n"
"v_accvgpr_write_b32 a68, v55\n"
"v_accvgpr_write_b32 a69, v56\n"
"v_accvgpr_write_b32 a70, v57\n"
"v_accvgpr_write_b32 a71, v58\n"
"v_accvgpr_read_b32 v55, a72\n"
"v_accvgpr_read_b32 v56, a73\n"
"v_accvgpr_read_b32 v57, a74\n"
"v_accvgpr_read_b32 v58, a75\n"
"v_mul_f32 v55, v55, v51\n"
"v_mul_f32 v56, v56, v51\n"
"v_mul_f32 v57, v57, v51\n"
"v_mul_f32 v58, v58, v51\n"
"v_accvgpr_write_b32 a72, v55\n"
"v_accvgpr_write_b32 a73, v56\n"
"v_accvgpr_write_b32 a74, v57\n"
"v_accvgpr_write_b32 a75, v58\n"
"v_accvgpr_read_b32 v55, a76\n"
"v_accvgpr_read_b32 v56, a77\n"
"v_accvgpr_read_b32 v57, a78\n"
"v_accvgpr_read_b32 v58, a79\n"
"v_mul_f32 v55, v55, v52\n"
"v_mul_f32 v56, v56, v52\n"
"v_mul_f32 v57, v57, v52\n"
"v_mul_f32 v58, v58, v52\n"
"v_accvgpr_write_b32 a76, v55\n"
"v_accvgpr_write_b32 a77, v56\n"
"v_accvgpr_write_b32 a78, v57\n"
"v_accvgpr_write_b32 a79, v58\n"
"v_accvgpr_read_b32 v55, a80\n"
"v_accvgpr_read_b32 v56, a81\n"
"v_accvgpr_read_b32 v57, a82\n"
"v_accvgpr_read_b32 v58, a83\n"
"v_mul_f32 v55, v55, v49\n"
"v_mul_f32 v56, v56, v49\n"
"v_mul_f32 v57, v57, v49\n"
"v_mul_f32 v58, v58, v49\n"
"v_accvgpr_write_b32 a80, v55\n"
"v_accvgpr_write_b32 a81, v56\n"
"v_accvgpr_write_b32 a82, v57\n"
"v_accvgpr_write_b32 a83, v58\n"
"v_accvgpr_read_b32 v55, a84\n"
"v_accvgpr_read_b32 v56, a85\n"
"v_accvgpr_read_b32 v57, a86\n"
"v_accvgpr_read_b32 v58, a87\n"
"v_mul_f32 v55, v55, v50\n"
"v_mul_f32 v56, v56, v50\n"
"v_mul_f32 v57, v57, v50\n"
"v_mul_f32 v58, v58, v50\n"
"v_accvgpr_write_b32 a84, v55\n"
"v_accvgpr_write_b32 a85, v56\n"
"v_accvgpr_write_b32 a86, v57\n"
"v_accvgpr_write_b32 a87, v58\n"
"v_accvgpr_read_b32 v55, a88\n"
"v_accvgpr_read_b32 v56, a89\n"
"v_accvgpr_read_b32 v57, a90\n"
"v_accvgpr_read_b32 v58, a91\n"
"v_mul_f32 v55, v55, v51\n"
"v_mul_f32 v56, v56, v51\n"
"v_mul_f32 v57, v57, v51\n"
"v_mul_f32 v58, v58, v51\n"
"v_accvgpr_write_b32 a88, v55\n"
"v_accvgpr_write_b32 a89, v56\n"
"v_accvgpr_write_b32 a90, v57\n"
"v_accvgpr_write_b32 a91, v58\n"
"v_accvgpr_read_b32 v55, a92\n"
"v_accvgpr_read_b32 v56, a93\n"
"v_accvgpr_read_b32 v57, a94\n"
"v_accvgpr_read_b32 v58, a95\n"
"v_mul_f32 v55, v55, v52\n"
"v_mul_f32 v56, v56, v52\n"
"v_mul_f32 v57, v57, v52\n"
"v_mul_f32 v58, v58, v52\n"
"v_accvgpr_write_b32 a92, v55\n"
"v_accvgpr_write_b32 a93, v56\n"
"v_accvgpr_write_b32 a94, v57\n"
"v_accvgpr_write_b32 a95, v58\n"
"v_accvgpr_read_b32 v55, a96\n"
"v_accvgpr_read_b32 v56, a97\n"
"v_accvgpr_read_b32 v57, a98\n"
"v_accvgpr_read_b32 v58, a99\n"
"v_mul_f32 v55, v55, v49\n"
"v_mul_f32 v56, v56, v49\n"
"v_mul_f32 v57, v57, v49\n"
"v_mul_f32 v58, v58, v49\n"
"v_accvgpr_write_b32 a96, v55\n"
"v_accvgpr_write_b32 a97, v56\n"
"v_accvgpr_write_b32 a98, v57\n"
"v_accvgpr_write_b32 a99, v58\n"
"v_accvgpr_read_b32 v55, a100\n"
"v_accvgpr_read_b32 v56, a101\n"
"v_accvgpr_read_b32 v57, a102\n"
"v_accvgpr_read_b32 v58, a103\n"
"v_mul_f32 v55, v55, v50\n"
"v_mul_f32 v56, v56, v50\n"
"v_mul_f32 v57, v57, v50\n"
"v_mul_f32 v58, v58, v50\n"
"v_accvgpr_write_b32 a100, v55\n"
"v_accvgpr_write_b32 a101, v56\n"
"v_accvgpr_write_b32 a102, v57\n"
"v_accvgpr_write_b32 a103, v58\n"
"v_accvgpr_read_b32 v55, a104\n"
"v_accvgpr_read_b32 v56, a105\n"
"v_accvgpr_read_b32 v57, a106\n"
"v_accvgpr_read_b32 v58, a107\n"
"v_mul_f32 v55, v55, v51\n"
"v_mul_f32 v56, v56, v51\n"
"v_mul_f32 v57, v57, v51\n"
"v_mul_f32 v58, v58, v51\n"
"v_accvgpr_write_b32 a104, v55\n"
"v_accvgpr_write_b32 a105, v56\n"
"v_accvgpr_write_b32 a106, v57\n"
"v_accvgpr_write_b32 a107, v58\n"
"v_accvgpr_read_b32 v55, a108\n"
"v_accvgpr_read_b32 v56, a109\n"
"v_accvgpr_read_b32 v57, a110\n"
"v_accvgpr_read_b32 v58, a111\n"
"v_mul_f32 v55, v55, v52\n"
"v_mul_f32 v56, v56, v52\n"
"v_mul_f32 v57, v57, v52\n"
"v_mul_f32 v58, v58, v52\n"
"v_accvgpr_write_b32 a108, v55\n"
"v_accvgpr_write_b32 a109, v56\n"
"v_accvgpr_write_b32 a110, v57\n"
"v_accvgpr_write_b32 a111, v58\n"
"v_accvgpr_read_b32 v55, a112\n"
"v_accvgpr_read_b32 v56, a113\n"
"v_accvgpr_read_b32 v57, a114\n"
"v_accvgpr_read_b32 v58, a115\n"
"v_mul_f32 v55, v55, v49\n"
"v_mul_f32 v56, v56, v49\n"
"v_mul_f32 v57, v57, v49\n"
"v_mul_f32 v58, v58, v49\n"
"v_accvgpr_write_b32 a112, v55\n"
"v_accvgpr_write_b32 a113, v56\n"
"v_accvgpr_write_b32 a114, v57\n"
"v_accvgpr_write_b32 a115, v58\n"
"v_accvgpr_read_b32 v55, a116\n"
"v_accvgpr_read_b32 v56, a117\n"
"v_accvgpr_read_b32 v57, a118\n"
"v_accvgpr_read_b32 v58, a119\n"
"v_mul_f32 v55, v55, v50\n"
"v_mul_f32 v56, v56, v50\n"
"v_mul_f32 v57, v57, v50\n"
"v_mul_f32 v58, v58, v50\n"
"v_accvgpr_write_b32 a116, v55\n"
"v_accvgpr_write_b32 a117, v56\n"
"v_accvgpr_write_b32 a118, v57\n"
"v_accvgpr_write_b32 a119, v58\n"
"v_accvgpr_read_b32 v55, a120\n"
"v_accvgpr_read_b32 v56, a121\n"
"v_accvgpr_read_b32 v57, a122\n"
"v_accvgpr_read_b32 v58, a123\n"
"v_mul_f32 v55, v55, v51\n"
"v_mul_f32 v56, v56, v51\n"
"v_mul_f32 v57, v57, v51\n"
"v_mul_f32 v58, v58, v51\n"
"v_accvgpr_write_b32 a120, v55\n"
"v_accvgpr_write_b32 a121, v56\n"
"v_accvgpr_write_b32 a122, v57\n"
"v_accvgpr_write_b32 a123, v58\n"
"v_accvgpr_read_b32 v55, a124\n"
"v_accvgpr_read_b32 v56, a125\n"
"v_accvgpr_read_b32 v57, a126\n"
"v_accvgpr_read_b32 v58, a127\n"
"v_mul_f32 v55, v55, v52\n"
"v_mul_f32 v56, v56, v52\n"
"v_mul_f32 v57, v57, v52\n"
"v_mul_f32 v58, v58, v52\n"
"v_accvgpr_write_b32 a124, v55\n"
"v_accvgpr_write_b32 a125, v56\n"
"v_accvgpr_write_b32 a126, v57\n"
"v_accvgpr_write_b32 a127, v58\n"
"v_accvgpr_read_b32 v55, a128\n"
"v_accvgpr_read_b32 v56, a129\n"
"v_accvgpr_read_b32 v57, a130\n"
"v_accvgpr_read_b32 v58, a131\n"
"v_mul_f32 v55, v55, v49\n"
"v_mul_f32 v56, v56, v49\n"
"v_mul_f32 v57, v57, v49\n"
"v_mul_f32 v58, v58, v49\n"
"v_accvgpr_write_b32 a128, v55\n"
"v_accvgpr_write_b32 a129, v56\n"
"v_accvgpr_write_b32 a130, v57\n"
"v_accvgpr_write_b32 a131, v58\n"
"v_accvgpr_read_b32 v55, a132\n"
"v_accvgpr_read_b32 v56, a133\n"
"v_accvgpr_read_b32 v57, a134\n"
"v_accvgpr_read_b32 v58, a135\n"
"v_mul_f32 v55, v55, v50\n"
"v_mul_f32 v56, v56, v50\n"
"v_mul_f32 v57, v57, v50\n"
"v_mul_f32 v58, v58, v50\n"
"v_accvgpr_write_b32 a132, v55\n"
"v_accvgpr_write_b32 a133, v56\n"
"v_accvgpr_write_b32 a134, v57\n"
"v_accvgpr_write_b32 a135, v58\n"
"v_accvgpr_read_b32 v55, a136\n"
"v_accvgpr_read_b32 v56, a137\n"
"v_accvgpr_read_b32 v57, a138\n"
"v_accvgpr_read_b32 v58, a139\n"
"v_mul_f32 v55, v55, v51\n"
"v_mul_f32 v56, v56, v51\n"
"v_mul_f32 v57, v57, v51\n"
"v_mul_f32 v58, v58, v51\n"
"v_accvgpr_write_b32 a136, v55\n"
"v_accvgpr_write_b32 a137, v56\n"
"v_accvgpr_write_b32 a138, v57\n"
"v_accvgpr_write_b32 a139, v58\n"
"v_accvgpr_read_b32 v55, a140\n"
"v_accvgpr_read_b32 v56, a141\n"
"v_accvgpr_read_b32 v57, a142\n"
"v_accvgpr_read_b32 v58, a143\n"
"v_mul_f32 v55, v55, v52\n"
"v_mul_f32 v56, v56, v52\n"
"v_mul_f32 v57, v57, v52\n"
"v_mul_f32 v58, v58, v52\n"
"v_accvgpr_write_b32 a140, v55\n"
"v_accvgpr_write_b32 a141, v56\n"
"v_accvgpr_write_b32 a142, v57\n"
"v_accvgpr_write_b32 a143, v58\n"
"v_accvgpr_read_b32 v55, a144\n"
"v_accvgpr_read_b32 v56, a145\n"
"v_accvgpr_read_b32 v57, a146\n"
"v_accvgpr_read_b32 v58, a147\n"
"v_mul_f32 v55, v55, v49\n"
"v_mul_f32 v56, v56, v49\n"
"v_mul_f32 v57, v57, v49\n"
"v_mul_f32 v58, v58, v49\n"
"v_accvgpr_write_b32 a144, v55\n"
"v_accvgpr_write_b32 a145, v56\n"
"v_accvgpr_write_b32 a146, v57\n"
"v_accvgpr_write_b32 a147, v58\n"
"v_accvgpr_read_b32 v55, a148\n"
"v_accvgpr_read_b32 v56, a149\n"
"v_accvgpr_read_b32 v57, a150\n"
"v_accvgpr_read_b32 v58, a151\n"
"v_mul_f32 v55, v55, v50\n"
"v_mul_f32 v56, v56, v50\n"
"v_mul_f32 v57, v57, v50\n"
"v_mul_f32 v58, v58, v50\n"
"v_accvgpr_write_b32 a148, v55\n"
"v_accvgpr_write_b32 a149, v56\n"
"v_accvgpr_write_b32 a150, v57\n"
"v_accvgpr_write_b32 a151, v58\n"
"v_accvgpr_read_b32 v55, a152\n"
"v_accvgpr_read_b32 v56, a153\n"
"v_accvgpr_read_b32 v57, a154\n"
"v_accvgpr_read_b32 v58, a155\n"
"v_mul_f32 v55, v55, v51\n"
"v_mul_f32 v56, v56, v51\n"
"v_mul_f32 v57, v57, v51\n"
"v_mul_f32 v58, v58, v51\n"
"v_accvgpr_write_b32 a152, v55\n"
"v_accvgpr_write_b32 a153, v56\n"
"v_accvgpr_write_b32 a154, v57\n"
"v_accvgpr_write_b32 a155, v58\n"
"v_accvgpr_read_b32 v55, a156\n"
"v_accvgpr_read_b32 v56, a157\n"
"v_accvgpr_read_b32 v57, a158\n"
"v_accvgpr_read_b32 v58, a159\n"
"v_mul_f32 v55, v55, v52\n"
"v_mul_f32 v56, v56, v52\n"
"v_mul_f32 v57, v57, v52\n"
"v_mul_f32 v58, v58, v52\n"
"v_accvgpr_write_b32 a156, v55\n"
"v_accvgpr_write_b32 a157, v56\n"
"v_accvgpr_write_b32 a158, v57\n"
"v_accvgpr_write_b32 a159, v58\n"
"v_accvgpr_read_b32 v55, a160\n"
"v_accvgpr_read_b32 v56, a161\n"
"v_accvgpr_read_b32 v57, a162\n"
"v_accvgpr_read_b32 v58, a163\n"
"v_mul_f32 v55, v55, v49\n"
"v_mul_f32 v56, v56, v49\n"
"v_mul_f32 v57, v57, v49\n"
"v_mul_f32 v58, v58, v49\n"
"v_accvgpr_write_b32 a160, v55\n"
"v_accvgpr_write_b32 a161, v56\n"
"v_accvgpr_write_b32 a162, v57\n"
"v_accvgpr_write_b32 a163, v58\n"
"v_accvgpr_read_b32 v55, a164\n"
"v_accvgpr_read_b32 v56, a165\n"
"v_accvgpr_read_b32 v57, a166\n"
"v_accvgpr_read_b32 v58, a167\n"
"v_mul_f32 v55, v55, v50\n"
"v_mul_f32 v56, v56, v50\n"
"v_mul_f32 v57, v57, v50\n"
"v_mul_f32 v58, v58, v50\n"
"v_accvgpr_write_b32 a164, v55\n"
"v_accvgpr_write_b32 a165, v56\n"
"v_accvgpr_write_b32 a166, v57\n"
"v_accvgpr_write_b32 a167, v58\n"
"v_accvgpr_read_b32 v55, a168\n"
"v_accvgpr_read_b32 v56, a169\n"
"v_accvgpr_read_b32 v57, a170\n"
"v_accvgpr_read_b32 v58, a171\n"
"v_mul_f32 v55, v55, v51\n"
"v_mul_f32 v56, v56, v51\n"
"v_mul_f32 v57, v57, v51\n"
"v_mul_f32 v58, v58, v51\n"
"v_accvgpr_write_b32 a168, v55\n"
"v_accvgpr_write_b32 a169, v56\n"
"v_accvgpr_write_b32 a170, v57\n"
"v_accvgpr_write_b32 a171, v58\n"
"v_accvgpr_read_b32 v55, a172\n"
"v_accvgpr_read_b32 v56, a173\n"
"v_accvgpr_read_b32 v57, a174\n"
"v_accvgpr_read_b32 v58, a175\n"
"v_mul_f32 v55, v55, v52\n"
"v_mul_f32 v56, v56, v52\n"
"v_mul_f32 v57, v57, v52\n"
"v_mul_f32 v58, v58, v52\n"
"v_accvgpr_write_b32 a172, v55\n"
"v_accvgpr_write_b32 a173, v56\n"
"v_accvgpr_write_b32 a174, v57\n"
"v_accvgpr_write_b32 a175, v58\n"
"v_accvgpr_read_b32 v55, a176\n"
"v_accvgpr_read_b32 v56, a177\n"
"v_accvgpr_read_b32 v57, a178\n"
"v_accvgpr_read_b32 v58, a179\n"
"v_mul_f32 v55, v55, v49\n"
"v_mul_f32 v56, v56, v49\n"
"v_mul_f32 v57, v57, v49\n"
"v_mul_f32 v58, v58, v49\n"
"v_accvgpr_write_b32 a176, v55\n"
"v_accvgpr_write_b32 a177, v56\n"
"v_accvgpr_write_b32 a178, v57\n"
"v_accvgpr_write_b32 a179, v58\n"
"v_accvgpr_read_b32 v55, a180\n"
"v_accvgpr_read_b32 v56, a181\n"
"v_accvgpr_read_b32 v57, a182\n"
"v_accvgpr_read_b32 v58, a183\n"
"v_mul_f32 v55, v55, v50\n"
"v_mul_f32 v56, v56, v50\n"
"v_mul_f32 v57, v57, v50\n"
"v_mul_f32 v58, v58, v50\n"
"v_accvgpr_write_b32 a180, v55\n"
"v_accvgpr_write_b32 a181, v56\n"
"v_accvgpr_write_b32 a182, v57\n"
"v_accvgpr_write_b32 a183, v58\n"
"v_accvgpr_read_b32 v55, a184\n"
"v_accvgpr_read_b32 v56, a185\n"
"v_accvgpr_read_b32 v57, a186\n"
"v_accvgpr_read_b32 v58, a187\n"
"v_mul_f32 v55, v55, v51\n"
"v_mul_f32 v56, v56, v51\n"
"v_mul_f32 v57, v57, v51\n"
"v_mul_f32 v58, v58, v51\n"
"v_accvgpr_write_b32 a184, v55\n"
"v_accvgpr_write_b32 a185, v56\n"
"v_accvgpr_write_b32 a186, v57\n"
"v_accvgpr_write_b32 a187, v58\n"
"v_accvgpr_read_b32 v55, a188\n"
"v_accvgpr_read_b32 v56, a189\n"
"v_accvgpr_read_b32 v57, a190\n"
"v_accvgpr_read_b32 v58, a191\n"
"v_mul_f32 v55, v55, v52\n"
"v_mul_f32 v56, v56, v52\n"
"v_mul_f32 v57, v57, v52\n"
"v_mul_f32 v58, v58, v52\n"
"v_accvgpr_write_b32 a188, v55\n"
"v_accvgpr_write_b32 a189, v56\n"
"v_accvgpr_write_b32 a190, v57\n"
"v_accvgpr_write_b32 a191, v58\n"
"s_nop 1\n"
"s_cmp_eq_u32 s65, 0\n"
"s_cbranch_scc1 K1A_ROb0\n"
"s_cmp_eq_u32 s65, 1\n"
"s_cbranch_scc1 K1A_ROb1\n"
"s_cmp_eq_u32 s65, 2\n"
"s_cbranch_scc1 K1A_ROb2\n"
"s_cmp_eq_u32 s65, 3\n"
"s_cbranch_scc1 K1A_ROb3\n"
"s_cmp_eq_u32 s65, 4\n"
"s_cbranch_scc1 K1A_ROb4\n"
"s_cmp_eq_u32 s65, 5\n"
"s_cbranch_scc1 K1A_ROb5\n"
"s_branch K1A_ROb6\n"
"K1B_START:\n"
"s_getpc_b64 s[76:77]\n"
"K1_PC0:\n"
"s_sub_u32 s76, s76, K1_PC0-K1_CODE_BEGIN\n"
"s_subb_u32 s77, s77, 0\n"
"s_lshr_b32 s66, %[bid], 3\n"
"s_mul_i32 s66, s66, (((K1_END-K1_CODE_BEGIN+127)/128+31)/32)\n"
"v_min_u32 v55, (((K1_END-K1_CODE_BEGIN+127)/128+31)/32)-1, v0\n"
"v_add_u32 v55, s66, v55\n"
"v_lshlrev_b32 v55, 7, v55\n"
"v_min_u32 v55, K1_END-K1_CODE_BEGIN-4, v55\n"
"global_load_dword v47, v55, s[76:77]\n"
"s_getpc_b64 s[76:77]\n"
"K1_PC1:\n"
"s_add_u32 s76, s76, K2_CODE_BEGIN-K1_PC1\n"
"s_addc_u32 s77, s77, 0\n"
"s_lshr_b32 s66, %[bid], 3\n"
"s_mul_i32 s66, s66, (((K2_CODE_END-K2_CODE_BEGIN+127)/128+31)/32)\n"
"v_min_u32 v55, (((K2_CODE_END-K2_CODE_BEGIN+127)/128+31)/32)-1, v0\n"
"v_add_u32 v55, s66, v55\n"
"v_lshlrev_b32 v55, 7, v55\n"
"v_min_u32 v55, K2_CODE_END-K2_CODE_BEGIN-4, v55\n"
"global_load_dword v47, v55, s[76:77]\n"
"v_lshlrev_b32 v43, 7, v0\n"
"v_add_u32 v55, 8192, v43\n"
"v_cmp_gt_u32 vcc, 32, v0\n"
"v_mov_b32 v56, 0x7fffff00\n"
"s_nop 0\n"
"v_cndmask_b32 v44, v56, v55, vcc\n"
"s_and_b32 s66, s72, 7\n"
"s_and_b32 s67, s73, 3\n"
"s_mul_i32 s66, s66, 30\n"
"v_add_u32 v55, s66, v0\n"
"v_mul_u32_u24 v55, 1280, v55\n"
"v_cmp_gt_u32 vcc, 30, v0\n"
"s_nop 1\n"
"v_cndmask_b32 v45, v56, v55, vcc\n"
"s_mul_i32 s67, s67, 48\n"
"v_add_u32 v55, s67, v0\n"
"v_mul_u32_u24 v57, 21846, v55\n"
"v_lshrrev_b32 v57, 16, v57\n"
"v_mul_u32_u24 v58, 3, v57\n"
"v_sub_u32 v58, v55, v58\n"
"v_mul_u32_u24 v57, 12288, v57\n"
"v_lshl_add_u32 v57, v58, 7, v57\n"
"v_cmp_gt_u32 vcc, 48, v0\n"
"s_nop 1\n"
"v_cndmask_b32 v46, v56, v57, vcc\n"
"s_mov_b32 s87, 0\n"
"s_mov_b32 s88, 0\n"
"s_mov_b32 s85, 24576\n"
"s_mov_b32 s86, 24576\n"
"buffer_load_dword v47, v46, s[40:43], s88 offen\n"
"buffer_load_dword v47, v45, s[36:39], s87 offen\n"
"s_add_u32 s88, s88, 786432\n"
"s_add_u32 s87, s87, 128\n"
"buffer_load_dword v47, v46, s[40:43], s88 offen\n"
"buffer_load_dword v47, v45, s[36:39], s87 offen\n"
"s_add_u32 s88, s88, 786432\n"
"s_add_u32 s87, s87, 128\n"
"buffer_load_dword v47, v46, s[40:43], s88 offen\n"
"buffer_load_dword v47, v45, s[36:39], s87 offen\n"
"s_add_u32 s88, s88, 786432\n"
"s_add_u32 s87, s87, 128\n"
"buffer_load_dword v47, v46, s[40:43], s88 offen\n"
"buffer_load_dword v47, v45, s[36:39], s87 offen\n"
"s_add_u32 s88, s88, 786432\n"
"s_add_u32 s87, s87, 128\n"
"s_lshl_b32 s66, s73, 5\n"
"s_add_u32 s66, s66, s72\n"
"s_mul_i32 s67, s66, 393216\n"
"s_add_u32 s44, %[klo], s67\n"
"s_addc_u32 s45, %[khi], 0\n"
"s_mov_b32 s46, 393216\n"
"s_mov_b32 s47, 0x20000\n"
"s_add_u32 s48, %[vlo], s67\n"
"s_addc_u32 s49, %[vhi], 0\n"
"s_mov_b32 s50, 393216\n"
"s_mov_b32 s51, 0x20000\n"
"s_mov_b32 s52, %[olo]\n"
"s_mov_b32 s53, %[ohi]\n"
"s_mov_b32 s54, 23592960\n"
"s_mov_b32 s55, 0x20000\n"
"s_mul_i32 s68, s72, 720\n"
"s_mul_i32 s66, s73, 15\n"
"s_add_u32 s68, s68, s66\n"
"s_lshl_b32 s66, s56, 2\n"
"s_add_u32 s68, s68, s66\n"
"s_lshl_b32 s68, s68, 10\n"
"s_mov_b32 s59, 0\n"
"s_mov_b32 s60, 0\n"
"s_mov_b32 s62, 0x3dd53b95\n"
"s_mov_b32 s63, 0x3dd53b95\n"
"s_mov_b32 s64, 0x41000000\n"
"v_lshrrev_b32 v55, 4, v0\n"
"v_and_b32 v56, 15, v0\n"
"v_bfe_u32 v57, v0, 2, 2\n"
"v_and_b32 v58, 3, v0\n"
"v_lshl_add_u32 v59, v55, 3, v57\n"
"v_mul_u32_u24 v59, 384, v59\n"
"v_lshl_add_u32 v59, v58, 3, v59\n"
"v_add_u32 v59, 24576, v59\n"
"v_lshrrev_b32 v60, 1, v57\n"
"v_and_b32 v61, 1, v55\n"
"v_lshl_add_u32 v60, v61, 1, v60\n"
"v_xor_b32 v61, 0, v60\n"
"v_lshl_add_u32 v37, v61, 5, v59\n"
"v_xor_b32 v61, 1, v60\n"
"v_lshl_add_u32 v38, v61, 5, v59\n"
"v_xor_b32 v61, 2, v60\n"
"v_lshl_add_u32 v39, v61, 5, v59\n"
"v_xor_b32 v61, 3, v60\n"
"v_lshl_add_u32 v40, v61, 5, v59\n"
"v_lshlrev_b32 v59, 7, v56\n"
"v_add_u32 v59, 147456, v59\n"
"v_bfe_u32 v60, v56, 1, 3\n"
"v_xor_b32 v61, v55, v60\n"
"v_lshl_add_u32 v41, v61, 4, v59\n"
"v_xor_b32 v61, 4, v61\n"
"v_lshl_add_u32 v42, v61, 4, v59\n"
"v_mul_u32_u24 v59, 384, v56\n"
"v_add_u32 v59, 73728, v59\n"
"v_xor_b32 v61, v55, v60\n"
"v_lshl_add_u32 v1, v61, 4, v59\n"
"v_xor_b32 v61, 4, v61\n"
"v_lshl_add_u32 v2, v61, 4, v59\n"
"v_add_u32 v3, 36864, v1\n"
"v_add_u32 v4, 36864, v2\n"
"v_lshl_add_u32 v59, v55, 2, v57\n"
"v_bfe_u32 v60, v59, 1, 2\n"
"v_mul_u32_u24 v59, 384, v59\n"
"v_lshl_add_u32 v59, v58, 3, v59\n"
"v_xor_b32 v61, 0, v60\n"
"v_lshl_add_u32 v5, v61, 5, v59\n"
"v_add_u32 v9, 36864, v5\n"
"v_xor_b32 v61, 1, v60\n"
"v_lshl_add_u32 v6, v61, 5, v59\n"
"v_add_u32 v10, 36864, v6\n"
"v_xor_b32 v61, 2, v60\n"
"v_lshl_add_u32 v7, v61, 5, v59\n"
"v_add_u32 v11, 36864, v7\n"
"v_xor_b32 v61, 3, v60\n"
"v_lshl_add_u32 v8, v61, 5, v59\n"
"v_add_u32 v12, 36864, v8\n"
"v_add_u32 v55, 0, v0\n"
"v_mul_u32_u24 v55, 1366, v55\n"
"v_bfe_u32 v55, v55, 16, 3\n"
"v_xor_b32 v55, v55, v0\n"
"v_lshlrev_b32 v13, 4, v55\n"
"v_add_u32 v55, 64, v0\n"
"v_mul_u32_u24 v55, 1366, v55\n"
"v_bfe_u32 v55, v55, 16, 3\n"
"v_xor_b32 v55, v55, v0\n"
"v_lshlrev_b32 v14, 4, v55\n"
"v_add_u32 v55, 128, v0\n"
"v_mul_u32_u24 v55, 1366, v55\n"
"v_bfe_u32 v55, v55, 16, 3\n"
"v_xor_b32 v55, v55, v0\n"
"v_lshlrev_b32 v15, 4, v55\n"
"v_add_u32 v55, 192, v0\n"
"v_mul_u32_u24 v55, 1366, v55\n"
"v_bfe_u32 v55, v55, 16, 3\n"
"v_xor_b32 v55, v55, v0\n"
"v_lshlrev_b32 v16, 4, v55\n"
"v_add_u32 v55, 256, v0\n"
"v_mul_u32_u24 v55, 1366, v55\n"
"v_bfe_u32 v55, v55, 16, 3\n"
"v_xor_b32 v55, v55, v0\n"
"v_lshlrev_b32 v17, 4, v55\n"
"v_add_u32 v55, 320, v0\n"
"v_mul_u32_u24 v55, 1366, v55\n"
"v_bfe_u32 v55, v55, 16, 3\n"
"v_xor_b32 v55, v55, v0\n"
"v_lshlrev_b32 v18, 4, v55\n"
"v_add_u32 v24, 8192, v18\n"
"v_add_u32 v23, 8192, v17\n"
"v_add_u32 v22, 8192, v16\n"
"v_add_u32 v21, 8192, v15\n"
"v_add_u32 v20, 4096, v14\n"
"v_add_u32 v19, 4096, v13\n"
"v_add_u32 v17, 4096, v17\n"
"v_add_u32 v18, 4096, v18\n"
"v_add_u32 v55, 0, v0\n"
"v_mul_u32_u24 v55, 1366, v55\n"
"v_bfe_u32 v55, v55, 16, 2\n"
"v_lshlrev_b32 v55, 1, v55\n"
"v_xor_b32 v55, v55, v0\n"
"v_lshlrev_b32 v25, 4, v55\n"
"v_add_u32 v55, 64, v0\n"
"v_mul_u32_u24 v55, 1366, v55\n"
"v_bfe_u32 v55, v55, 16, 2\n"
"v_lshlrev_b32 v55, 1, v55\n"
"v_xor_b32 v55, v55, v0\n"
"v_lshlrev_b32 v26, 4, v55\n"
"v_add_u32 v55, 128, v0\n"
"v_mul_u32_u24 v55, 1366, v55\n"
"v_bfe_u32 v55, v55, 16, 2\n"
"v_lshlrev_b32 v55, 1, v55\n"
"v_xor_b32 v55, v55, v0\n"
"v_lshlrev_b32 v27, 4, v55\n"
"v_add_u32 v36, 8192, v27\n"
"v_add_u32 v35, 8192, v26\n"
"v_add_u32 v34, 8192, v25\n"
"v_add_u32 v33, 8192, v27\n"
"v_add_u32 v32, 4096, v26\n"
"v_add_u32 v31, 4096, v25\n"
"v_add_u32 v30, 4096, v27\n"
"v_add_u32 v29, 4096, v26\n"
"v_mov_b32 v28, v25\n"
"s_barrier\n"
"ds_read_b128 a[0:3], v41 offset:0\n"
"ds_read_b128 a[4:7], v41 offset:2048\n"
"ds_read_b128 a[8:11], v41 offset:4096\n"
"ds_read_b64_tr_b16 a[64:65], v40 offset:256\n"
"ds_read_b64_tr_b16 a[66:67], v40 offset:1792\n"
"ds_read_b128 a[16:19], v42 offset:0\n"
"ds_read_b128 a[20:23], v42 offset:2048\n"
"ds_read_b128 a[24:27], v42 offset:4096\n"
"ds_read_b64_tr_b16 a[68:69], v40 offset:12544\n"
"ds_read_b64_tr_b16 a[70:71], v40 offset:14080\n"
"ds_read_b64_tr_b16 a[72:73], v39 offset:256\n"
"ds_read_b64_tr_b16 a[74:75], v39 offset:1792\n"
"s_waitcnt lgkmcnt(0)\n"
"v_mfma_f32_16x16x32_bf16 v[228:231], a[64:67], a[0:3], 0\n"
"ds_read_b64_tr_b16 a[76:77], v39 offset:12544\n"
"ds_read_b64_tr_b16 a[78:79], v39 offset:14080\n"
"ds_read_b64_tr_b16 a[80:81], v38 offset:256\n"
"v_mfma_f32_16x16x32_bf16 v[236:239], a[64:67], a[4:7], 0\n"
"ds_read_b64_tr_b16 a[82:83], v38 offset:1792\n"
"ds_read_b64_tr_b16 a[84:85], v38 offset:12544\n"
"ds_read_b64_tr_b16 a[86:87], v38 offset:14080\n"
"v_mfma_f32_16x16x32_bf16 v[244:247], a[64:67], a[8:11], 0\n"
"buffer_load_dword v47, v46, s[40:43], s88 offen\n"
"ds_read_b64_tr_b16 a[88:89], v37 offset:256\n"
"v_mfma_f32_16x16x32_bf16 v[228:231], a[68:71], a[16:19], v[228:231]\n"
"ds_read_b64_tr_b16 a[90:91], v37 offset:1792\n"
"ds_read_b64_tr_b16 a[92:93], v37 offset:12544\n"
"ds_read_b64_tr_b16 a[94:95], v37 offset:14080\n"
"v_mfma_f32_16x16x32_bf16 v[236:239], a[68:71], a[20:23], v[236:239]\n"
"v_mfma_f32_16x16x32_bf16 v[244:247], a[68:71], a[24:27], v[244:247]\n"
"v_mfma_f32_16x16x32_bf16 v[224:227], a[72:75], a[0:3], 0\n"
"ds_read_b64_tr_b16 a[64:65], v40 offset:128\n"
"ds_read_b64_tr_b16 a[66:67], v40 offset:1664\n"
"v_mfma_f32_16x16x32_bf16 v[232:235], a[72:75], a[4:7], 0\n"
"v_mfma_f32_16x16x32_bf16 v[240:243], a[72:75], a[8:11], 0\n"
"s_waitcnt lgkmcnt(6)\n"
"v_mfma_f32_16x16x32_bf16 v[224:227], a[76:79], a[16:19], v[224:227]\n"
"ds_read_b64_tr_b16 a[68:69], v40 offset:12416\n"
"ds_read_b64_tr_b16 a[70:71], v40 offset:13952\n"
"v_mfma_f32_16x16x32_bf16 v[232:235], a[76:79], a[20:23], v[232:235]\n"
"v_mfma_f32_16x16x32_bf16 v[240:243], a[76:79], a[24:27], v[240:243]\n"
"v_mfma_f32_16x16x32_bf16 v[196:199], a[80:83], a[0:3], 0\n"
"ds_read_b64_tr_b16 a[72:73], v39 offset:128\n"
"ds_read_b64_tr_b16 a[74:75], v39 offset:1664\n"
"v_mfma_f32_16x16x32_bf16 v[204:207], a[80:83], a[4:7], 0\n"
"v_mfma_f32_16x16x32_bf16 v[212:215], a[80:83], a[8:11], 0\n"
"v_mfma_f32_16x16x32_bf16 v[196:199], a[84:87], a[16:19], v[196:199]\n"
"ds_read_b64_tr_b16 a[76:77], v39 offset:12416\n"
"ds_read_b64_tr_b16 a[78:79], v39 offset:13952\n"
"v_mfma_f32_16x16x32_bf16 v[204:207], a[84:87], a[20:23], v[204:207]\n"
"v_mfma_f32_16x16x32_bf16 v[212:215], a[84:87], a[24:27], v[212:215]\n"
"s_waitcnt lgkmcnt(6)\n"
"v_mfma_f32_16x16x32_bf16 v[192:195], a[88:91], a[0:3], 0\n"
"ds_read_b64_tr_b16 a[80:81], v38 offset:128\n"
"ds_read_b64_tr_b16 a[82:83], v38 offset:1664\n"
"v_mfma_f32_16x16x32_bf16 v[200:203], a[88:91], a[4:7], 0\n"
"v_mfma_f32_16x16x32_bf16 v[208:211], a[88:91], a[8:11], 0\n"
"v_mfma_f32_16x16x32_bf16 v[192:195], a[92:95], a[16:19], v[192:195]\n"
"ds_read_b64_tr_b16 a[84:85], v38 offset:12416\n"
"ds_read_b64_tr_b16 a[86:87], v38 offset:13952\n"
"v_mfma_f32_16x16x32_bf16 v[200:203], a[92:95], a[20:23], v[200:203]\n"
"v_mfma_f32_16x16x32_bf16 v[208:211], a[92:95], a[24:27], v[208:211]\n"
"v_mfma_f32_16x16x32_bf16 v[164:167], a[64:67], a[0:3], 0\n"
"ds_read_b64_tr_b16 a[88:89], v37 offset:128\n"
"ds_read_b64_tr_b16 a[90:91], v37 offset:1664\n"
"v_mfma_f32_16x16x32_bf16 v[172:175], a[64:67], a[4:7], 0\n"
"v_mfma_f32_16x16x32_bf16 v[180:183], a[64:67], a[8:11], 0\n"
"s_waitcnt lgkmcnt(6)\n"
"v_mfma_f32_16x16x32_bf16 v[164:167], a[68:71], a[16:19], v[164:167]\n"
"buffer_load_dword v47, v45, s[36:39], s87 offen\n"
"ds_read_b64_tr_b16 a[92:93], v37 offset:12416\n"
"ds_read_b64_tr_b16 a[94:95], v37 offset:13952\n"
"v_mfma_f32_16x16x32_bf16 v[172:175], a[68:71], a[20:23], v[172:175]\n"
"v_mfma_f32_16x16x32_bf16 v[180:183], a[68:71], a[24:27], v[180:183]\n"
"v_mfma_f32_16x16x32_bf16 v[160:163], a[72:75], a[0:3], 0\n"
"ds_read_b64_tr_b16 a[64:65], v40 offset:0\n"
"ds_read_b64_tr_b16 a[66:67], v40 offset:1536\n"
"v_mfma_f32_16x16x32_bf16 v[168:171], a[72:75], a[4:7], 0\n"
"v_mfma_f32_16x16x32_bf16 v[176:179], a[72:75], a[8:11], 0\n"
"v_mfma_f32_16x16x32_bf16 v[160:163], a[76:79], a[16:19], v[160:163]\n"
"ds_read_b64_tr_b16 a[68:69], v40 offset:12288\n"
"ds_read_b64_tr_b16 a[70:71], v40 offset:13824\n"
"v_mfma_f32_16x16x32_bf16 v[168:171], a[76:79], a[20:23], v[168:171]\n"
"v_mfma_f32_16x16x32_bf16 v[176:179], a[76:79], a[24:27], v[176:179]\n"
"s_waitcnt lgkmcnt(6)\n"
"v_mfma_f32_16x16x32_bf16 v[132:135], a[80:83], a[0:3], 0\n"
"ds_read_b64_tr_b16 a[72:73], v39 offset:0\n"
"ds_read_b64_tr_b16 a[74:75], v39 offset:1536\n"
"v_mfma_f32_16x16x32_bf16 v[140:143], a[80:83], a[4:7], 0\n"
"v_mfma_f32_16x16x32_bf16 v[148:151], a[80:83], a[8:11], 0\n"
"v_mfma_f32_16x16x32_bf16 v[132:135], a[84:87], a[16:19], v[132:135]\n"
"ds_read_b64_tr_b16 a[76:77], v39 offset:12288\n"
"ds_read_b64_tr_b16 a[78:79], v39 offset:13824\n"
"v_mfma_f32_16x16x32_bf16 v[140:143], a[84:87], a[20:23], v[140:143]\n"
"v_mfma_f32_16x16x32_bf16 v[148:151], a[84:87], a[24:27], v[148:151]\n"
"v_mfma_f32_16x16x32_bf16 v[128:131], a[88:91], a[0:3], 0\n"
"ds_read_b64_tr_b16 a[80:81], v38 offset:0\n"
"ds_read_b64_tr_b16 a[82:83], v38 offset:1536\n"
"v_mfma_f32_16x16x32_bf16 v[136:139], a[88:91], a[4:7], 0\n"
"v_mfma_f32_16x16x32_bf16 v[144:147], a[88:91], a[8:11], 0\n"
"s_waitcnt lgkmcnt(6)\n"
"v_mfma_f32_16x16x32_bf16 v[128:131], a[92:95], a[16:19], v[128:131]\n"
"ds_read_b64_tr_b16 a[84:85], v38 offset:12288\n"
"ds_read_b64_tr_b16 a[86:87], v38 offset:13824\n"
"v_mfma_f32_16x16x32_bf16 v[136:139], a[92:95], a[20:23], v[136:139]\n"
"v_mfma_f32_16x16x32_bf16 v[144:147], a[92:95], a[24:27], v[144:147]\n"
"v_mfma_f32_16x16x32_bf16 v[100:103], a[64:67], a[0:3], 0\n"
"ds_read_b64_tr_b16 a[88:89], v37 offset:0\n"
"ds_read_b64_tr_b16 a[90:91], v37 offset:1536\n"
"v_mfma_f32_16x16x32_bf16 v[108:111], a[64:67], a[4:7], 0\n"
"v_mfma_f32_16x16x32_bf16 v[116:119], a[64:67], a[8:11], 0\n"
"v_mfma_f32_16x16x32_bf16 v[100:103], a[68:71], a[16:19], v[100:103]\n"
"ds_read_b64_tr_b16 a[92:93], v37 offset:12288\n"
"ds_read_b64_tr_b16 a[94:95], v37 offset:13824\n"
"v_mfma_f32_16x16x32_bf16 v[108:111], a[68:71], a[20:23], v[108:111]\n"
"v_mfma_f32_16x16x32_bf16 v[116:119], a[68:71], a[24:27], v[116:119]\n"
"s_add_u32 s88, s88, 786432\n"
"s_add_u32 s87, s87, 128\n"
"s_waitcnt lgkmcnt(6)\n"
"v_mfma_f32_16x16x32_bf16 v[96:99], a[72:75], a[0:3], 0\n"
"v_mfma_f32_16x16x32_bf16 v[104:107], a[72:75], a[4:7], 0\n"
"v_mfma_f32_16x16x32_bf16 v[112:115], a[72:75], a[8:11], 0\n"
"v_mfma_f32_16x16x32_bf16 v[96:99], a[76:79], a[16:19], v[96:99]\n"
"s_waitcnt lgkmcnt(0)\n"
"s_barrier\n"
"v_mfma_f32_16x16x32_bf16 v[104:107], a[76:79], a[20:23], v[104:107]\n"
"ds_read_b128 a[32:35], v41 offset:6144\n"
"ds_read_b128 a[36:39], v41 offset:8192\n"
"ds_read_b128 a[40:43], v41 offset:10240\n"
"v_mfma_f32_16x16x32_bf16 v[112:115], a[76:79], a[24:27], v[112:115]\n"
"ds_read_b64_tr_b16 a[64:65], v40 offset:24832\n"
"ds_read_b64_tr_b16 a[66:67], v40 offset:26368\n"
"ds_read_b128 a[48:51], v42 offset:6144\n"
"v_mfma_f32_16x16x32_bf16 v[68:71], a[80:83], a[0:3], 0\n"
"ds_read_b128 a[52:55], v42 offset:8192\n"
"ds_read_b128 a[56:59], v42 offset:10240\n"
"ds_read_b64_tr_b16 a[68:69], v40 offset:37120\n"
"v_mfma_f32_16x16x32_bf16 v[76:79], a[80:83], a[4:7], 0\n"
"ds_read_b64_tr_b16 a[70:71], v40 offset:38656\n"
"ds_read_b64_tr_b16 a[72:73], v39 offset:24832\n"
"ds_read_b64_tr_b16 a[74:75], v39 offset:26368\n"
"v_mfma_f32_16x16x32_bf16 v[84:87], a[80:83], a[8:11], 0\n"
"v_mfma_f32_16x16x32_bf16 v[68:71], a[84:87], a[16:19], v[68:71]\n"
"v_mfma_f32_16x16x32_bf16 v[76:79], a[84:87], a[20:23], v[76:79]\n"
"v_mfma_f32_16x16x32_bf16 v[84:87], a[84:87], a[24:27], v[84:87]\n"
"v_mfma_f32_16x16x32_bf16 v[64:67], a[88:91], a[0:3], 0\n"
"v_mfma_f32_16x16x32_bf16 v[72:75], a[88:91], a[4:7], 0\n"
"v_mfma_f32_16x16x32_bf16 v[80:83], a[88:91], a[8:11], 0\n"
"v_mfma_f32_16x16x32_bf16 v[64:67], a[92:95], a[16:19], v[64:67]\n"
"v_mfma_f32_16x16x32_bf16 v[72:75], a[92:95], a[20:23], v[72:75]\n"
"v_mfma_f32_16x16x32_bf16 v[80:83], a[92:95], a[24:27], v[80:83]\n"
"s_waitcnt lgkmcnt(0)\n"
"v_mfma_f32_16x16x32_bf16 v[228:231], a[64:67], a[32:35], v[228:231]\n"
"ds_read_b64_tr_b16 a[76:77], v39 offset:37120\n"
"ds_read_b64_tr_b16 a[78:79], v39 offset:38656\n"
"ds_read_b64_tr_b16 a[80:81], v38 offset:24832\n"
"v_mfma_f32_16x16x32_bf16 v[236:239], a[64:67], a[36:39], v[236:239]\n"
"ds_read_b64_tr_b16 a[82:83], v38 offset:26368\n"
"ds_read_b64_tr_b16 a[84:85], v38 offset:37120\n"
"ds_read_b64_tr_b16 a[86:87], v38 offset:38656\n"
"v_mfma_f32_16x16x32_bf16 v[244:247], a[64:67], a[40:43], v[244:247]\n"
"buffer_load_dword v47, v46, s[40:43], s88 offen\n"
"ds_read_b64_tr_b16 a[88:89], v37 offset:24832\n"
"v_mfma_f32_16x16x32_bf16 v[228:231], a[68:71], a[48:51], v[228:231]\n"
"ds_read_b64_tr_b16 a[90:91], v37 offset:26368\n"
"ds_read_b64_tr_b16 a[92:93], v37 offset:37120\n"
"ds_read_b64_tr_b16 a[94:95], v37 offset:38656\n"
"v_mfma_f32_16x16x32_bf16 v[236:239], a[68:71], a[52:55], v[236:239]\n"
"v_mfma_f32_16x16x32_bf16 v[244:247], a[68:71], a[56:59], v[244:247]\n"
"v_mfma_f32_16x16x32_bf16 v[224:227], a[72:75], a[32:35], v[224:227]\n"
"ds_read_b64_tr_b16 a[64:65], v40 offset:24704\n"
"ds_read_b64_tr_b16 a[66:67], v40 offset:26240\n"
"v_mfma_f32_16x16x32_bf16 v[232:235], a[72:75], a[36:39], v[232:235]\n"
"v_mfma_f32_16x16x32_bf16 v[240:243], a[72:75], a[40:43], v[240:243]\n"
"s_waitcnt lgkmcnt(6)\n"
"v_mfma_f32_16x16x32_bf16 v[224:227], a[76:79], a[48:51], v[224:227]\n"
"ds_read_b64_tr_b16 a[68:69], v40 offset:36992\n"
"ds_read_b64_tr_b16 a[70:71], v40 offset:38528\n"
"v_mfma_f32_16x16x32_bf16 v[232:235], a[76:79], a[52:55], v[232:235]\n"
"v_mfma_f32_16x16x32_bf16 v[240:243], a[76:79], a[56:59], v[240:243]\n"
"v_mfma_f32_16x16x32_bf16 v[196:199], a[80:83], a[32:35], v[196:199]\n"
"ds_read_b64_tr_b16 a[72:73], v39 offset:24704\n"
"ds_read_b64_tr_b16 a[74:75], v39 offset:26240\n"
"v_mfma_f32_16x16x32_bf16 v[204:207], a[80:83], a[36:39], v[204:207]\n"
"v_mfma_f32_16x16x32_bf16 v[212:215], a[80:83], a[40:43], v[212:215]\n"
"v_mfma_f32_16x16x32_bf16 v[196:199], a[84:87], a[48:51], v[196:199]\n"
"ds_read_b64_tr_b16 a[76:77], v39 offset:36992\n"
"ds_read_b64_tr_b16 a[78:79], v39 offset:38528\n"
"v_mfma_f32_16x16x32_bf16 v[204:207], a[84:87], a[52:55], v[204:207]\n"
"v_mfma_f32_16x16x32_bf16 v[212:215], a[84:87], a[56:59], v[212:215]\n"
"s_waitcnt lgkmcnt(6)\n"
"v_mfma_f32_16x16x32_bf16 v[192:195], a[88:91], a[32:35], v[192:195]\n"
"ds_read_b64_tr_b16 a[80:81], v38 offset:24704\n"
"ds_read_b64_tr_b16 a[82:83], v38 offset:26240\n"
"v_mfma_f32_16x16x32_bf16 v[200:203], a[88:91], a[36:39], v[200:203]\n"
"v_mfma_f32_16x16x32_bf16 v[208:211], a[88:91], a[40:43], v[208:211]\n"
"v_mfma_f32_16x16x32_bf16 v[192:195], a[92:95], a[48:51], v[192:195]\n"
"ds_read_b64_tr_b16 a[84:85], v38 offset:36992\n"
"ds_read_b64_tr_b16 a[86:87], v38 offset:38528\n"
"v_mfma_f32_16x16x32_bf16 v[200:203], a[92:95], a[52:55], v[200:203]\n"
"v_mfma_f32_16x16x32_bf16 v[208:211], a[92:95], a[56:59], v[208:211]\n"
"v_mfma_f32_16x16x32_bf16 v[164:167], a[64:67], a[32:35], v[164:167]\n"
"ds_read_b64_tr_b16 a[88:89], v37 offset:24704\n"
"ds_read_b64_tr_b16 a[90:91], v37 offset:26240\n"
"v_mfma_f32_16x16x32_bf16 v[172:175], a[64:67], a[36:39], v[172:175]\n"
"v_mfma_f32_16x16x32_bf16 v[180:183], a[64:67], a[40:43], v[180:183]\n"
"s_waitcnt lgkmcnt(6)\n"
"v_mfma_f32_16x16x32_bf16 v[164:167], a[68:71], a[48:51], v[164:167]\n"
"buffer_load_dword v47, v45, s[36:39], s87 offen\n"
"ds_read_b64_tr_b16 a[92:93], v37 offset:36992\n"
"ds_read_b64_tr_b16 a[94:95], v37 offset:38528\n"
"v_mfma_f32_16x16x32_bf16 v[172:175], a[68:71], a[52:55], v[172:175]\n"
"v_mfma_f32_16x16x32_bf16 v[180:183], a[68:71], a[56:59], v[180:183]\n"
"v_mfma_f32_16x16x32_bf16 v[160:163], a[72:75], a[32:35], v[160:163]\n"
"ds_read_b64_tr_b16 a[64:65], v40 offset:24576\n"
"ds_read_b64_tr_b16 a[66:67], v40 offset:26112\n"
"v_mfma_f32_16x16x32_bf16 v[168:171], a[72:75], a[36:39], v[168:171]\n"
"v_mfma_f32_16x16x32_bf16 v[176:179], a[72:75], a[40:43], v[176:179]\n"
"v_mfma_f32_16x16x32_bf16 v[160:163], a[76:79], a[48:51], v[160:163]\n"
"ds_read_b64_tr_b16 a[68:69], v40 offset:36864\n"
"ds_read_b64_tr_b16 a[70:71], v40 offset:38400\n"
"v_mfma_f32_16x16x32_bf16 v[168:171], a[76:79], a[52:55], v[168:171]\n"
"v_mfma_f32_16x16x32_bf16 v[176:179], a[76:79], a[56:59], v[176:179]\n"
"s_waitcnt lgkmcnt(6)\n"
"v_mfma_f32_16x16x32_bf16 v[132:135], a[80:83], a[32:35], v[132:135]\n"
"ds_read_b64_tr_b16 a[72:73], v39 offset:24576\n"
"ds_read_b64_tr_b16 a[74:75], v39 offset:26112\n"
"v_mfma_f32_16x16x32_bf16 v[140:143], a[80:83], a[36:39], v[140:143]\n"
"v_mfma_f32_16x16x32_bf16 v[148:151], a[80:83], a[40:43], v[148:151]\n"
"v_mfma_f32_16x16x32_bf16 v[132:135], a[84:87], a[48:51], v[132:135]\n"
"ds_read_b64_tr_b16 a[76:77], v39 offset:36864\n"
"ds_read_b64_tr_b16 a[78:79], v39 offset:38400\n"
"v_mfma_f32_16x16x32_bf16 v[140:143], a[84:87], a[52:55], v[140:143]\n"
"v_mfma_f32_16x16x32_bf16 v[148:151], a[84:87], a[56:59], v[148:151]\n"
"v_mfma_f32_16x16x32_bf16 v[128:131], a[88:91], a[32:35], v[128:131]\n"
"ds_read_b64_tr_b16 a[80:81], v38 offset:24576\n"
"ds_read_b64_tr_b16 a[82:83], v38 offset:26112\n"
"v_mfma_f32_16x16x32_bf16 v[136:139], a[88:91], a[36:39], v[136:139]\n"
"v_mfma_f32_16x16x32_bf16 v[144:147], a[88:91], a[40:43], v[144:147]\n"
"s_waitcnt lgkmcnt(6)\n"
"v_mfma_f32_16x16x32_bf16 v[128:131], a[92:95], a[48:51], v[128:131]\n"
"ds_read_b64_tr_b16 a[84:85], v38 offset:36864\n"
"ds_read_b64_tr_b16 a[86:87], v38 offset:38400\n"
"v_mfma_f32_16x16x32_bf16 v[136:139], a[92:95], a[52:55], v[136:139]\n"
"v_mfma_f32_16x16x32_bf16 v[144:147], a[92:95], a[56:59], v[144:147]\n"
"v_mfma_f32_16x16x32_bf16 v[100:103], a[64:67], a[32:35], v[100:103]\n"
"ds_read_b64_tr_b16 a[88:89], v37 offset:24576\n"
"ds_read_b64_tr_b16 a[90:91], v37 offset:26112\n"
"v_mfma_f32_16x16x32_bf16 v[108:111], a[64:67], a[36:39], v[108:111]\n"
"v_mfma_f32_16x16x32_bf16 v[116:119], a[64:67], a[40:43], v[116:119]\n"
"v_mfma_f32_16x16x32_bf16 v[100:103], a[68:71], a[48:51], v[100:103]\n"
"ds_read_b64_tr_b16 a[92:93], v37 offset:36864\n"
"ds_read_b64_tr_b16 a[94:95], v37 offset:38400\n"
"v_mfma_f32_16x16x32_bf16 v[108:111], a[68:71], a[52:55], v[108:111]\n"
"v_mfma_f32_16x16x32_bf16 v[116:119], a[68:71], a[56:59], v[116:119]\n"
"s_add_u32 s88, s88, 786432\n"
"s_add_u32 s87, s87, 128\n"
"s_waitcnt lgkmcnt(6)\n"
"v_mfma_f32_16x16x32_bf16 v[96:99], a[72:75], a[32:35], v[96:99]\n"
"v_mfma_f32_16x16x32_bf16 v[104:107], a[72:75], a[36:39], v[104:107]\n"
"v_mfma_f32_16x16x32_bf16 v[112:115], a[72:75], a[40:43], v[112:115]\n"
"v_mfma_f32_16x16x32_bf16 v[96:99], a[76:79], a[48:51], v[96:99]\n"
"s_waitcnt lgkmcnt(0)\n"
"s_barrier\n"
"v_mfma_f32_16x16x32_bf16 v[104:107], a[76:79], a[52:55], v[104:107]\n"
"ds_read_b128 a[0:3], v41 offset:0\n"
"ds_read_b128 a[4:7], v41 offset:2048\n"
"ds_read_b128 a[8:11], v41 offset:4096\n"
"v_mfma_f32_16x16x32_bf16 v[112:115], a[76:79], a[56:59], v[112:115]\n"
"ds_read_b64_tr_b16 a[64:65], v40 offset:256\n"
"ds_read_b64_tr_b16 a[66:67], v40 offset:1792\n"
"ds_read_b128 a[16:19], v42 offset:0\n"
"v_mfma_f32_16x16x32_bf16 v[68:71], a[80:83], a[32:35], v[68:71]\n"
"ds_read_b128 a[20:23], v42 offset:2048\n"
"ds_read_b128 a[24:27], v42 offset:4096\n"
"ds_read_b64_tr_b16 a[68:69], v40 offset:12544\n"
"v_mfma_f32_16x16x32_bf16 v[76:79], a[80:83], a[36:39], v[76:79]\n"
"ds_read_b64_tr_b16 a[70:71], v40 offset:14080\n"
"ds_read_b64_tr_b16 a[72:73], v39 offset:256\n"
"ds_read_b64_tr_b16 a[74:75], v39 offset:1792\n"
"v_mfma_f32_16x16x32_bf16 v[84:87], a[80:83], a[40:43], v[84:87]\n"
"v_mfma_f32_16x16x32_bf16 v[68:71], a[84:87], a[48:51], v[68:71]\n"
"v_mfma_f32_16x16x32_bf16 v[76:79], a[84:87], a[52:55], v[76:79]\n"
"v_mfma_f32_16x16x32_bf16 v[84:87], a[84:87], a[56:59], v[84:87]\n"
"v_mfma_f32_16x16x32_bf16 v[64:67], a[88:91], a[32:35], v[64:67]\n"
"v_mfma_f32_16x16x32_bf16 v[72:75], a[88:91], a[36:39], v[72:75]\n"
"v_mfma_f32_16x16x32_bf16 v[80:83], a[88:91], a[40:43], v[80:83]\n"
"v_mfma_f32_16x16x32_bf16 v[64:67], a[92:95], a[48:51], v[64:67]\n"
"v_mfma_f32_16x16x32_bf16 v[72:75], a[92:95], a[52:55], v[72:75]\n"
"v_mfma_f32_16x16x32_bf16 v[80:83], a[92:95], a[56:59], v[80:83]\n"
"s_waitcnt lgkmcnt(0)\n"
"v_mfma_f32_16x16x32_bf16 v[228:231], a[64:67], a[0:3], v[228:231]\n"
"ds_read_b64_tr_b16 a[76:77], v39 offset:12544\n"
"ds_read_b64_tr_b16 a[78:79], v39 offset:14080\n"
"ds_read_b64_tr_b16 a[80:81], v38 offset:256\n"
"v_mfma_f32_16x16x32_bf16 v[236:239], a[64:67], a[4:7], v[236:239]\n"
"ds_read_b64_tr_b16 a[82:83], v38 offset:1792\n"
"ds_read_b64_tr_b16 a[84:85], v38 offset:12544\n"
"ds_read_b64_tr_b16 a[86:87], v38 offset:14080\n"
"v_mfma_f32_16x16x32_bf16 v[244:247], a[64:67], a[8:11], v[244:247]\n"
"s_waitcnt vmcnt(2)\n"
"buffer_load_dword v47, v46, s[40:43], s88 offen\n"
"v_mfma_f32_16x16x32_bf16 v[228:231], a[68:71], a[16:19], v[228:231]\n"
"ds_read_b64_tr_b16 a[88:89], v37 offset:256\n"
"ds_read_b64_tr_b16 a[90:91], v37 offset:1792\n"
"ds_read_b64_tr_b16 a[92:93], v37 offset:12544\n"
"v_mfma_f32_16x16x32_bf16 v[236:239], a[68:71], a[20:23], v[236:239]\n"
"ds_read_b64_tr_b16 a[94:95], v37 offset:14080\n"
"v_mfma_f32_16x16x32_bf16 v[244:247], a[68:71], a[24:27], v[244:247]\n"
"v_mfma_f32_16x16x32_bf16 v[224:227], a[72:75], a[0:3], v[224:227]\n"
"ds_read_b64_tr_b16 a[64:65], v40 offset:128\n"
"ds_read_b64_tr_b16 a[66:67], v40 offset:1664\n"
"v_mfma_f32_16x16x32_bf16 v[232:235], a[72:75], a[4:7], v[232:235]\n"
"v_mfma_f32_16x16x32_bf16 v[240:243], a[72:75], a[8:11], v[240:243]\n"
"s_waitcnt lgkmcnt(6)\n"
"v_mfma_f32_16x16x32_bf16 v[224:227], a[76:79], a[16:19], v[224:227]\n"
"ds_read_b64_tr_b16 a[68:69], v40 offset:12416\n"
"ds_read_b64_tr_b16 a[70:71], v40 offset:13952\n"
"v_mfma_f32_16x16x32_bf16 v[232:235], a[76:79], a[20:23], v[232:235]\n"
"v_mfma_f32_16x16x32_bf16 v[240:243], a[76:79], a[24:27], v[240:243]\n"
"v_mfma_f32_16x16x32_bf16 v[196:199], a[80:83], a[0:3], v[196:199]\n"
"ds_read_b64_tr_b16 a[72:73], v39 offset:128\n"
"ds_read_b64_tr_b16 a[74:75], v39 offset:1664\n"
"v_mfma_f32_16x16x32_bf16 v[204:207], a[80:83], a[4:7], v[204:207]\n"
"v_mfma_f32_16x16x32_bf16 v[212:215], a[80:83], a[8:11], v[212:215]\n"
"v_mfma_f32_16x16x32_bf16 v[196:199], a[84:87], a[16:19], v[196:199]\n"
"ds_read_b64_tr_b16 a[76:77], v39 offset:12416\n"
"ds_read_b64_tr_b16 a[78:79], v39 offset:13952\n"
"v_mfma_f32_16x16x32_bf16 v[204:207], a[84:87], a[20:23], v[204:207]\n"
"v_mfma_f32_16x16x32_bf16 v[212:215], a[84:87], a[24:27], v[212:215]\n"
"s_waitcnt lgkmcnt(6)\n"
"v_mfma_f32_16x16x32_bf16 v[192:195], a[88:91], a[0:3], v[192:195]\n"
"ds_read_b64_tr_b16 a[80:81], v38 offset:128\n"
"ds_read_b64_tr_b16 a[82:83], v38 offset:1664\n"
"v_mfma_f32_16x16x32_bf16 v[200:203], a[88:91], a[4:7], v[200:203]\n"
"v_mfma_f32_16x16x32_bf16 v[208:211], a[88:91], a[8:11], v[208:211]\n"
"v_mfma_f32_16x16x32_bf16 v[192:195], a[92:95], a[16:19], v[192:195]\n"
"ds_read_b64_tr_b16 a[84:85], v38 offset:12416\n"
"ds_read_b64_tr_b16 a[86:87], v38 offset:13952\n"
"v_mfma_f32_16x16x32_bf16 v[200:203], a[92:95], a[20:23], v[200:203]\n"
"v_mfma_f32_16x16x32_bf16 v[208:211], a[92:95], a[24:27], v[208:211]\n"
"v_mfma_f32_16x16x32_bf16 v[164:167], a[64:67], a[0:3], v[164:167]\n"
"ds_read_b64_tr_b16 a[88:89], v37 offset:128\n"
"ds_read_b64_tr_b16 a[90:91], v37 offset:1664\n"
"v_mfma_f32_16x16x32_bf16 v[172:175], a[64:67], a[4:7], v[172:175]\n"
"v_mfma_f32_16x16x32_bf16 v[180:183], a[64:67], a[8:11], v[180:183]\n"
"s_waitcnt lgkmcnt(6)\n"
"v_mfma_f32_16x16x32_bf16 v[164:167], a[68:71], a[16:19], v[164:167]\n"
"buffer_load_dword v47, v45, s[36:39], s87 offen\n"
"ds_read_b64_tr_b16 a[92:93], v37 offset:12416\n"
"ds_read_b64_tr_b16 a[94:95], v37 offset:13952\n"
"v_mfma_f32_16x16x32_bf16 v[172:175], a[68:71], a[20:23], v[172:175]\n"
"v_mfma_f32_16x16x32_bf16 v[180:183], a[68:71], a[24:27], v[180:183]\n"
"v_mfma_f32_16x16x32_bf16 v[160:163], a[72:75], a[0:3], v[160:163]\n"
"ds_read_b64_tr_b16 a[64:65], v40 offset:0\n"
"ds_read_b64_tr_b16 a[66:67], v40 offset:1536\n"
"v_mfma_f32_16x16x32_bf16 v[168:171], a[72:75], a[4:7], v[168:171]\n"
"v_mfma_f32_16x16x32_bf16 v[176:179], a[72:75], a[8:11], v[176:179]\n"
"v_mfma_f32_16x16x32_bf16 v[160:163], a[76:79], a[16:19], v[160:163]\n"
"ds_read_b64_tr_b16 a[68:69], v40 offset:12288\n"
"ds_read_b64_tr_b16 a[70:71], v40 offset:13824\n"
"v_mfma_f32_16x16x32_bf16 v[168:171], a[76:79], a[20:23], v[168:171]\n"
"v_mfma_f32_16x16x32_bf16 v[176:179], a[76:79], a[24:27], v[176:179]\n"
"s_waitcnt lgkmcnt(6)\n"
"v_mfma_f32_16x16x32_bf16 v[132:135], a[80:83], a[0:3], v[132:135]\n"
"ds_read_b64_tr_b16 a[72:73], v39 offset:0\n"
"ds_read_b64_tr_b16 a[74:75], v39 offset:1536\n"
"v_mfma_f32_16x16x32_bf16 v[140:143], a[80:83], a[4:7], v[140:143]\n"
"v_mfma_f32_16x16x32_bf16 v[148:151], a[80:83], a[8:11], v[148:151]\n"
"v_mfma_f32_16x16x32_bf16 v[132:135], a[84:87], a[16:19], v[132:135]\n"
"ds_read_b64_tr_b16 a[76:77], v39 offset:12288\n"
"ds_read_b64_tr_b16 a[78:79], v39 offset:13824\n"
"v_mfma_f32_16x16x32_bf16 v[140:143], a[84:87], a[20:23], v[140:143]\n"
"v_mfma_f32_16x16x32_bf16 v[148:151], a[84:87], a[24:27], v[148:151]\n"
"v_mfma_f32_16x16x32_bf16 v[128:131], a[88:91], a[0:3], v[128:131]\n"
"ds_read_b64_tr_b16 a[80:81], v38 offset:0\n"
"ds_read_b64_tr_b16 a[82:83], v38 offset:1536\n"
"v_mfma_f32_16x16x32_bf16 v[136:139], a[88:91], a[4:7], v[136:139]\n"
"v_mfma_f32_16x16x32_bf16 v[144:147], a[88:91], a[8:11], v[144:147]\n"
"s_waitcnt lgkmcnt(6)\n"
"v_mfma_f32_16x16x32_bf16 v[128:131], a[92:95], a[16:19], v[128:131]\n"
"ds_read_b64_tr_b16 a[84:85], v38 offset:12288\n"
"ds_read_b64_tr_b16 a[86:87], v38 offset:13824\n"
"v_mfma_f32_16x16x32_bf16 v[136:139], a[92:95], a[20:23], v[136:139]\n"
"v_mfma_f32_16x16x32_bf16 v[144:147], a[92:95], a[24:27], v[144:147]\n"
"v_mfma_f32_16x16x32_bf16 v[100:103], a[64:67], a[0:3], v[100:103]\n"
"ds_read_b64_tr_b16 a[88:89], v37 offset:0\n"
"ds_read_b64_tr_b16 a[90:91], v37 offset:1536\n"
"v_mfma_f32_16x16x32_bf16 v[108:111], a[64:67], a[4:7], v[108:111]\n"
"v_mfma_f32_16x16x32_bf16 v[116:119], a[64:67], a[8:11], v[116:119]\n"
"v_mfma_f32_16x16x32_bf16 v[100:103], a[68:71], a[16:19], v[100:103]\n"
"ds_read_b64_tr_b16 a[92:93], v37 offset:12288\n"
"ds_read_b64_tr_b16 a[94:95], v37 offset:13824\n"
"v_mfma_f32_16x16x32_bf16 v[108:111], a[68:71], a[20:23], v[108:111]\n"
"v_mfma_f32_16x16x32_bf16 v[116:119], a[68:71], a[24:27], v[116:119]\n"
"s_add_u32 s88, s88, 786432\n"
"s_add_u32 s87, s87, 128\n"
"s_waitcnt lgkmcnt(6)\n"
"v_mfma_f32_16x16x32_bf16 v[96:99], a[72:75], a[0:3], v[96:99]\n"
"v_mfma_f32_16x16x32_bf16 v[104:107], a[72:75], a[4:7], v[104:107]\n"
"v_mfma_f32_16x16x32_bf16 v[112:115], a[72:75], a[8:11], v[112:115]\n"
"v_mfma_f32_16x16x32_bf16 v[96:99], a[76:79], a[16:19], v[96:99]\n"
"s_waitcnt lgkmcnt(0)\n"
"s_barrier\n"
"v_mfma_f32_16x16x32_bf16 v[104:107], a[76:79], a[20:23], v[104:107]\n"
"ds_read_b128 a[32:35], v41 offset:6144\n"
"ds_read_b128 a[36:39], v41 offset:8192\n"
"ds_read_b128 a[40:43], v41 offset:10240\n"
"v_mfma_f32_16x16x32_bf16 v[112:115], a[76:79], a[24:27], v[112:115]\n"
"ds_read_b64_tr_b16 a[64:65], v40 offset:24832\n"
"ds_read_b64_tr_b16 a[66:67], v40 offset:26368\n"
"ds_read_b128 a[48:51], v42 offset:6144\n"
"v_mfma_f32_16x16x32_bf16 v[68:71], a[80:83], a[0:3], v[68:71]\n"
"ds_read_b128 a[52:55], v42 offset:8192\n"
"ds_read_b128 a[56:59], v42 offset:10240\n"
"ds_read_b64_tr_b16 a[68:69], v40 offset:37120\n"
"v_mfma_f32_16x16x32_bf16 v[76:79], a[80:83], a[4:7], v[76:79]\n"
"ds_read_b64_tr_b16 a[70:71], v40 offset:38656\n"
"ds_read_b64_tr_b16 a[72:73], v39 offset:24832\n"
"ds_read_b64_tr_b16 a[74:75], v39 offset:26368\n"
"v_mfma_f32_16x16x32_bf16 v[84:87], a[80:83], a[8:11], v[84:87]\n"
"v_mfma_f32_16x16x32_bf16 v[68:71], a[84:87], a[16:19], v[68:71]\n"
"v_mfma_f32_16x16x32_bf16 v[76:79], a[84:87], a[20:23], v[76:79]\n"
"v_mfma_f32_16x16x32_bf16 v[84:87], a[84:87], a[24:27], v[84:87]\n"
"v_mfma_f32_16x16x32_bf16 v[64:67], a[88:91], a[0:3], v[64:67]\n"
"v_mfma_f32_16x16x32_bf16 v[72:75], a[88:91], a[4:7], v[72:75]\n"
"v_mfma_f32_16x16x32_bf16 v[80:83], a[88:91], a[8:11], v[80:83]\n"
"v_mfma_f32_16x16x32_bf16 v[64:67], a[92:95], a[16:19], v[64:67]\n"
"v_mfma_f32_16x16x32_bf16 v[72:75], a[92:95], a[20:23], v[72:75]\n"
"v_mfma_f32_16x16x32_bf16 v[80:83], a[92:95], a[24:27], v[80:83]\n"
"s_waitcnt lgkmcnt(0)\n"
"v_mfma_f32_16x16x32_bf16 v[228:231], a[64:67], a[32:35], v[228:231]\n"
"ds_read_b64_tr_b16 a[76:77], v39 offset:37120\n"
"ds_read_b64_tr_b16 a[78:79], v39 offset:38656\n"
"ds_read_b64_tr_b16 a[80:81], v38 offset:24832\n"
"v_mfma_f32_16x16x32_bf16 v[236:239], a[64:67], a[36:39], v[236:239]\n"
"ds_read_b64_tr_b16 a[82:83], v38 offset:26368\n"
"ds_read_b64_tr_b16 a[84:85], v38 offset:37120\n"
"ds_read_b64_tr_b16 a[86:87], v38 offset:38656\n"
"v_mfma_f32_16x16x32_bf16 v[244:247], a[64:67], a[40:43], v[244:247]\n"
"s_waitcnt vmcnt(2)\n"
"buffer_load_dword v47, v46, s[40:43], s88 offen\n"
"v_mfma_f32_16x16x32_bf16 v[228:231], a[68:71], a[48:51], v[228:231]\n"
"ds_read_b64_tr_b16 a[88:89], v37 offset:24832\n"
"ds_read_b64_tr_b16 a[90:91], v37 offset:26368\n"
"ds_read_b64_tr_b16 a[92:93], v37 offset:37120\n"
"v_mfma_f32_16x16x32_bf16 v[236:239], a[68:71], a[52:55], v[236:239]\n"
"ds_read_b64_tr_b16 a[94:95], v37 offset:38656\n"
"v_mfma_f32_16x16x32_bf16 v[244:247], a[68:71], a[56:59], v[244:247]\n"
"v_mfma_f32_16x16x32_bf16 v[224:227], a[72:75], a[32:35], v[224:227]\n"
"ds_read_b64_tr_b16 a[64:65], v40 offset:24704\n"
"ds_read_b64_tr_b16 a[66:67], v40 offset:26240\n"
"v_mfma_f32_16x16x32_bf16 v[232:235], a[72:75], a[36:39], v[232:235]\n"
"v_mfma_f32_16x16x32_bf16 v[240:243], a[72:75], a[40:43], v[240:243]\n"
"s_waitcnt lgkmcnt(6)\n"
"v_mfma_f32_16x16x32_bf16 v[224:227], a[76:79], a[48:51], v[224:227]\n"
"ds_read_b64_tr_b16 a[68:69], v40 offset:36992\n"
"ds_read_b64_tr_b16 a[70:71], v40 offset:38528\n"
"v_mfma_f32_16x16x32_bf16 v[232:235], a[76:79], a[52:55], v[232:235]\n"
"v_mfma_f32_16x16x32_bf16 v[240:243], a[76:79], a[56:59], v[240:243]\n"
"v_mfma_f32_16x16x32_bf16 v[196:199], a[80:83], a[32:35], v[196:199]\n"
"ds_read_b64_tr_b16 a[72:73], v39 offset:24704\n"
"ds_read_b64_tr_b16 a[74:75], v39 offset:26240\n"
"v_mfma_f32_16x16x32_bf16 v[204:207], a[80:83], a[36:39], v[204:207]\n"
"v_mfma_f32_16x16x32_bf16 v[212:215], a[80:83], a[40:43], v[212:215]\n"
"v_mfma_f32_16x16x32_bf16 v[196:199], a[84:87], a[48:51], v[196:199]\n"
"ds_read_b64_tr_b16 a[76:77], v39 offset:36992\n"
"ds_read_b64_tr_b16 a[78:79], v39 offset:38528\n"
"v_mfma_f32_16x16x32_bf16 v[204:207], a[84:87], a[52:55], v[204:207]\n"
"v_mfma_f32_16x16x32_bf16 v[212:215], a[84:87], a[56:59], v[212:215]\n"
"s_waitcnt lgkmcnt(6)\n"
"v_mfma_f32_16x16x32_bf16 v[192:195], a[88:91], a[32:35], v[192:195]\n"
"ds_read_b64_tr_b16 a[80:81], v38 offset:24704\n"
"ds_read_b64_tr_b16 a[82:83], v38 offset:26240\n"
"v_mfma_f32_16x16x32_bf16 v[200:203], a[88:91], a[36:39], v[200:203]\n"
"v_mfma_f32_16x16x32_bf16 v[208:211], a[88:91], a[40:43], v[208:211]\n"
"v_mfma_f32_16x16x32_bf16 v[192:195], a[92:95], a[48:51], v[192:195]\n"
"ds_read_b64_tr_b16 a[84:85], v38 offset:36992\n"
"ds_read_b64_tr_b16 a[86:87], v38 offset:38528\n"
"v_mfma_f32_16x16x32_bf16 v[200:203], a[92:95], a[52:55], v[200:203]\n"
"v_mfma_f32_16x16x32_bf16 v[208:211], a[92:95], a[56:59], v[208:211]\n"
"v_mfma_f32_16x16x32_bf16 v[164:167], a[64:67], a[32:35], v[164:167]\n"
"ds_read_b64_tr_b16 a[88:89], v37 offset:24704\n"
"ds_read_b64_tr_b16 a[90:91], v37 offset:26240\n"
"v_mfma_f32_16x16x32_bf16 v[172:175], a[64:67], a[36:39], v[172:175]\n"
"v_mfma_f32_16x16x32_bf16 v[180:183], a[64:67], a[40:43], v[180:183]\n"
"s_waitcnt lgkmcnt(6)\n"
"v_mfma_f32_16x16x32_bf16 v[164:167], a[68:71], a[48:51], v[164:167]\n"
"buffer_load_dword v47, v45, s[36:39], s87 offen\n"
"ds_read_b64_tr_b16 a[92:93], v37 offset:36992\n"
"ds_read_b64_tr_b16 a[94:95], v37 offset:38528\n"
"v_mfma_f32_16x16x32_bf16 v[172:175], a[68:71], a[52:55], v[172:175]\n"
"v_mfma_f32_16x16x32_bf16 v[180:183], a[68:71], a[56:59], v[180:183]\n"
"v_mfma_f32_16x16x32_bf16 v[160:163], a[72:75], a[32:35], v[160:163]\n"
"ds_read_b64_tr_b16 a[64:65], v40 offset:24576\n"
"ds_read_b64_tr_b16 a[66:67], v40 offset:26112\n"
"v_mfma_f32_16x16x32_bf16 v[168:171], a[72:75], a[36:39], v[168:171]\n"
"v_mfma_f32_16x16x32_bf16 v[176:179], a[72:75], a[40:43], v[176:179]\n"
"v_mfma_f32_16x16x32_bf16 v[160:163], a[76:79], a[48:51], v[160:163]\n"
"ds_read_b64_tr_b16 a[68:69], v40 offset:36864\n"
"ds_read_b64_tr_b16 a[70:71], v40 offset:38400\n"
"v_mfma_f32_16x16x32_bf16 v[168:171], a[76:79], a[52:55], v[168:171]\n"
"v_mfma_f32_16x16x32_bf16 v[176:179], a[76:79], a[56:59], v[176:179]\n"
"s_waitcnt lgkmcnt(6)\n"
"v_mfma_f32_16x16x32_bf16 v[132:135], a[80:83], a[32:35], v[132:135]\n"
"ds_read_b64_tr_b16 a[72:73], v39 offset:24576\n"
"ds_read_b64_tr_b16 a[74:75], v39 offset:26112\n"
"v_mfma_f32_16x16x32_bf16 v[140:143], a[80:83], a[36:39], v[140:143]\n"
"v_mfma_f32_16x16x32_bf16 v[148:151], a[80:83], a[40:43], v[148:151]\n"
"v_mfma_f32_16x16x32_bf16 v[132:135], a[84:87], a[48:51], v[132:135]\n"
"ds_read_b64_tr_b16 a[76:77], v39 offset:36864\n"
"ds_read_b64_tr_b16 a[78:79], v39 offset:38400\n"
"v_mfma_f32_16x16x32_bf16 v[140:143], a[84:87], a[52:55], v[140:143]\n"
"v_mfma_f32_16x16x32_bf16 v[148:151], a[84:87], a[56:59], v[148:151]\n"
"v_mfma_f32_16x16x32_bf16 v[128:131], a[88:91], a[32:35], v[128:131]\n"
"ds_read_b64_tr_b16 a[80:81], v38 offset:24576\n"
"ds_read_b64_tr_b16 a[82:83], v38 offset:26112\n"
"v_mfma_f32_16x16x32_bf16 v[136:139], a[88:91], a[36:39], v[136:139]\n"
"v_mfma_f32_16x16x32_bf16 v[144:147], a[88:91], a[40:43], v[144:147]\n"
"s_waitcnt lgkmcnt(6)\n"
"v_mfma_f32_16x16x32_bf16 v[128:131], a[92:95], a[48:51], v[128:131]\n"
"ds_read_b64_tr_b16 a[84:85], v38 offset:36864\n"
"ds_read_b64_tr_b16 a[86:87], v38 offset:38400\n"
"v_mfma_f32_16x16x32_bf16 v[136:139], a[92:95], a[52:55], v[136:139]\n"
"v_mfma_f32_16x16x32_bf16 v[144:147], a[92:95], a[56:59], v[144:147]\n"
"v_mfma_f32_16x16x32_bf16 v[100:103], a[64:67], a[32:35], v[100:103]\n"
"ds_read_b64_tr_b16 a[88:89], v37 offset:24576\n"
"ds_read_b64_tr_b16 a[90:91], v37 offset:26112\n"
"v_mfma_f32_16x16x32_bf16 v[108:111], a[64:67], a[36:39], v[108:111]\n"
"v_mfma_f32_16x16x32_bf16 v[116:119], a[64:67], a[40:43], v[116:119]\n"
"v_mfma_f32_16x16x32_bf16 v[100:103], a[68:71], a[48:51], v[100:103]\n"
"ds_read_b64_tr_b16 a[92:93], v37 offset:36864\n"
"ds_read_b64_tr_b16 a[94:95], v37 offset:38400\n"
"v_mfma_f32_16x16x32_bf16 v[108:111], a[68:71], a[52:55], v[108:111]\n"
"v_mfma_f32_16x16x32_bf16 v[116:119], a[68:71], a[56:59], v[116:119]\n"
"s_add_u32 s88, s88, 786432\n"
"s_add_u32 s87, s87, 128\n"
"s_waitcnt lgkmcnt(6)\n"
"v_mfma_f32_16x16x32_bf16 v[96:99], a[72:75], a[32:35], v[96:99]\n"
"v_mfma_f32_16x16x32_bf16 v[104:107], a[72:75], a[36:39], v[104:107]\n"
"v_mfma_f32_16x16x32_bf16 v[112:115], a[72:75], a[40:43], v[112:115]\n"
"v_mfma_f32_16x16x32_bf16 v[96:99], a[76:79], a[48:51], v[96:99]\n"
"s_waitcnt lgkmcnt(0)\n"
"s_barrier\n"
"v_mfma_f32_16x16x32_bf16 v[104:107], a[76:79], a[52:55], v[104:107]\n"
"ds_read_b128 a[0:3], v41 offset:0\n"
"ds_read_b128 a[4:7], v41 offset:2048\n"
"ds_read_b128 a[8:11], v41 offset:4096\n"
"v_mfma_f32_16x16x32_bf16 v[112:115], a[76:79], a[56:59], v[112:115]\n"
"ds_read_b64_tr_b16 a[64:65], v40 offset:256\n"
"ds_read_b64_tr_b16 a[66:67], v40 offset:1792\n"
"ds_read_b128 a[16:19], v42 offset:0\n"
"v_mfma_f32_16x16x32_bf16 v[68:71], a[80:83], a[32:35], v[68:71]\n"
"ds_read_b128 a[20:23], v42 offset:2048\n"
"ds_read_b128 a[24:27], v42 offset:4096\n"
"ds_read_b64_tr_b16 a[68:69], v40 offset:12544\n"
"v_mfma_f32_16x16x32_bf16 v[76:79], a[80:83], a[36:39], v[76:79]\n"
"ds_read_b64_tr_b16 a[70:71], v40 offset:14080\n"
"ds_read_b64_tr_b16 a[72:73], v39 offset:256\n"
"ds_read_b64_tr_b16 a[74:75], v39 offset:1792\n"
"v_mfma_f32_16x16x32_bf16 v[84:87], a[80:83], a[40:43], v[84:87]\n"
"v_mfma_f32_16x16x32_bf16 v[68:71], a[84:87], a[48:51], v[68:71]\n"
"v_mfma_f32_16x16x32_bf16 v[76:79], a[84:87], a[52:55], v[76:79]\n"
"v_mfma_f32_16x16x32_bf16 v[84:87], a[84:87], a[56:59], v[84:87]\n"
"v_mfma_f32_16x16x32_bf16 v[64:67], a[88:91], a[32:35], v[64:67]\n"
"v_mfma_f32_16x16x32_bf16 v[72:75], a[88:91], a[36:39], v[72:75]\n"
"v_mfma_f32_16x16x32_bf16 v[80:83], a[88:91], a[40:43], v[80:83]\n"
"v_mfma_f32_16x16x32_bf16 v[64:67], a[92:95], a[48:51], v[64:67]\n"
"v_mfma_f32_16x16x32_bf16 v[72:75], a[92:95], a[52:55], v[72:75]\n"
"v_mfma_f32_16x16x32_bf16 v[80:83], a[92:95], a[56:59], v[80:83]\n"
"s_waitcnt lgkmcnt(0)\n"
"v_mfma_f32_16x16x32_bf16 v[228:231], a[64:67], a[0:3], v[228:231]\n"
"ds_read_b64_tr_b16 a[76:77], v39 offset:12544\n"
"ds_read_b64_tr_b16 a[78:79], v39 offset:14080\n"
"ds_read_b64_tr_b16 a[80:81], v38 offset:256\n"
"v_mfma_f32_16x16x32_bf16 v[236:239], a[64:67], a[4:7], v[236:239]\n"
"ds_read_b64_tr_b16 a[82:83], v38 offset:1792\n"
"ds_read_b64_tr_b16 a[84:85], v38 offset:12544\n"
"ds_read_b64_tr_b16 a[86:87], v38 offset:14080\n"
"v_mfma_f32_16x16x32_bf16 v[244:247], a[64:67], a[8:11], v[244:247]\n"
"s_waitcnt vmcnt(2)\n"
"buffer_load_dword v47, v46, s[40:43], s88 offen\n"
"v_mfma_f32_16x16x32_bf16 v[228:231], a[68:71], a[16:19], v[228:231]\n"
"ds_read_b64_tr_b16 a[88:89], v37 offset:256\n"
"ds_read_b64_tr_b16 a[90:91], v37 offset:1792\n"
"ds_read_b64_tr_b16 a[92:93], v37 offset:12544\n"
"v_mfma_f32_16x16x32_bf16 v[236:239], a[68:71], a[20:23], v[236:239]\n"
"ds_read_b64_tr_b16 a[94:95], v37 offset:14080\n"
"v_mfma_f32_16x16x32_bf16 v[244:247], a[68:71], a[24:27], v[244:247]\n"
"v_mfma_f32_16x16x32_bf16 v[224:227], a[72:75], a[0:3], v[224:227]\n"
"ds_read_b64_tr_b16 a[64:65], v40 offset:128\n"
"ds_read_b64_tr_b16 a[66:67], v40 offset:1664\n"
"v_mfma_f32_16x16x32_bf16 v[232:235], a[72:75], a[4:7], v[232:235]\n"
"v_mfma_f32_16x16x32_bf16 v[240:243], a[72:75], a[8:11], v[240:243]\n"
"s_waitcnt lgkmcnt(6)\n"
"v_mfma_f32_16x16x32_bf16 v[224:227], a[76:79], a[16:19], v[224:227]\n"
"ds_read_b64_tr_b16 a[68:69], v40 offset:12416\n"
"ds_read_b64_tr_b16 a[70:71], v40 offset:13952\n"
"v_mfma_f32_16x16x32_bf16 v[232:235], a[76:79], a[20:23], v[232:235]\n"
"v_mfma_f32_16x16x32_bf16 v[240:243], a[76:79], a[24:27], v[240:243]\n"
"v_mfma_f32_16x16x32_bf16 v[196:199], a[80:83], a[0:3], v[196:199]\n"
"ds_read_b64_tr_b16 a[72:73], v39 offset:128\n"
"ds_read_b64_tr_b16 a[74:75], v39 offset:1664\n"
"v_mfma_f32_16x16x32_bf16 v[204:207], a[80:83], a[4:7], v[204:207]\n"
"v_mfma_f32_16x16x32_bf16 v[212:215], a[80:83], a[8:11], v[212:215]\n"
"v_mfma_f32_16x16x32_bf16 v[196:199], a[84:87], a[16:19], v[196:199]\n"
"ds_read_b64_tr_b16 a[76:77], v39 offset:12416\n"
"ds_read_b64_tr_b16 a[78:79], v39 offset:13952\n"
"v_mfma_f32_16x16x32_bf16 v[204:207], a[84:87], a[20:23], v[204:207]\n"
"v_mfma_f32_16x16x32_bf16 v[212:215], a[84:87], a[24:27], v[212:215]\n"
"s_waitcnt lgkmcnt(6)\n"
"v_mfma_f32_16x16x32_bf16 v[192:195], a[88:91], a[0:3], v[192:195]\n"
"ds_read_b64_tr_b16 a[80:81], v38 offset:128\n"
"ds_read_b64_tr_b16 a[82:83], v38 offset:1664\n"
"v_mfma_f32_16x16x32_bf16 v[200:203], a[88:91], a[4:7], v[200:203]\n"
"v_mfma_f32_16x16x32_bf16 v[208:211], a[88:91], a[8:11], v[208:211]\n"
"v_mfma_f32_16x16x32_bf16 v[192:195], a[92:95], a[16:19], v[192:195]\n"
"ds_read_b64_tr_b16 a[84:85], v38 offset:12416\n"
"ds_read_b64_tr_b16 a[86:87], v38 offset:13952\n"
"v_mfma_f32_16x16x32_bf16 v[200:203], a[92:95], a[20:23], v[200:203]\n"
"v_mfma_f32_16x16x32_bf16 v[208:211], a[92:95], a[24:27], v[208:211]\n"
"v_mfma_f32_16x16x32_bf16 v[164:167], a[64:67], a[0:3], v[164:167]\n"
"ds_read_b64_tr_b16 a[88:89], v37 offset:128\n"
"ds_read_b64_tr_b16 a[90:91], v37 offset:1664\n"
"v_mfma_f32_16x16x32_bf16 v[172:175], a[64:67], a[4:7], v[172:175]\n"
"v_mfma_f32_16x16x32_bf16 v[180:183], a[64:67], a[8:11], v[180:183]\n"
"s_waitcnt lgkmcnt(6)\n"
"v_mfma_f32_16x16x32_bf16 v[164:167], a[68:71], a[16:19], v[164:167]\n"
"buffer_load_dword v47, v45, s[36:39], s87 offen\n"
"ds_read_b64_tr_b16 a[92:93], v37 offset:12416\n"
"ds_read_b64_tr_b16 a[94:95], v37 offset:13952\n"
"v_mfma_f32_16x16x32_bf16 v[172:175], a[68:71], a[20:23], v[172:175]\n"
"v_mfma_f32_16x16x32_bf16 v[180:183], a[68:71], a[24:27], v[180:183]\n"
"v_mfma_f32_16x16x32_bf16 v[160:163], a[72:75], a[0:3], v[160:163]\n"
"ds_read_b64_tr_b16 a[64:65], v40 offset:0\n"
"ds_read_b64_tr_b16 a[66:67], v40 offset:1536\n"
"v_mfma_f32_16x16x32_bf16 v[168:171], a[72:75], a[4:7], v[168:171]\n"
"v_mfma_f32_16x16x32_bf16 v[176:179], a[72:75], a[8:11], v[176:179]\n"
"v_mfma_f32_16x16x32_bf16 v[160:163], a[76:79], a[16:19], v[160:163]\n"
"ds_read_b64_tr_b16 a[68:69], v40 offset:12288\n"
"ds_read_b64_tr_b16 a[70:71], v40 offset:13824\n"
"v_mfma_f32_16x16x32_bf16 v[168:171], a[76:79], a[20:23], v[168:171]\n"
"v_mfma_f32_16x16x32_bf16 v[176:179], a[76:79], a[24:27], v[176:179]\n"
"s_waitcnt lgkmcnt(6)\n"
"v_mfma_f32_16x16x32_bf16 v[132:135], a[80:83], a[0:3], v[132:135]\n"
"ds_read_b64_tr_b16 a[72:73], v39 offset:0\n"
"ds_read_b64_tr_b16 a[74:75], v39 offset:1536\n"
"v_mfma_f32_16x16x32_bf16 v[140:143], a[80:83], a[4:7], v[140:143]\n"
"v_mfma_f32_16x16x32_bf16 v[148:151], a[80:83], a[8:11], v[148:151]\n"
"v_mfma_f32_16x16x32_bf16 v[132:135], a[84:87], a[16:19], v[132:135]\n"
"ds_read_b64_tr_b16 a[76:77], v39 offset:12288\n"
"ds_read_b64_tr_b16 a[78:79], v39 offset:13824\n"
"v_mfma_f32_16x16x32_bf16 v[140:143], a[84:87], a[20:23], v[140:143]\n"
"v_mfma_f32_16x16x32_bf16 v[148:151], a[84:87], a[24:27], v[148:151]\n"
"v_mfma_f32_16x16x32_bf16 v[128:131], a[88:91], a[0:3], v[128:131]\n"
"ds_read_b64_tr_b16 a[80:81], v38 offset:0\n"
"ds_read_b64_tr_b16 a[82:83], v38 offset:1536\n"
"v_mfma_f32_16x16x32_bf16 v[136:139], a[88:91], a[4:7], v[136:139]\n"
"v_mfma_f32_16x16x32_bf16 v[144:147], a[88:91], a[8:11], v[144:147]\n"
"s_waitcnt lgkmcnt(6)\n"
"v_mfma_f32_16x16x32_bf16 v[128:131], a[92:95], a[16:19], v[128:131]\n"
"ds_read_b64_tr_b16 a[84:85], v38 offset:12288\n"
"ds_read_b64_tr_b16 a[86:87], v38 offset:13824\n"
"v_mfma_f32_16x16x32_bf16 v[136:139], a[92:95], a[20:23], v[136:139]\n"
"v_mfma_f32_16x16x32_bf16 v[144:147], a[92:95], a[24:27], v[144:147]\n"
"v_mfma_f32_16x16x32_bf16 v[100:103], a[64:67], a[0:3], v[100:103]\n"
"ds_read_b64_tr_b16 a[88:89], v37 offset:0\n"
"ds_read_b64_tr_b16 a[90:91], v37 offset:1536\n"
"v_mfma_f32_16x16x32_bf16 v[108:111], a[64:67], a[4:7], v[108:111]\n"
"v_mfma_f32_16x16x32_bf16 v[116:119], a[64:67], a[8:11], v[116:119]\n"
"v_mfma_f32_16x16x32_bf16 v[100:103], a[68:71], a[16:19], v[100:103]\n"
"ds_read_b64_tr_b16 a[92:93], v37 offset:12288\n"
"ds_read_b64_tr_b16 a[94:95], v37 offset:13824\n"
"v_mfma_f32_16x16x32_bf16 v[108:111], a[68:71], a[20:23], v[108:111]\n"
"v_mfma_f32_16x16x32_bf16 v[116:119], a[68:71], a[24:27], v[116:119]\n"
"s_add_u32 s88, s88, 786432\n"
"s_add_u32 s87, s87, 128\n"
"s_waitcnt lgkmcnt(6)\n"
"v_mfma_f32_16x16x32_bf16 v[96:99], a[72:75], a[0:3], v[96:99]\n"
"v_mfma_f32_16x16x32_bf16 v[104:107], a[72:75], a[4:7], v[104:107]\n"
"v_mfma_f32_16x16x32_bf16 v[112:115], a[72:75], a[8:11], v[112:115]\n"
"v_mfma_f32_16x16x32_bf16 v[96:99], a[76:79], a[16:19], v[96:99]\n"
"s_waitcnt lgkmcnt(0)\n"
"s_barrier\n"
"v_mfma_f32_16x16x32_bf16 v[104:107], a[76:79], a[20:23], v[104:107]\n"
"ds_read_b128 a[32:35], v41 offset:6144\n"
"ds_read_b128 a[36:39], v41 offset:8192\n"
"ds_read_b128 a[40:43], v41 offset:10240\n"
"v_mfma_f32_16x16x32_bf16 v[112:115], a[76:79], a[24:27], v[112:115]\n"
"ds_read_b64_tr_b16 a[64:65], v40 offset:24832\n"
"ds_read_b64_tr_b16 a[66:67], v40 offset:26368\n"
"ds_read_b128 a[48:51], v42 offset:6144\n"
"v_mfma_f32_16x16x32_bf16 v[68:71], a[80:83], a[0:3], v[68:71]\n"
"ds_read_b128 a[52:55], v42 offset:8192\n"
"ds_read_b128 a[56:59], v42 offset:10240\n"
"ds_read_b64_tr_b16 a[68:69], v40 offset:37120\n"
"v_mfma_f32_16x16x32_bf16 v[76:79], a[80:83], a[4:7], v[76:79]\n"
"ds_read_b64_tr_b16 a[70:71], v40 offset:38656\n"
"ds_read_b64_tr_b16 a[72:73], v39 offset:24832\n"
"ds_read_b64_tr_b16 a[74:75], v39 offset:26368\n"
"v_mfma_f32_16x16x32_bf16 v[84:87], a[80:83], a[8:11], v[84:87]\n"
"v_mfma_f32_16x16x32_bf16 v[68:71], a[84:87], a[16:19], v[68:71]\n"
"v_mfma_f32_16x16x32_bf16 v[76:79], a[84:87], a[20:23], v[76:79]\n"
"v_mfma_f32_16x16x32_bf16 v[84:87], a[84:87], a[24:27], v[84:87]\n"
"v_mfma_f32_16x16x32_bf16 v[64:67], a[88:91], a[0:3], v[64:67]\n"
"v_mfma_f32_16x16x32_bf16 v[72:75], a[88:91], a[4:7], v[72:75]\n"
"v_mfma_f32_16x16x32_bf16 v[80:83], a[88:91], a[8:11], v[80:83]\n"
"v_mfma_f32_16x16x32_bf16 v[64:67], a[92:95], a[16:19], v[64:67]\n"
"v_mfma_f32_16x16x32_bf16 v[72:75], a[92:95], a[20:23], v[72:75]\n"
"v_mfma_f32_16x16x32_bf16 v[80:83], a[92:95], a[24:27], v[80:83]\n"
"s_waitcnt lgkmcnt(0)\n"
"v_mfma_f32_16x16x32_bf16 v[228:231], a[64:67], a[32:35], v[228:231]\n"
"ds_read_b64_tr_b16 a[76:77], v39 offset:37120\n"
"ds_read_b64_tr_b16 a[78:79], v39 offset:38656\n"
"ds_read_b64_tr_b16 a[80:81], v38 offset:24832\n"
"v_mfma_f32_16x16x32_bf16 v[236:239], a[64:67], a[36:39], v[236:239]\n"
"ds_read_b64_tr_b16 a[82:83], v38 offset:26368\n"
"ds_read_b64_tr_b16 a[84:85], v38 offset:37120\n"
"ds_read_b64_tr_b16 a[86:87], v38 offset:38656\n"
"v_mfma_f32_16x16x32_bf16 v[244:247], a[64:67], a[40:43], v[244:247]\n"
"s_waitcnt vmcnt(2)\n"
"buffer_load_dword v47, v46, s[40:43], s88 offen\n"
"v_mfma_f32_16x16x32_bf16 v[228:231], a[68:71], a[48:51], v[228:231]\n"
"ds_read_b64_tr_b16 a[88:89], v37 offset:24832\n"
"ds_read_b64_tr_b16 a[90:91], v37 offset:26368\n"
"ds_read_b64_tr_b16 a[92:93], v37 offset:37120\n"
"v_mfma_f32_16x16x32_bf16 v[236:239], a[68:71], a[52:55], v[236:239]\n"
"ds_read_b64_tr_b16 a[94:95], v37 offset:38656\n"
"v_mfma_f32_16x16x32_bf16 v[244:247], a[68:71], a[56:59], v[244:247]\n"
"v_mfma_f32_16x16x32_bf16 v[224:227], a[72:75], a[32:35], v[224:227]\n"
"ds_read_b64_tr_b16 a[64:65], v40 offset:24704\n"
"ds_read_b64_tr_b16 a[66:67], v40 offset:26240\n"
"v_mfma_f32_16x16x32_bf16 v[232:235], a[72:75], a[36:39], v[232:235]\n"
"v_mfma_f32_16x16x32_bf16 v[240:243], a[72:75], a[40:43], v[240:243]\n"
"s_waitcnt lgkmcnt(6)\n"
"v_mfma_f32_16x16x32_bf16 v[224:227], a[76:79], a[48:51], v[224:227]\n"
"ds_read_b64_tr_b16 a[68:69], v40 offset:36992\n"
"ds_read_b64_tr_b16 a[70:71], v40 offset:38528\n"
"v_mfma_f32_16x16x32_bf16 v[232:235], a[76:79], a[52:55], v[232:235]\n"
"v_mfma_f32_16x16x32_bf16 v[240:243], a[76:79], a[56:59], v[240:243]\n"
"v_mfma_f32_16x16x32_bf16 v[196:199], a[80:83], a[32:35], v[196:199]\n"
"ds_read_b64_tr_b16 a[72:73], v39 offset:24704\n"
"ds_read_b64_tr_b16 a[74:75], v39 offset:26240\n"
"v_mfma_f32_16x16x32_bf16 v[204:207], a[80:83], a[36:39], v[204:207]\n"
"v_mfma_f32_16x16x32_bf16 v[212:215], a[80:83], a[40:43], v[212:215]\n"
"v_mfma_f32_16x16x32_bf16 v[196:199], a[84:87], a[48:51], v[196:199]\n"
"ds_read_b64_tr_b16 a[76:77], v39 offset:36992\n"
"ds_read_b64_tr_b16 a[78:79], v39 offset:38528\n"
"v_mfma_f32_16x16x32_bf16 v[204:207], a[84:87], a[52:55], v[204:207]\n"
"v_mfma_f32_16x16x32_bf16 v[212:215], a[84:87], a[56:59], v[212:215]\n"
"s_waitcnt lgkmcnt(6)\n"
"v_mfma_f32_16x16x32_bf16 v[192:195], a[88:91], a[32:35], v[192:195]\n"
"ds_read_b64_tr_b16 a[80:81], v38 offset:24704\n"
"ds_read_b64_tr_b16 a[82:83], v38 offset:26240\n"
"v_mfma_f32_16x16x32_bf16 v[200:203], a[88:91], a[36:39], v[200:203]\n"
"v_mfma_f32_16x16x32_bf16 v[208:211], a[88:91], a[40:43], v[208:211]\n"
"v_mfma_f32_16x16x32_bf16 v[192:195], a[92:95], a[48:51], v[192:195]\n"
"ds_read_b64_tr_b16 a[84:85], v38 offset:36992\n"
"ds_read_b64_tr_b16 a[86:87], v38 offset:38528\n"
"v_mfma_f32_16x16x32_bf16 v[200:203], a[92:95], a[52:55], v[200:203]\n"
"v_mfma_f32_16x16x32_bf16 v[208:211], a[92:95], a[56:59], v[208:211]\n"
"v_mfma_f32_16x16x32_bf16 v[164:167], a[64:67], a[32:35], v[164:167]\n"
"ds_read_b64_tr_b16 a[88:89], v37 offset:24704\n"
"ds_read_b64_tr_b16 a[90:91], v37 offset:26240\n"
"v_mfma_f32_16x16x32_bf16 v[172:175], a[64:67], a[36:39], v[172:175]\n"
"v_mfma_f32_16x16x32_bf16 v[180:183], a[64:67], a[40:43], v[180:183]\n"
"s_waitcnt lgkmcnt(6)\n"
"v_mfma_f32_16x16x32_bf16 v[164:167], a[68:71], a[48:51], v[164:167]\n"
"buffer_load_dword v47, v45, s[36:39], s87 offen\n"
"ds_read_b64_tr_b16 a[92:93], v37 offset:36992\n"
"ds_read_b64_tr_b16 a[94:95], v37 offset:38528\n"
"v_mfma_f32_16x16x32_bf16 v[172:175], a[68:71], a[52:55], v[172:175]\n"
"v_mfma_f32_16x16x32_bf16 v[180:183], a[68:71], a[56:59], v[180:183]\n"
"v_mfma_f32_16x16x32_bf16 v[160:163], a[72:75], a[32:35], v[160:163]\n"
"ds_read_b64_tr_b16 a[64:65], v40 offset:24576\n"
"ds_read_b64_tr_b16 a[66:67], v40 offset:26112\n"
"v_mfma_f32_16x16x32_bf16 v[168:171], a[72:75], a[36:39], v[168:171]\n"
"v_mfma_f32_16x16x32_bf16 v[176:179], a[72:75], a[40:43], v[176:179]\n"
"v_mfma_f32_16x16x32_bf16 v[160:163], a[76:79], a[48:51], v[160:163]\n"
"ds_read_b64_tr_b16 a[68:69], v40 offset:36864\n"
"ds_read_b64_tr_b16 a[70:71], v40 offset:38400\n"
"v_mfma_f32_16x16x32_bf16 v[168:171], a[76:79], a[52:55], v[168:171]\n"
"v_mfma_f32_16x16x32_bf16 v[176:179], a[76:79], a[56:59], v[176:179]\n"
"s_waitcnt lgkmcnt(6)\n"
"v_mfma_f32_16x16x32_bf16 v[132:135], a[80:83], a[32:35], v[132:135]\n"
"ds_read_b64_tr_b16 a[72:73], v39 offset:24576\n"
"ds_read_b64_tr_b16 a[74:75], v39 offset:26112\n"
"v_mfma_f32_16x16x32_bf16 v[140:143], a[80:83], a[36:39], v[140:143]\n"
"v_mfma_f32_16x16x32_bf16 v[148:151], a[80:83], a[40:43], v[148:151]\n"
"v_mfma_f32_16x16x32_bf16 v[132:135], a[84:87], a[48:51], v[132:135]\n"
"ds_read_b64_tr_b16 a[76:77], v39 offset:36864\n"
"ds_read_b64_tr_b16 a[78:79], v39 offset:38400\n"
"v_mfma_f32_16x16x32_bf16 v[140:143], a[84:87], a[52:55], v[140:143]\n"
"v_mfma_f32_16x16x32_bf16 v[148:151], a[84:87], a[56:59], v[148:151]\n"
"v_mfma_f32_16x16x32_bf16 v[128:131], a[88:91], a[32:35], v[128:131]\n"
"ds_read_b64_tr_b16 a[80:81], v38 offset:24576\n"
"ds_read_b64_tr_b16 a[82:83], v38 offset:26112\n"
"v_mfma_f32_16x16x32_bf16 v[136:139], a[88:91], a[36:39], v[136:139]\n"
"v_mfma_f32_16x16x32_bf16 v[144:147], a[88:91], a[40:43], v[144:147]\n"
"s_waitcnt lgkmcnt(6)\n"
"v_mfma_f32_16x16x32_bf16 v[128:131], a[92:95], a[48:51], v[128:131]\n"
"ds_read_b64_tr_b16 a[84:85], v38 offset:36864\n"
"ds_read_b64_tr_b16 a[86:87], v38 offset:38400\n"
"v_mfma_f32_16x16x32_bf16 v[136:139], a[92:95], a[52:55], v[136:139]\n"
"v_mfma_f32_16x16x32_bf16 v[144:147], a[92:95], a[56:59], v[144:147]\n"
"v_mfma_f32_16x16x32_bf16 v[100:103], a[64:67], a[32:35], v[100:103]\n"
"ds_read_b64_tr_b16 a[88:89], v37 offset:24576\n"
"ds_read_b64_tr_b16 a[90:91], v37 offset:26112\n"
"v_mfma_f32_16x16x32_bf16 v[108:111], a[64:67], a[36:39], v[108:111]\n"
"v_mfma_f32_16x16x32_bf16 v[116:119], a[64:67], a[40:43], v[116:119]\n"
"v_mfma_f32_16x16x32_bf16 v[100:103], a[68:71], a[48:51], v[100:103]\n"
"ds_read_b64_tr_b16 a[92:93], v37 offset:36864\n"
"ds_read_b64_tr_b16 a[94:95], v37 offset:38400\n"
"v_mfma_f32_16x16x32_bf16 v[108:111], a[68:71], a[52:55], v[108:111]\n"
"v_mfma_f32_16x16x32_bf16 v[116:119], a[68:71], a[56:59], v[116:119]\n"
"s_add_u32 s88, s88, 786432\n"
"s_add_u32 s87, s87, 128\n"
"s_waitcnt lgkmcnt(6)\n"
"v_mfma_f32_16x16x32_bf16 v[96:99], a[72:75], a[32:35], v[96:99]\n"
"v_mfma_f32_16x16x32_bf16 v[104:107], a[72:75], a[36:39], v[104:107]\n"
"v_mfma_f32_16x16x32_bf16 v[112:115], a[72:75], a[40:43], v[112:115]\n"
"v_mfma_f32_16x16x32_bf16 v[96:99], a[76:79], a[48:51], v[96:99]\n"
"s_waitcnt lgkmcnt(0)\n"
"s_barrier\n"
"v_mfma_f32_16x16x32_bf16 v[104:107], a[76:79], a[52:55], v[104:107]\n"
"ds_read_b128 a[0:3], v41 offset:0\n"
"ds_read_b128 a[4:7], v41 offset:2048\n"
"ds_read_b128 a[8:11], v41 offset:4096\n"
"v_mfma_f32_16x16x32_bf16 v[112:115], a[76:79], a[56:59], v[112:115]\n"
"ds_read_b64_tr_b16 a[64:65], v40 offset:256\n"
"ds_read_b64_tr_b16 a[66:67], v40 offset:1792\n"
"ds_read_b128 a[16:19], v42 offset:0\n"
"v_mfma_f32_16x16x32_bf16 v[68:71], a[80:83], a[32:35], v[68:71]\n"
"ds_read_b128 a[20:23], v42 offset:2048\n"
"ds_read_b128 a[24:27], v42 offset:4096\n"
"ds_read_b64_tr_b16 a[68:69], v40 offset:12544\n"
"v_mfma_f32_16x16x32_bf16 v[76:79], a[80:83], a[36:39], v[76:79]\n"
"ds_read_b64_tr_b16 a[70:71], v40 offset:14080\n"
"ds_read_b64_tr_b16 a[72:73], v39 offset:256\n"
"ds_read_b64_tr_b16 a[74:75], v39 offset:1792\n"
"v_mfma_f32_16x16x32_bf16 v[84:87], a[80:83], a[40:43], v[84:87]\n"
"v_mfma_f32_16x16x32_bf16 v[68:71], a[84:87], a[48:51], v[68:71]\n"
"v_mfma_f32_16x16x32_bf16 v[76:79], a[84:87], a[52:55], v[76:79]\n"
"v_mfma_f32_16x16x32_bf16 v[84:87], a[84:87], a[56:59], v[84:87]\n"
"v_mfma_f32_16x16x32_bf16 v[64:67], a[88:91], a[32:35], v[64:67]\n"
"v_mfma_f32_16x16x32_bf16 v[72:75], a[88:91], a[36:39], v[72:75]\n"
"v_mfma_f32_16x16x32_bf16 v[80:83], a[88:91], a[40:43], v[80:83]\n"
"v_mfma_f32_16x16x32_bf16 v[64:67], a[92:95], a[48:51], v[64:67]\n"
"v_mfma_f32_16x16x32_bf16 v[72:75], a[92:95], a[52:55], v[72:75]\n"
"v_mfma_f32_16x16x32_bf16 v[80:83], a[92:95], a[56:59], v[80:83]\n"
"s_waitcnt lgkmcnt(0)\n"
"v_mfma_f32_16x16x32_bf16 v[228:231], a[64:67], a[0:3], v[228:231]\n"
"ds_read_b64_tr_b16 a[76:77], v39 offset:12544\n"
"ds_read_b64_tr_b16 a[78:79], v39 offset:14080\n"
"ds_read_b64_tr_b16 a[80:81], v38 offset:256\n"
"v_mfma_f32_16x16x32_bf16 v[236:239], a[64:67], a[4:7], v[236:239]\n"
"ds_read_b64_tr_b16 a[82:83], v38 offset:1792\n"
"ds_read_b64_tr_b16 a[84:85], v38 offset:12544\n"
"ds_read_b64_tr_b16 a[86:87], v38 offset:14080\n"
"v_mfma_f32_16x16x32_bf16 v[244:247], a[64:67], a[8:11], v[244:247]\n"
"s_waitcnt vmcnt(2)\n"
"s_mov_b32 m0, 73728\n"
"v_mfma_f32_16x16x32_bf16 v[228:231], a[68:71], a[16:19], v[228:231]\n"
"buffer_load_dwordx4 v13, s[44:47], s59 offen offset:0 nt lds\n"
"ds_read_b64_tr_b16 a[88:89], v37 offset:256\n"
"ds_read_b64_tr_b16 a[90:91], v37 offset:1792\n"
"v_mfma_f32_16x16x32_bf16 v[236:239], a[68:71], a[20:23], v[236:239]\n"
"buffer_load_dwordx4 v14, s[44:47], s59 offen offset:1024 nt lds\n"
"ds_read_b64_tr_b16 a[92:93], v37 offset:12544\n"
"ds_read_b64_tr_b16 a[94:95], v37 offset:14080\n"
"v_mfma_f32_16x16x32_bf16 v[244:247], a[68:71], a[24:27], v[244:247]\n"
"v_mfma_f32_16x16x32_bf16 v[224:227], a[72:75], a[0:3], v[224:227]\n"
"buffer_load_dwordx4 v15, s[44:47], s59 offen offset:2048 nt lds\n"
"ds_read_b64_tr_b16 a[64:65], v40 offset:128\n"
"ds_read_b64_tr_b16 a[66:67], v40 offset:1664\n"
"v_mfma_f32_16x16x32_bf16 v[232:235], a[72:75], a[4:7], v[232:235]\n"
"v_mfma_f32_16x16x32_bf16 v[240:243], a[72:75], a[8:11], v[240:243]\n"
"buffer_load_dwordx4 v16, s[44:47], s59 offen offset:3072 nt lds\n"
"s_waitcnt lgkmcnt(6)\n"
"v_mfma_f32_16x16x32_bf16 v[224:227], a[76:79], a[16:19], v[224:227]\n"
"ds_read_b64_tr_b16 a[68:69], v40 offset:12416\n"
"ds_read_b64_tr_b16 a[70:71], v40 offset:13952\n"
"v_mfma_f32_16x16x32_bf16 v[232:235], a[76:79], a[20:23], v[232:235]\n"
"s_mov_b32 m0, 77824\n"
"v_mfma_f32_16x16x32_bf16 v[240:243], a[76:79], a[24:27], v[240:243]\n"
"buffer_load_dwordx4 v17, s[44:47], s59 offen offset:0 nt lds\n"
"v_mfma_f32_16x16x32_bf16 v[196:199], a[80:83], a[0:3], v[196:199]\n"
"buffer_load_dwordx4 v18, s[44:47], s59 offen offset:1024 nt lds\n"
"ds_read_b64_tr_b16 a[72:73], v39 offset:128\n"
"ds_read_b64_tr_b16 a[74:75], v39 offset:1664\n"
"v_mfma_f32_16x16x32_bf16 v[204:207], a[80:83], a[4:7], v[204:207]\n"
"v_mfma_f32_16x16x32_bf16 v[212:215], a[80:83], a[8:11], v[212:215]\n"
"buffer_load_dwordx4 v19, s[44:47], s59 offen offset:2048 nt lds\n"
"v_mfma_f32_16x16x32_bf16 v[196:199], a[84:87], a[16:19], v[196:199]\n"
"ds_read_b64_tr_b16 a[76:77], v39 offset:12416\n"
"ds_read_b64_tr_b16 a[78:79], v39 offset:13952\n"
"v_mfma_f32_16x16x32_bf16 v[204:207], a[84:87], a[20:23], v[204:207]\n"
"buffer_load_dwordx4 v20, s[44:47], s59 offen offset:3072 nt lds\n"
"v_mfma_f32_16x16x32_bf16 v[212:215], a[84:87], a[24:27], v[212:215]\n"
"s_waitcnt lgkmcnt(6)\n"
"v_mfma_f32_16x16x32_bf16 v[192:195], a[88:91], a[0:3], v[192:195]\n"
"ds_read_b64_tr_b16 a[80:81], v38 offset:128\n"
"ds_read_b64_tr_b16 a[82:83], v38 offset:1664\n"
"v_mfma_f32_16x16x32_bf16 v[200:203], a[88:91], a[4:7], v[200:203]\n"
"s_mov_b32 m0, 81920\n"
"v_mfma_f32_16x16x32_bf16 v[208:211], a[88:91], a[8:11], v[208:211]\n"
"buffer_load_dwordx4 v21, s[44:47], s59 offen offset:0 nt lds\n"
"v_mfma_f32_16x16x32_bf16 v[192:195], a[92:95], a[16:19], v[192:195]\n"
"buffer_load_dwordx4 v22, s[44:47], s59 offen offset:1024 nt lds\n"
"ds_read_b64_tr_b16 a[84:85], v38 offset:12416\n"
"ds_read_b64_tr_b16 a[86:87], v38 offset:13952\n"
"v_mfma_f32_16x16x32_bf16 v[200:203], a[92:95], a[20:23], v[200:203]\n"
"v_mfma_f32_16x16x32_bf16 v[208:211], a[92:95], a[24:27], v[208:211]\n"
"buffer_load_dwordx4 v23, s[44:47], s59 offen offset:2048 nt lds\n"
"v_mfma_f32_16x16x32_bf16 v[164:167], a[64:67], a[0:3], v[164:167]\n"
"ds_read_b64_tr_b16 a[88:89], v37 offset:128\n"
"ds_read_b64_tr_b16 a[90:91], v37 offset:1664\n"
"v_mfma_f32_16x16x32_bf16 v[172:175], a[64:67], a[4:7], v[172:175]\n"
"buffer_load_dwordx4 v24, s[44:47], s59 offen offset:3072 nt lds\n"
"v_mfma_f32_16x16x32_bf16 v[180:183], a[64:67], a[8:11], v[180:183]\n"
"s_waitcnt lgkmcnt(6)\n"
"v_mfma_f32_16x16x32_bf16 v[164:167], a[68:71], a[16:19], v[164:167]\n"
"s_add_u32 s59, s59, 12288\n"
"s_mov_b32 m0, 0\n"
"ds_read_b64_tr_b16 a[92:93], v37 offset:12416\n"
"v_mfma_f32_16x16x32_bf16 v[172:175], a[68:71], a[20:23], v[172:175]\n"
"buffer_load_dwordx4 v25, s[48:51], s60 offen offset:0 nt lds\n"
"ds_read_b64_tr_b16 a[94:95], v37 offset:13952\n"
"v_mfma_f32_16x16x32_bf16 v[180:183], a[68:71], a[24:27], v[180:183]\n"
"buffer_load_dwordx4 v26, s[48:51], s60 offen offset:1024 nt lds\n"
"v_mfma_f32_16x16x32_bf16 v[160:163], a[72:75], a[0:3], v[160:163]\n"
"ds_read_b64_tr_b16 a[64:65], v40 offset:0\n"
"ds_read_b64_tr_b16 a[66:67], v40 offset:1536\n"
"v_mfma_f32_16x16x32_bf16 v[168:171], a[72:75], a[4:7], v[168:171]\n"
"buffer_load_dwordx4 v27, s[48:51], s60 offen offset:2048 nt lds\n"
"v_mfma_f32_16x16x32_bf16 v[176:179], a[72:75], a[8:11], v[176:179]\n"
"v_mfma_f32_16x16x32_bf16 v[160:163], a[76:79], a[16:19], v[160:163]\n"
"buffer_load_dwordx4 v28, s[48:51], s60 offen offset:3072 nt lds\n"
"ds_read_b64_tr_b16 a[68:69], v40 offset:12288\n"
"ds_read_b64_tr_b16 a[70:71], v40 offset:13824\n"
"v_mfma_f32_16x16x32_bf16 v[168:171], a[76:79], a[20:23], v[168:171]\n"
"v_mfma_f32_16x16x32_bf16 v[176:179], a[76:79], a[24:27], v[176:179]\n"
"s_waitcnt lgkmcnt(6)\n"
"v_mfma_f32_16x16x32_bf16 v[132:135], a[80:83], a[0:3], v[132:135]\n"
"s_mov_b32 m0, 4096\n"
"ds_read_b64_tr_b16 a[72:73], v39 offset:0\n"
"ds_read_b64_tr_b16 a[74:75], v39 offset:1536\n"
"v_mfma_f32_16x16x32_bf16 v[140:143], a[80:83], a[4:7], v[140:143]\n"
"buffer_load_dwordx4 v29, s[48:51], s60 offen offset:0 nt lds\n"
"v_mfma_f32_16x16x32_bf16 v[148:151], a[80:83], a[8:11], v[148:151]\n"
"buffer_load_dwordx4 v30, s[48:51], s60 offen offset:1024 nt lds\n"
"v_mfma_f32_16x16x32_bf16 v[132:135], a[84:87], a[16:19], v[132:135]\n"
"ds_read_b64_tr_b16 a[76:77], v39 offset:12288\n"
"ds_read_b64_tr_b16 a[78:79], v39 offset:13824\n"
"v_mfma_f32_16x16x32_bf16 v[140:143], a[84:87], a[20:23], v[140:143]\n"
"buffer_load_dwordx4 v31, s[48:51], s60 offen offset:2048 nt lds\n"
"v_mfma_f32_16x16x32_bf16 v[148:151], a[84:87], a[24:27], v[148:151]\n"
"v_mfma_f32_16x16x32_bf16 v[128:131], a[88:91], a[0:3], v[128:131]\n"
"buffer_load_dwordx4 v32, s[48:51], s60 offen offset:3072 nt lds\n"
"ds_read_b64_tr_b16 a[80:81], v38 offset:0\n"
"ds_read_b64_tr_b16 a[82:83], v38 offset:1536\n"
"v_mfma_f32_16x16x32_bf16 v[136:139], a[88:91], a[4:7], v[136:139]\n"
"v_mfma_f32_16x16x32_bf16 v[144:147], a[88:91], a[8:11], v[144:147]\n"
"s_mov_b32 m0, 8192\n"
"s_waitcnt lgkmcnt(6)\n"
"v_mfma_f32_16x16x32_bf16 v[128:131], a[92:95], a[16:19], v[128:131]\n"
"buffer_load_dwordx4 v33, s[48:51], s60 offen offset:0 nt lds\n"
"ds_read_b64_tr_b16 a[84:85], v38 offset:12288\n"
"ds_read_b64_tr_b16 a[86:87], v38 offset:13824\n"
"v_mfma_f32_16x16x32_bf16 v[136:139], a[92:95], a[20:23], v[136:139]\n"
"buffer_load_dwordx4 v34, s[48:51], s60 offen offset:1024 nt lds\n"
"v_mfma_f32_16x16x32_bf16 v[144:147], a[92:95], a[24:27], v[144:147]\n"
"v_mfma_f32_16x16x32_bf16 v[100:103], a[64:67], a[0:3], v[100:103]\n"
"buffer_load_dwordx4 v35, s[48:51], s60 offen offset:2048 nt lds\n"
"ds_read_b64_tr_b16 a[88:89], v37 offset:0\n"
"ds_read_b64_tr_b16 a[90:91], v37 offset:1536\n"
"v_mfma_f32_16x16x32_bf16 v[108:111], a[64:67], a[4:7], v[108:111]\n"
"v_mfma_f32_16x16x32_bf16 v[116:119], a[64:67], a[8:11], v[116:119]\n"
"buffer_load_dwordx4 v36, s[48:51], s60 offen offset:3072 nt lds\n"
"v_mfma_f32_16x16x32_bf16 v[100:103], a[68:71], a[16:19], v[100:103]\n"
"ds_read_b64_tr_b16 a[92:93], v37 offset:12288\n"
"ds_read_b64_tr_b16 a[94:95], v37 offset:13824\n"
"v_mfma_f32_16x16x32_bf16 v[108:111], a[68:71], a[20:23], v[108:111]\n"
"v_mfma_f32_16x16x32_bf16 v[116:119], a[68:71], a[24:27], v[116:119]\n"
"s_add_u32 s60, s60, 12288\n"
"s_waitcnt lgkmcnt(6)\n"
"v_mfma_f32_16x16x32_bf16 v[96:99], a[72:75], a[0:3], v[96:99]\n"
"v_mfma_f32_16x16x32_bf16 v[104:107], a[72:75], a[4:7], v[104:107]\n"
"v_mfma_f32_16x16x32_bf16 v[112:115], a[72:75], a[8:11], v[112:115]\n"
"v_mfma_f32_16x16x32_bf16 v[96:99], a[76:79], a[16:19], v[96:99]\n"
"s_waitcnt lgkmcnt(0)\n"
"s_barrier\n"
"v_mfma_f32_16x16x32_bf16 v[104:107], a[76:79], a[20:23], v[104:107]\n"
"ds_read_b128 a[32:35], v41 offset:6144\n"
"ds_read_b128 a[36:39], v41 offset:8192\n"
"ds_read_b128 a[40:43], v41 offset:10240\n"
"v_mfma_f32_16x16x32_bf16 v[112:115], a[76:79], a[24:27], v[112:115]\n"
"ds_read_b64_tr_b16 a[64:65], v40 offset:24832\n"
"ds_read_b64_tr_b16 a[66:67], v40 offset:26368\n"
"ds_read_b128 a[48:51], v42 offset:6144\n"
"v_mfma_f32_16x16x32_bf16 v[68:71], a[80:83], a[0:3], v[68:71]\n"
"ds_read_b128 a[52:55], v42 offset:8192\n"
"ds_read_b128 a[56:59], v42 offset:10240\n"
"ds_read_b64_tr_b16 a[68:69], v40 offset:37120\n"
"v_mfma_f32_16x16x32_bf16 v[76:79], a[80:83], a[4:7], v[76:79]\n"
"ds_read_b64_tr_b16 a[70:71], v40 offset:38656\n"
"ds_read_b64_tr_b16 a[72:73], v39 offset:24832\n"
"ds_read_b64_tr_b16 a[74:75], v39 offset:26368\n"
"v_mfma_f32_16x16x32_bf16 v[84:87], a[80:83], a[8:11], v[84:87]\n"
"v_mfma_f32_16x16x32_bf16 v[68:71], a[84:87], a[16:19], v[68:71]\n"
"v_mfma_f32_16x16x32_bf16 v[76:79], a[84:87], a[20:23], v[76:79]\n"
"v_mfma_f32_16x16x32_bf16 v[84:87], a[84:87], a[24:27], v[84:87]\n"
"v_mfma_f32_16x16x32_bf16 v[64:67], a[88:91], a[0:3], v[64:67]\n"
"v_mfma_f32_16x16x32_bf16 v[72:75], a[88:91], a[4:7], v[72:75]\n"
"v_mfma_f32_16x16x32_bf16 v[80:83], a[88:91], a[8:11], v[80:83]\n"
"v_mfma_f32_16x16x32_bf16 v[64:67], a[92:95], a[16:19], v[64:67]\n"
"v_mfma_f32_16x16x32_bf16 v[72:75], a[92:95], a[20:23], v[72:75]\n"
"v_mfma_f32_16x16x32_bf16 v[80:83], a[92:95], a[24:27], v[80:83]\n"
"s_waitcnt lgkmcnt(0)\n"
"v_mfma_f32_16x16x32_bf16 v[228:231], a[64:67], a[32:35], v[228:231]\n"
"ds_read_b64_tr_b16 a[76:77], v39 offset:37120\n"
"ds_read_b64_tr_b16 a[78:79], v39 offset:38656\n"
"ds_read_b64_tr_b16 a[80:81], v38 offset:24832\n"
"v_mfma_f32_16x16x32_bf16 v[236:239], a[64:67], a[36:39], v[236:239]\n"
"ds_read_b64_tr_b16 a[82:83], v38 offset:26368\n"
"ds_read_b64_tr_b16 a[84:85], v38 offset:37120\n"
"ds_read_b64_tr_b16 a[86:87], v38 offset:38656\n"
"v_mfma_f32_16x16x32_bf16 v[244:247], a[64:67], a[40:43], v[244:247]\n"
"s_waitcnt vmcnt(24)\n"
"s_mov_b32 m0, 86016\n"
"v_mfma_f32_16x16x32_bf16 v[228:231], a[68:71], a[48:51], v[228:231]\n"
"buffer_load_dwordx4 v13, s[44:47], s59 offen offset:0 nt lds\n"
"ds_read_b64_tr_b16 a[88:89], v37 offset:24832\n"
"ds_read_b64_tr_b16 a[90:91], v37 offset:26368\n"
"v_mfma_f32_16x16x32_bf16 v[236:239], a[68:71], a[52:55], v[236:239]\n"
"buffer_load_dwordx4 v14, s[44:47], s59 offen offset:1024 nt lds\n"
"ds_read_b64_tr_b16 a[92:93], v37 offset:37120\n"
"ds_read_b64_tr_b16 a[94:95], v37 offset:38656\n"
"v_mfma_f32_16x16x32_bf16 v[244:247], a[68:71], a[56:59], v[244:247]\n"
"v_mfma_f32_16x16x32_bf16 v[224:227], a[72:75], a[32:35], v[224:227]\n"
"buffer_load_dwordx4 v15, s[44:47], s59 offen offset:2048 nt lds\n"
"ds_read_b64_tr_b16 a[64:65], v40 offset:24704\n"
"ds_read_b64_tr_b16 a[66:67], v40 offset:26240\n"
"v_mfma_f32_16x16x32_bf16 v[232:235], a[72:75], a[36:39], v[232:235]\n"
"v_mfma_f32_16x16x32_bf16 v[240:243], a[72:75], a[40:43], v[240:243]\n"
"buffer_load_dwordx4 v16, s[44:47], s59 offen offset:3072 nt lds\n"
"s_waitcnt lgkmcnt(6)\n"
"v_mfma_f32_16x16x32_bf16 v[224:227], a[76:79], a[48:51], v[224:227]\n"
"ds_read_b64_tr_b16 a[68:69], v40 offset:36992\n"
"ds_read_b64_tr_b16 a[70:71], v40 offset:38528\n"
"v_mfma_f32_16x16x32_bf16 v[232:235], a[76:79], a[52:55], v[232:235]\n"
"s_mov_b32 m0, 90112\n"
"v_mfma_f32_16x16x32_bf16 v[240:243], a[76:79], a[56:59], v[240:243]\n"
"buffer_load_dwordx4 v17, s[44:47], s59 offen offset:0 nt lds\n"
"v_mfma_f32_16x16x32_bf16 v[196:199], a[80:83], a[32:35], v[196:199]\n"
"buffer_load_dwordx4 v18, s[44:47], s59 offen offset:1024 nt lds\n"
"ds_read_b64_tr_b16 a[72:73], v39 offset:24704\n"
"ds_read_b64_tr_b16 a[74:75], v39 offset:26240\n"
"v_mfma_f32_16x16x32_bf16 v[204:207], a[80:83], a[36:39], v[204:207]\n"
"v_mfma_f32_16x16x32_bf16 v[212:215], a[80:83], a[40:43], v[212:215]\n"
"buffer_load_dwordx4 v19, s[44:47], s59 offen offset:2048 nt lds\n"
"v_mfma_f32_16x16x32_bf16 v[196:199], a[84:87], a[48:51], v[196:199]\n"
"ds_read_b64_tr_b16 a[76:77], v39 offset:36992\n"
"ds_read_b64_tr_b16 a[78:79], v39 offset:38528\n"
"v_mfma_f32_16x16x32_bf16 v[204:207], a[84:87], a[52:55], v[204:207]\n"
"buffer_load_dwordx4 v20, s[44:47], s59 offen offset:3072 nt lds\n"
"v_mfma_f32_16x16x32_bf16 v[212:215], a[84:87], a[56:59], v[212:215]\n"
"s_waitcnt lgkmcnt(6)\n"
"v_mfma_f32_16x16x32_bf16 v[192:195], a[88:91], a[32:35], v[192:195]\n"
"ds_read_b64_tr_b16 a[80:81], v38 offset:24704\n"
"ds_read_b64_tr_b16 a[82:83], v38 offset:26240\n"
"v_mfma_f32_16x16x32_bf16 v[200:203], a[88:91], a[36:39], v[200:203]\n"
"s_mov_b32 m0, 94208\n"
"v_mfma_f32_16x16x32_bf16 v[208:211], a[88:91], a[40:43], v[208:211]\n"
"buffer_load_dwordx4 v21, s[44:47], s59 offen offset:0 nt lds\n"
"v_mfma_f32_16x16x32_bf16 v[192:195], a[92:95], a[48:51], v[192:195]\n"
"buffer_load_dwordx4 v22, s[44:47], s59 offen offset:1024 nt lds\n"
"ds_read_b64_tr_b16 a[84:85], v38 offset:36992\n"
"ds_read_b64_tr_b16 a[86:87], v38 offset:38528\n"
"v_mfma_f32_16x16x32_bf16 v[200:203], a[92:95], a[52:55], v[200:203]\n"
"v_mfma_f32_16x16x32_bf16 v[208:211], a[92:95], a[56:59], v[208:211]\n"
"buffer_load_dwordx4 v23, s[44:47], s59 offen offset:2048 nt lds\n"
"v_mfma_f32_16x16x32_bf16 v[164:167], a[64:67], a[32:35], v[164:167]\n"
"ds_read_b64_tr_b16 a[88:89], v37 offset:24704\n"
"ds_read_b64_tr_b16 a[90:91], v37 offset:26240\n"
"v_mfma_f32_16x16x32_bf16 v[172:175], a[64:67], a[36:39], v[172:175]\n"
"buffer_load_dwordx4 v24, s[44:47], s59 offen offset:3072 nt lds\n"
"v_mfma_f32_16x16x32_bf16 v[180:183], a[64:67], a[40:43], v[180:183]\n"
"s_waitcnt lgkmcnt(6)\n"
"v_mfma_f32_16x16x32_bf16 v[164:167], a[68:71], a[48:51], v[164:167]\n"
"s_add_u32 s59, s59, 12288\n"
"s_mov_b32 m0, 12288\n"
"ds_read_b64_tr_b16 a[92:93], v37 offset:36992\n"
"v_mfma_f32_16x16x32_bf16 v[172:175], a[68:71], a[52:55], v[172:175]\n"
"buffer_load_dwordx4 v25, s[48:51], s60 offen offset:0 nt lds\n"
"ds_read_b64_tr_b16 a[94:95], v37 offset:38528\n"
"v_mfma_f32_16x16x32_bf16 v[180:183], a[68:71], a[56:59], v[180:183]\n"
"buffer_load_dwordx4 v26, s[48:51], s60 offen offset:1024 nt lds\n"
"v_mfma_f32_16x16x32_bf16 v[160:163], a[72:75], a[32:35], v[160:163]\n"
"ds_read_b64_tr_b16 a[64:65], v40 offset:24576\n"
"ds_read_b64_tr_b16 a[66:67], v40 offset:26112\n"
"v_mfma_f32_16x16x32_bf16 v[168:171], a[72:75], a[36:39], v[168:171]\n"
"buffer_load_dwordx4 v27, s[48:51], s60 offen offset:2048 nt lds\n"
"v_mfma_f32_16x16x32_bf16 v[176:179], a[72:75], a[40:43], v[176:179]\n"
"v_mfma_f32_16x16x32_bf16 v[160:163], a[76:79], a[48:51], v[160:163]\n"
"buffer_load_dwordx4 v28, s[48:51], s60 offen offset:3072 nt lds\n"
"ds_read_b64_tr_b16 a[68:69], v40 offset:36864\n"
"ds_read_b64_tr_b16 a[70:71], v40 offset:38400\n"
"v_mfma_f32_16x16x32_bf16 v[168:171], a[76:79], a[52:55], v[168:171]\n"
"v_mfma_f32_16x16x32_bf16 v[176:179], a[76:79], a[56:59], v[176:179]\n"
"s_waitcnt lgkmcnt(6)\n"
"v_mfma_f32_16x16x32_bf16 v[132:135], a[80:83], a[32:35], v[132:135]\n"
"s_mov_b32 m0, 16384\n"
"ds_read_b64_tr_b16 a[72:73], v39 offset:24576\n"
"ds_read_b64_tr_b16 a[74:75], v39 offset:26112\n"
"v_mfma_f32_16x16x32_bf16 v[140:143], a[80:83], a[36:39], v[140:143]\n"
"buffer_load_dwordx4 v29, s[48:51], s60 offen offset:0 nt lds\n"
"v_mfma_f32_16x16x32_bf16 v[148:151], a[80:83], a[40:43], v[148:151]\n"
"buffer_load_dwordx4 v30, s[48:51], s60 offen offset:1024 nt lds\n"
"v_mfma_f32_16x16x32_bf16 v[132:135], a[84:87], a[48:51], v[132:135]\n"
"ds_read_b64_tr_b16 a[76:77], v39 offset:36864\n"
"ds_read_b64_tr_b16 a[78:79], v39 offset:38400\n"
"v_mfma_f32_16x16x32_bf16 v[140:143], a[84:87], a[52:55], v[140:143]\n"
"buffer_load_dwordx4 v31, s[48:51], s60 offen offset:2048 nt lds\n"
"v_mfma_f32_16x16x32_bf16 v[148:151], a[84:87], a[56:59], v[148:151]\n"
"v_mfma_f32_16x16x32_bf16 v[128:131], a[88:91], a[32:35], v[128:131]\n"
"buffer_load_dwordx4 v32, s[48:51], s60 offen offset:3072 nt lds\n"
"ds_read_b64_tr_b16 a[80:81], v38 offset:24576\n"
"ds_read_b64_tr_b16 a[82:83], v38 offset:26112\n"
"v_mfma_f32_16x16x32_bf16 v[136:139], a[88:91], a[36:39], v[136:139]\n"
"v_mfma_f32_16x16x32_bf16 v[144:147], a[88:91], a[40:43], v[144:147]\n"
"s_mov_b32 m0, 20480\n"
"s_waitcnt lgkmcnt(6)\n"
"v_mfma_f32_16x16x32_bf16 v[128:131], a[92:95], a[48:51], v[128:131]\n"
"buffer_load_dwordx4 v33, s[48:51], s60 offen offset:0 nt lds\n"
"ds_read_b64_tr_b16 a[84:85], v38 offset:36864\n"
"ds_read_b64_tr_b16 a[86:87], v38 offset:38400\n"
"v_mfma_f32_16x16x32_bf16 v[136:139], a[92:95], a[52:55], v[136:139]\n"
"buffer_load_dwordx4 v34, s[48:51], s60 offen offset:1024 nt lds\n"
"v_mfma_f32_16x16x32_bf16 v[144:147], a[92:95], a[56:59], v[144:147]\n"
"v_mfma_f32_16x16x32_bf16 v[100:103], a[64:67], a[32:35], v[100:103]\n"
"buffer_load_dwordx4 v35, s[48:51], s60 offen offset:2048 nt lds\n"
"ds_read_b64_tr_b16 a[88:89], v37 offset:24576\n"
"ds_read_b64_tr_b16 a[90:91], v37 offset:26112\n"
"v_mfma_f32_16x16x32_bf16 v[108:111], a[64:67], a[36:39], v[108:111]\n"
"v_mfma_f32_16x16x32_bf16 v[116:119], a[64:67], a[40:43], v[116:119]\n"
"buffer_load_dwordx4 v36, s[48:51], s60 offen offset:3072 nt lds\n"
"v_mfma_f32_16x16x32_bf16 v[100:103], a[68:71], a[48:51], v[100:103]\n"
"ds_read_b64_tr_b16 a[92:93], v37 offset:36864\n"
"ds_read_b64_tr_b16 a[94:95], v37 offset:38400\n"
"v_mfma_f32_16x16x32_bf16 v[108:111], a[68:71], a[52:55], v[108:111]\n"
"v_mfma_f32_16x16x32_bf16 v[116:119], a[68:71], a[56:59], v[116:119]\n"
"s_add_u32 s60, s60, 12288\n"
"s_waitcnt lgkmcnt(6)\n"
"v_mfma_f32_16x16x32_bf16 v[96:99], a[72:75], a[32:35], v[96:99]\n"
"v_mfma_f32_16x16x32_bf16 v[104:107], a[72:75], a[36:39], v[104:107]\n"
"v_mfma_f32_16x16x32_bf16 v[112:115], a[72:75], a[40:43], v[112:115]\n"
"v_mfma_f32_16x16x32_bf16 v[96:99], a[76:79], a[48:51], v[96:99]\n"
"s_waitcnt lgkmcnt(0)\n"
"s_barrier\n"
"v_mfma_f32_16x16x32_bf16 v[104:107], a[76:79], a[52:55], v[104:107]\n"
"ds_read_b128 a[0:3], v41 offset:0\n"
"ds_read_b128 a[4:7], v41 offset:2048\n"
"ds_read_b128 a[8:11], v41 offset:4096\n"
"v_mfma_f32_16x16x32_bf16 v[112:115], a[76:79], a[56:59], v[112:115]\n"
"ds_read_b64_tr_b16 a[64:65], v40 offset:256\n"
"ds_read_b64_tr_b16 a[66:67], v40 offset:1792\n"
"ds_read_b128 a[16:19], v42 offset:0\n"
"v_mfma_f32_16x16x32_bf16 v[68:71], a[80:83], a[32:35], v[68:71]\n"
"ds_read_b128 a[20:23], v42 offset:2048\n"
"ds_read_b128 a[24:27], v42 offset:4096\n"
"ds_read_b64_tr_b16 a[68:69], v40 offset:12544\n"
"v_mfma_f32_16x16x32_bf16 v[76:79], a[80:83], a[36:39], v[76:79]\n"
"ds_read_b64_tr_b16 a[70:71], v40 offset:14080\n"
"ds_read_b64_tr_b16 a[72:73], v39 offset:256\n"
"ds_read_b64_tr_b16 a[74:75], v39 offset:1792\n"
"v_mfma_f32_16x16x32_bf16 v[84:87], a[80:83], a[40:43], v[84:87]\n"
"v_mfma_f32_16x16x32_bf16 v[68:71], a[84:87], a[48:51], v[68:71]\n"
"v_mfma_f32_16x16x32_bf16 v[76:79], a[84:87], a[52:55], v[76:79]\n"
"v_mfma_f32_16x16x32_bf16 v[84:87], a[84:87], a[56:59], v[84:87]\n"
"v_mfma_f32_16x16x32_bf16 v[64:67], a[88:91], a[32:35], v[64:67]\n"
"v_mfma_f32_16x16x32_bf16 v[72:75], a[88:91], a[36:39], v[72:75]\n"
"v_mfma_f32_16x16x32_bf16 v[80:83], a[88:91], a[40:43], v[80:83]\n"
"v_mfma_f32_16x16x32_bf16 v[64:67], a[92:95], a[48:51], v[64:67]\n"
"v_mfma_f32_16x16x32_bf16 v[72:75], a[92:95], a[52:55], v[72:75]\n"
"v_mfma_f32_16x16x32_bf16 v[80:83], a[92:95], a[56:59], v[80:83]\n"
"s_waitcnt lgkmcnt(0)\n"
"v_mfma_f32_16x16x32_bf16 v[228:231], a[64:67], a[0:3], v[228:231]\n"
"ds_read_b64_tr_b16 a[76:77], v39 offset:12544\n"
"ds_read_b64_tr_b16 a[78:79], v39 offset:14080\n"
"ds_read_b64_tr_b16 a[80:81], v38 offset:256\n"
"v_mfma_f32_16x16x32_bf16 v[236:239], a[64:67], a[4:7], v[236:239]\n"
"ds_read_b64_tr_b16 a[82:83], v38 offset:1792\n"
"ds_read_b64_tr_b16 a[84:85], v38 offset:12544\n"
"ds_read_b64_tr_b16 a[86:87], v38 offset:14080\n"
"v_mfma_f32_16x16x32_bf16 v[244:247], a[64:67], a[8:11], v[244:247]\n"
"ds_read_b64_tr_b16 a[88:89], v37 offset:256\n"
"v_mfma_f32_16x16x32_bf16 v[228:231], a[68:71], a[16:19], v[228:231]\n"
"ds_read_b64_tr_b16 a[90:91], v37 offset:1792\n"
"ds_read_b64_tr_b16 a[92:93], v37 offset:12544\n"
"ds_read_b64_tr_b16 a[94:95], v37 offset:14080\n"
"v_mfma_f32_16x16x32_bf16 v[236:239], a[68:71], a[20:23], v[236:239]\n"
"v_mfma_f32_16x16x32_bf16 v[244:247], a[68:71], a[24:27], v[244:247]\n"
"v_mfma_f32_16x16x32_bf16 v[224:227], a[72:75], a[0:3], v[224:227]\n"
"ds_read_b64_tr_b16 a[64:65], v40 offset:128\n"
"ds_read_b64_tr_b16 a[66:67], v40 offset:1664\n"
"v_mfma_f32_16x16x32_bf16 v[232:235], a[72:75], a[4:7], v[232:235]\n"
"v_mfma_f32_16x16x32_bf16 v[240:243], a[72:75], a[8:11], v[240:243]\n"
"s_waitcnt lgkmcnt(6)\n"
"v_mfma_f32_16x16x32_bf16 v[224:227], a[76:79], a[16:19], v[224:227]\n"
"ds_read_b64_tr_b16 a[68:69], v40 offset:12416\n"
"ds_read_b64_tr_b16 a[70:71], v40 offset:13952\n"
"v_mfma_f32_16x16x32_bf16 v[232:235], a[76:79], a[20:23], v[232:235]\n"
"v_mfma_f32_16x16x32_bf16 v[240:243], a[76:79], a[24:27], v[240:243]\n"
"v_mfma_f32_16x16x32_bf16 v[196:199], a[80:83], a[0:3], v[196:199]\n"
"ds_read_b64_tr_b16 a[72:73], v39 offset:128\n"
"ds_read_b64_tr_b16 a[74:75], v39 offset:1664\n"
"v_mfma_f32_16x16x32_bf16 v[204:207], a[80:83], a[4:7], v[204:207]\n"
"v_mfma_f32_16x16x32_bf16 v[212:215], a[80:83], a[8:11], v[212:215]\n"
"v_mfma_f32_16x16x32_bf16 v[196:199], a[84:87], a[16:19], v[196:199]\n"
"ds_read_b64_tr_b16 a[76:77], v39 offset:12416\n"
"ds_read_b64_tr_b16 a[78:79], v39 offset:13952\n"
"v_mfma_f32_16x16x32_bf16 v[204:207], a[84:87], a[20:23], v[204:207]\n"
"v_mfma_f32_16x16x32_bf16 v[212:215], a[84:87], a[24:27], v[212:215]\n"
"s_waitcnt lgkmcnt(6)\n"
"v_mfma_f32_16x16x32_bf16 v[192:195], a[88:91], a[0:3], v[192:195]\n"
"ds_read_b64_tr_b16 a[80:81], v38 offset:128\n"
"ds_read_b64_tr_b16 a[82:83], v38 offset:1664\n"
"v_mfma_f32_16x16x32_bf16 v[200:203], a[88:91], a[4:7], v[200:203]\n"
"v_mfma_f32_16x16x32_bf16 v[208:211], a[88:91], a[8:11], v[208:211]\n"
"v_mfma_f32_16x16x32_bf16 v[192:195], a[92:95], a[16:19], v[192:195]\n"
"ds_read_b64_tr_b16 a[84:85], v38 offset:12416\n"
"ds_read_b64_tr_b16 a[86:87], v38 offset:13952\n"
"v_mfma_f32_16x16x32_bf16 v[200:203], a[92:95], a[20:23], v[200:203]\n"
"v_mfma_f32_16x16x32_bf16 v[208:211], a[92:95], a[24:27], v[208:211]\n"
"v_mfma_f32_16x16x32_bf16 v[164:167], a[64:67], a[0:3], v[164:167]\n"
"ds_read_b64_tr_b16 a[88:89], v37 offset:128\n"
"ds_read_b64_tr_b16 a[90:91], v37 offset:1664\n"
"v_mfma_f32_16x16x32_bf16 v[172:175], a[64:67], a[4:7], v[172:175]\n"
"v_mfma_f32_16x16x32_bf16 v[180:183], a[64:67], a[8:11], v[180:183]\n"
"s_waitcnt lgkmcnt(6)\n"
"v_mfma_f32_16x16x32_bf16 v[164:167], a[68:71], a[16:19], v[164:167]\n"
"ds_read_b64_tr_b16 a[92:93], v37 offset:12416\n"
"ds_read_b64_tr_b16 a[94:95], v37 offset:13952\n"
"v_mfma_f32_16x16x32_bf16 v[172:175], a[68:71], a[20:23], v[172:175]\n"
"v_mfma_f32_16x16x32_bf16 v[180:183], a[68:71], a[24:27], v[180:183]\n"
"v_mfma_f32_16x16x32_bf16 v[160:163], a[72:75], a[0:3], v[160:163]\n"
"ds_read_b64_tr_b16 a[64:65], v40 offset:0\n"
"ds_read_b64_tr_b16 a[66:67], v40 offset:1536\n"
"v_mfma_f32_16x16x32_bf16 v[168:171], a[72:75], a[4:7], v[168:171]\n"
"v_mfma_f32_16x16x32_bf16 v[176:179], a[72:75], a[8:11], v[176:179]\n"
"v_mfma_f32_16x16x32_bf16 v[160:163], a[76:79], a[16:19], v[160:163]\n"
"ds_read_b64_tr_b16 a[68:69], v40 offset:12288\n"
"ds_read_b64_tr_b16 a[70:71], v40 offset:13824\n"
"v_mfma_f32_16x16x32_bf16 v[168:171], a[76:79], a[20:23], v[168:171]\n"
"v_mfma_f32_16x16x32_bf16 v[176:179], a[76:79], a[24:27], v[176:179]\n"
"s_waitcnt lgkmcnt(6)\n"
"v_mfma_f32_16x16x32_bf16 v[132:135], a[80:83], a[0:3], v[132:135]\n"
"ds_read_b64_tr_b16 a[72:73], v39 offset:0\n"
"ds_read_b64_tr_b16 a[74:75], v39 offset:1536\n"
"v_mfma_f32_16x16x32_bf16 v[140:143], a[80:83], a[4:7], v[140:143]\n"
"v_mfma_f32_16x16x32_bf16 v[148:151], a[80:83], a[8:11], v[148:151]\n"
"v_mfma_f32_16x16x32_bf16 v[132:135], a[84:87], a[16:19], v[132:135]\n"
"ds_read_b64_tr_b16 a[76:77], v39 offset:12288\n"
"ds_read_b64_tr_b16 a[78:79], v39 offset:13824\n"
"v_mfma_f32_16x16x32_bf16 v[140:143], a[84:87], a[20:23], v[140:143]\n"
"v_mfma_f32_16x16x32_bf16 v[148:151], a[84:87], a[24:27], v[148:151]\n"
"v_mfma_f32_16x16x32_bf16 v[128:131], a[88:91], a[0:3], v[128:131]\n"
"ds_read_b64_tr_b16 a[80:81], v38 offset:0\n"
"ds_read_b64_tr_b16 a[82:83], v38 offset:1536\n"
"v_mfma_f32_16x16x32_bf16 v[136:139], a[88:91], a[4:7], v[136:139]\n"
"v_mfma_f32_16x16x32_bf16 v[144:147], a[88:91], a[8:11], v[144:147]\n"
"s_waitcnt lgkmcnt(6)\n"
"v_mfma_f32_16x16x32_bf16 v[128:131], a[92:95], a[16:19], v[128:131]\n"
"ds_read_b64_tr_b16 a[84:85], v38 offset:12288\n"
"ds_read_b64_tr_b16 a[86:87], v38 offset:13824\n"
"v_mfma_f32_16x16x32_bf16 v[136:139], a[92:95], a[20:23], v[136:139]\n"
"v_mfma_f32_16x16x32_bf16 v[144:147], a[92:95], a[24:27], v[144:147]\n"
"v_mfma_f32_16x16x32_bf16 v[100:103], a[64:67], a[0:3], v[100:103]\n"
"ds_read_b64_tr_b16 a[88:89], v37 offset:0\n"
"ds_read_b64_tr_b16 a[90:91], v37 offset:1536\n"
"v_mfma_f32_16x16x32_bf16 v[108:111], a[64:67], a[4:7], v[108:111]\n"
"v_mfma_f32_16x16x32_bf16 v[116:119], a[64:67], a[8:11], v[116:119]\n"
"v_mfma_f32_16x16x32_bf16 v[100:103], a[68:71], a[16:19], v[100:103]\n"
"ds_read_b64_tr_b16 a[92:93], v37 offset:12288\n"
"ds_read_b64_tr_b16 a[94:95], v37 offset:13824\n"
"v_mfma_f32_16x16x32_bf16 v[108:111], a[68:71], a[20:23], v[108:111]\n"
"v_mfma_f32_16x16x32_bf16 v[116:119], a[68:71], a[24:27], v[116:119]\n"
"s_waitcnt lgkmcnt(6)\n"
"v_mfma_f32_16x16x32_bf16 v[96:99], a[72:75], a[0:3], v[96:99]\n"
"v_mfma_f32_16x16x32_bf16 v[104:107], a[72:75], a[4:7], v[104:107]\n"
"v_mfma_f32_16x16x32_bf16 v[112:115], a[72:75], a[8:11], v[112:115]\n"
"v_mfma_f32_16x16x32_bf16 v[96:99], a[76:79], a[16:19], v[96:99]\n"
"s_waitcnt lgkmcnt(0)\n"
"s_barrier\n"
"v_mfma_f32_16x16x32_bf16 v[104:107], a[76:79], a[20:23], v[104:107]\n"
"ds_read_b128 a[32:35], v41 offset:6144\n"
"ds_read_b128 a[36:39], v41 offset:8192\n"
"ds_read_b128 a[40:43], v41 offset:10240\n"
"v_mfma_f32_16x16x32_bf16 v[112:115], a[76:79], a[24:27], v[112:115]\n"
"ds_read_b64_tr_b16 a[64:65], v40 offset:24832\n"
"ds_read_b64_tr_b16 a[66:67], v40 offset:26368\n"
"ds_read_b128 a[48:51], v42 offset:6144\n"
"v_mfma_f32_16x16x32_bf16 v[68:71], a[80:83], a[0:3], v[68:71]\n"
"ds_read_b128 a[52:55], v42 offset:8192\n"
"ds_read_b128 a[56:59], v42 offset:10240\n"
"ds_read_b64_tr_b16 a[68:69], v40 offset:37120\n"
"v_mfma_f32_16x16x32_bf16 v[76:79], a[80:83], a[4:7], v[76:79]\n"
"ds_read_b64_tr_b16 a[70:71], v40 offset:38656\n"
"ds_read_b64_tr_b16 a[72:73], v39 offset:24832\n"
"ds_read_b64_tr_b16 a[74:75], v39 offset:26368\n"
"v_mfma_f32_16x16x32_bf16 v[84:87], a[80:83], a[8:11], v[84:87]\n"
"v_mfma_f32_16x16x32_bf16 v[68:71], a[84:87], a[16:19], v[68:71]\n"
"v_mfma_f32_16x16x32_bf16 v[76:79], a[84:87], a[20:23], v[76:79]\n"
"v_mfma_f32_16x16x32_bf16 v[84:87], a[84:87], a[24:27], v[84:87]\n"
"v_mfma_f32_16x16x32_bf16 v[64:67], a[88:91], a[0:3], v[64:67]\n"
"v_mfma_f32_16x16x32_bf16 v[72:75], a[88:91], a[4:7], v[72:75]\n"
"v_mfma_f32_16x16x32_bf16 v[80:83], a[88:91], a[8:11], v[80:83]\n"
"v_mfma_f32_16x16x32_bf16 v[64:67], a[92:95], a[16:19], v[64:67]\n"
"v_mfma_f32_16x16x32_bf16 v[72:75], a[92:95], a[20:23], v[72:75]\n"
"v_mfma_f32_16x16x32_bf16 v[80:83], a[92:95], a[24:27], v[80:83]\n"
"s_waitcnt lgkmcnt(0)\n"
"v_mfma_f32_16x16x32_bf16 v[228:231], a[64:67], a[32:35], v[228:231]\n"
"ds_read_b64_tr_b16 a[76:77], v39 offset:37120\n"
"ds_read_b64_tr_b16 a[78:79], v39 offset:38656\n"
"ds_read_b64_tr_b16 a[80:81], v38 offset:24832\n"
"v_mfma_f32_16x16x32_bf16 v[236:239], a[64:67], a[36:39], v[236:239]\n"
"ds_read_b64_tr_b16 a[82:83], v38 offset:26368\n"
"ds_read_b64_tr_b16 a[84:85], v38 offset:37120\n"
"ds_read_b64_tr_b16 a[86:87], v38 offset:38656\n"
"v_mfma_f32_16x16x32_bf16 v[244:247], a[64:67], a[40:43], v[244:247]\n"
"s_waitcnt vmcnt(12)\n"
"v_mfma_f32_16x16x32_bf16 v[228:231], a[68:71], a[48:51], v[228:231]\n"
"s_mov_b32 m0, 98304\n"
"ds_read_b64_tr_b16 a[88:89], v37 offset:24832\n"
"ds_read_b64_tr_b16 a[90:91], v37 offset:26368\n"
"v_mfma_f32_16x16x32_bf16 v[236:239], a[68:71], a[52:55], v[236:239]\n"
"buffer_load_dwordx4 v13, s[44:47], s59 offen offset:0 nt lds\n"
"buffer_load_dwordx4 v14, s[44:47], s59 offen offset:1024 nt lds\n"
"buffer_load_dwordx4 v15, s[44:47], s59 offen offset:2048 nt lds\n"
"v_mfma_f32_16x16x32_bf16 v[244:247], a[68:71], a[56:59], v[244:247]\n"
"ds_read_b64_tr_b16 a[92:93], v37 offset:37120\n"
"ds_read_b64_tr_b16 a[94:95], v37 offset:38656\n"
"v_mfma_f32_16x16x32_bf16 v[224:227], a[72:75], a[32:35], v[224:227]\n"
"buffer_load_dwordx4 v16, s[44:47], s59 offen offset:3072 nt lds\n"
"ds_read_b64_tr_b16 a[64:65], v40 offset:24704\n"
"ds_read_b64_tr_b16 a[66:67], v40 offset:26240\n"
"v_mfma_f32_16x16x32_bf16 v[232:235], a[72:75], a[36:39], v[232:235]\n"
"s_mov_b32 m0, 102400\n"
"v_mfma_f32_16x16x32_bf16 v[240:243], a[72:75], a[40:43], v[240:243]\n"
"buffer_load_dwordx4 v17, s[44:47], s59 offen offset:0 nt lds\n"
"s_waitcnt lgkmcnt(6)\n"
"v_mfma_f32_16x16x32_bf16 v[224:227], a[76:79], a[48:51], v[224:227]\n"
"buffer_load_dwordx4 v18, s[44:47], s59 offen offset:1024 nt lds\n"
"ds_read_b64_tr_b16 a[68:69], v40 offset:36992\n"
"ds_read_b64_tr_b16 a[70:71], v40 offset:38528\n"
"v_mfma_f32_16x16x32_bf16 v[232:235], a[76:79], a[52:55], v[232:235]\n"
"buffer_load_dwordx4 v19, s[44:47], s59 offen offset:2048 nt lds\n"
"v_mfma_f32_16x16x32_bf16 v[240:243], a[76:79], a[56:59], v[240:243]\n"
"buffer_load_dwordx4 v20, s[44:47], s59 offen offset:3072 nt lds\n"
"v_mfma_f32_16x16x32_bf16 v[196:199], a[80:83], a[32:35], v[196:199]\n"
"ds_read_b64_tr_b16 a[72:73], v39 offset:24704\n"
"ds_read_b64_tr_b16 a[74:75], v39 offset:26240\n"
"v_mfma_f32_16x16x32_bf16 v[204:207], a[80:83], a[36:39], v[204:207]\n"
"s_mov_b32 m0, 106496\n"
"v_mfma_f32_16x16x32_bf16 v[212:215], a[80:83], a[40:43], v[212:215]\n"
"buffer_load_dwordx4 v21, s[44:47], s59 offen offset:0 nt lds\n"
"buffer_load_dwordx4 v22, s[44:47], s59 offen offset:1024 nt lds\n"
"v_mfma_f32_16x16x32_bf16 v[196:199], a[84:87], a[48:51], v[196:199]\n"
"ds_read_b64_tr_b16 a[76:77], v39 offset:36992\n"
"ds_read_b64_tr_b16 a[78:79], v39 offset:38528\n"
"v_mfma_f32_16x16x32_bf16 v[204:207], a[84:87], a[52:55], v[204:207]\n"
"buffer_load_dwordx4 v23, s[44:47], s59 offen offset:2048 nt lds\n"
"v_pk_mul_f32 v[240:241], v[240:241], s[62:63]\n"
"v_pk_mul_f32 v[242:243], v[242:243], s[62:63]\n"
"v_mfma_f32_16x16x32_bf16 v[212:215], a[84:87], a[56:59], v[212:215]\n"
"buffer_load_dwordx4 v24, s[44:47], s59 offen offset:3072 nt lds\n"
"v_pk_mul_f32 v[244:245], v[244:245], s[62:63]\n"
"s_waitcnt lgkmcnt(6)\n"
"v_mfma_f32_16x16x32_bf16 v[192:195], a[88:91], a[32:35], v[192:195]\n"
"ds_read_b64_tr_b16 a[80:81], v38 offset:24704\n"
"ds_read_b64_tr_b16 a[82:83], v38 offset:26240\n"
"v_pk_mul_f32 v[246:247], v[246:247], s[62:63]\n"
"v_mfma_f32_16x16x32_bf16 v[200:203], a[88:91], a[36:39], v[200:203]\n"
"s_add_u32 s59, s59, 12288\n"
"s_mov_b32 m0, 110592\n"
"v_cvt_pk_bf16_f32 v251, v246, v247\n"
"v_mfma_f32_16x16x32_bf16 v[208:211], a[88:91], a[40:43], v[208:211]\n"
"buffer_load_dwordx4 v13, s[44:47], s59 offen offset:0 nt lds\n"
"buffer_load_dwordx4 v14, s[44:47], s59 offen offset:1024 nt lds\n"
"v_cvt_pk_bf16_f32 v250, v244, v245\n"
"v_mfma_f32_16x16x32_bf16 v[192:195], a[92:95], a[48:51], v[192:195]\n"
"buffer_load_dwordx4 v15, s[44:47], s59 offen offset:2048 nt lds\n"
"ds_read_b64_tr_b16 a[84:85], v38 offset:36992\n"
"ds_read_b64_tr_b16 a[86:87], v38 offset:38528\n"
"v_mfma_f32_16x16x32_bf16 v[200:203], a[92:95], a[52:55], v[200:203]\n"
"v_cvt_pk_bf16_f32 v249, v242, v243\n"
"v_cvt_pk_bf16_f32 v248, v240, v241\n"
"v_mfma_f32_16x16x32_bf16 v[208:211], a[92:95], a[56:59], v[208:211]\n"
"buffer_load_dwordx4 v16, s[44:47], s59 offen offset:3072 nt lds\n"
"v_mfma_f32_16x16x32_bf16 v[164:167], a[64:67], a[32:35], v[164:167]\n"
"s_mov_b32 m0, 114688\n"
"ds_read_b64_tr_b16 a[88:89], v37 offset:24704\n"
"ds_read_b64_tr_b16 a[90:91], v37 offset:26240\n"
"v_mfma_f32_16x16x32_bf16 v[172:175], a[64:67], a[36:39], v[172:175]\n"
"buffer_load_dwordx4 v17, s[44:47], s59 offen offset:0 nt lds\n"
"v_permlane32_swap_b32 v248, v250\n"
"v_permlane32_swap_b32 v249, v251\n"
"v_mfma_f32_16x16x32_bf16 v[180:183], a[64:67], a[40:43], v[180:183]\n"
"buffer_load_dwordx4 v18, s[44:47], s59 offen offset:1024 nt lds\n"
"v_permlane16_swap_b32 v248, v250\n"
"s_waitcnt lgkmcnt(6)\n"
"v_mfma_f32_16x16x32_bf16 v[164:167], a[68:71], a[48:51], v[164:167]\n"
"buffer_load_dwordx4 v19, s[44:47], s59 offen offset:2048 nt lds\n"
"ds_read_b64_tr_b16 a[92:93], v37 offset:36992\n"
"ds_read_b64_tr_b16 a[94:95], v37 offset:38528\n"
"v_mfma_f32_16x16x32_bf16 v[172:175], a[68:71], a[52:55], v[172:175]\n"
"buffer_load_dwordx4 v20, s[44:47], s59 offen offset:3072 nt lds\n"
"v_permlane16_swap_b32 v249, v251\n"
"v_pk_mul_f32 v[232:233], v[232:233], s[62:63]\n"
"v_mfma_f32_16x16x32_bf16 v[180:183], a[68:71], a[56:59], v[180:183]\n"
"v_pk_mul_f32 v[234:235], v[234:235], s[62:63]\n"
"v_pk_mul_f32 v[236:237], v[236:237], s[62:63]\n"
"v_pk_mul_f32 v[238:239], v[238:239], s[62:63]\n"
"v_mfma_f32_16x16x32_bf16 v[160:163], a[72:75], a[32:35], v[160:163]\n"
"s_mov_b32 m0, 118784\n"
"ds_read_b64_tr_b16 a[64:65], v40 offset:24576\n"
"ds_read_b64_tr_b16 a[66:67], v40 offset:26112\n"
"v_mfma_f32_16x16x32_bf16 v[168:171], a[72:75], a[36:39], v[168:171]\n"
"buffer_load_dwordx4 v21, s[44:47], s59 offen offset:0 nt lds\n"
"buffer_load_dwordx4 v22, s[44:47], s59 offen offset:1024 nt lds\n"
"v_cvt_pk_bf16_f32 v247, v238, v239\n"
"v_mfma_f32_16x16x32_bf16 v[176:179], a[72:75], a[40:43], v[176:179]\n"
"v_cvt_pk_bf16_f32 v246, v236, v237\n"
"v_cvt_pk_bf16_f32 v245, v234, v235\n"
"v_cvt_pk_bf16_f32 v244, v232, v233\n"
"v_mfma_f32_16x16x32_bf16 v[160:163], a[76:79], a[48:51], v[160:163]\n"
"buffer_load_dwordx4 v23, s[44:47], s59 offen offset:2048 nt lds\n"
"ds_read_b64_tr_b16 a[68:69], v40 offset:36864\n"
"ds_read_b64_tr_b16 a[70:71], v40 offset:38400\n"
"v_mfma_f32_16x16x32_bf16 v[168:171], a[76:79], a[52:55], v[168:171]\n"
"buffer_load_dwordx4 v24, s[44:47], s59 offen offset:3072 nt lds\n"
"v_permlane32_swap_b32 v244, v246\n"
"v_permlane32_swap_b32 v245, v247\n"
"v_mfma_f32_16x16x32_bf16 v[176:179], a[76:79], a[56:59], v[176:179]\n"
"v_permlane16_swap_b32 v244, v246\n"
"v_permlane16_swap_b32 v245, v247\n"
"s_waitcnt lgkmcnt(6)\n"
"v_mfma_f32_16x16x32_bf16 v[132:135], a[80:83], a[32:35], v[132:135]\n"
"s_add_u32 s59, s59, 12288\n"
"s_mov_b32 m0, 24576\n"
"ds_read_b64_tr_b16 a[72:73], v39 offset:24576\n"
"v_mfma_f32_16x16x32_bf16 v[140:143], a[80:83], a[36:39], v[140:143]\n"
"buffer_load_dwordx4 v25, s[48:51], s60 offen offset:0 nt lds\n"
"buffer_load_dwordx4 v26, s[48:51], s60 offen offset:1024 nt lds\n"
"ds_read_b64_tr_b16 a[74:75], v39 offset:26112\n"
"v_mfma_f32_16x16x32_bf16 v[148:151], a[80:83], a[40:43], v[148:151]\n"
"buffer_load_dwordx4 v27, s[48:51], s60 offen offset:2048 nt lds\n"
"v_pk_mul_f32 v[224:225], v[224:225], s[62:63]\n"
"v_pk_mul_f32 v[226:227], v[226:227], s[62:63]\n"
"v_mfma_f32_16x16x32_bf16 v[132:135], a[84:87], a[48:51], v[132:135]\n"
"ds_read_b64_tr_b16 a[76:77], v39 offset:36864\n"
"ds_read_b64_tr_b16 a[78:79], v39 offset:38400\n"
"v_pk_mul_f32 v[228:229], v[228:229], s[62:63]\n"
"v_mfma_f32_16x16x32_bf16 v[140:143], a[84:87], a[52:55], v[140:143]\n"
"buffer_load_dwordx4 v28, s[48:51], s60 offen offset:3072 nt lds\n"
"v_pk_mul_f32 v[230:231], v[230:231], s[62:63]\n"
"v_cvt_pk_bf16_f32 v243, v230, v231\n"
"v_mfma_f32_16x16x32_bf16 v[148:151], a[84:87], a[56:59], v[148:151]\n"
"s_mov_b32 m0, 28672\n"
"v_cvt_pk_bf16_f32 v242, v228, v229\n"
"v_cvt_pk_bf16_f32 v241, v226, v227\n"
"v_mfma_f32_16x16x32_bf16 v[128:131], a[88:91], a[32:35], v[128:131]\n"
"buffer_load_dwordx4 v29, s[48:51], s60 offen offset:0 nt lds\n"
"ds_read_b64_tr_b16 a[80:81], v38 offset:24576\n"
"ds_read_b64_tr_b16 a[82:83], v38 offset:26112\n"
"v_mfma_f32_16x16x32_bf16 v[136:139], a[88:91], a[36:39], v[136:139]\n"
"buffer_load_dwordx4 v30, s[48:51], s60 offen offset:1024 nt lds\n"
"v_cvt_pk_bf16_f32 v240, v224, v225\n"
"v_mfma_f32_16x16x32_bf16 v[144:147], a[88:91], a[40:43], v[144:147]\n"
"buffer_load_dwordx4 v31, s[48:51], s60 offen offset:2048 nt lds\n"
"s_waitcnt lgkmcnt(6)\n"
"v_mfma_f32_16x16x32_bf16 v[128:131], a[92:95], a[48:51], v[128:131]\n"
"buffer_load_dwordx4 v32, s[48:51], s60 offen offset:3072 nt lds\n"
"ds_read_b64_tr_b16 a[84:85], v38 offset:36864\n"
"ds_read_b64_tr_b16 a[86:87], v38 offset:38400\n"
"v_mfma_f32_16x16x32_bf16 v[136:139], a[92:95], a[52:55], v[136:139]\n"
"v_permlane32_swap_b32 v240, v242\n"
"v_permlane32_swap_b32 v241, v243\n"
"v_mfma_f32_16x16x32_bf16 v[144:147], a[92:95], a[56:59], v[144:147]\n"
"s_mov_b32 m0, 32768\n"
"v_permlane16_swap_b32 v240, v242\n"
"v_permlane16_swap_b32 v241, v243\n"
"v_mfma_f32_16x16x32_bf16 v[100:103], a[64:67], a[32:35], v[100:103]\n"
"buffer_load_dwordx4 v33, s[48:51], s60 offen offset:0 nt lds\n"
"buffer_load_dwordx4 v34, s[48:51], s60 offen offset:1024 nt lds\n"
"ds_read_b64_tr_b16 a[88:89], v37 offset:24576\n"
"v_mfma_f32_16x16x32_bf16 v[108:111], a[64:67], a[36:39], v[108:111]\n"
"ds_read_b64_tr_b16 a[90:91], v37 offset:26112\n"
"v_pk_mul_f32 v[208:209], v[208:209], s[62:63]\n"
"v_pk_mul_f32 v[210:211], v[210:211], s[62:63]\n"
"v_mfma_f32_16x16x32_bf16 v[116:119], a[64:67], a[40:43], v[116:119]\n"
"buffer_load_dwordx4 v35, s[48:51], s60 offen offset:2048 nt lds\n"
"v_pk_mul_f32 v[212:213], v[212:213], s[62:63]\n"
"v_pk_mul_f32 v[214:215], v[214:215], s[62:63]\n"
"v_mfma_f32_16x16x32_bf16 v[100:103], a[68:71], a[48:51], v[100:103]\n"
"buffer_load_dwordx4 v36, s[48:51], s60 offen offset:3072 nt lds\n"
"ds_read_b64_tr_b16 a[92:93], v37 offset:36864\n"
"ds_read_b64_tr_b16 a[94:95], v37 offset:38400\n"
"v_mfma_f32_16x16x32_bf16 v[108:111], a[68:71], a[52:55], v[108:111]\n"
"v_cvt_pk_bf16_f32 v235, v214, v215\n"
"v_cvt_pk_bf16_f32 v234, v212, v213\n"
"v_cvt_pk_bf16_f32 v233, v210, v211\n"
"v_mfma_f32_16x16x32_bf16 v[116:119], a[68:71], a[56:59], v[116:119]\n"
"s_add_u32 s60, s60, 12288\n"
"v_cvt_pk_bf16_f32 v232, v208, v209\n"
"s_waitcnt lgkmcnt(6)\n"
"v_mfma_f32_16x16x32_bf16 v[96:99], a[72:75], a[32:35], v[96:99]\n"
"v_mfma_f32_16x16x32_bf16 v[104:107], a[72:75], a[36:39], v[104:107]\n"
"v_permlane32_swap_b32 v232, v234\n"
"v_permlane32_swap_b32 v233, v235\n"
"v_mfma_f32_16x16x32_bf16 v[112:115], a[72:75], a[40:43], v[112:115]\n"
"v_permlane16_swap_b32 v232, v234\n"
"v_permlane16_swap_b32 v233, v235\n"
"v_pk_mul_f32 v[200:201], v[200:201], s[62:63]\n"
"v_mfma_f32_16x16x32_bf16 v[96:99], a[76:79], a[48:51], v[96:99]\n"
"s_waitcnt vmcnt(36) lgkmcnt(0)\n"
"s_barrier\n"
"v_pk_mul_f32 v[202:203], v[202:203], s[62:63]\n"
"v_mfma_f32_16x16x32_bf16 v[104:107], a[76:79], a[52:55], v[104:107]\n"
"v_pk_mul_f32 v[204:205], v[204:205], s[62:63]\n"
"v_pk_mul_f32 v[206:207], v[206:207], s[62:63]\n"
"v_cvt_pk_bf16_f32 v231, v206, v207\n"
"v_mfma_f32_16x16x32_bf16 v[112:115], a[76:79], a[56:59], v[112:115]\n"
"v_cvt_pk_bf16_f32 v230, v204, v205\n"
"v_cvt_pk_bf16_f32 v229, v202, v203\n"
"v_cvt_pk_bf16_f32 v228, v200, v201\n"
"v_mfma_f32_16x16x32_bf16 v[68:71], a[80:83], a[32:35], v[68:71]\n"
"ds_read_b128 a[192:195], v1 offset:0\n"
"ds_read_b128 a[196:199], v1 offset:6144\n"
"ds_read_b128 a[200:203], v2 offset:0\n"
"v_mfma_f32_16x16x32_bf16 v[76:79], a[80:83], a[36:39], v[76:79]\n"
"v_permlane32_swap_b32 v228, v230\n"
"v_permlane32_swap_b32 v229, v231\n"
"ds_read_b128 a[204:207], v2 offset:6144\n"
"v_mfma_f32_16x16x32_bf16 v[84:87], a[80:83], a[40:43], v[84:87]\n"
"v_permlane16_swap_b32 v228, v230\n"
"v_permlane16_swap_b32 v229, v231\n"
"v_mfma_f32_16x16x32_bf16 v[68:71], a[84:87], a[48:51], v[68:71]\n"
"v_pk_mul_f32 v[192:193], v[192:193], s[62:63]\n"
"v_pk_mul_f32 v[194:195], v[194:195], s[62:63]\n"
"v_pk_mul_f32 v[196:197], v[196:197], s[62:63]\n"
"v_mfma_f32_16x16x32_bf16 v[76:79], a[84:87], a[52:55], v[76:79]\n"
"v_pk_mul_f32 v[198:199], v[198:199], s[62:63]\n"
"v_cvt_pk_bf16_f32 v227, v198, v199\n"
"v_cvt_pk_bf16_f32 v226, v196, v197\n"
"v_mfma_f32_16x16x32_bf16 v[84:87], a[84:87], a[56:59], v[84:87]\n"
"v_cvt_pk_bf16_f32 v225, v194, v195\n"
"v_cvt_pk_bf16_f32 v224, v192, v193\n"
"ds_read_b128 a[208:211], v1 offset:128\n"
"v_mfma_f32_16x16x32_bf16 v[64:67], a[88:91], a[32:35], v[64:67]\n"
"v_mfma_f32_16x16x32_bf16 v[72:75], a[88:91], a[36:39], v[72:75]\n"
"v_permlane32_swap_b32 v224, v226\n"
"v_permlane32_swap_b32 v225, v227\n"
"v_mfma_f32_16x16x32_bf16 v[80:83], a[88:91], a[40:43], v[80:83]\n"
"v_permlane16_swap_b32 v224, v226\n"
"v_permlane16_swap_b32 v225, v227\n"
"v_mfma_f32_16x16x32_bf16 v[64:67], a[92:95], a[48:51], v[64:67]\n"
"v_mfma_f32_16x16x32_bf16 v[72:75], a[92:95], a[52:55], v[72:75]\n"
"v_mfma_f32_16x16x32_bf16 v[80:83], a[92:95], a[56:59], v[80:83]\n"
"s_nop 7\n"
"v_pk_mul_f32 v[176:177], v[176:177], s[62:63]\n"
"v_pk_mul_f32 v[178:179], v[178:179], s[62:63]\n"
"v_pk_mul_f32 v[180:181], v[180:181], s[62:63]\n"
"v_pk_mul_f32 v[182:183], v[182:183], s[62:63]\n"
"s_mov_b32 m0, 122880\n"
"s_nop 0\n"
"buffer_load_dwordx4 v13, s[44:47], s59 offen offset:0 nt lds\n"
"buffer_load_dwordx4 v14, s[44:47], s59 offen offset:1024 nt lds\n"
"v_cvt_pk_bf16_f32 v219, v182, v183\n"
"v_cvt_pk_bf16_f32 v218, v180, v181\n"
"v_cvt_pk_bf16_f32 v217, v178, v179\n"
"v_cvt_pk_bf16_f32 v216, v176, v177\n"
"v_pk_mul_f32 v[168:169], v[168:169], s[62:63]\n"
"v_pk_mul_f32 v[170:171], v[170:171], s[62:63]\n"
"v_pk_mul_f32 v[172:173], v[172:173], s[62:63]\n"
"v_pk_mul_f32 v[174:175], v[174:175], s[62:63]\n"
"v_permlane32_swap_b32 v216, v218\n"
"v_permlane32_swap_b32 v217, v219\n"
"buffer_load_dwordx4 v15, s[44:47], s59 offen offset:2048 nt lds\n"
"buffer_load_dwordx4 v16, s[44:47], s59 offen offset:3072 nt lds\n"
"s_mov_b32 m0, 126976\n"
"s_nop 0\n"
"v_cvt_pk_bf16_f32 v215, v174, v175\n"
"v_cvt_pk_bf16_f32 v214, v172, v173\n"
"v_cvt_pk_bf16_f32 v213, v170, v171\n"
"v_cvt_pk_bf16_f32 v212, v168, v169\n"
"v_permlane16_swap_b32 v216, v218\n"
"v_permlane16_swap_b32 v217, v219\n"
"v_pk_mul_f32 v[160:161], v[160:161], s[62:63]\n"
"v_pk_mul_f32 v[162:163], v[162:163], s[62:63]\n"
"v_pk_mul_f32 v[164:165], v[164:165], s[62:63]\n"
"v_pk_mul_f32 v[166:167], v[166:167], s[62:63]\n"
"v_permlane32_swap_b32 v212, v214\n"
"v_permlane32_swap_b32 v213, v215\n"
"buffer_load_dwordx4 v17, s[44:47], s59 offen offset:0 nt lds\n"
"buffer_load_dwordx4 v18, s[44:47], s59 offen offset:1024 nt lds\n"
"buffer_load_dwordx4 v19, s[44:47], s59 offen offset:2048 nt lds\n"
"v_cvt_pk_bf16_f32 v211, v166, v167\n"
"v_cvt_pk_bf16_f32 v210, v164, v165\n"
"v_cvt_pk_bf16_f32 v209, v162, v163\n"
"v_cvt_pk_bf16_f32 v208, v160, v161\n"
"v_permlane16_swap_b32 v212, v214\n"
"v_permlane16_swap_b32 v213, v215\n"
"v_pk_mul_f32 v[144:145], v[144:145], s[62:63]\n"
"v_pk_mul_f32 v[146:147], v[146:147], s[62:63]\n"
"v_pk_mul_f32 v[148:149], v[148:149], s[62:63]\n"
"v_pk_mul_f32 v[150:151], v[150:151], s[62:63]\n"
"v_permlane32_swap_b32 v208, v210\n"
"v_permlane32_swap_b32 v209, v211\n"
"buffer_load_dwordx4 v20, s[44:47], s59 offen offset:3072 nt lds\n"
"s_mov_b32 m0, 131072\n"
"s_nop 0\n"
"buffer_load_dwordx4 v21, s[44:47], s59 offen offset:0 nt lds\n"
"v_cvt_pk_bf16_f32 v203, v150, v151\n"
"v_cvt_pk_bf16_f32 v202, v148, v149\n"
"v_cvt_pk_bf16_f32 v201, v146, v147\n"
"v_cvt_pk_bf16_f32 v200, v144, v145\n"
"v_permlane16_swap_b32 v208, v210\n"
"v_permlane16_swap_b32 v209, v211\n"
"v_pk_mul_f32 v[136:137], v[136:137], s[62:63]\n"
"v_pk_mul_f32 v[138:139], v[138:139], s[62:63]\n"
"v_pk_mul_f32 v[140:141], v[140:141], s[62:63]\n"
"v_pk_mul_f32 v[142:143], v[142:143], s[62:63]\n"
"v_permlane32_swap_b32 v200, v202\n"
"v_permlane32_swap_b32 v201, v203\n"
"buffer_load_dwordx4 v22, s[44:47], s59 offen offset:1024 nt lds\n"
"buffer_load_dwordx4 v23, s[44:47], s59 offen offset:2048 nt lds\n"
"buffer_load_dwordx4 v24, s[44:47], s59 offen offset:3072 nt lds\n"
"v_cvt_pk_bf16_f32 v199, v142, v143\n"
"v_cvt_pk_bf16_f32 v198, v140, v141\n"
"v_cvt_pk_bf16_f32 v197, v138, v139\n"
"v_cvt_pk_bf16_f32 v196, v136, v137\n"
"v_permlane16_swap_b32 v200, v202\n"
"v_permlane16_swap_b32 v201, v203\n"
"v_pk_mul_f32 v[128:129], v[128:129], s[62:63]\n"
"v_pk_mul_f32 v[130:131], v[130:131], s[62:63]\n"
"v_pk_mul_f32 v[132:133], v[132:133], s[62:63]\n"
"v_pk_mul_f32 v[134:135], v[134:135], s[62:63]\n"
"v_permlane32_swap_b32 v196, v198\n"
"v_permlane32_swap_b32 v197, v199\n"
"s_add_u32 s59, s59, 12288\n"
"s_mov_b32 m0, 36864\n"
"s_nop 0\n"
"buffer_load_dwordx4 v25, s[48:51], s60 offen offset:0 nt lds\n"
"v_cvt_pk_bf16_f32 v195, v134, v135\n"
"v_cvt_pk_bf16_f32 v194, v132, v133\n"
"v_cvt_pk_bf16_f32 v193, v130, v131\n"
"v_cvt_pk_bf16_f32 v192, v128, v129\n"
"v_permlane16_swap_b32 v196, v198\n"
"v_permlane16_swap_b32 v197, v199\n"
"v_pk_mul_f32 v[112:113], v[112:113], s[62:63]\n"
"v_pk_mul_f32 v[114:115], v[114:115], s[62:63]\n"
"v_pk_mul_f32 v[116:117], v[116:117], s[62:63]\n"
"v_pk_mul_f32 v[118:119], v[118:119], s[62:63]\n"
"v_permlane32_swap_b32 v192, v194\n"
"v_permlane32_swap_b32 v193, v195\n"
"buffer_load_dwordx4 v26, s[48:51], s60 offen offset:1024 nt lds\n"
"buffer_load_dwordx4 v27, s[48:51], s60 offen offset:2048 nt lds\n"
"buffer_load_dwordx4 v28, s[48:51], s60 offen offset:3072 nt lds\n"
"v_cvt_pk_bf16_f32 v187, v118, v119\n"
"v_cvt_pk_bf16_f32 v186, v116, v117\n"
"v_cvt_pk_bf16_f32 v185, v114, v115\n"
"v_cvt_pk_bf16_f32 v184, v112, v113\n"
"v_permlane16_swap_b32 v192, v194\n"
"v_permlane16_swap_b32 v193, v195\n"
"v_pk_mul_f32 v[104:105], v[104:105], s[62:63]\n"
"v_pk_mul_f32 v[106:107], v[106:107], s[62:63]\n"
"v_pk_mul_f32 v[108:109], v[108:109], s[62:63]\n"
"v_pk_mul_f32 v[110:111], v[110:111], s[62:63]\n"
"v_permlane32_swap_b32 v184, v186\n"
"v_permlane32_swap_b32 v185, v187\n"
"s_mov_b32 m0, 40960\n"
"s_nop 0\n"
"buffer_load_dwordx4 v29, s[48:51], s60 offen offset:0 nt lds\n"
"buffer_load_dwordx4 v30, s[48:51], s60 offen offset:1024 nt lds\n"
"v_cvt_pk_bf16_f32 v183, v110, v111\n"
"v_cvt_pk_bf16_f32 v182, v108, v109\n"
"v_cvt_pk_bf16_f32 v181, v106, v107\n"
"v_cvt_pk_bf16_f32 v180, v104, v105\n"
"v_permlane16_swap_b32 v184, v186\n"
"v_permlane16_swap_b32 v185, v187\n"
"v_pk_mul_f32 v[96:97], v[96:97], s[62:63]\n"
"v_pk_mul_f32 v[98:99], v[98:99], s[62:63]\n"
"v_pk_mul_f32 v[100:101], v[100:101], s[62:63]\n"
"v_pk_mul_f32 v[102:103], v[102:103], s[62:63]\n"
"v_permlane32_swap_b32 v180, v182\n"
"v_permlane32_swap_b32 v181, v183\n"
"buffer_load_dwordx4 v31, s[48:51], s60 offen offset:2048 nt lds\n"
"buffer_load_dwordx4 v32, s[48:51], s60 offen offset:3072 nt lds\n"
"s_mov_b32 m0, 45056\n"
"s_nop 0\n"
"v_cvt_pk_bf16_f32 v179, v102, v103\n"
"v_cvt_pk_bf16_f32 v178, v100, v101\n"
"v_cvt_pk_bf16_f32 v177, v98, v99\n"
"v_cvt_pk_bf16_f32 v176, v96, v97\n"
"v_permlane16_swap_b32 v180, v182\n"
"v_permlane16_swap_b32 v181, v183\n"
"v_pk_mul_f32 v[80:81], v[80:81], s[62:63]\n"
"v_pk_mul_f32 v[82:83], v[82:83], s[62:63]\n"
"v_pk_mul_f32 v[84:85], v[84:85], s[62:63]\n"
"v_pk_mul_f32 v[86:87], v[86:87], s[62:63]\n"
"v_permlane32_swap_b32 v176, v178\n"
"v_permlane32_swap_b32 v177, v179\n"
"buffer_load_dwordx4 v33, s[48:51], s60 offen offset:0 nt lds\n"
"buffer_load_dwordx4 v34, s[48:51], s60 offen offset:1024 nt lds\n"
"buffer_load_dwordx4 v35, s[48:51], s60 offen offset:2048 nt lds\n"
"v_cvt_pk_bf16_f32 v171, v86, v87\n"
"v_cvt_pk_bf16_f32 v170, v84, v85\n"
"v_cvt_pk_bf16_f32 v169, v82, v83\n"
"v_cvt_pk_bf16_f32 v168, v80, v81\n"
"v_permlane16_swap_b32 v176, v178\n"
"v_permlane16_swap_b32 v177, v179\n"
"v_pk_mul_f32 v[72:73], v[72:73], s[62:63]\n"
"v_pk_mul_f32 v[74:75], v[74:75], s[62:63]\n"
"v_pk_mul_f32 v[76:77], v[76:77], s[62:63]\n"
"v_pk_mul_f32 v[78:79], v[78:79], s[62:63]\n"
"v_permlane32_swap_b32 v168, v170\n"
"v_permlane32_swap_b32 v169, v171\n"
"buffer_load_dwordx4 v36, s[48:51], s60 offen offset:3072 nt lds\n"
"s_add_u32 s60, s60, 12288\n"
"v_cvt_pk_bf16_f32 v167, v78, v79\n"
"v_cvt_pk_bf16_f32 v166, v76, v77\n"
"v_cvt_pk_bf16_f32 v165, v74, v75\n"
"v_cvt_pk_bf16_f32 v164, v72, v73\n"
"v_permlane16_swap_b32 v168, v170\n"
"v_permlane16_swap_b32 v169, v171\n"
"v_pk_mul_f32 v[64:65], v[64:65], s[62:63]\n"
"v_pk_mul_f32 v[66:67], v[66:67], s[62:63]\n"
"v_pk_mul_f32 v[68:69], v[68:69], s[62:63]\n"
"v_pk_mul_f32 v[70:71], v[70:71], s[62:63]\n"
"v_permlane32_swap_b32 v164, v166\n"
"v_permlane32_swap_b32 v165, v167\n"
"v_cvt_pk_bf16_f32 v163, v70, v71\n"
"v_cvt_pk_bf16_f32 v162, v68, v69\n"
"v_cvt_pk_bf16_f32 v161, v66, v67\n"
"v_cvt_pk_bf16_f32 v160, v64, v65\n"
"v_permlane16_swap_b32 v164, v166\n"
"v_permlane16_swap_b32 v165, v167\n"
"s_nop 1\n"
"v_permlane32_swap_b32 v160, v162\n"
"v_permlane32_swap_b32 v161, v163\n"
"s_nop 1\n"
"v_permlane16_swap_b32 v160, v162\n"
"v_permlane16_swap_b32 v161, v163\n"
"s_nop 1\n"
"s_waitcnt lgkmcnt(2)\n"
"v_mfma_f32_16x16x32_bf16 v[64:67], a[192:195], v[160:163], 0\n"
"ds_read_b128 a[212:215], v1 offset:6272\n"
"v_mfma_f32_16x16x32_bf16 v[72:75], a[192:195], v[164:167], 0\n"
"v_mfma_f32_16x16x32_bf16 v[80:83], a[192:195], v[168:171], 0\n"
"v_mfma_f32_16x16x32_bf16 v[68:71], a[196:199], v[160:163], 0\n"
"ds_read_b128 a[216:219], v2 offset:128\n"
"v_mfma_f32_16x16x32_bf16 v[76:79], a[196:199], v[164:167], 0\n"
"v_mfma_f32_16x16x32_bf16 v[84:87], a[196:199], v[168:171], 0\n"
"v_mfma_f32_16x16x32_bf16 v[64:67], a[200:203], v[176:179], v[64:67]\n"
"ds_read_b128 a[220:223], v2 offset:6272\n"
"v_mfma_f32_16x16x32_bf16 v[72:75], a[200:203], v[180:183], v[72:75]\n"
"v_mfma_f32_16x16x32_bf16 v[80:83], a[200:203], v[184:187], v[80:83]\n"
"s_waitcnt lgkmcnt(2)\n"
"v_mfma_f32_16x16x32_bf16 v[68:71], a[204:207], v[176:179], v[68:71]\n"
"ds_read_b128 a[192:195], v1 offset:256\n"
"v_mfma_f32_16x16x32_bf16 v[76:79], a[204:207], v[180:183], v[76:79]\n"
"v_mfma_f32_16x16x32_bf16 v[84:87], a[204:207], v[184:187], v[84:87]\n"
"v_mfma_f32_16x16x32_bf16 v[64:67], a[208:211], v[192:195], v[64:67]\n"
"ds_read_b128 a[196:199], v1 offset:6400\n"
"v_mfma_f32_16x16x32_bf16 v[72:75], a[208:211], v[196:199], v[72:75]\n"
"v_mfma_f32_16x16x32_bf16 v[80:83], a[208:211], v[200:203], v[80:83]\n"
"v_mfma_f32_16x16x32_bf16 v[68:71], a[212:215], v[192:195], v[68:71]\n"
"ds_read_b128 a[200:203], v2 offset:256\n"
"v_mfma_f32_16x16x32_bf16 v[76:79], a[212:215], v[196:199], v[76:79]\n"
"v_mfma_f32_16x16x32_bf16 v[84:87], a[212:215], v[200:203], v[84:87]\n"
"s_waitcnt lgkmcnt(2)\n"
"v_mfma_f32_16x16x32_bf16 v[64:67], a[216:219], v[208:211], v[64:67]\n"
"ds_read_b128 a[204:207], v2 offset:6400\n"
"v_mfma_f32_16x16x32_bf16 v[72:75], a[216:219], v[212:215], v[72:75]\n"
"v_mfma_f32_16x16x32_bf16 v[80:83], a[216:219], v[216:219], v[80:83]\n"
"v_mfma_f32_16x16x32_bf16 v[68:71], a[220:223], v[208:211], v[68:71]\n"
"v_mfma_f32_16x16x32_bf16 v[76:79], a[220:223], v[212:215], v[76:79]\n"
"v_mfma_f32_16x16x32_bf16 v[84:87], a[220:223], v[216:219], v[84:87]\n"
"v_mfma_f32_16x16x32_bf16 v[64:67], a[192:195], v[224:227], v[64:67]\n"
"v_mfma_f32_16x16x32_bf16 v[72:75], a[192:195], v[228:231], v[72:75]\n"
"v_mfma_f32_16x16x32_bf16 v[80:83], a[192:195], v[232:235], v[80:83]\n"
"s_waitcnt lgkmcnt(0)\n"
"v_mfma_f32_16x16x32_bf16 v[68:71], a[196:199], v[224:227], v[68:71]\n"
"v_mfma_f32_16x16x32_bf16 v[76:79], a[196:199], v[228:231], v[76:79]\n"
"v_mfma_f32_16x16x32_bf16 v[84:87], a[196:199], v[232:235], v[84:87]\n"
"v_mfma_f32_16x16x32_bf16 v[64:67], a[200:203], v[240:243], v[64:67]\n"
"v_mfma_f32_16x16x32_bf16 v[72:75], a[200:203], v[244:247], v[72:75]\n"
"v_mfma_f32_16x16x32_bf16 v[80:83], a[200:203], v[248:251], v[80:83]\n"
"v_mfma_f32_16x16x32_bf16 v[68:71], a[204:207], v[240:243], v[68:71]\n"
"v_mfma_f32_16x16x32_bf16 v[76:79], a[204:207], v[244:247], v[76:79]\n"
"v_mfma_f32_16x16x32_bf16 v[84:87], a[204:207], v[248:251], v[84:87]\n"
"ds_read_b128 a[208:211], v1 offset:12288\n"
"ds_read_b128 a[212:215], v1 offset:18432\n"
"ds_read_b128 a[216:219], v2 offset:12288\n"
"ds_read_b128 a[220:223], v2 offset:18432\n"
"ds_read_b128 a[192:195], v1 offset:12416\n"
"s_nop 7\n"
"v_max3_f32 v55, v64, v65, v66\n"
"v_max3_f32 v56, v72, v73, v74\n"
"v_max3_f32 v57, v80, v81, v82\n"
"s_nop 0\n"
"v_max3_f32 v55, v55, v67, v68\n"
"v_max3_f32 v56, v56, v75, v76\n"
"v_max3_f32 v57, v57, v83, v84\n"
"s_nop 0\n"
"v_max3_f32 v55, v55, v69, v70\n"
"v_max3_f32 v56, v56, v77, v78\n"
"v_max3_f32 v57, v57, v85, v86\n"
"s_nop 0\n"
"v_max_f32 v55, v55, v71\n"
"v_max_f32 v56, v56, v79\n"
"v_max_f32 v57, v57, v87\n"
"s_nop 0\n"
"v_mov_b32 v49, v55\n"
"v_mov_b32 v50, v56\n"
"v_mov_b32 v51, v57\n"
"s_nop 0\n"
"v_permlane32_swap_b32 v49, v55\n"
"v_permlane32_swap_b32 v50, v56\n"
"v_permlane32_swap_b32 v51, v57\n"
"s_nop 0\n"
"v_max_f32 v55, v55, v49\n"
"v_max_f32 v56, v56, v50\n"
"v_max_f32 v57, v57, v51\n"
"s_nop 0\n"
"v_mov_b32 v49, v55\n"
"v_mov_b32 v50, v56\n"
"v_mov_b32 v51, v57\n"
"s_nop 0\n"
"v_permlane16_swap_b32 v49, v55\n"
"v_permlane16_swap_b32 v50, v56\n"
"v_permlane16_swap_b32 v51, v57\n"
"s_nop 0\n"
"v_max_f32 v55, v55, v49\n"
"v_max_f32 v56, v56, v50\n"
"v_max_f32 v57, v57, v51\n"
"s_nop 0\n"
"v_sub_f32 v144, 0, v55\n"
"v_sub_f32 v145, 0, v55\n"
"v_sub_f32 v146, 0, v55\n"
"v_sub_f32 v147, 0, v55\n"
"v_sub_f32 v64, v64, v55\n"
"v_sub_f32 v65, v65, v55\n"
"v_sub_f32 v66, v66, v55\n"
"v_sub_f32 v67, v67, v55\n"
"v_sub_f32 v68, v68, v55\n"
"v_sub_f32 v69, v69, v55\n"
"v_sub_f32 v70, v70, v55\n"
"v_sub_f32 v71, v71, v55\n"
"v_mov_b32 v37, 0\n"
"v_mov_b32 v38, 0\n"
"v_sub_f32 v148, 0, v56\n"
"v_sub_f32 v149, 0, v56\n"
"v_sub_f32 v150, 0, v56\n"
"v_sub_f32 v151, 0, v56\n"
"v_sub_f32 v72, v72, v56\n"
"v_sub_f32 v73, v73, v56\n"
"v_sub_f32 v74, v74, v56\n"
"v_sub_f32 v75, v75, v56\n"
"v_sub_f32 v76, v76, v56\n"
"v_sub_f32 v77, v77, v56\n"
"v_sub_f32 v78, v78, v56\n"
"v_sub_f32 v79, v79, v56\n"
"v_mov_b32 v39, 0\n"
"v_mov_b32 v40, 0\n"
"v_sub_f32 v152, 0, v57\n"
"v_sub_f32 v153, 0, v57\n"
"v_sub_f32 v154, 0, v57\n"
"v_sub_f32 v155, 0, v57\n"
"v_sub_f32 v80, v80, v57\n"
"v_sub_f32 v81, v81, v57\n"
"v_sub_f32 v82, v82, v57\n"
"v_sub_f32 v83, v83, v57\n"
"v_sub_f32 v84, v84, v57\n"
"v_sub_f32 v85, v85, v57\n"
"v_sub_f32 v86, v86, v57\n"
"v_sub_f32 v87, v87, v57\n"
"v_mov_b32 v41, 0\n"
"v_mov_b32 v42, 0\n"
"v_exp_f32 v64, v64\n"
"v_exp_f32 v65, v65\n"
"v_exp_f32 v66, v66\n"
"v_exp_f32 v67, v67\n"
"v_exp_f32 v68, v68\n"
"v_exp_f32 v69, v69\n"
"v_exp_f32 v70, v70\n"
"v_exp_f32 v71, v71\n"
"v_exp_f32 v72, v72\n"
"v_exp_f32 v73, v73\n"
"v_exp_f32 v74, v74\n"
"v_exp_f32 v75, v75\n"
"s_nop 0\n"
"v_add_f32 v37, v37, v64\n"
"v_add_f32 v38, v38, v65\n"
"v_add_f32 v37, v37, v66\n"
"v_add_f32 v38, v38, v67\n"
"v_add_f32 v37, v37, v68\n"
"v_add_f32 v38, v38, v69\n"
"v_add_f32 v37, v37, v70\n"
"v_add_f32 v38, v38, v71\n"
"v_add_f32 v39, v39, v72\n"
"v_add_f32 v40, v40, v73\n"
"v_add_f32 v39, v39, v74\n"
"v_add_f32 v40, v40, v75\n"
"s_mov_b32 s95, 0\n"
"s_waitcnt lgkmcnt(2)\n"
"v_mfma_f32_16x16x32_bf16 v[96:99], a[208:211], v[160:163], v[144:147]\n"
"ds_read_b128 a[196:199], v1 offset:18560\n"
"v_exp_f32 v76, v76\n"
"v_mfma_f32_16x16x32_bf16 v[104:107], a[208:211], v[164:167], v[148:151]\n"
"v_exp_f32 v77, v77\n"
"v_cvt_pk_bf16_f32 v128, v64, v65\n"
"v_mfma_f32_16x16x32_bf16 v[112:115], a[208:211], v[168:171], v[152:155]\n"
"v_exp_f32 v78, v78\n"
"v_cvt_pk_bf16_f32 v129, v66, v67\n"
"v_mfma_f32_16x16x32_bf16 v[100:103], a[212:215], v[160:163], v[144:147]\n"
"ds_read_b128 a[200:203], v2 offset:12416\n"
"v_exp_f32 v79, v79\n"
"v_mfma_f32_16x16x32_bf16 v[108:111], a[212:215], v[164:167], v[148:151]\n"
"v_exp_f32 v80, v80\n"
"v_cvt_pk_bf16_f32 v130, v68, v69\n"
"v_mfma_f32_16x16x32_bf16 v[116:119], a[212:215], v[168:171], v[152:155]\n"
"v_exp_f32 v81, v81\n"
"v_cvt_pk_bf16_f32 v131, v70, v71\n"
"v_mfma_f32_16x16x32_bf16 v[96:99], a[216:219], v[176:179], v[96:99]\n"
"ds_read_b128 a[204:207], v2 offset:18560\n"
"v_exp_f32 v82, v82\n"
"v_mfma_f32_16x16x32_bf16 v[104:107], a[216:219], v[180:183], v[104:107]\n"
"v_exp_f32 v83, v83\n"
"v_cvt_pk_bf16_f32 v132, v72, v73\n"
"v_mfma_f32_16x16x32_bf16 v[112:115], a[216:219], v[184:187], v[112:115]\n"
"v_exp_f32 v84, v84\n"
"s_waitcnt lgkmcnt(2)\n"
"v_mfma_f32_16x16x32_bf16 v[100:103], a[220:223], v[176:179], v[100:103]\n"
"ds_read_b128 a[208:211], v1 offset:12544\n"
"v_exp_f32 v85, v85\n"
"v_mfma_f32_16x16x32_bf16 v[108:111], a[220:223], v[180:183], v[108:111]\n"
"v_exp_f32 v86, v86\n"
"v_cvt_pk_bf16_f32 v133, v74, v75\n"
"v_mfma_f32_16x16x32_bf16 v[116:119], a[220:223], v[184:187], v[116:119]\n"
"v_exp_f32 v87, v87\n"
"v_cvt_pk_bf16_f32 v134, v76, v77\n"
"v_mfma_f32_16x16x32_bf16 v[96:99], a[192:195], v[192:195], v[96:99]\n"
"ds_read_b128 a[212:215], v1 offset:18688\n"
"v_cvt_pk_bf16_f32 v135, v78, v79\n"
"v_cvt_pk_bf16_f32 v136, v80, v81\n"
"v_mfma_f32_16x16x32_bf16 v[104:107], a[192:195], v[196:199], v[104:107]\n"
"v_cvt_pk_bf16_f32 v137, v82, v83\n"
"v_cvt_pk_bf16_f32 v138, v84, v85\n"
"v_cvt_pk_bf16_f32 v139, v86, v87\n"
"v_mfma_f32_16x16x32_bf16 v[112:115], a[192:195], v[200:203], v[112:115]\n"
"v_add_f32 v39, v39, v76\n"
"v_add_f32 v40, v40, v77\n"
"v_add_f32 v39, v39, v78\n"
"v_mfma_f32_16x16x32_bf16 v[100:103], a[196:199], v[192:195], v[100:103]\n"
"ds_read_b128 a[216:219], v2 offset:12544\n"
"v_add_f32 v40, v40, v79\n"
"v_add_f32 v41, v41, v80\n"
"v_mfma_f32_16x16x32_bf16 v[108:111], a[196:199], v[196:199], v[108:111]\n"
"v_add_f32 v42, v42, v81\n"
"v_add_f32 v41, v41, v82\n"
"v_add_f32 v42, v42, v83\n"
"v_mfma_f32_16x16x32_bf16 v[116:119], a[196:199], v[200:203], v[116:119]\n"
"v_add_f32 v41, v41, v84\n"
"v_add_f32 v42, v42, v85\n"
"s_waitcnt lgkmcnt(2)\n"
"v_mfma_f32_16x16x32_bf16 v[96:99], a[200:203], v[208:211], v[96:99]\n"
"ds_read_b128 a[220:223], v2 offset:18688\n"
"v_add_f32 v41, v41, v86\n"
"v_add_f32 v42, v42, v87\n"
"v_mfma_f32_16x16x32_bf16 v[104:107], a[200:203], v[212:215], v[104:107]\n"
"v_mfma_f32_16x16x32_bf16 v[112:115], a[200:203], v[216:219], v[112:115]\n"
"v_mfma_f32_16x16x32_bf16 v[100:103], a[204:207], v[208:211], v[100:103]\n"
"ds_read_b64_tr_b16 a[224:225], v5 offset:0\n"
"ds_read_b64_tr_b16 a[226:227], v5 offset:6144\n"
"v_mfma_f32_16x16x32_bf16 v[108:111], a[204:207], v[212:215], v[108:111]\n"
"v_mfma_f32_16x16x32_bf16 v[116:119], a[204:207], v[216:219], v[116:119]\n"
"v_mfma_f32_16x16x32_bf16 v[96:99], a[208:211], v[224:227], v[96:99]\n"
"ds_read_b64_tr_b16 a[228:229], v6 offset:0\n"
"ds_read_b64_tr_b16 a[230:231], v6 offset:6144\n"
"v_mfma_f32_16x16x32_bf16 v[104:107], a[208:211], v[228:231], v[104:107]\n"
"v_mfma_f32_16x16x32_bf16 v[112:115], a[208:211], v[232:235], v[112:115]\n"
"s_waitcnt lgkmcnt(4)\n"
"v_mfma_f32_16x16x32_bf16 v[100:103], a[212:215], v[224:227], v[100:103]\n"
"ds_read_b64_tr_b16 a[232:233], v7 offset:0\n"
"ds_read_b64_tr_b16 a[234:235], v7 offset:6144\n"
"v_mfma_f32_16x16x32_bf16 v[108:111], a[212:215], v[228:231], v[108:111]\n"
"v_mfma_f32_16x16x32_bf16 v[116:119], a[212:215], v[232:235], v[116:119]\n"
"v_mfma_f32_16x16x32_bf16 v[96:99], a[216:219], v[240:243], v[96:99]\n"
"ds_read_b64_tr_b16 a[236:237], v8 offset:0\n"
"ds_read_b64_tr_b16 a[238:239], v8 offset:6144\n"
"v_mfma_f32_16x16x32_bf16 v[104:107], a[216:219], v[244:247], v[104:107]\n"
"v_mfma_f32_16x16x32_bf16 v[112:115], a[216:219], v[248:251], v[112:115]\n"
"v_mfma_f32_16x16x32_bf16 v[100:103], a[220:223], v[240:243], v[100:103]\n"
"ds_read_b64_tr_b16 a[240:241], v5 offset:128\n"
"ds_read_b64_tr_b16 a[242:243], v5 offset:6272\n"
"v_mfma_f32_16x16x32_bf16 v[108:111], a[220:223], v[244:247], v[108:111]\n"
"v_mfma_f32_16x16x32_bf16 v[116:119], a[220:223], v[248:251], v[116:119]\n"
"v_max3_f32 v53, v96, v97, v98\n"
"s_waitcnt lgkmcnt(4)\n"
"v_mfma_f32_16x16x32_bf16 a[0:3], a[224:227], v[128:131], 0\n"
"ds_read_b64_tr_b16 a[244:245], v6 offset:128\n"
"ds_read_b64_tr_b16 a[246:247], v6 offset:6272\n"
"v_max3_f32 v54, v99, v104, v105\n"
"v_mfma_f32_16x16x32_bf16 a[4:7], a[224:227], v[132:135], 0\n"
"v_max3_f32 v53, v53, v106, v107\n"
"v_max3_f32 v54, v54, v112, v113\n"
"v_max3_f32 v53, v53, v114, v115\n"
"v_mfma_f32_16x16x32_bf16 a[8:11], a[224:227], v[136:139], 0\n"
"v_max3_f32 v54, v54, v100, v101\n"
"v_max3_f32 v53, v53, v102, v103\n"
"v_mfma_f32_16x16x32_bf16 a[16:19], a[228:231], v[128:131], 0\n"
"v_max3_f32 v54, v54, v108, v109\n"
"v_max3_f32 v53, v53, v110, v111\n"
"ds_read_b64_tr_b16 a[248:249], v7 offset:128\n"
"v_mfma_f32_16x16x32_bf16 a[20:23], a[228:231], v[132:135], 0\n"
"v_max3_f32 v54, v54, v116, v117\n"
"v_max3_f32 v53, v53, v118, v119\n"
"v_max_f32 v53, v53, v54\n"
"v_mfma_f32_16x16x32_bf16 a[24:27], a[228:231], v[136:139], 0\n"
"v_cmp_lt_f32 vcc, s64, v53\n"
"ds_read_b64_tr_b16 a[250:251], v7 offset:6272\n"
"v_mfma_f32_16x16x32_bf16 a[32:35], a[232:235], v[128:131], 0\n"
"ds_read_b64_tr_b16 a[252:253], v8 offset:128\n"
"ds_read_b64_tr_b16 a[254:255], v8 offset:6272\n"
"v_mfma_f32_16x16x32_bf16 a[36:39], a[232:235], v[132:135], 0\n"
"v_mfma_f32_16x16x32_bf16 a[40:43], a[232:235], v[136:139], 0\n"
"s_waitcnt lgkmcnt(4)\n"
"v_mfma_f32_16x16x32_bf16 a[48:51], a[236:239], v[128:131], 0\n"
"s_cbranch_vccnz K1B_RS0\n"
"K1B_RSb0:\n"
"ds_read_b64_tr_b16 a[224:225], v5 offset:256\n"
"ds_read_b64_tr_b16 a[226:227], v5 offset:6400\n"
"v_mfma_f32_16x16x32_bf16 a[52:55], a[236:239], v[132:135], 0\n"
"v_exp_f32 v96, v96\n"
"v_mfma_f32_16x16x32_bf16 a[56:59], a[236:239], v[136:139], 0\n"
"v_exp_f32 v97, v97\n"
"v_add_f32 v37, v37, v96\n"
"v_mfma_f32_16x16x32_bf16 a[64:67], a[240:243], v[128:131], 0\n"
"ds_read_b64_tr_b16 a[228:229], v6 offset:256\n"
"ds_read_b64_tr_b16 a[230:231], v6 offset:6400\n"
"v_add_f32 v38, v38, v97\n"
"v_mfma_f32_16x16x32_bf16 a[68:71], a[240:243], v[132:135], 0\n"
"v_exp_f32 v98, v98\n"
"v_mfma_f32_16x16x32_bf16 a[72:75], a[240:243], v[136:139], 0\n"
"v_exp_f32 v99, v99\n"
"v_add_f32 v37, v37, v98\n"
"v_mfma_f32_16x16x32_bf16 a[80:83], a[244:247], v[128:131], 0\n"
"ds_read_b64_tr_b16 a[232:233], v7 offset:256\n"
"ds_read_b64_tr_b16 a[234:235], v7 offset:6400\n"
"v_add_f32 v38, v38, v99\n"
"v_mfma_f32_16x16x32_bf16 a[84:87], a[244:247], v[132:135], 0\n"
"v_exp_f32 v100, v100\n"
"v_mfma_f32_16x16x32_bf16 a[88:91], a[244:247], v[136:139], 0\n"
"v_exp_f32 v101, v101\n"
"s_waitcnt lgkmcnt(4)\n"
"v_mfma_f32_16x16x32_bf16 a[96:99], a[248:251], v[128:131], 0\n"
"ds_read_b64_tr_b16 a[236:237], v8 offset:256\n"
"ds_read_b64_tr_b16 a[238:239], v8 offset:6400\n"
"v_add_f32 v37, v37, v100\n"
"v_mfma_f32_16x16x32_bf16 a[100:103], a[248:251], v[132:135], 0\n"
"s_waitcnt vmcnt(48)\n"
"K1B_PC0:\n"
"s_memrealtime s[92:93]\n"
"s_waitcnt lgkmcnt(0)\n"
"s_sub_u32 s94, s92, s95\n"
"s_cmp_lt_u32 s94, 96\n"
"s_cbranch_scc1 K1B_PC0\n"
"s_mov_b32 s95, s92\n"
"s_barrier\n"
"v_add_f32 v38, v38, v101\n"
"v_mfma_f32_16x16x32_bf16 a[104:107], a[248:251], v[136:139], 0\n"
"s_mov_b32 m0, 135168\n"
"v_exp_f32 v102, v102\n"
"v_mfma_f32_16x16x32_bf16 a[112:115], a[252:255], v[128:131], 0\n"
"ds_read_b128 a[192:195], v1 offset:24576\n"
"buffer_load_dwordx4 v13, s[44:47], s59 offen offset:0 nt lds\n"
"v_add_f32 v37, v37, v102\n"
"v_mfma_f32_16x16x32_bf16 a[116:119], a[252:255], v[132:135], 0\n"
"v_exp_f32 v103, v103\n"
"v_mfma_f32_16x16x32_bf16 a[120:123], a[252:255], v[136:139], 0\n"
"buffer_load_dwordx4 v14, s[44:47], s59 offen offset:1024 nt lds\n"
"v_exp_f32 v104, v104\n"
"v_mfma_f32_16x16x32_bf16 a[128:131], a[224:227], v[128:131], 0\n"
"ds_read_b128 a[196:199], v1 offset:30720\n"
"v_exp_f32 v105, v105\n"
"v_mfma_f32_16x16x32_bf16 a[132:135], a[224:227], v[132:135], 0\n"
"v_exp_f32 v106, v106\n"
"v_add_f32 v38, v38, v103\n"
"v_mfma_f32_16x16x32_bf16 a[136:139], a[224:227], v[136:139], 0\n"
"buffer_load_dwordx4 v15, s[44:47], s59 offen offset:2048 nt lds\n"
"v_add_f32 v39, v39, v104\n"
"v_mfma_f32_16x16x32_bf16 a[144:147], a[228:231], v[128:131], 0\n"
"ds_read_b128 a[200:203], v2 offset:24576\n"
"v_exp_f32 v107, v107\n"
"v_mfma_f32_16x16x32_bf16 a[148:151], a[228:231], v[132:135], 0\n"
"v_add_f32 v40, v40, v105\n"
"v_add_f32 v39, v39, v106\n"
"v_add_f32 v40, v40, v107\n"
"v_mfma_f32_16x16x32_bf16 a[152:155], a[228:231], v[136:139], 0\n"
"buffer_load_dwordx4 v16, s[44:47], s59 offen offset:3072 nt lds\n"
"v_mfma_f32_16x16x32_bf16 a[160:163], a[232:235], v[128:131], 0\n"
"ds_read_b128 a[204:207], v2 offset:30720\n"
"v_mfma_f32_16x16x32_bf16 a[164:167], a[232:235], v[132:135], 0\n"
"v_mfma_f32_16x16x32_bf16 a[168:171], a[232:235], v[136:139], 0\n"
"s_mov_b32 m0, 139264\n"
"v_mfma_f32_16x16x32_bf16 a[176:179], a[236:239], v[128:131], 0\n"
"ds_read_b128 a[208:211], v1 offset:24704\n"
"buffer_load_dwordx4 v17, s[44:47], s59 offen offset:0 nt lds\n"
"v_mfma_f32_16x16x32_bf16 a[180:183], a[236:239], v[132:135], 0\n"
"v_mfma_f32_16x16x32_bf16 a[184:187], a[236:239], v[136:139], 0\n"
"s_cbranch_vccnz K1B_RO0\n"
"K1B_ROb0:\n"
"s_mov_b32 s61, 5\n"
"K1B_AL:\n"
"s_waitcnt lgkmcnt(2)\n"
"v_mfma_f32_16x16x32_bf16 v[64:67], a[192:195], v[160:163], v[144:147]\n"
"ds_read_b128 a[212:215], v1 offset:30848\n"
"buffer_load_dwordx4 v18, s[44:47], s59 offen offset:1024 nt lds\n"
"v_cvt_pk_bf16_f32 v128, v96, v97\n"
"v_mfma_f32_16x16x32_bf16 v[72:75], a[192:195], v[164:167], v[148:151]\n"
"v_exp_f32 v108, v108\n"
"v_cvt_pk_bf16_f32 v129, v98, v99\n"
"v_mfma_f32_16x16x32_bf16 v[80:83], a[192:195], v[168:171], v[152:155]\n"
"buffer_load_dwordx4 v19, s[44:47], s59 offen offset:2048 nt lds\n"
"v_exp_f32 v109, v109\n"
"v_mfma_f32_16x16x32_bf16 v[68:71], a[196:199], v[160:163], v[144:147]\n"
"ds_read_b128 a[216:219], v2 offset:24704\n"
"v_exp_f32 v110, v110\n"
"v_mfma_f32_16x16x32_bf16 v[76:79], a[196:199], v[164:167], v[148:151]\n"
"v_exp_f32 v111, v111\n"
"v_cvt_pk_bf16_f32 v130, v100, v101\n"
"v_mfma_f32_16x16x32_bf16 v[84:87], a[196:199], v[168:171], v[152:155]\n"
"buffer_load_dwordx4 v20, s[44:47], s59 offen offset:3072 nt lds\n"
"v_exp_f32 v112, v112\n"
"v_mfma_f32_16x16x32_bf16 v[64:67], a[200:203], v[176:179], v[64:67]\n"
"ds_read_b128 a[220:223], v2 offset:30848\n"
"v_exp_f32 v113, v113\n"
"v_mfma_f32_16x16x32_bf16 v[72:75], a[200:203], v[180:183], v[72:75]\n"
"v_exp_f32 v114, v114\n"
"v_cvt_pk_bf16_f32 v131, v102, v103\n"
"v_mfma_f32_16x16x32_bf16 v[80:83], a[200:203], v[184:187], v[80:83]\n"
"s_mov_b32 m0, 143360\n"
"v_cvt_pk_bf16_f32 v132, v104, v105\n"
"s_waitcnt lgkmcnt(2)\n"
"v_mfma_f32_16x16x32_bf16 v[68:71], a[204:207], v[176:179], v[68:71]\n"
"ds_read_b128 a[192:195], v1 offset:24832\n"
"buffer_load_dwordx4 v21, s[44:47], s59 offen offset:0 nt lds\n"
"v_cvt_pk_bf16_f32 v133, v106, v107\n"
"v_mfma_f32_16x16x32_bf16 v[76:79], a[204:207], v[180:183], v[76:79]\n"
"buffer_load_dwordx4 v22, s[44:47], s59 offen offset:1024 nt lds\n"
"v_exp_f32 v115, v115\n"
"v_mfma_f32_16x16x32_bf16 v[84:87], a[204:207], v[184:187], v[84:87]\n"
"v_exp_f32 v116, v116\n"
"v_cvt_pk_bf16_f32 v134, v108, v109\n"
"v_mfma_f32_16x16x32_bf16 v[64:67], a[208:211], v[192:195], v[64:67]\n"
"ds_read_b128 a[196:199], v1 offset:30976\n"
"v_exp_f32 v117, v117\n"
"v_mfma_f32_16x16x32_bf16 v[72:75], a[208:211], v[196:199], v[72:75]\n"
"buffer_load_dwordx4 v23, s[44:47], s59 offen offset:2048 nt lds\n"
"v_exp_f32 v118, v118\n"
"v_mfma_f32_16x16x32_bf16 v[80:83], a[208:211], v[200:203], v[80:83]\n"
"v_exp_f32 v119, v119\n"
"v_cvt_pk_bf16_f32 v135, v110, v111\n"
"v_mfma_f32_16x16x32_bf16 v[68:71], a[212:215], v[192:195], v[68:71]\n"
"ds_read_b128 a[200:203], v2 offset:24832\n"
"v_cvt_pk_bf16_f32 v136, v112, v113\n"
"v_cvt_pk_bf16_f32 v137, v114, v115\n"
"v_mfma_f32_16x16x32_bf16 v[76:79], a[212:215], v[196:199], v[76:79]\n"
"buffer_load_dwordx4 v24, s[44:47], s59 offen offset:3072 nt lds\n"
"v_cvt_pk_bf16_f32 v138, v116, v117\n"
"v_cvt_pk_bf16_f32 v139, v118, v119\n"
"v_mfma_f32_16x16x32_bf16 v[84:87], a[212:215], v[200:203], v[84:87]\n"
"v_add_f32 v39, v39, v108\n"
"v_add_f32 v40, v40, v109\n"
"s_waitcnt lgkmcnt(2)\n"
"v_mfma_f32_16x16x32_bf16 v[64:67], a[216:219], v[208:211], v[64:67]\n"
"ds_read_b128 a[204:207], v2 offset:30976\n"
"v_add_f32 v39, v39, v110\n"
"v_add_f32 v40, v40, v111\n"
"v_mfma_f32_16x16x32_bf16 v[72:75], a[216:219], v[212:215], v[72:75]\n"
"s_add_u32 s59, s59, 12288\n"
"s_waitcnt vmcnt(48)\n"
"s_mov_b32 m0, 49152\n"
"v_mfma_f32_16x16x32_bf16 v[80:83], a[216:219], v[216:219], v[80:83]\n"
"buffer_load_dwordx4 v25, s[48:51], s60 offen offset:0 nt lds\n"
"v_add_f32 v41, v41, v112\n"
"v_add_f32 v42, v42, v113\n"
"v_mfma_f32_16x16x32_bf16 v[68:71], a[220:223], v[208:211], v[68:71]\n"
"buffer_load_dwordx4 v26, s[48:51], s60 offen offset:1024 nt lds\n"
"ds_read_b64_tr_b16 a[240:241], v5 offset:12288\n"
"ds_read_b64_tr_b16 a[242:243], v5 offset:18432\n"
"v_mfma_f32_16x16x32_bf16 v[76:79], a[220:223], v[212:215], v[76:79]\n"
"v_add_f32 v41, v41, v114\n"
"v_add_f32 v42, v42, v115\n"
"v_add_f32 v41, v41, v116\n"
"v_mfma_f32_16x16x32_bf16 v[84:87], a[220:223], v[216:219], v[84:87]\n"
"v_add_f32 v42, v42, v117\n"
"v_add_f32 v41, v41, v118\n"
"v_add_f32 v42, v42, v119\n"
"v_mfma_f32_16x16x32_bf16 v[64:67], a[192:195], v[224:227], v[64:67]\n"
"buffer_load_dwordx4 v27, s[48:51], s60 offen offset:2048 nt lds\n"
"ds_read_b64_tr_b16 a[244:245], v6 offset:12288\n"
"ds_read_b64_tr_b16 a[246:247], v6 offset:18432\n"
"v_mfma_f32_16x16x32_bf16 v[72:75], a[192:195], v[228:231], v[72:75]\n"
"v_mfma_f32_16x16x32_bf16 v[80:83], a[192:195], v[232:235], v[80:83]\n"
"s_waitcnt lgkmcnt(4)\n"
"v_mfma_f32_16x16x32_bf16 v[68:71], a[196:199], v[224:227], v[68:71]\n"
"ds_read_b64_tr_b16 a[248:249], v7 offset:12288\n"
"ds_read_b64_tr_b16 a[250:251], v7 offset:18432\n"
"buffer_load_dwordx4 v28, s[48:51], s60 offen offset:3072 nt lds\n"
"v_mfma_f32_16x16x32_bf16 v[76:79], a[196:199], v[228:231], v[76:79]\n"
"v_mfma_f32_16x16x32_bf16 v[84:87], a[196:199], v[232:235], v[84:87]\n"
"v_mfma_f32_16x16x32_bf16 v[64:67], a[200:203], v[240:243], v[64:67]\n"
"s_mov_b32 m0, 53248\n"
"ds_read_b64_tr_b16 a[252:253], v8 offset:12288\n"
"ds_read_b64_tr_b16 a[254:255], v8 offset:18432\n"
"v_mfma_f32_16x16x32_bf16 v[72:75], a[200:203], v[244:247], v[72:75]\n"
"buffer_load_dwordx4 v29, s[48:51], s60 offen offset:0 nt lds\n"
"v_mfma_f32_16x16x32_bf16 v[80:83], a[200:203], v[248:251], v[80:83]\n"
"buffer_load_dwordx4 v30, s[48:51], s60 offen offset:1024 nt lds\n"
"v_mfma_f32_16x16x32_bf16 v[68:71], a[204:207], v[240:243], v[68:71]\n"
"ds_read_b64_tr_b16 a[224:225], v5 offset:12416\n"
"ds_read_b64_tr_b16 a[226:227], v5 offset:18560\n"
"v_mfma_f32_16x16x32_bf16 v[76:79], a[204:207], v[244:247], v[76:79]\n"
"v_mfma_f32_16x16x32_bf16 v[84:87], a[204:207], v[248:251], v[84:87]\n"
"buffer_load_dwordx4 v31, s[48:51], s60 offen offset:2048 nt lds\n"
"v_max3_f32 v53, v64, v65, v66\n"
"s_waitcnt lgkmcnt(4)\n"
"v_mfma_f32_16x16x32_bf16 a[0:3], a[240:243], v[128:131], a[0:3]\n"
"ds_read_b64_tr_b16 a[228:229], v6 offset:12416\n"
"ds_read_b64_tr_b16 a[230:231], v6 offset:18560\n"
"v_max3_f32 v54, v67, v72, v73\n"
"v_mfma_f32_16x16x32_bf16 a[4:7], a[240:243], v[132:135], a[4:7]\n"
"v_max3_f32 v53, v53, v74, v75\n"
"v_max3_f32 v54, v54, v80, v81\n"
"v_max3_f32 v53, v53, v82, v83\n"
"v_mfma_f32_16x16x32_bf16 a[8:11], a[240:243], v[136:139], a[8:11]\n"
"buffer_load_dwordx4 v32, s[48:51], s60 offen offset:3072 nt lds\n"
"v_max3_f32 v54, v54, v68, v69\n"
"v_max3_f32 v53, v53, v70, v71\n"
"v_mfma_f32_16x16x32_bf16 a[16:19], a[244:247], v[128:131], a[16:19]\n"
"v_max3_f32 v54, v54, v76, v77\n"
"v_max3_f32 v53, v53, v78, v79\n"
"ds_read_b64_tr_b16 a[232:233], v7 offset:12416\n"
"v_mfma_f32_16x16x32_bf16 a[20:23], a[244:247], v[132:135], a[20:23]\n"
"v_max3_f32 v54, v54, v84, v85\n"
"v_max3_f32 v53, v53, v86, v87\n"
"v_max_f32 v53, v53, v54\n"
"v_mfma_f32_16x16x32_bf16 a[24:27], a[244:247], v[136:139], a[24:27]\n"
"v_cmp_lt_f32 vcc, s64, v53\n"
"s_mov_b32 m0, 57344\n"
"ds_read_b64_tr_b16 a[234:235], v7 offset:18560\n"
"v_mfma_f32_16x16x32_bf16 a[32:35], a[248:251], v[128:131], a[32:35]\n"
"buffer_load_dwordx4 v33, s[48:51], s60 offen offset:0 nt lds\n"
"ds_read_b64_tr_b16 a[236:237], v8 offset:12416\n"
"ds_read_b64_tr_b16 a[238:239], v8 offset:18560\n"
"v_mfma_f32_16x16x32_bf16 a[36:39], a[248:251], v[132:135], a[36:39]\n"
"buffer_load_dwordx4 v34, s[48:51], s60 offen offset:1024 nt lds\n"
"v_mfma_f32_16x16x32_bf16 a[40:43], a[248:251], v[136:139], a[40:43]\n"
"s_waitcnt lgkmcnt(4)\n"
"v_mfma_f32_16x16x32_bf16 a[48:51], a[252:255], v[128:131], a[48:51]\n"
"s_cbranch_vccnz K1B_RS1\n"
"K1B_RSb1:\n"
"ds_read_b64_tr_b16 a[240:241], v5 offset:12544\n"
"ds_read_b64_tr_b16 a[242:243], v5 offset:18688\n"
"v_mfma_f32_16x16x32_bf16 a[52:55], a[252:255], v[132:135], a[52:55]\n"
"buffer_load_dwordx4 v35, s[48:51], s60 offen offset:2048 nt lds\n"
"v_exp_f32 v64, v64\n"
"v_mfma_f32_16x16x32_bf16 a[56:59], a[252:255], v[136:139], a[56:59]\n"
"v_exp_f32 v65, v65\n"
"v_add_f32 v37, v37, v64\n"
"v_mfma_f32_16x16x32_bf16 a[64:67], a[224:227], v[128:131], a[64:67]\n"
"ds_read_b64_tr_b16 a[244:245], v6 offset:12544\n"
"ds_read_b64_tr_b16 a[246:247], v6 offset:18688\n"
"v_add_f32 v38, v38, v65\n"
"v_mfma_f32_16x16x32_bf16 a[68:71], a[224:227], v[132:135], a[68:71]\n"
"buffer_load_dwordx4 v36, s[48:51], s60 offen offset:3072 nt lds\n"
"v_exp_f32 v66, v66\n"
"v_mfma_f32_16x16x32_bf16 a[72:75], a[224:227], v[136:139], a[72:75]\n"
"v_exp_f32 v67, v67\n"
"v_add_f32 v37, v37, v66\n"
"v_mfma_f32_16x16x32_bf16 a[80:83], a[228:231], v[128:131], a[80:83]\n"
"ds_read_b64_tr_b16 a[248:249], v7 offset:12544\n"
"ds_read_b64_tr_b16 a[250:251], v7 offset:18688\n"
"v_add_f32 v38, v38, v67\n"
"v_mfma_f32_16x16x32_bf16 a[84:87], a[228:231], v[132:135], a[84:87]\n"
"s_add_u32 s60, s60, 12288\n"
"v_exp_f32 v68, v68\n"
"v_mfma_f32_16x16x32_bf16 a[88:91], a[228:231], v[136:139], a[88:91]\n"
"v_exp_f32 v69, v69\n"
"s_waitcnt lgkmcnt(4)\n"
"v_mfma_f32_16x16x32_bf16 a[96:99], a[232:235], v[128:131], a[96:99]\n"
"ds_read_b64_tr_b16 a[252:253], v8 offset:12544\n"
"ds_read_b64_tr_b16 a[254:255], v8 offset:18688\n"
"v_add_f32 v37, v37, v68\n"
"v_mfma_f32_16x16x32_bf16 a[100:103], a[232:235], v[132:135], a[100:103]\n"
"s_waitcnt vmcnt(48)\n"
"K1B_PC1:\n"
"s_memrealtime s[92:93]\n"
"s_waitcnt lgkmcnt(0)\n"
"s_sub_u32 s94, s92, s95\n"
"s_cmp_lt_u32 s94, 96\n"
"s_cbranch_scc1 K1B_PC1\n"
"s_mov_b32 s95, s92\n"
"s_barrier\n"
"v_add_f32 v38, v38, v69\n"
"v_mfma_f32_16x16x32_bf16 a[104:107], a[232:235], v[136:139], a[104:107]\n"
"s_mov_b32 m0, 73728\n"
"v_exp_f32 v70, v70\n"
"v_mfma_f32_16x16x32_bf16 a[112:115], a[236:239], v[128:131], a[112:115]\n"
"ds_read_b128 a[208:211], v3 offset:0\n"
"buffer_load_dwordx4 v13, s[44:47], s59 offen offset:0 nt lds\n"
"v_add_f32 v37, v37, v70\n"
"v_mfma_f32_16x16x32_bf16 a[116:119], a[236:239], v[132:135], a[116:119]\n"
"v_exp_f32 v71, v71\n"
"v_mfma_f32_16x16x32_bf16 a[120:123], a[236:239], v[136:139], a[120:123]\n"
"buffer_load_dwordx4 v14, s[44:47], s59 offen offset:1024 nt lds\n"
"v_exp_f32 v72, v72\n"
"v_mfma_f32_16x16x32_bf16 a[128:131], a[240:243], v[128:131], a[128:131]\n"
"ds_read_b128 a[212:215], v3 offset:6144\n"
"v_exp_f32 v73, v73\n"
"v_mfma_f32_16x16x32_bf16 a[132:135], a[240:243], v[132:135], a[132:135]\n"
"v_exp_f32 v74, v74\n"
"v_add_f32 v38, v38, v71\n"
"v_mfma_f32_16x16x32_bf16 a[136:139], a[240:243], v[136:139], a[136:139]\n"
"buffer_load_dwordx4 v15, s[44:47], s59 offen offset:2048 nt lds\n"
"v_add_f32 v39, v39, v72\n"
"v_mfma_f32_16x16x32_bf16 a[144:147], a[244:247], v[128:131], a[144:147]\n"
"ds_read_b128 a[216:219], v4 offset:0\n"
"v_exp_f32 v75, v75\n"
"v_mfma_f32_16x16x32_bf16 a[148:151], a[244:247], v[132:135], a[148:151]\n"
"v_add_f32 v40, v40, v73\n"
"v_add_f32 v39, v39, v74\n"
"v_add_f32 v40, v40, v75\n"
"v_mfma_f32_16x16x32_bf16 a[152:155], a[244:247], v[136:139], a[152:155]\n"
"buffer_load_dwordx4 v16, s[44:47], s59 offen offset:3072 nt lds\n"
"v_mfma_f32_16x16x32_bf16 a[160:163], a[248:251], v[128:131], a[160:163]\n"
"ds_read_b128 a[220:223], v4 offset:6144\n"
"v_mfma_f32_16x16x32_bf16 a[164:167], a[248:251], v[132:135], a[164:167]\n"
"v_mfma_f32_16x16x32_bf16 a[168:171], a[248:251], v[136:139], a[168:171]\n"
"s_mov_b32 m0, 77824\n"
"v_mfma_f32_16x16x32_bf16 a[176:179], a[252:255], v[128:131], a[176:179]\n"
"ds_read_b128 a[192:195], v3 offset:128\n"
"buffer_load_dwordx4 v17, s[44:47], s59 offen offset:0 nt lds\n"
"v_mfma_f32_16x16x32_bf16 a[180:183], a[252:255], v[132:135], a[180:183]\n"
"v_mfma_f32_16x16x32_bf16 a[184:187], a[252:255], v[136:139], a[184:187]\n"
"s_cbranch_vccnz K1B_RO1\n"
"K1B_ROb1:\n"
"s_waitcnt lgkmcnt(2)\n"
"v_mfma_f32_16x16x32_bf16 v[96:99], a[208:211], v[160:163], v[144:147]\n"
"ds_read_b128 a[196:199], v3 offset:6272\n"
"buffer_load_dwordx4 v18, s[44:47], s59 offen offset:1024 nt lds\n"
"v_cvt_pk_bf16_f32 v128, v64, v65\n"
"v_mfma_f32_16x16x32_bf16 v[104:107], a[208:211], v[164:167], v[148:151]\n"
"v_exp_f32 v76, v76\n"
"v_cvt_pk_bf16_f32 v129, v66, v67\n"
"v_mfma_f32_16x16x32_bf16 v[112:115], a[208:211], v[168:171], v[152:155]\n"
"buffer_load_dwordx4 v19, s[44:47], s59 offen offset:2048 nt lds\n"
"v_exp_f32 v77, v77\n"
"v_mfma_f32_16x16x32_bf16 v[100:103], a[212:215], v[160:163], v[144:147]\n"
"ds_read_b128 a[200:203], v4 offset:128\n"
"v_exp_f32 v78, v78\n"
"v_mfma_f32_16x16x32_bf16 v[108:111], a[212:215], v[164:167], v[148:151]\n"
"v_exp_f32 v79, v79\n"
"v_cvt_pk_bf16_f32 v130, v68, v69\n"
"v_mfma_f32_16x16x32_bf16 v[116:119], a[212:215], v[168:171], v[152:155]\n"
"buffer_load_dwordx4 v20, s[44:47], s59 offen offset:3072 nt lds\n"
"v_exp_f32 v80, v80\n"
"v_mfma_f32_16x16x32_bf16 v[96:99], a[216:219], v[176:179], v[96:99]\n"
"ds_read_b128 a[204:207], v4 offset:6272\n"
"v_exp_f32 v81, v81\n"
"v_mfma_f32_16x16x32_bf16 v[104:107], a[216:219], v[180:183], v[104:107]\n"
"v_exp_f32 v82, v82\n"
"v_cvt_pk_bf16_f32 v131, v70, v71\n"
"v_mfma_f32_16x16x32_bf16 v[112:115], a[216:219], v[184:187], v[112:115]\n"
"s_mov_b32 m0, 81920\n"
"v_cvt_pk_bf16_f32 v132, v72, v73\n"
"s_waitcnt lgkmcnt(2)\n"
"v_mfma_f32_16x16x32_bf16 v[100:103], a[220:223], v[176:179], v[100:103]\n"
"ds_read_b128 a[208:211], v3 offset:256\n"
"buffer_load_dwordx4 v21, s[44:47], s59 offen offset:0 nt lds\n"
"v_cvt_pk_bf16_f32 v133, v74, v75\n"
"v_mfma_f32_16x16x32_bf16 v[108:111], a[220:223], v[180:183], v[108:111]\n"
"buffer_load_dwordx4 v22, s[44:47], s59 offen offset:1024 nt lds\n"
"v_exp_f32 v83, v83\n"
"v_mfma_f32_16x16x32_bf16 v[116:119], a[220:223], v[184:187], v[116:119]\n"
"v_exp_f32 v84, v84\n"
"v_cvt_pk_bf16_f32 v134, v76, v77\n"
"v_mfma_f32_16x16x32_bf16 v[96:99], a[192:195], v[192:195], v[96:99]\n"
"ds_read_b128 a[212:215], v3 offset:6400\n"
"v_exp_f32 v85, v85\n"
"v_mfma_f32_16x16x32_bf16 v[104:107], a[192:195], v[196:199], v[104:107]\n"
"buffer_load_dwordx4 v23, s[44:47], s59 offen offset:2048 nt lds\n"
"v_exp_f32 v86, v86\n"
"v_mfma_f32_16x16x32_bf16 v[112:115], a[192:195], v[200:203], v[112:115]\n"
"v_exp_f32 v87, v87\n"
"v_cvt_pk_bf16_f32 v135, v78, v79\n"
"v_mfma_f32_16x16x32_bf16 v[100:103], a[196:199], v[192:195], v[100:103]\n"
"ds_read_b128 a[216:219], v4 offset:256\n"
"v_cvt_pk_bf16_f32 v136, v80, v81\n"
"v_cvt_pk_bf16_f32 v137, v82, v83\n"
"v_mfma_f32_16x16x32_bf16 v[108:111], a[196:199], v[196:199], v[108:111]\n"
"buffer_load_dwordx4 v24, s[44:47], s59 offen offset:3072 nt lds\n"
"v_cvt_pk_bf16_f32 v138, v84, v85\n"
"v_cvt_pk_bf16_f32 v139, v86, v87\n"
"v_mfma_f32_16x16x32_bf16 v[116:119], a[196:199], v[200:203], v[116:119]\n"
"v_add_f32 v39, v39, v76\n"
"v_add_f32 v40, v40, v77\n"
"s_waitcnt lgkmcnt(2)\n"
"v_mfma_f32_16x16x32_bf16 v[96:99], a[200:203], v[208:211], v[96:99]\n"
"ds_read_b128 a[220:223], v4 offset:6400\n"
"v_add_f32 v39, v39, v78\n"
"v_add_f32 v40, v40, v79\n"
"v_mfma_f32_16x16x32_bf16 v[104:107], a[200:203], v[212:215], v[104:107]\n"
"s_add_u32 s59, s59, 12288\n"
"s_waitcnt vmcnt(48)\n"
"s_mov_b32 m0, 61440\n"
"v_mfma_f32_16x16x32_bf16 v[112:115], a[200:203], v[216:219], v[112:115]\n"
"buffer_load_dwordx4 v25, s[48:51], s60 offen offset:0 nt lds\n"
"v_add_f32 v41, v41, v80\n"
"v_add_f32 v42, v42, v81\n"
"v_mfma_f32_16x16x32_bf16 v[100:103], a[204:207], v[208:211], v[100:103]\n"
"buffer_load_dwordx4 v26, s[48:51], s60 offen offset:1024 nt lds\n"
"ds_read_b64_tr_b16 a[224:225], v5 offset:24576\n"
"ds_read_b64_tr_b16 a[226:227], v5 offset:30720\n"
"v_mfma_f32_16x16x32_bf16 v[108:111], a[204:207], v[212:215], v[108:111]\n"
"v_add_f32 v41, v41, v82\n"
"v_add_f32 v42, v42, v83\n"
"v_add_f32 v41, v41, v84\n"
"v_mfma_f32_16x16x32_bf16 v[116:119], a[204:207], v[216:219], v[116:119]\n"
"v_add_f32 v42, v42, v85\n"
"v_add_f32 v41, v41, v86\n"
"v_add_f32 v42, v42, v87\n"
"v_mfma_f32_16x16x32_bf16 v[96:99], a[208:211], v[224:227], v[96:99]\n"
"buffer_load_dwordx4 v27, s[48:51], s60 offen offset:2048 nt lds\n"
"ds_read_b64_tr_b16 a[228:229], v6 offset:24576\n"
"ds_read_b64_tr_b16 a[230:231], v6 offset:30720\n"
"v_mfma_f32_16x16x32_bf16 v[104:107], a[208:211], v[228:231], v[104:107]\n"
"v_mfma_f32_16x16x32_bf16 v[112:115], a[208:211], v[232:235], v[112:115]\n"
"s_waitcnt lgkmcnt(4)\n"
"v_mfma_f32_16x16x32_bf16 v[100:103], a[212:215], v[224:227], v[100:103]\n"
"ds_read_b64_tr_b16 a[232:233], v7 offset:24576\n"
"ds_read_b64_tr_b16 a[234:235], v7 offset:30720\n"
"buffer_load_dwordx4 v28, s[48:51], s60 offen offset:3072 nt lds\n"
"v_mfma_f32_16x16x32_bf16 v[108:111], a[212:215], v[228:231], v[108:111]\n"
"v_mfma_f32_16x16x32_bf16 v[116:119], a[212:215], v[232:235], v[116:119]\n"
"v_mfma_f32_16x16x32_bf16 v[96:99], a[216:219], v[240:243], v[96:99]\n"
"s_mov_b32 m0, 65536\n"
"ds_read_b64_tr_b16 a[236:237], v8 offset:24576\n"
"ds_read_b64_tr_b16 a[238:239], v8 offset:30720\n"
"v_mfma_f32_16x16x32_bf16 v[104:107], a[216:219], v[244:247], v[104:107]\n"
"buffer_load_dwordx4 v29, s[48:51], s60 offen offset:0 nt lds\n"
"v_mfma_f32_16x16x32_bf16 v[112:115], a[216:219], v[248:251], v[112:115]\n"
"buffer_load_dwordx4 v30, s[48:51], s60 offen offset:1024 nt lds\n"
"v_mfma_f32_16x16x32_bf16 v[100:103], a[220:223], v[240:243], v[100:103]\n"
"ds_read_b64_tr_b16 a[240:241], v5 offset:24704\n"
"ds_read_b64_tr_b16 a[242:243], v5 offset:30848\n"
"v_mfma_f32_16x16x32_bf16 v[108:111], a[220:223], v[244:247], v[108:111]\n"
"v_mfma_f32_16x16x32_bf16 v[116:119], a[220:223], v[248:251], v[116:119]\n"
"buffer_load_dwordx4 v31, s[48:51], s60 offen offset:2048 nt lds\n"
"v_max3_f32 v53, v96, v97, v98\n"
"s_waitcnt lgkmcnt(4)\n"
"v_mfma_f32_16x16x32_bf16 a[0:3], a[224:227], v[128:131], a[0:3]\n"
"ds_read_b64_tr_b16 a[244:245], v6 offset:24704\n"
"ds_read_b64_tr_b16 a[246:247], v6 offset:30848\n"
"v_max3_f32 v54, v99, v104, v105\n"
"v_mfma_f32_16x16x32_bf16 a[4:7], a[224:227], v[132:135], a[4:7]\n"
"v_max3_f32 v53, v53, v106, v107\n"
"v_max3_f32 v54, v54, v112, v113\n"
"v_max3_f32 v53, v53, v114, v115\n"
"v_mfma_f32_16x16x32_bf16 a[8:11], a[224:227], v[136:139], a[8:11]\n"
"buffer_load_dwordx4 v32, s[48:51], s60 offen offset:3072 nt lds\n"
"v_max3_f32 v54, v54, v100, v101\n"
"v_max3_f32 v53, v53, v102, v103\n"
"v_mfma_f32_16x16x32_bf16 a[16:19], a[228:231], v[128:131], a[16:19]\n"
"v_max3_f32 v54, v54, v108, v109\n"
"v_max3_f32 v53, v53, v110, v111\n"
"ds_read_b64_tr_b16 a[248:249], v7 offset:24704\n"
"v_mfma_f32_16x16x32_bf16 a[20:23], a[228:231], v[132:135], a[20:23]\n"
"v_max3_f32 v54, v54, v116, v117\n"
"v_max3_f32 v53, v53, v118, v119\n"
"v_max_f32 v53, v53, v54\n"
"v_mfma_f32_16x16x32_bf16 a[24:27], a[228:231], v[136:139], a[24:27]\n"
"v_cmp_lt_f32 vcc, s64, v53\n"
"s_mov_b32 m0, 69632\n"
"ds_read_b64_tr_b16 a[250:251], v7 offset:30848\n"
"v_mfma_f32_16x16x32_bf16 a[32:35], a[232:235], v[128:131], a[32:35]\n"
"buffer_load_dwordx4 v33, s[48:51], s60 offen offset:0 nt lds\n"
"ds_read_b64_tr_b16 a[252:253], v8 offset:24704\n"
"ds_read_b64_tr_b16 a[254:255], v8 offset:30848\n"
"v_mfma_f32_16x16x32_bf16 a[36:39], a[232:235], v[132:135], a[36:39]\n"
"buffer_load_dwordx4 v34, s[48:51], s60 offen offset:1024 nt lds\n"
"v_mfma_f32_16x16x32_bf16 a[40:43], a[232:235], v[136:139], a[40:43]\n"
"s_waitcnt lgkmcnt(4)\n"
"v_mfma_f32_16x16x32_bf16 a[48:51], a[236:239], v[128:131], a[48:51]\n"
"s_cbranch_vccnz K1B_RS2\n"
"K1B_RSb2:\n"
"ds_read_b64_tr_b16 a[224:225], v5 offset:24832\n"
"ds_read_b64_tr_b16 a[226:227], v5 offset:30976\n"
"v_mfma_f32_16x16x32_bf16 a[52:55], a[236:239], v[132:135], a[52:55]\n"
"buffer_load_dwordx4 v35, s[48:51], s60 offen offset:2048 nt lds\n"
"v_exp_f32 v96, v96\n"
"v_mfma_f32_16x16x32_bf16 a[56:59], a[236:239], v[136:139], a[56:59]\n"
"v_exp_f32 v97, v97\n"
"v_add_f32 v37, v37, v96\n"
"v_mfma_f32_16x16x32_bf16 a[64:67], a[240:243], v[128:131], a[64:67]\n"
"ds_read_b64_tr_b16 a[228:229], v6 offset:24832\n"
"ds_read_b64_tr_b16 a[230:231], v6 offset:30976\n"
"v_add_f32 v38, v38, v97\n"
"v_mfma_f32_16x16x32_bf16 a[68:71], a[240:243], v[132:135], a[68:71]\n"
"buffer_load_dwordx4 v36, s[48:51], s60 offen offset:3072 nt lds\n"
"v_exp_f32 v98, v98\n"
"v_mfma_f32_16x16x32_bf16 a[72:75], a[240:243], v[136:139], a[72:75]\n"
"v_exp_f32 v99, v99\n"
"v_add_f32 v37, v37, v98\n"
"v_mfma_f32_16x16x32_bf16 a[80:83], a[244:247], v[128:131], a[80:83]\n"
"ds_read_b64_tr_b16 a[232:233], v7 offset:24832\n"
"ds_read_b64_tr_b16 a[234:235], v7 offset:30976\n"
"v_add_f32 v38, v38, v99\n"
"v_mfma_f32_16x16x32_bf16 a[84:87], a[244:247], v[132:135], a[84:87]\n"
"s_add_u32 s60, s60, 12288\n"
"v_exp_f32 v100, v100\n"
"v_mfma_f32_16x16x32_bf16 a[88:91], a[244:247], v[136:139], a[88:91]\n"
"v_exp_f32 v101, v101\n"
"s_waitcnt lgkmcnt(4)\n"
"v_mfma_f32_16x16x32_bf16 a[96:99], a[248:251], v[128:131], a[96:99]\n"
"ds_read_b64_tr_b16 a[236:237], v8 offset:24832\n"
"ds_read_b64_tr_b16 a[238:239], v8 offset:30976\n"
"v_add_f32 v37, v37, v100\n"
"v_mfma_f32_16x16x32_bf16 a[100:103], a[248:251], v[132:135], a[100:103]\n"
"s_waitcnt vmcnt(48)\n"
"K1B_PC2:\n"
"s_memrealtime s[92:93]\n"
"s_waitcnt lgkmcnt(0)\n"
"s_sub_u32 s94, s92, s95\n"
"s_cmp_lt_u32 s94, 96\n"
"s_cbranch_scc1 K1B_PC2\n"
"s_mov_b32 s95, s92\n"
"s_barrier\n"
"v_add_f32 v38, v38, v101\n"
"v_mfma_f32_16x16x32_bf16 a[104:107], a[248:251], v[136:139], a[104:107]\n"
"s_mov_b32 m0, 86016\n"
"v_exp_f32 v102, v102\n"
"v_mfma_f32_16x16x32_bf16 a[112:115], a[252:255], v[128:131], a[112:115]\n"
"ds_read_b128 a[192:195], v3 offset:12288\n"
"buffer_load_dwordx4 v13, s[44:47], s59 offen offset:0 nt lds\n"
"v_add_f32 v37, v37, v102\n"
"v_mfma_f32_16x16x32_bf16 a[116:119], a[252:255], v[132:135], a[116:119]\n"
"v_exp_f32 v103, v103\n"
"v_mfma_f32_16x16x32_bf16 a[120:123], a[252:255], v[136:139], a[120:123]\n"
"buffer_load_dwordx4 v14, s[44:47], s59 offen offset:1024 nt lds\n"
"v_exp_f32 v104, v104\n"
"v_mfma_f32_16x16x32_bf16 a[128:131], a[224:227], v[128:131], a[128:131]\n"
"ds_read_b128 a[196:199], v3 offset:18432\n"
"v_exp_f32 v105, v105\n"
"v_mfma_f32_16x16x32_bf16 a[132:135], a[224:227], v[132:135], a[132:135]\n"
"v_exp_f32 v106, v106\n"
"v_add_f32 v38, v38, v103\n"
"v_mfma_f32_16x16x32_bf16 a[136:139], a[224:227], v[136:139], a[136:139]\n"
"buffer_load_dwordx4 v15, s[44:47], s59 offen offset:2048 nt lds\n"
"v_add_f32 v39, v39, v104\n"
"v_mfma_f32_16x16x32_bf16 a[144:147], a[228:231], v[128:131], a[144:147]\n"
"ds_read_b128 a[200:203], v4 offset:12288\n"
"v_exp_f32 v107, v107\n"
"v_mfma_f32_16x16x32_bf16 a[148:151], a[228:231], v[132:135], a[148:151]\n"
"v_add_f32 v40, v40, v105\n"
"v_add_f32 v39, v39, v106\n"
"v_add_f32 v40, v40, v107\n"
"v_mfma_f32_16x16x32_bf16 a[152:155], a[228:231], v[136:139], a[152:155]\n"
"buffer_load_dwordx4 v16, s[44:47], s59 offen offset:3072 nt lds\n"
"v_mfma_f32_16x16x32_bf16 a[160:163], a[232:235], v[128:131], a[160:163]\n"
"ds_read_b128 a[204:207], v4 offset:18432\n"
"v_mfma_f32_16x16x32_bf16 a[164:167], a[232:235], v[132:135], a[164:167]\n"
"v_mfma_f32_16x16x32_bf16 a[168:171], a[232:235], v[136:139], a[168:171]\n"
"s_mov_b32 m0, 90112\n"
"v_mfma_f32_16x16x32_bf16 a[176:179], a[236:239], v[128:131], a[176:179]\n"
"ds_read_b128 a[208:211], v3 offset:12416\n"
"buffer_load_dwordx4 v17, s[44:47], s59 offen offset:0 nt lds\n"
"v_mfma_f32_16x16x32_bf16 a[180:183], a[236:239], v[132:135], a[180:183]\n"
"v_mfma_f32_16x16x32_bf16 a[184:187], a[236:239], v[136:139], a[184:187]\n"
"s_cbranch_vccnz K1B_RO2\n"
"K1B_ROb2:\n"
"s_waitcnt lgkmcnt(2)\n"
"v_mfma_f32_16x16x32_bf16 v[64:67], a[192:195], v[160:163], v[144:147]\n"
"ds_read_b128 a[212:215], v3 offset:18560\n"
"buffer_load_dwordx4 v18, s[44:47], s59 offen offset:1024 nt lds\n"
"v_cvt_pk_bf16_f32 v128, v96, v97\n"
"v_mfma_f32_16x16x32_bf16 v[72:75], a[192:195], v[164:167], v[148:151]\n"
"v_exp_f32 v108, v108\n"
"v_cvt_pk_bf16_f32 v129, v98, v99\n"
"v_mfma_f32_16x16x32_bf16 v[80:83], a[192:195], v[168:171], v[152:155]\n"
"buffer_load_dwordx4 v19, s[44:47], s59 offen offset:2048 nt lds\n"
"v_exp_f32 v109, v109\n"
"v_mfma_f32_16x16x32_bf16 v[68:71], a[196:199], v[160:163], v[144:147]\n"
"ds_read_b128 a[216:219], v4 offset:12416\n"
"v_exp_f32 v110, v110\n"
"v_mfma_f32_16x16x32_bf16 v[76:79], a[196:199], v[164:167], v[148:151]\n"
"v_exp_f32 v111, v111\n"
"v_cvt_pk_bf16_f32 v130, v100, v101\n"
"v_mfma_f32_16x16x32_bf16 v[84:87], a[196:199], v[168:171], v[152:155]\n"
"buffer_load_dwordx4 v20, s[44:47], s59 offen offset:3072 nt lds\n"
"v_exp_f32 v112, v112\n"
"v_mfma_f32_16x16x32_bf16 v[64:67], a[200:203], v[176:179], v[64:67]\n"
"ds_read_b128 a[220:223], v4 offset:18560\n"
"v_exp_f32 v113, v113\n"
"v_mfma_f32_16x16x32_bf16 v[72:75], a[200:203], v[180:183], v[72:75]\n"
"v_exp_f32 v114, v114\n"
"v_cvt_pk_bf16_f32 v131, v102, v103\n"
"v_mfma_f32_16x16x32_bf16 v[80:83], a[200:203], v[184:187], v[80:83]\n"
"s_mov_b32 m0, 94208\n"
"v_cvt_pk_bf16_f32 v132, v104, v105\n"
"s_waitcnt lgkmcnt(2)\n"
"v_mfma_f32_16x16x32_bf16 v[68:71], a[204:207], v[176:179], v[68:71]\n"
"ds_read_b128 a[192:195], v3 offset:12544\n"
"buffer_load_dwordx4 v21, s[44:47], s59 offen offset:0 nt lds\n"
"v_cvt_pk_bf16_f32 v133, v106, v107\n"
"v_mfma_f32_16x16x32_bf16 v[76:79], a[204:207], v[180:183], v[76:79]\n"
"buffer_load_dwordx4 v22, s[44:47], s59 offen offset:1024 nt lds\n"
"v_exp_f32 v115, v115\n"
"v_mfma_f32_16x16x32_bf16 v[84:87], a[204:207], v[184:187], v[84:87]\n"
"v_exp_f32 v116, v116\n"
"v_cvt_pk_bf16_f32 v134, v108, v109\n"
"v_mfma_f32_16x16x32_bf16 v[64:67], a[208:211], v[192:195], v[64:67]\n"
"ds_read_b128 a[196:199], v3 offset:18688\n"
"v_exp_f32 v117, v117\n"
"v_mfma_f32_16x16x32_bf16 v[72:75], a[208:211], v[196:199], v[72:75]\n"
"buffer_load_dwordx4 v23, s[44:47], s59 offen offset:2048 nt lds\n"
"v_exp_f32 v118, v118\n"
"v_mfma_f32_16x16x32_bf16 v[80:83], a[208:211], v[200:203], v[80:83]\n"
"v_exp_f32 v119, v119\n"
"v_cvt_pk_bf16_f32 v135, v110, v111\n"
"v_mfma_f32_16x16x32_bf16 v[68:71], a[212:215], v[192:195], v[68:71]\n"
"ds_read_b128 a[200:203], v4 offset:12544\n"
"v_cvt_pk_bf16_f32 v136, v112, v113\n"
"v_cvt_pk_bf16_f32 v137, v114, v115\n"
"v_mfma_f32_16x16x32_bf16 v[76:79], a[212:215], v[196:199], v[76:79]\n"
"buffer_load_dwordx4 v24, s[44:47], s59 offen offset:3072 nt lds\n"
"v_cvt_pk_bf16_f32 v138, v116, v117\n"
"v_cvt_pk_bf16_f32 v139, v118, v119\n"
"v_mfma_f32_16x16x32_bf16 v[84:87], a[212:215], v[200:203], v[84:87]\n"
"v_add_f32 v39, v39, v108\n"
"v_add_f32 v40, v40, v109\n"
"s_waitcnt lgkmcnt(2)\n"
"v_mfma_f32_16x16x32_bf16 v[64:67], a[216:219], v[208:211], v[64:67]\n"
"ds_read_b128 a[204:207], v4 offset:18688\n"
"v_add_f32 v39, v39, v110\n"
"v_add_f32 v40, v40, v111\n"
"v_mfma_f32_16x16x32_bf16 v[72:75], a[216:219], v[212:215], v[72:75]\n"
"s_add_u32 s59, s59, 12288\n"
"s_waitcnt vmcnt(48)\n"
"s_mov_b32 m0, 0\n"
"v_mfma_f32_16x16x32_bf16 v[80:83], a[216:219], v[216:219], v[80:83]\n"
"buffer_load_dwordx4 v25, s[48:51], s60 offen offset:0 nt lds\n"
"v_add_f32 v41, v41, v112\n"
"v_add_f32 v42, v42, v113\n"
"v_mfma_f32_16x16x32_bf16 v[68:71], a[220:223], v[208:211], v[68:71]\n"
"buffer_load_dwordx4 v26, s[48:51], s60 offen offset:1024 nt lds\n"
"ds_read_b64_tr_b16 a[240:241], v9 offset:0\n"
"ds_read_b64_tr_b16 a[242:243], v9 offset:6144\n"
"v_mfma_f32_16x16x32_bf16 v[76:79], a[220:223], v[212:215], v[76:79]\n"
"v_add_f32 v41, v41, v114\n"
"v_add_f32 v42, v42, v115\n"
"v_add_f32 v41, v41, v116\n"
"v_mfma_f32_16x16x32_bf16 v[84:87], a[220:223], v[216:219], v[84:87]\n"
"v_add_f32 v42, v42, v117\n"
"v_add_f32 v41, v41, v118\n"
"v_add_f32 v42, v42, v119\n"
"v_mfma_f32_16x16x32_bf16 v[64:67], a[192:195], v[224:227], v[64:67]\n"
"buffer_load_dwordx4 v27, s[48:51], s60 offen offset:2048 nt lds\n"
"ds_read_b64_tr_b16 a[244:245], v10 offset:0\n"
"ds_read_b64_tr_b16 a[246:247], v10 offset:6144\n"
"v_mfma_f32_16x16x32_bf16 v[72:75], a[192:195], v[228:231], v[72:75]\n"
"v_mfma_f32_16x16x32_bf16 v[80:83], a[192:195], v[232:235], v[80:83]\n"
"s_waitcnt lgkmcnt(4)\n"
"v_mfma_f32_16x16x32_bf16 v[68:71], a[196:199], v[224:227], v[68:71]\n"
"ds_read_b64_tr_b16 a[248:249], v11 offset:0\n"
"ds_read_b64_tr_b16 a[250:251], v11 offset:6144\n"
"buffer_load_dwordx4 v28, s[48:51], s60 offen offset:3072 nt lds\n"
"v_mfma_f32_16x16x32_bf16 v[76:79], a[196:199], v[228:231], v[76:79]\n"
"v_mfma_f32_16x16x32_bf16 v[84:87], a[196:199], v[232:235], v[84:87]\n"
"v_mfma_f32_16x16x32_bf16 v[64:67], a[200:203], v[240:243], v[64:67]\n"
"s_mov_b32 m0, 4096\n"
"ds_read_b64_tr_b16 a[252:253], v12 offset:0\n"
"ds_read_b64_tr_b16 a[254:255], v12 offset:6144\n"
"v_mfma_f32_16x16x32_bf16 v[72:75], a[200:203], v[244:247], v[72:75]\n"
"buffer_load_dwordx4 v29, s[48:51], s60 offen offset:0 nt lds\n"
"v_mfma_f32_16x16x32_bf16 v[80:83], a[200:203], v[248:251], v[80:83]\n"
"buffer_load_dwordx4 v30, s[48:51], s60 offen offset:1024 nt lds\n"
"v_mfma_f32_16x16x32_bf16 v[68:71], a[204:207], v[240:243], v[68:71]\n"
"ds_read_b64_tr_b16 a[224:225], v9 offset:128\n"
"ds_read_b64_tr_b16 a[226:227], v9 offset:6272\n"
"v_mfma_f32_16x16x32_bf16 v[76:79], a[204:207], v[244:247], v[76:79]\n"
"v_mfma_f32_16x16x32_bf16 v[84:87], a[204:207], v[248:251], v[84:87]\n"
"buffer_load_dwordx4 v31, s[48:51], s60 offen offset:2048 nt lds\n"
"v_max3_f32 v53, v64, v65, v66\n"
"s_waitcnt lgkmcnt(4)\n"
"v_mfma_f32_16x16x32_bf16 a[0:3], a[240:243], v[128:131], a[0:3]\n"
"ds_read_b64_tr_b16 a[228:229], v10 offset:128\n"
"ds_read_b64_tr_b16 a[230:231], v10 offset:6272\n"
"v_max3_f32 v54, v67, v72, v73\n"
"v_mfma_f32_16x16x32_bf16 a[4:7], a[240:243], v[132:135], a[4:7]\n"
"v_max3_f32 v53, v53, v74, v75\n"
"v_max3_f32 v54, v54, v80, v81\n"
"v_max3_f32 v53, v53, v82, v83\n"
"v_mfma_f32_16x16x32_bf16 a[8:11], a[240:243], v[136:139], a[8:11]\n"
"buffer_load_dwordx4 v32, s[48:51], s60 offen offset:3072 nt lds\n"
"v_max3_f32 v54, v54, v68, v69\n"
"v_max3_f32 v53, v53, v70, v71\n"
"v_mfma_f32_16x16x32_bf16 a[16:19], a[244:247], v[128:131], a[16:19]\n"
"v_max3_f32 v54, v54, v76, v77\n"
"v_max3_f32 v53, v53, v78, v79\n"
"ds_read_b64_tr_b16 a[232:233], v11 offset:128\n"
"v_mfma_f32_16x16x32_bf16 a[20:23], a[244:247], v[132:135], a[20:23]\n"
"v_max3_f32 v54, v54, v84, v85\n"
"v_max3_f32 v53, v53, v86, v87\n"
"v_max_f32 v53, v53, v54\n"
"v_mfma_f32_16x16x32_bf16 a[24:27], a[244:247], v[136:139], a[24:27]\n"
"v_cmp_lt_f32 vcc, s64, v53\n"
"s_mov_b32 m0, 8192\n"
"ds_read_b64_tr_b16 a[234:235], v11 offset:6272\n"
"v_mfma_f32_16x16x32_bf16 a[32:35], a[248:251], v[128:131], a[32:35]\n"
"buffer_load_dwordx4 v33, s[48:51], s60 offen offset:0 nt lds\n"
"ds_read_b64_tr_b16 a[236:237], v12 offset:128\n"
"ds_read_b64_tr_b16 a[238:239], v12 offset:6272\n"
"v_mfma_f32_16x16x32_bf16 a[36:39], a[248:251], v[132:135], a[36:39]\n"
"buffer_load_dwordx4 v34, s[48:51], s60 offen offset:1024 nt lds\n"
"v_mfma_f32_16x16x32_bf16 a[40:43], a[248:251], v[136:139], a[40:43]\n"
"s_waitcnt lgkmcnt(4)\n"
"v_mfma_f32_16x16x32_bf16 a[48:51], a[252:255], v[128:131], a[48:51]\n"
"s_cbranch_vccnz K1B_RS3\n"
"K1B_RSb3:\n"
"ds_read_b64_tr_b16 a[240:241], v9 offset:256\n"
"ds_read_b64_tr_b16 a[242:243], v9 offset:6400\n"
"v_mfma_f32_16x16x32_bf16 a[52:55], a[252:255], v[132:135], a[52:55]\n"
"buffer_load_dwordx4 v35, s[48:51], s60 offen offset:2048 nt lds\n"
"v_exp_f32 v64, v64\n"
"v_mfma_f32_16x16x32_bf16 a[56:59], a[252:255], v[136:139], a[56:59]\n"
"v_exp_f32 v65, v65\n"
"v_add_f32 v37, v37, v64\n"
"v_mfma_f32_16x16x32_bf16 a[64:67], a[224:227], v[128:131], a[64:67]\n"
"ds_read_b64_tr_b16 a[244:245], v10 offset:256\n"
"ds_read_b64_tr_b16 a[246:247], v10 offset:6400\n"
"v_add_f32 v38, v38, v65\n"
"v_mfma_f32_16x16x32_bf16 a[68:71], a[224:227], v[132:135], a[68:71]\n"
"buffer_load_dwordx4 v36, s[48:51], s60 offen offset:3072 nt lds\n"
"v_exp_f32 v66, v66\n"
"v_mfma_f32_16x16x32_bf16 a[72:75], a[224:227], v[136:139], a[72:75]\n"
"v_exp_f32 v67, v67\n"
"v_add_f32 v37, v37, v66\n"
"v_mfma_f32_16x16x32_bf16 a[80:83], a[228:231], v[128:131], a[80:83]\n"
"ds_read_b64_tr_b16 a[248:249], v11 offset:256\n"
"ds_read_b64_tr_b16 a[250:251], v11 offset:6400\n"
"v_add_f32 v38, v38, v67\n"
"v_mfma_f32_16x16x32_bf16 a[84:87], a[228:231], v[132:135], a[84:87]\n"
"s_add_u32 s60, s60, 12288\n"
"v_exp_f32 v68, v68\n"
"v_mfma_f32_16x16x32_bf16 a[88:91], a[228:231], v[136:139], a[88:91]\n"
"v_exp_f32 v69, v69\n"
"s_waitcnt lgkmcnt(4)\n"
"v_mfma_f32_16x16x32_bf16 a[96:99], a[232:235], v[128:131], a[96:99]\n"
"ds_read_b64_tr_b16 a[252:253], v12 offset:256\n"
"ds_read_b64_tr_b16 a[254:255], v12 offset:6400\n"
"v_add_f32 v37, v37, v68\n"
"v_mfma_f32_16x16x32_bf16 a[100:103], a[232:235], v[132:135], a[100:103]\n"
"s_waitcnt vmcnt(48)\n"
"K1B_PC3:\n"
"s_memrealtime s[92:93]\n"
"s_waitcnt lgkmcnt(0)\n"
"s_sub_u32 s94, s92, s95\n"
"s_cmp_lt_u32 s94, 96\n"
"s_cbranch_scc1 K1B_PC3\n"
"s_mov_b32 s95, s92\n"
"s_barrier\n"
"v_add_f32 v38, v38, v69\n"
"v_mfma_f32_16x16x32_bf16 a[104:107], a[232:235], v[136:139], a[104:107]\n"
"s_mov_b32 m0, 98304\n"
"v_exp_f32 v70, v70\n"
"v_mfma_f32_16x16x32_bf16 a[112:115], a[236:239], v[128:131], a[112:115]\n"
"ds_read_b128 a[208:211], v3 offset:24576\n"
"buffer_load_dwordx4 v13, s[44:47], s59 offen offset:0 nt lds\n"
"v_add_f32 v37, v37, v70\n"
"v_mfma_f32_16x16x32_bf16 a[116:119], a[236:239], v[132:135], a[116:119]\n"
"v_exp_f32 v71, v71\n"
"v_mfma_f32_16x16x32_bf16 a[120:123], a[236:239], v[136:139], a[120:123]\n"
"buffer_load_dwordx4 v14, s[44:47], s59 offen offset:1024 nt lds\n"
"v_exp_f32 v72, v72\n"
"v_mfma_f32_16x16x32_bf16 a[128:131], a[240:243], v[128:131], a[128:131]\n"
"ds_read_b128 a[212:215], v3 offset:30720\n"
"v_exp_f32 v73, v73\n"
"v_mfma_f32_16x16x32_bf16 a[132:135], a[240:243], v[132:135], a[132:135]\n"
"v_exp_f32 v74, v74\n"
"v_add_f32 v38, v38, v71\n"
"v_mfma_f32_16x16x32_bf16 a[136:139], a[240:243], v[136:139], a[136:139]\n"
"buffer_load_dwordx4 v15, s[44:47], s59 offen offset:2048 nt lds\n"
"v_add_f32 v39, v39, v72\n"
"v_mfma_f32_16x16x32_bf16 a[144:147], a[244:247], v[128:131], a[144:147]\n"
"ds_read_b128 a[216:219], v4 offset:24576\n"
"v_exp_f32 v75, v75\n"
"v_mfma_f32_16x16x32_bf16 a[148:151], a[244:247], v[132:135], a[148:151]\n"
"v_add_f32 v40, v40, v73\n"
"v_add_f32 v39, v39, v74\n"
"v_add_f32 v40, v40, v75\n"
"v_mfma_f32_16x16x32_bf16 a[152:155], a[244:247], v[136:139], a[152:155]\n"
"buffer_load_dwordx4 v16, s[44:47], s59 offen offset:3072 nt lds\n"
"v_mfma_f32_16x16x32_bf16 a[160:163], a[248:251], v[128:131], a[160:163]\n"
"ds_read_b128 a[220:223], v4 offset:30720\n"
"v_mfma_f32_16x16x32_bf16 a[164:167], a[248:251], v[132:135], a[164:167]\n"
"v_mfma_f32_16x16x32_bf16 a[168:171], a[248:251], v[136:139], a[168:171]\n"
"s_mov_b32 m0, 102400\n"
"v_mfma_f32_16x16x32_bf16 a[176:179], a[252:255], v[128:131], a[176:179]\n"
"ds_read_b128 a[192:195], v3 offset:24704\n"
"buffer_load_dwordx4 v17, s[44:47], s59 offen offset:0 nt lds\n"
"v_mfma_f32_16x16x32_bf16 a[180:183], a[252:255], v[132:135], a[180:183]\n"
"v_mfma_f32_16x16x32_bf16 a[184:187], a[252:255], v[136:139], a[184:187]\n"
"s_cbranch_vccnz K1B_RO3\n"
"K1B_ROb3:\n"
"s_waitcnt lgkmcnt(2)\n"
"v_mfma_f32_16x16x32_bf16 v[96:99], a[208:211], v[160:163], v[144:147]\n"
"ds_read_b128 a[196:199], v3 offset:30848\n"
"buffer_load_dwordx4 v18, s[44:47], s59 offen offset:1024 nt lds\n"
"v_cvt_pk_bf16_f32 v128, v64, v65\n"
"v_mfma_f32_16x16x32_bf16 v[104:107], a[208:211], v[164:167], v[148:151]\n"
"v_exp_f32 v76, v76\n"
"v_cvt_pk_bf16_f32 v129, v66, v67\n"
"v_mfma_f32_16x16x32_bf16 v[112:115], a[208:211], v[168:171], v[152:155]\n"
"buffer_load_dwordx4 v19, s[44:47], s59 offen offset:2048 nt lds\n"
"v_exp_f32 v77, v77\n"
"v_mfma_f32_16x16x32_bf16 v[100:103], a[212:215], v[160:163], v[144:147]\n"
"ds_read_b128 a[200:203], v4 offset:24704\n"
"v_exp_f32 v78, v78\n"
"v_mfma_f32_16x16x32_bf16 v[108:111], a[212:215], v[164:167], v[148:151]\n"
"v_exp_f32 v79, v79\n"
"v_cvt_pk_bf16_f32 v130, v68, v69\n"
"v_mfma_f32_16x16x32_bf16 v[116:119], a[212:215], v[168:171], v[152:155]\n"
"buffer_load_dwordx4 v20, s[44:47], s59 offen offset:3072 nt lds\n"
"v_exp_f32 v80, v80\n"
"v_mfma_f32_16x16x32_bf16 v[96:99], a[216:219], v[176:179], v[96:99]\n"
"ds_read_b128 a[204:207], v4 offset:30848\n"
"v_exp_f32 v81, v81\n"
"v_mfma_f32_16x16x32_bf16 v[104:107], a[216:219], v[180:183], v[104:107]\n"
"v_exp_f32 v82, v82\n"
"v_cvt_pk_bf16_f32 v131, v70, v71\n"
"v_mfma_f32_16x16x32_bf16 v[112:115], a[216:219], v[184:187], v[112:115]\n"
"s_mov_b32 m0, 106496\n"
"v_cvt_pk_bf16_f32 v132, v72, v73\n"
"s_waitcnt lgkmcnt(2)\n"
"v_mfma_f32_16x16x32_bf16 v[100:103], a[220:223], v[176:179], v[100:103]\n"
"ds_read_b128 a[208:211], v3 offset:24832\n"
"buffer_load_dwordx4 v21, s[44:47], s59 offen offset:0 nt lds\n"
"v_cvt_pk_bf16_f32 v133, v74, v75\n"
"v_mfma_f32_16x16x32_bf16 v[108:111], a[220:223], v[180:183], v[108:111]\n"
"buffer_load_dwordx4 v22, s[44:47], s59 offen offset:1024 nt lds\n"
"v_exp_f32 v83, v83\n"
"v_mfma_f32_16x16x32_bf16 v[116:119], a[220:223], v[184:187], v[116:119]\n"
"v_exp_f32 v84, v84\n"
"v_cvt_pk_bf16_f32 v134, v76, v77\n"
"v_mfma_f32_16x16x32_bf16 v[96:99], a[192:195], v[192:195], v[96:99]\n"
"ds_read_b128 a[212:215], v3 offset:30976\n"
"v_exp_f32 v85, v85\n"
"v_mfma_f32_16x16x32_bf16 v[104:107], a[192:195], v[196:199], v[104:107]\n"
"buffer_load_dwordx4 v23, s[44:47], s59 offen offset:2048 nt lds\n"
"v_exp_f32 v86, v86\n"
"v_mfma_f32_16x16x32_bf16 v[112:115], a[192:195], v[200:203], v[112:115]\n"
"v_exp_f32 v87, v87\n"
"v_cvt_pk_bf16_f32 v135, v78, v79\n"
"v_mfma_f32_16x16x32_bf16 v[100:103], a[196:199], v[192:195], v[100:103]\n"
"ds_read_b128 a[216:219], v4 offset:24832\n"
"v_cvt_pk_bf16_f32 v136, v80, v81\n"
"v_cvt_pk_bf16_f32 v137, v82, v83\n"
"v_mfma_f32_16x16x32_bf16 v[108:111], a[196:199], v[196:199], v[108:111]\n"
"buffer_load_dwordx4 v24, s[44:47], s59 offen offset:3072 nt lds\n"
"v_cvt_pk_bf16_f32 v138, v84, v85\n"
"v_cvt_pk_bf16_f32 v139, v86, v87\n"
"v_mfma_f32_16x16x32_bf16 v[116:119], a[196:199], v[200:203], v[116:119]\n"
"v_add_f32 v39, v39, v76\n"
"v_add_f32 v40, v40, v77\n"
"s_waitcnt lgkmcnt(2)\n"
"v_mfma_f32_16x16x32_bf16 v[96:99], a[200:203], v[208:211], v[96:99]\n"
"ds_read_b128 a[220:223], v4 offset:30976\n"
"v_add_f32 v39, v39, v78\n"
"v_add_f32 v40, v40, v79\n"
"v_mfma_f32_16x16x32_bf16 v[104:107], a[200:203], v[212:215], v[104:107]\n"
"s_add_u32 s59, s59, 12288\n"
"s_waitcnt vmcnt(48)\n"
"s_mov_b32 m0, 12288\n"
"v_mfma_f32_16x16x32_bf16 v[112:115], a[200:203], v[216:219], v[112:115]\n"
"buffer_load_dwordx4 v25, s[48:51], s60 offen offset:0 nt lds\n"
"v_add_f32 v41, v41, v80\n"
"v_add_f32 v42, v42, v81\n"
"v_mfma_f32_16x16x32_bf16 v[100:103], a[204:207], v[208:211], v[100:103]\n"
"buffer_load_dwordx4 v26, s[48:51], s60 offen offset:1024 nt lds\n"
"ds_read_b64_tr_b16 a[224:225], v9 offset:12288\n"
"ds_read_b64_tr_b16 a[226:227], v9 offset:18432\n"
"v_mfma_f32_16x16x32_bf16 v[108:111], a[204:207], v[212:215], v[108:111]\n"
"v_add_f32 v41, v41, v82\n"
"v_add_f32 v42, v42, v83\n"
"v_add_f32 v41, v41, v84\n"
"v_mfma_f32_16x16x32_bf16 v[116:119], a[204:207], v[216:219], v[116:119]\n"
"v_add_f32 v42, v42, v85\n"
"v_add_f32 v41, v41, v86\n"
"v_add_f32 v42, v42, v87\n"
"v_mfma_f32_16x16x32_bf16 v[96:99], a[208:211], v[224:227], v[96:99]\n"
"buffer_load_dwordx4 v27, s[48:51], s60 offen offset:2048 nt lds\n"
"ds_read_b64_tr_b16 a[228:229], v10 offset:12288\n"
"ds_read_b64_tr_b16 a[230:231], v10 offset:18432\n"
"v_mfma_f32_16x16x32_bf16 v[104:107], a[208:211], v[228:231], v[104:107]\n"
"v_mfma_f32_16x16x32_bf16 v[112:115], a[208:211], v[232:235], v[112:115]\n"
"s_waitcnt lgkmcnt(4)\n"
"v_mfma_f32_16x16x32_bf16 v[100:103], a[212:215], v[224:227], v[100:103]\n"
"ds_read_b64_tr_b16 a[232:233], v11 offset:12288\n"
"ds_read_b64_tr_b16 a[234:235], v11 offset:18432\n"
"buffer_load_dwordx4 v28, s[48:51], s60 offen offset:3072 nt lds\n"
"v_mfma_f32_16x16x32_bf16 v[108:111], a[212:215], v[228:231], v[108:111]\n"
"v_mfma_f32_16x16x32_bf16 v[116:119], a[212:215], v[232:235], v[116:119]\n"
"v_mfma_f32_16x16x32_bf16 v[96:99], a[216:219], v[240:243], v[96:99]\n"
"s_mov_b32 m0, 16384\n"
"ds_read_b64_tr_b16 a[236:237], v12 offset:12288\n"
"ds_read_b64_tr_b16 a[238:239], v12 offset:18432\n"
"v_mfma_f32_16x16x32_bf16 v[104:107], a[216:219], v[244:247], v[104:107]\n"
"buffer_load_dwordx4 v29, s[48:51], s60 offen offset:0 nt lds\n"
"v_mfma_f32_16x16x32_bf16 v[112:115], a[216:219], v[248:251], v[112:115]\n"
"buffer_load_dwordx4 v30, s[48:51], s60 offen offset:1024 nt lds\n"
"v_mfma_f32_16x16x32_bf16 v[100:103], a[220:223], v[240:243], v[100:103]\n"
"ds_read_b64_tr_b16 a[240:241], v9 offset:12416\n"
"ds_read_b64_tr_b16 a[242:243], v9 offset:18560\n"
"v_mfma_f32_16x16x32_bf16 v[108:111], a[220:223], v[244:247], v[108:111]\n"
"v_mfma_f32_16x16x32_bf16 v[116:119], a[220:223], v[248:251], v[116:119]\n"
"buffer_load_dwordx4 v31, s[48:51], s60 offen offset:2048 nt lds\n"
"v_max3_f32 v53, v96, v97, v98\n"
"s_waitcnt lgkmcnt(4)\n"
"v_mfma_f32_16x16x32_bf16 a[0:3], a[224:227], v[128:131], a[0:3]\n"
"ds_read_b64_tr_b16 a[244:245], v10 offset:12416\n"
"ds_read_b64_tr_b16 a[246:247], v10 offset:18560\n"
"v_max3_f32 v54, v99, v104, v105\n"
"v_mfma_f32_16x16x32_bf16 a[4:7], a[224:227], v[132:135], a[4:7]\n"
"v_max3_f32 v53, v53, v106, v107\n"
"v_max3_f32 v54, v54, v112, v113\n"
"v_max3_f32 v53, v53, v114, v115\n"
"v_mfma_f32_16x16x32_bf16 a[8:11], a[224:227], v[136:139], a[8:11]\n"
"buffer_load_dwordx4 v32, s[48:51], s60 offen offset:3072 nt lds\n"
"v_max3_f32 v54, v54, v100, v101\n"
"v_max3_f32 v53, v53, v102, v103\n"
"v_mfma_f32_16x16x32_bf16 a[16:19], a[228:231], v[128:131], a[16:19]\n"
"v_max3_f32 v54, v54, v108, v109\n"
"v_max3_f32 v53, v53, v110, v111\n"
"ds_read_b64_tr_b16 a[248:249], v11 offset:12416\n"
"v_mfma_f32_16x16x32_bf16 a[20:23], a[228:231], v[132:135], a[20:23]\n"
"v_max3_f32 v54, v54, v116, v117\n"
"v_max3_f32 v53, v53, v118, v119\n"
"v_max_f32 v53, v53, v54\n"
"v_mfma_f32_16x16x32_bf16 a[24:27], a[228:231], v[136:139], a[24:27]\n"
"v_cmp_lt_f32 vcc, s64, v53\n"
"s_mov_b32 m0, 20480\n"
"ds_read_b64_tr_b16 a[250:251], v11 offset:18560\n"
"v_mfma_f32_16x16x32_bf16 a[32:35], a[232:235], v[128:131], a[32:35]\n"
"buffer_load_dwordx4 v33, s[48:51], s60 offen offset:0 nt lds\n"
"ds_read_b64_tr_b16 a[252:253], v12 offset:12416\n"
"ds_read_b64_tr_b16 a[254:255], v12 offset:18560\n"
"v_mfma_f32_16x16x32_bf16 a[36:39], a[232:235], v[132:135], a[36:39]\n"
"buffer_load_dwordx4 v34, s[48:51], s60 offen offset:1024 nt lds\n"
"v_mfma_f32_16x16x32_bf16 a[40:43], a[232:235], v[136:139], a[40:43]\n"
"s_waitcnt lgkmcnt(4)\n"
"v_mfma_f32_16x16x32_bf16 a[48:51], a[236:239], v[128:131], a[48:51]\n"
"s_cbranch_vccnz K1B_RS4\n"
"K1B_RSb4:\n"
"ds_read_b64_tr_b16 a[224:225], v9 offset:12544\n"
"ds_read_b64_tr_b16 a[226:227], v9 offset:18688\n"
"v_mfma_f32_16x16x32_bf16 a[52:55], a[236:239], v[132:135], a[52:55]\n"
"buffer_load_dwordx4 v35, s[48:51], s60 offen offset:2048 nt lds\n"
"v_exp_f32 v96, v96\n"
"v_mfma_f32_16x16x32_bf16 a[56:59], a[236:239], v[136:139], a[56:59]\n"
"v_exp_f32 v97, v97\n"
"v_add_f32 v37, v37, v96\n"
"v_mfma_f32_16x16x32_bf16 a[64:67], a[240:243], v[128:131], a[64:67]\n"
"ds_read_b64_tr_b16 a[228:229], v10 offset:12544\n"
"ds_read_b64_tr_b16 a[230:231], v10 offset:18688\n"
"v_add_f32 v38, v38, v97\n"
"v_mfma_f32_16x16x32_bf16 a[68:71], a[240:243], v[132:135], a[68:71]\n"
"buffer_load_dwordx4 v36, s[48:51], s60 offen offset:3072 nt lds\n"
"v_exp_f32 v98, v98\n"
"v_mfma_f32_16x16x32_bf16 a[72:75], a[240:243], v[136:139], a[72:75]\n"
"v_exp_f32 v99, v99\n"
"v_add_f32 v37, v37, v98\n"
"v_mfma_f32_16x16x32_bf16 a[80:83], a[244:247], v[128:131], a[80:83]\n"
"ds_read_b64_tr_b16 a[232:233], v11 offset:12544\n"
"ds_read_b64_tr_b16 a[234:235], v11 offset:18688\n"
"v_add_f32 v38, v38, v99\n"
"v_mfma_f32_16x16x32_bf16 a[84:87], a[244:247], v[132:135], a[84:87]\n"
"s_add_u32 s60, s60, 12288\n"
"v_exp_f32 v100, v100\n"
"v_mfma_f32_16x16x32_bf16 a[88:91], a[244:247], v[136:139], a[88:91]\n"
"v_exp_f32 v101, v101\n"
"s_waitcnt lgkmcnt(4)\n"
"v_mfma_f32_16x16x32_bf16 a[96:99], a[248:251], v[128:131], a[96:99]\n"
"ds_read_b64_tr_b16 a[236:237], v12 offset:12544\n"
"ds_read_b64_tr_b16 a[238:239], v12 offset:18688\n"
"v_add_f32 v37, v37, v100\n"
"v_mfma_f32_16x16x32_bf16 a[100:103], a[248:251], v[132:135], a[100:103]\n"
"s_waitcnt vmcnt(48)\n"
"K1B_PC4:\n"
"s_memrealtime s[92:93]\n"
"s_waitcnt lgkmcnt(0)\n"
"s_sub_u32 s94, s92, s95\n"
"s_cmp_lt_u32 s94, 96\n"
"s_cbranch_scc1 K1B_PC4\n"
"s_mov_b32 s95, s92\n"
"s_barrier\n"
"v_add_f32 v38, v38, v101\n"
"v_mfma_f32_16x16x32_bf16 a[104:107], a[248:251], v[136:139], a[104:107]\n"
"s_mov_b32 m0, 110592\n"
"v_exp_f32 v102, v102\n"
"v_mfma_f32_16x16x32_bf16 a[112:115], a[252:255], v[128:131], a[112:115]\n"
"ds_read_b128 a[192:195], v1 offset:0\n"
"buffer_load_dwordx4 v13, s[44:47], s59 offen offset:0 nt lds\n"
"v_add_f32 v37, v37, v102\n"
"v_mfma_f32_16x16x32_bf16 a[116:119], a[252:255], v[132:135], a[116:119]\n"
"v_exp_f32 v103, v103\n"
"v_mfma_f32_16x16x32_bf16 a[120:123], a[252:255], v[136:139], a[120:123]\n"
"buffer_load_dwordx4 v14, s[44:47], s59 offen offset:1024 nt lds\n"
"v_exp_f32 v104, v104\n"
"v_mfma_f32_16x16x32_bf16 a[128:131], a[224:227], v[128:131], a[128:131]\n"
"ds_read_b128 a[196:199], v1 offset:6144\n"
"v_exp_f32 v105, v105\n"
"v_mfma_f32_16x16x32_bf16 a[132:135], a[224:227], v[132:135], a[132:135]\n"
"v_exp_f32 v106, v106\n"
"v_add_f32 v38, v38, v103\n"
"v_mfma_f32_16x16x32_bf16 a[136:139], a[224:227], v[136:139], a[136:139]\n"
"buffer_load_dwordx4 v15, s[44:47], s59 offen offset:2048 nt lds\n"
"v_add_f32 v39, v39, v104\n"
"v_mfma_f32_16x16x32_bf16 a[144:147], a[228:231], v[128:131], a[144:147]\n"
"ds_read_b128 a[200:203], v2 offset:0\n"
"v_exp_f32 v107, v107\n"
"v_mfma_f32_16x16x32_bf16 a[148:151], a[228:231], v[132:135], a[148:151]\n"
"v_add_f32 v40, v40, v105\n"
"v_add_f32 v39, v39, v106\n"
"v_add_f32 v40, v40, v107\n"
"v_mfma_f32_16x16x32_bf16 a[152:155], a[228:231], v[136:139], a[152:155]\n"
"buffer_load_dwordx4 v16, s[44:47], s59 offen offset:3072 nt lds\n"
"v_mfma_f32_16x16x32_bf16 a[160:163], a[232:235], v[128:131], a[160:163]\n"
"ds_read_b128 a[204:207], v2 offset:6144\n"
"v_mfma_f32_16x16x32_bf16 a[164:167], a[232:235], v[132:135], a[164:167]\n"
"v_mfma_f32_16x16x32_bf16 a[168:171], a[232:235], v[136:139], a[168:171]\n"
"s_mov_b32 m0, 114688\n"
"v_mfma_f32_16x16x32_bf16 a[176:179], a[236:239], v[128:131], a[176:179]\n"
"ds_read_b128 a[208:211], v1 offset:128\n"
"buffer_load_dwordx4 v17, s[44:47], s59 offen offset:0 nt lds\n"
"v_mfma_f32_16x16x32_bf16 a[180:183], a[236:239], v[132:135], a[180:183]\n"
"v_mfma_f32_16x16x32_bf16 a[184:187], a[236:239], v[136:139], a[184:187]\n"
"s_cbranch_vccnz K1B_RO4\n"
"K1B_ROb4:\n"
"s_waitcnt lgkmcnt(2)\n"
"v_mfma_f32_16x16x32_bf16 v[64:67], a[192:195], v[160:163], v[144:147]\n"
"ds_read_b128 a[212:215], v1 offset:6272\n"
"buffer_load_dwordx4 v18, s[44:47], s59 offen offset:1024 nt lds\n"
"v_cvt_pk_bf16_f32 v128, v96, v97\n"
"v_mfma_f32_16x16x32_bf16 v[72:75], a[192:195], v[164:167], v[148:151]\n"
"v_exp_f32 v108, v108\n"
"v_cvt_pk_bf16_f32 v129, v98, v99\n"
"v_mfma_f32_16x16x32_bf16 v[80:83], a[192:195], v[168:171], v[152:155]\n"
"buffer_load_dwordx4 v19, s[44:47], s59 offen offset:2048 nt lds\n"
"v_exp_f32 v109, v109\n"
"v_mfma_f32_16x16x32_bf16 v[68:71], a[196:199], v[160:163], v[144:147]\n"
"ds_read_b128 a[216:219], v2 offset:128\n"
"v_exp_f32 v110, v110\n"
"v_mfma_f32_16x16x32_bf16 v[76:79], a[196:199], v[164:167], v[148:151]\n"
"v_exp_f32 v111, v111\n"
"v_cvt_pk_bf16_f32 v130, v100, v101\n"
"v_mfma_f32_16x16x32_bf16 v[84:87], a[196:199], v[168:171], v[152:155]\n"
"buffer_load_dwordx4 v20, s[44:47], s59 offen offset:3072 nt lds\n"
"v_exp_f32 v112, v112\n"
"v_mfma_f32_16x16x32_bf16 v[64:67], a[200:203], v[176:179], v[64:67]\n"
"ds_read_b128 a[220:223], v2 offset:6272\n"
"v_exp_f32 v113, v113\n"
"v_mfma_f32_16x16x32_bf16 v[72:75], a[200:203], v[180:183], v[72:75]\n"
"v_exp_f32 v114, v114\n"
"v_cvt_pk_bf16_f32 v131, v102, v103\n"
"v_mfma_f32_16x16x32_bf16 v[80:83], a[200:203], v[184:187], v[80:83]\n"
"s_mov_b32 m0, 118784\n"
"v_cvt_pk_bf16_f32 v132, v104, v105\n"
"s_waitcnt lgkmcnt(2)\n"
"v_mfma_f32_16x16x32_bf16 v[68:71], a[204:207], v[176:179], v[68:71]\n"
"ds_read_b128 a[192:195], v1 offset:256\n"
"buffer_load_dwordx4 v21, s[44:47], s59 offen offset:0 nt lds\n"
"v_cvt_pk_bf16_f32 v133, v106, v107\n"
"v_mfma_f32_16x16x32_bf16 v[76:79], a[204:207], v[180:183], v[76:79]\n"
"buffer_load_dwordx4 v22, s[44:47], s59 offen offset:1024 nt lds\n"
"v_exp_f32 v115, v115\n"
"v_mfma_f32_16x16x32_bf16 v[84:87], a[204:207], v[184:187], v[84:87]\n"
"v_exp_f32 v116, v116\n"
"v_cvt_pk_bf16_f32 v134, v108, v109\n"
"v_mfma_f32_16x16x32_bf16 v[64:67], a[208:211], v[192:195], v[64:67]\n"
"ds_read_b128 a[196:199], v1 offset:6400\n"
"v_exp_f32 v117, v117\n"
"v_mfma_f32_16x16x32_bf16 v[72:75], a[208:211], v[196:199], v[72:75]\n"
"buffer_load_dwordx4 v23, s[44:47], s59 offen offset:2048 nt lds\n"
"v_exp_f32 v118, v118\n"
"v_mfma_f32_16x16x32_bf16 v[80:83], a[208:211], v[200:203], v[80:83]\n"
"v_exp_f32 v119, v119\n"
"v_cvt_pk_bf16_f32 v135, v110, v111\n"
"v_mfma_f32_16x16x32_bf16 v[68:71], a[212:215], v[192:195], v[68:71]\n"
"ds_read_b128 a[200:203], v2 offset:256\n"
"v_cvt_pk_bf16_f32 v136, v112, v113\n"
"v_cvt_pk_bf16_f32 v137, v114, v115\n"
"v_mfma_f32_16x16x32_bf16 v[76:79], a[212:215], v[196:199], v[76:79]\n"
"buffer_load_dwordx4 v24, s[44:47], s59 offen offset:3072 nt lds\n"
"v_cvt_pk_bf16_f32 v138, v116, v117\n"
"v_cvt_pk_bf16_f32 v139, v118, v119\n"
"v_mfma_f32_16x16x32_bf16 v[84:87], a[212:215], v[200:203], v[84:87]\n"
"v_add_f32 v39, v39, v108\n"
"v_add_f32 v40, v40, v109\n"
"s_waitcnt lgkmcnt(2)\n"
"v_mfma_f32_16x16x32_bf16 v[64:67], a[216:219], v[208:211], v[64:67]\n"
"ds_read_b128 a[204:207], v2 offset:6400\n"
"v_add_f32 v39, v39, v110\n"
"v_add_f32 v40, v40, v111\n"
"v_mfma_f32_16x16x32_bf16 v[72:75], a[216:219], v[212:215], v[72:75]\n"
"s_add_u32 s59, s59, 12288\n"
"s_waitcnt vmcnt(48)\n"
"s_mov_b32 m0, 24576\n"
"v_mfma_f32_16x16x32_bf16 v[80:83], a[216:219], v[216:219], v[80:83]\n"
"buffer_load_dwordx4 v25, s[48:51], s60 offen offset:0 nt lds\n"
"v_add_f32 v41, v41, v112\n"
"v_add_f32 v42, v42, v113\n"
"v_mfma_f32_16x16x32_bf16 v[68:71], a[220:223], v[208:211], v[68:71]\n"
"buffer_load_dwordx4 v26, s[48:51], s60 offen offset:1024 nt lds\n"
"ds_read_b64_tr_b16 a[240:241], v9 offset:24576\n"
"ds_read_b64_tr_b16 a[242:243], v9 offset:30720\n"
"v_mfma_f32_16x16x32_bf16 v[76:79], a[220:223], v[212:215], v[76:79]\n"
"v_add_f32 v41, v41, v114\n"
"v_add_f32 v42, v42, v115\n"
"v_add_f32 v41, v41, v116\n"
"v_mfma_f32_16x16x32_bf16 v[84:87], a[220:223], v[216:219], v[84:87]\n"
"v_add_f32 v42, v42, v117\n"
"v_add_f32 v41, v41, v118\n"
"v_add_f32 v42, v42, v119\n"
"v_mfma_f32_16x16x32_bf16 v[64:67], a[192:195], v[224:227], v[64:67]\n"
"buffer_load_dwordx4 v27, s[48:51], s60 offen offset:2048 nt lds\n"
"ds_read_b64_tr_b16 a[244:245], v10 offset:24576\n"
"ds_read_b64_tr_b16 a[246:247], v10 offset:30720\n"
"v_mfma_f32_16x16x32_bf16 v[72:75], a[192:195], v[228:231], v[72:75]\n"
"v_mfma_f32_16x16x32_bf16 v[80:83], a[192:195], v[232:235], v[80:83]\n"
"s_waitcnt lgkmcnt(4)\n"
"v_mfma_f32_16x16x32_bf16 v[68:71], a[196:199], v[224:227], v[68:71]\n"
"ds_read_b64_tr_b16 a[248:249], v11 offset:24576\n"
"ds_read_b64_tr_b16 a[250:251], v11 offset:30720\n"
"buffer_load_dwordx4 v28, s[48:51], s60 offen offset:3072 nt lds\n"
"v_mfma_f32_16x16x32_bf16 v[76:79], a[196:199], v[228:231], v[76:79]\n"
"v_mfma_f32_16x16x32_bf16 v[84:87], a[196:199], v[232:235], v[84:87]\n"
"v_mfma_f32_16x16x32_bf16 v[64:67], a[200:203], v[240:243], v[64:67]\n"
"s_mov_b32 m0, 28672\n"
"ds_read_b64_tr_b16 a[252:253], v12 offset:24576\n"
"ds_read_b64_tr_b16 a[254:255], v12 offset:30720\n"
"v_mfma_f32_16x16x32_bf16 v[72:75], a[200:203], v[244:247], v[72:75]\n"
"buffer_load_dwordx4 v29, s[48:51], s60 offen offset:0 nt lds\n"
"v_mfma_f32_16x16x32_bf16 v[80:83], a[200:203], v[248:251], v[80:83]\n"
"buffer_load_dwordx4 v30, s[48:51], s60 offen offset:1024 nt lds\n"
"v_mfma_f32_16x16x32_bf16 v[68:71], a[204:207], v[240:243], v[68:71]\n"
"ds_read_b64_tr_b16 a[224:225], v9 offset:24704\n"
"ds_read_b64_tr_b16 a[226:227], v9 offset:30848\n"
"v_mfma_f32_16x16x32_bf16 v[76:79], a[204:207], v[244:247], v[76:79]\n"
"v_mfma_f32_16x16x32_bf16 v[84:87], a[204:207], v[248:251], v[84:87]\n"
"buffer_load_dwordx4 v31, s[48:51], s60 offen offset:2048 nt lds\n"
"v_max3_f32 v53, v64, v65, v66\n"
"s_waitcnt lgkmcnt(4)\n"
"v_mfma_f32_16x16x32_bf16 a[0:3], a[240:243], v[128:131], a[0:3]\n"
"ds_read_b64_tr_b16 a[228:229], v10 offset:24704\n"
"ds_read_b64_tr_b16 a[230:231], v10 offset:30848\n"
"v_max3_f32 v54, v67, v72, v73\n"
"v_mfma_f32_16x16x32_bf16 a[4:7], a[240:243], v[132:135], a[4:7]\n"
"v_max3_f32 v53, v53, v74, v75\n"
"v_max3_f32 v54, v54, v80, v81\n"
"v_max3_f32 v53, v53, v82, v83\n"
"v_mfma_f32_16x16x32_bf16 a[8:11], a[240:243], v[136:139], a[8:11]\n"
"buffer_load_dwordx4 v32, s[48:51], s60 offen offset:3072 nt lds\n"
"v_max3_f32 v54, v54, v68, v69\n"
"v_max3_f32 v53, v53, v70, v71\n"
"v_mfma_f32_16x16x32_bf16 a[16:19], a[244:247], v[128:131], a[16:19]\n"
"v_max3_f32 v54, v54, v76, v77\n"
"v_max3_f32 v53, v53, v78, v79\n"
"ds_read_b64_tr_b16 a[232:233], v11 offset:24704\n"
"v_mfma_f32_16x16x32_bf16 a[20:23], a[244:247], v[132:135], a[20:23]\n"
"v_max3_f32 v54, v54, v84, v85\n"
"v_max3_f32 v53, v53, v86, v87\n"
"v_max_f32 v53, v53, v54\n"
"v_mfma_f32_16x16x32_bf16 a[24:27], a[244:247], v[136:139], a[24:27]\n"
"v_cmp_lt_f32 vcc, s64, v53\n"
"s_mov_b32 m0, 32768\n"
"ds_read_b64_tr_b16 a[234:235], v11 offset:30848\n"
"v_mfma_f32_16x16x32_bf16 a[32:35], a[248:251], v[128:131], a[32:35]\n"
"buffer_load_dwordx4 v33, s[48:51], s60 offen offset:0 nt lds\n"
"ds_read_b64_tr_b16 a[236:237], v12 offset:24704\n"
"ds_read_b64_tr_b16 a[238:239], v12 offset:30848\n"
"v_mfma_f32_16x16x32_bf16 a[36:39], a[248:251], v[132:135], a[36:39]\n"
"buffer_load_dwordx4 v34, s[48:51], s60 offen offset:1024 nt lds\n"
"v_mfma_f32_16x16x32_bf16 a[40:43], a[248:251], v[136:139], a[40:43]\n"
"s_waitcnt lgkmcnt(4)\n"
"v_mfma_f32_16x16x32_bf16 a[48:51], a[252:255], v[128:131], a[48:51]\n"
"s_cbranch_vccnz K1B_RS5\n"
"K1B_RSb5:\n"
"ds_read_b64_tr_b16 a[240:241], v9 offset:24832\n"
"ds_read_b64_tr_b16 a[242:243], v9 offset:30976\n"
"v_mfma_f32_16x16x32_bf16 a[52:55], a[252:255], v[132:135], a[52:55]\n"
"buffer_load_dwordx4 v35, s[48:51], s60 offen offset:2048 nt lds\n"
"v_exp_f32 v64, v64\n"
"v_mfma_f32_16x16x32_bf16 a[56:59], a[252:255], v[136:139], a[56:59]\n"
"v_exp_f32 v65, v65\n"
"v_add_f32 v37, v37, v64\n"
"v_mfma_f32_16x16x32_bf16 a[64:67], a[224:227], v[128:131], a[64:67]\n"
"ds_read_b64_tr_b16 a[244:245], v10 offset:24832\n"
"ds_read_b64_tr_b16 a[246:247], v10 offset:30976\n"
"v_add_f32 v38, v38, v65\n"
"v_mfma_f32_16x16x32_bf16 a[68:71], a[224:227], v[132:135], a[68:71]\n"
"buffer_load_dwordx4 v36, s[48:51], s60 offen offset:3072 nt lds\n"
"v_exp_f32 v66, v66\n"
"v_mfma_f32_16x16x32_bf16 a[72:75], a[224:227], v[136:139], a[72:75]\n"
"v_exp_f32 v67, v67\n"
"v_add_f32 v37, v37, v66\n"
"v_mfma_f32_16x16x32_bf16 a[80:83], a[228:231], v[128:131], a[80:83]\n"
"ds_read_b64_tr_b16 a[248:249], v11 offset:24832\n"
"ds_read_b64_tr_b16 a[250:251], v11 offset:30976\n"
"v_add_f32 v38, v38, v67\n"
"v_mfma_f32_16x16x32_bf16 a[84:87], a[228:231], v[132:135], a[84:87]\n"
"s_add_u32 s60, s60, 12288\n"
"v_exp_f32 v68, v68\n"
"v_mfma_f32_16x16x32_bf16 a[88:91], a[228:231], v[136:139], a[88:91]\n"
"v_exp_f32 v69, v69\n"
"s_waitcnt lgkmcnt(4)\n"
"v_mfma_f32_16x16x32_bf16 a[96:99], a[232:235], v[128:131], a[96:99]\n"
"ds_read_b64_tr_b16 a[252:253], v12 offset:24832\n"
"ds_read_b64_tr_b16 a[254:255], v12 offset:30976\n"
"v_add_f32 v37, v37, v68\n"
"v_mfma_f32_16x16x32_bf16 a[100:103], a[232:235], v[132:135], a[100:103]\n"
"s_waitcnt vmcnt(48)\n"
"K1B_PC5:\n"
"s_memrealtime s[92:93]\n"
"s_waitcnt lgkmcnt(0)\n"
"s_sub_u32 s94, s92, s95\n"
"s_cmp_lt_u32 s94, 96\n"
"s_cbranch_scc1 K1B_PC5\n"
"s_mov_b32 s95, s92\n"
"s_barrier\n"
"v_add_f32 v38, v38, v69\n"
"v_mfma_f32_16x16x32_bf16 a[104:107], a[232:235], v[136:139], a[104:107]\n"
"s_mov_b32 m0, 122880\n"
"v_exp_f32 v70, v70\n"
"v_mfma_f32_16x16x32_bf16 a[112:115], a[236:239], v[128:131], a[112:115]\n"
"ds_read_b128 a[208:211], v1 offset:12288\n"
"buffer_load_dwordx4 v13, s[44:47], s59 offen offset:0 nt lds\n"
"v_add_f32 v37, v37, v70\n"
"v_mfma_f32_16x16x32_bf16 a[116:119], a[236:239], v[132:135], a[116:119]\n"
"v_exp_f32 v71, v71\n"
"v_mfma_f32_16x16x32_bf16 a[120:123], a[236:239], v[136:139], a[120:123]\n"
"buffer_load_dwordx4 v14, s[44:47], s59 offen offset:1024 nt lds\n"
"v_exp_f32 v72, v72\n"
"v_mfma_f32_16x16x32_bf16 a[128:131], a[240:243], v[128:131], a[128:131]\n"
"ds_read_b128 a[212:215], v1 offset:18432\n"
"v_exp_f32 v73, v73\n"
"v_mfma_f32_16x16x32_bf16 a[132:135], a[240:243], v[132:135], a[132:135]\n"
"v_exp_f32 v74, v74\n"
"v_add_f32 v38, v38, v71\n"
"v_mfma_f32_16x16x32_bf16 a[136:139], a[240:243], v[136:139], a[136:139]\n"
"buffer_load_dwordx4 v15, s[44:47], s59 offen offset:2048 nt lds\n"
"v_add_f32 v39, v39, v72\n"
"v_mfma_f32_16x16x32_bf16 a[144:147], a[244:247], v[128:131], a[144:147]\n"
"ds_read_b128 a[216:219], v2 offset:12288\n"
"v_exp_f32 v75, v75\n"
"v_mfma_f32_16x16x32_bf16 a[148:151], a[244:247], v[132:135], a[148:151]\n"
"v_add_f32 v40, v40, v73\n"
"v_add_f32 v39, v39, v74\n"
"v_add_f32 v40, v40, v75\n"
"v_mfma_f32_16x16x32_bf16 a[152:155], a[244:247], v[136:139], a[152:155]\n"
"buffer_load_dwordx4 v16, s[44:47], s59 offen offset:3072 nt lds\n"
"v_mfma_f32_16x16x32_bf16 a[160:163], a[248:251], v[128:131], a[160:163]\n"
"ds_read_b128 a[220:223], v2 offset:18432\n"
"v_mfma_f32_16x16x32_bf16 a[164:167], a[248:251], v[132:135], a[164:167]\n"
"v_mfma_f32_16x16x32_bf16 a[168:171], a[248:251], v[136:139], a[168:171]\n"
"s_mov_b32 m0, 126976\n"
"v_mfma_f32_16x16x32_bf16 a[176:179], a[252:255], v[128:131], a[176:179]\n"
"ds_read_b128 a[192:195], v1 offset:12416\n"
"buffer_load_dwordx4 v17, s[44:47], s59 offen offset:0 nt lds\n"
"v_mfma_f32_16x16x32_bf16 a[180:183], a[252:255], v[132:135], a[180:183]\n"
"v_mfma_f32_16x16x32_bf16 a[184:187], a[252:255], v[136:139], a[184:187]\n"
"s_cbranch_vccnz K1B_RO5\n"
"K1B_ROb5:\n"
"s_waitcnt lgkmcnt(2)\n"
"v_mfma_f32_16x16x32_bf16 v[96:99], a[208:211], v[160:163], v[144:147]\n"
"ds_read_b128 a[196:199], v1 offset:18560\n"
"buffer_load_dwordx4 v18, s[44:47], s59 offen offset:1024 nt lds\n"
"v_cvt_pk_bf16_f32 v128, v64, v65\n"
"v_mfma_f32_16x16x32_bf16 v[104:107], a[208:211], v[164:167], v[148:151]\n"
"v_exp_f32 v76, v76\n"
"v_cvt_pk_bf16_f32 v129, v66, v67\n"
"v_mfma_f32_16x16x32_bf16 v[112:115], a[208:211], v[168:171], v[152:155]\n"
"buffer_load_dwordx4 v19, s[44:47], s59 offen offset:2048 nt lds\n"
"v_exp_f32 v77, v77\n"
"v_mfma_f32_16x16x32_bf16 v[100:103], a[212:215], v[160:163], v[144:147]\n"
"ds_read_b128 a[200:203], v2 offset:12416\n"
"v_exp_f32 v78, v78\n"
"v_mfma_f32_16x16x32_bf16 v[108:111], a[212:215], v[164:167], v[148:151]\n"
"v_exp_f32 v79, v79\n"
"v_cvt_pk_bf16_f32 v130, v68, v69\n"
"v_mfma_f32_16x16x32_bf16 v[116:119], a[212:215], v[168:171], v[152:155]\n"
"buffer_load_dwordx4 v20, s[44:47], s59 offen offset:3072 nt lds\n"
"v_exp_f32 v80, v80\n"
"v_mfma_f32_16x16x32_bf16 v[96:99], a[216:219], v[176:179], v[96:99]\n"
"ds_read_b128 a[204:207], v2 offset:18560\n"
"v_exp_f32 v81, v81\n"
"v_mfma_f32_16x16x32_bf16 v[104:107], a[216:219], v[180:183], v[104:107]\n"
"v_exp_f32 v82, v82\n"
"v_cvt_pk_bf16_f32 v131, v70, v71\n"
"v_mfma_f32_16x16x32_bf16 v[112:115], a[216:219], v[184:187], v[112:115]\n"
"s_mov_b32 m0, 131072\n"
"v_cvt_pk_bf16_f32 v132, v72, v73\n"
"s_waitcnt lgkmcnt(2)\n"
"v_mfma_f32_16x16x32_bf16 v[100:103], a[220:223], v[176:179], v[100:103]\n"
"ds_read_b128 a[208:211], v1 offset:12544\n"
"buffer_load_dwordx4 v21, s[44:47], s59 offen offset:0 nt lds\n"
"v_cvt_pk_bf16_f32 v133, v74, v75\n"
"v_mfma_f32_16x16x32_bf16 v[108:111], a[220:223], v[180:183], v[108:111]\n"
"buffer_load_dwordx4 v22, s[44:47], s59 offen offset:1024 nt lds\n"
"v_exp_f32 v83, v83\n"
"v_mfma_f32_16x16x32_bf16 v[116:119], a[220:223], v[184:187], v[116:119]\n"
"v_exp_f32 v84, v84\n"
"v_cvt_pk_bf16_f32 v134, v76, v77\n"
"v_mfma_f32_16x16x32_bf16 v[96:99], a[192:195], v[192:195], v[96:99]\n"
"ds_read_b128 a[212:215], v1 offset:18688\n"
"v_exp_f32 v85, v85\n"
"v_mfma_f32_16x16x32_bf16 v[104:107], a[192:195], v[196:199], v[104:107]\n"
"buffer_load_dwordx4 v23, s[44:47], s59 offen offset:2048 nt lds\n"
"v_exp_f32 v86, v86\n"
"v_mfma_f32_16x16x32_bf16 v[112:115], a[192:195], v[200:203], v[112:115]\n"
"v_exp_f32 v87, v87\n"
"v_cvt_pk_bf16_f32 v135, v78, v79\n"
"v_mfma_f32_16x16x32_bf16 v[100:103], a[196:199], v[192:195], v[100:103]\n"
"ds_read_b128 a[216:219], v2 offset:12544\n"
"v_cvt_pk_bf16_f32 v136, v80, v81\n"
"v_cvt_pk_bf16_f32 v137, v82, v83\n"
"v_mfma_f32_16x16x32_bf16 v[108:111], a[196:199], v[196:199], v[108:111]\n"
"buffer_load_dwordx4 v24, s[44:47], s59 offen offset:3072 nt lds\n"
"v_cvt_pk_bf16_f32 v138, v84, v85\n"
"v_cvt_pk_bf16_f32 v139, v86, v87\n"
"v_mfma_f32_16x16x32_bf16 v[116:119], a[196:199], v[200:203], v[116:119]\n"
"v_add_f32 v39, v39, v76\n"
"v_add_f32 v40, v40, v77\n"
"s_waitcnt lgkmcnt(2)\n"
"v_mfma_f32_16x16x32_bf16 v[96:99], a[200:203], v[208:211], v[96:99]\n"
"ds_read_b128 a[220:223], v2 offset:18688\n"
"v_add_f32 v39, v39, v78\n"
"v_add_f32 v40, v40, v79\n"
"v_mfma_f32_16x16x32_bf16 v[104:107], a[200:203], v[212:215], v[104:107]\n"
"s_add_u32 s59, s59, 12288\n"
"s_waitcnt vmcnt(48)\n"
"s_mov_b32 m0, 36864\n"
"v_mfma_f32_16x16x32_bf16 v[112:115], a[200:203], v[216:219], v[112:115]\n"
"buffer_load_dwordx4 v25, s[48:51], s60 offen offset:0 nt lds\n"
"v_add_f32 v41, v41, v80\n"
"v_add_f32 v42, v42, v81\n"
"v_mfma_f32_16x16x32_bf16 v[100:103], a[204:207], v[208:211], v[100:103]\n"
"buffer_load_dwordx4 v26, s[48:51], s60 offen offset:1024 nt lds\n"
"ds_read_b64_tr_b16 a[224:225], v5 offset:0\n"
"ds_read_b64_tr_b16 a[226:227], v5 offset:6144\n"
"v_mfma_f32_16x16x32_bf16 v[108:111], a[204:207], v[212:215], v[108:111]\n"
"v_add_f32 v41, v41, v82\n"
"v_add_f32 v42, v42, v83\n"
"v_add_f32 v41, v41, v84\n"
"v_mfma_f32_16x16x32_bf16 v[116:119], a[204:207], v[216:219], v[116:119]\n"
"v_add_f32 v42, v42, v85\n"
"v_add_f32 v41, v41, v86\n"
"v_add_f32 v42, v42, v87\n"
"v_mfma_f32_16x16x32_bf16 v[96:99], a[208:211], v[224:227], v[96:99]\n"
"buffer_load_dwordx4 v27, s[48:51], s60 offen offset:2048 nt lds\n"
"ds_read_b64_tr_b16 a[228:229], v6 offset:0\n"
"ds_read_b64_tr_b16 a[230:231], v6 offset:6144\n"
"v_mfma_f32_16x16x32_bf16 v[104:107], a[208:211], v[228:231], v[104:107]\n"
"v_mfma_f32_16x16x32_bf16 v[112:115], a[208:211], v[232:235], v[112:115]\n"
"s_waitcnt lgkmcnt(4)\n"
"v_mfma_f32_16x16x32_bf16 v[100:103], a[212:215], v[224:227], v[100:103]\n"
"ds_read_b64_tr_b16 a[232:233], v7 offset:0\n"
"ds_read_b64_tr_b16 a[234:235], v7 offset:6144\n"
"buffer_load_dwordx4 v28, s[48:51], s60 offen offset:3072 nt lds\n"
"v_mfma_f32_16x16x32_bf16 v[108:111], a[212:215], v[228:231], v[108:111]\n"
"v_mfma_f32_16x16x32_bf16 v[116:119], a[212:215], v[232:235], v[116:119]\n"
"v_mfma_f32_16x16x32_bf16 v[96:99], a[216:219], v[240:243], v[96:99]\n"
"s_mov_b32 m0, 40960\n"
"ds_read_b64_tr_b16 a[236:237], v8 offset:0\n"
"ds_read_b64_tr_b16 a[238:239], v8 offset:6144\n"
"v_mfma_f32_16x16x32_bf16 v[104:107], a[216:219], v[244:247], v[104:107]\n"
"buffer_load_dwordx4 v29, s[48:51], s60 offen offset:0 nt lds\n"
"v_mfma_f32_16x16x32_bf16 v[112:115], a[216:219], v[248:251], v[112:115]\n"
"buffer_load_dwordx4 v30, s[48:51], s60 offen offset:1024 nt lds\n"
"v_mfma_f32_16x16x32_bf16 v[100:103], a[220:223], v[240:243], v[100:103]\n"
"ds_read_b64_tr_b16 a[240:241], v5 offset:128\n"
"ds_read_b64_tr_b16 a[242:243], v5 offset:6272\n"
"v_mfma_f32_16x16x32_bf16 v[108:111], a[220:223], v[244:247], v[108:111]\n"
"v_mfma_f32_16x16x32_bf16 v[116:119], a[220:223], v[248:251], v[116:119]\n"
"buffer_load_dwordx4 v31, s[48:51], s60 offen offset:2048 nt lds\n"
"v_max3_f32 v53, v96, v97, v98\n"
"s_waitcnt lgkmcnt(4)\n"
"v_mfma_f32_16x16x32_bf16 a[0:3], a[224:227], v[128:131], a[0:3]\n"
"ds_read_b64_tr_b16 a[244:245], v6 offset:128\n"
"ds_read_b64_tr_b16 a[246:247], v6 offset:6272\n"
"v_max3_f32 v54, v99, v104, v105\n"
"v_mfma_f32_16x16x32_bf16 a[4:7], a[224:227], v[132:135], a[4:7]\n"
"v_max3_f32 v53, v53, v106, v107\n"
"v_max3_f32 v54, v54, v112, v113\n"
"v_max3_f32 v53, v53, v114, v115\n"
"v_mfma_f32_16x16x32_bf16 a[8:11], a[224:227], v[136:139], a[8:11]\n"
"buffer_load_dwordx4 v32, s[48:51], s60 offen offset:3072 nt lds\n"
"v_max3_f32 v54, v54, v100, v101\n"
"v_max3_f32 v53, v53, v102, v103\n"
"v_mfma_f32_16x16x32_bf16 a[16:19], a[228:231], v[128:131], a[16:19]\n"
"v_max3_f32 v54, v54, v108, v109\n"
"v_max3_f32 v53, v53, v110, v111\n"
"ds_read_b64_tr_b16 a[248:249], v7 offset:128\n"
"v_mfma_f32_16x16x32_bf16 a[20:23], a[228:231], v[132:135], a[20:23]\n"
"v_max3_f32 v54, v54, v116, v117\n"
"v_max3_f32 v53, v53, v118, v119\n"
"v_max_f32 v53, v53, v54\n"
"v_mfma_f32_16x16x32_bf16 a[24:27], a[228:231], v[136:139], a[24:27]\n"
"v_cmp_lt_f32 vcc, s64, v53\n"
"s_mov_b32 m0, 45056\n"
"ds_read_b64_tr_b16 a[250:251], v7 offset:6272\n"
"v_mfma_f32_16x16x32_bf16 a[32:35], a[232:235], v[128:131], a[32:35]\n"
"buffer_load_dwordx4 v33, s[48:51], s60 offen offset:0 nt lds\n"
"ds_read_b64_tr_b16 a[252:253], v8 offset:128\n"
"ds_read_b64_tr_b16 a[254:255], v8 offset:6272\n"
"v_mfma_f32_16x16x32_bf16 a[36:39], a[232:235], v[132:135], a[36:39]\n"
"buffer_load_dwordx4 v34, s[48:51], s60 offen offset:1024 nt lds\n"
"v_mfma_f32_16x16x32_bf16 a[40:43], a[232:235], v[136:139], a[40:43]\n"
"s_waitcnt lgkmcnt(4)\n"
"v_mfma_f32_16x16x32_bf16 a[48:51], a[236:239], v[128:131], a[48:51]\n"
"s_cbranch_vccnz K1B_RS6\n"
"K1B_RSb6:\n"
"ds_read_b64_tr_b16 a[224:225], v5 offset:256\n"
"ds_read_b64_tr_b16 a[226:227], v5 offset:6400\n"
"v_mfma_f32_16x16x32_bf16 a[52:55], a[236:239], v[132:135], a[52:55]\n"
"buffer_load_dwordx4 v35, s[48:51], s60 offen offset:2048 nt lds\n"
"v_exp_f32 v96, v96\n"
"v_mfma_f32_16x16x32_bf16 a[56:59], a[236:239], v[136:139], a[56:59]\n"
"v_exp_f32 v97, v97\n"
"v_add_f32 v37, v37, v96\n"
"v_mfma_f32_16x16x32_bf16 a[64:67], a[240:243], v[128:131], a[64:67]\n"
"ds_read_b64_tr_b16 a[228:229], v6 offset:256\n"
"ds_read_b64_tr_b16 a[230:231], v6 offset:6400\n"
"v_add_f32 v38, v38, v97\n"
"v_mfma_f32_16x16x32_bf16 a[68:71], a[240:243], v[132:135], a[68:71]\n"
"buffer_load_dwordx4 v36, s[48:51], s60 offen offset:3072 nt lds\n"
"v_exp_f32 v98, v98\n"
"v_mfma_f32_16x16x32_bf16 a[72:75], a[240:243], v[136:139], a[72:75]\n"
"v_exp_f32 v99, v99\n"
"v_add_f32 v37, v37, v98\n"
"v_mfma_f32_16x16x32_bf16 a[80:83], a[244:247], v[128:131], a[80:83]\n"
"ds_read_b64_tr_b16 a[232:233], v7 offset:256\n"
"ds_read_b64_tr_b16 a[234:235], v7 offset:6400\n"
"v_add_f32 v38, v38, v99\n"
"v_mfma_f32_16x16x32_bf16 a[84:87], a[244:247], v[132:135], a[84:87]\n"
"s_add_u32 s60, s60, 12288\n"
"v_exp_f32 v100, v100\n"
"v_mfma_f32_16x16x32_bf16 a[88:91], a[244:247], v[136:139], a[88:91]\n"
"v_exp_f32 v101, v101\n"
"s_waitcnt lgkmcnt(4)\n"
"v_mfma_f32_16x16x32_bf16 a[96:99], a[248:251], v[128:131], a[96:99]\n"
"ds_read_b64_tr_b16 a[236:237], v8 offset:256\n"
"ds_read_b64_tr_b16 a[238:239], v8 offset:6400\n"
"v_add_f32 v37, v37, v100\n"
"v_mfma_f32_16x16x32_bf16 a[100:103], a[248:251], v[132:135], a[100:103]\n"
"s_waitcnt vmcnt(48)\n"
"K1B_PC6:\n"
"s_memrealtime s[92:93]\n"
"s_waitcnt lgkmcnt(0)\n"
"s_sub_u32 s94, s92, s95\n"
"s_cmp_lt_u32 s94, 96\n"
"s_cbranch_scc1 K1B_PC6\n"
"s_mov_b32 s95, s92\n"
"s_barrier\n"
"v_add_f32 v38, v38, v101\n"
"v_mfma_f32_16x16x32_bf16 a[104:107], a[248:251], v[136:139], a[104:107]\n"
"s_mov_b32 m0, 135168\n"
"v_exp_f32 v102, v102\n"
"v_mfma_f32_16x16x32_bf16 a[112:115], a[252:255], v[128:131], a[112:115]\n"
"ds_read_b128 a[192:195], v1 offset:24576\n"
"buffer_load_dwordx4 v13, s[44:47], s59 offen offset:0 nt lds\n"
"v_add_f32 v37, v37, v102\n"
"v_mfma_f32_16x16x32_bf16 a[116:119], a[252:255], v[132:135], a[116:119]\n"
"v_exp_f32 v103, v103\n"
"v_mfma_f32_16x16x32_bf16 a[120:123], a[252:255], v[136:139], a[120:123]\n"
"buffer_load_dwordx4 v14, s[44:47], s59 offen offset:1024 nt lds\n"
"v_exp_f32 v104, v104\n"
"v_mfma_f32_16x16x32_bf16 a[128:131], a[224:227], v[128:131], a[128:131]\n"
"ds_read_b128 a[196:199], v1 offset:30720\n"
"v_exp_f32 v105, v105\n"
"v_mfma_f32_16x16x32_bf16 a[132:135], a[224:227], v[132:135], a[132:135]\n"
"v_exp_f32 v106, v106\n"
"v_add_f32 v38, v38, v103\n"
"v_mfma_f32_16x16x32_bf16 a[136:139], a[224:227], v[136:139], a[136:139]\n"
"buffer_load_dwordx4 v15, s[44:47], s59 offen offset:2048 nt lds\n"
"v_add_f32 v39, v39, v104\n"
"v_mfma_f32_16x16x32_bf16 a[144:147], a[228:231], v[128:131], a[144:147]\n"
"ds_read_b128 a[200:203], v2 offset:24576\n"
"v_exp_f32 v107, v107\n"
"v_mfma_f32_16x16x32_bf16 a[148:151], a[228:231], v[132:135], a[148:151]\n"
"v_add_f32 v40, v40, v105\n"
"v_add_f32 v39, v39, v106\n"
"v_add_f32 v40, v40, v107\n"
"v_mfma_f32_16x16x32_bf16 a[152:155], a[228:231], v[136:139], a[152:155]\n"
"buffer_load_dwordx4 v16, s[44:47], s59 offen offset:3072 nt lds\n"
"v_mfma_f32_16x16x32_bf16 a[160:163], a[232:235], v[128:131], a[160:163]\n"
"ds_read_b128 a[204:207], v2 offset:30720\n"
"v_mfma_f32_16x16x32_bf16 a[164:167], a[232:235], v[132:135], a[164:167]\n"
"v_mfma_f32_16x16x32_bf16 a[168:171], a[232:235], v[136:139], a[168:171]\n"
"s_mov_b32 m0, 139264\n"
"v_mfma_f32_16x16x32_bf16 a[176:179], a[236:239], v[128:131], a[176:179]\n"
"ds_read_b128 a[208:211], v1 offset:24704\n"
"buffer_load_dwordx4 v17, s[44:47], s59 offen offset:0 nt lds\n"
"v_mfma_f32_16x16x32_bf16 a[180:183], a[236:239], v[132:135], a[180:183]\n"
"v_mfma_f32_16x16x32_bf16 a[184:187], a[236:239], v[136:139], a[184:187]\n"
"s_cbranch_vccnz K1B_RO6\n"
"K1B_ROb6:\n"
"s_sub_u32 s61, s61, 1\n"
"s_cmp_lg_u32 s61, 0\n"
"s_cbranch_scc1 K1B_AL\n"
"s_waitcnt vmcnt(0)\n"
"s_add_u32 s92, %[olo], 50331648\n"
"s_addc_u32 s93, %[ohi], 0\n"
"s_mov_b32 s94, 7372800\n"
"s_mov_b32 s95, 0x20000\n"
"s_mul_i32 s75, %[bid], 29696\n"
"v_mov_b32 v160, -1\n"
"v_mov_b32 v161, -1\n"
"v_mov_b32 v162, -1\n"
"v_mov_b32 v163, -1\n"
"v_lshlrev_b32 v164, 4, v0\n"
"v_add_u32 v165, 4096, v164\n"
"buffer_store_dwordx4 v[160:163], v164, s[92:95], s75 offen offset:0 sc1\n"
"buffer_store_dwordx4 v[160:163], v164, s[92:95], s75 offen offset:1024 sc1\n"
"buffer_store_dwordx4 v[160:163], v164, s[92:95], s75 offen offset:2048 sc1\n"
"buffer_store_dwordx4 v[160:163], v164, s[92:95], s75 offen offset:3072 sc1\n"
"buffer_store_dwordx4 v[160:163], v165, s[92:95], s75 offen offset:0 sc1\n"
"buffer_store_dwordx4 v[160:163], v165, s[92:95], s75 offen offset:1024 sc1\n"
"buffer_store_dwordx4 v[160:163], v165, s[92:95], s75 offen offset:2048 sc1\n"
"buffer_store_dwordx4 v[160:163], v165, s[92:95], s75 offen offset:3072 sc1\n"
"s_add_u32 s75, s75, 8192\n"
"buffer_store_dwordx4 v[160:163], v164, s[92:95], s75 offen offset:0 sc1\n"
"buffer_store_dwordx4 v[160:163], v164, s[92:95], s75 offen offset:1024 sc1\n"
"buffer_store_dwordx4 v[160:163], v164, s[92:95], s75 offen offset:2048 sc1\n"
"buffer_store_dwordx4 v[160:163], v164, s[92:95], s75 offen offset:3072 sc1\n"
"buffer_store_dwordx4 v[160:163], v165, s[92:95], s75 offen offset:0 sc1\n"
"buffer_store_dwordx4 v[160:163], v165, s[92:95], s75 offen offset:1024 sc1\n"
"buffer_store_dwordx4 v[160:163], v165, s[92:95], s75 offen offset:2048 sc1\n"
"buffer_store_dwordx4 v[160:163], v165, s[92:95], s75 offen offset:3072 sc1\n"
"s_add_u32 s75, s75, 8192\n"
"buffer_store_dwordx4 v[160:163], v164, s[92:95], s75 offen offset:0 sc1\n"
"buffer_store_dwordx4 v[160:163], v164, s[92:95], s75 offen offset:1024 sc1\n"
"buffer_store_dwordx4 v[160:163], v164, s[92:95], s75 offen offset:2048 sc1\n"
"buffer_store_dwordx4 v[160:163], v164, s[92:95], s75 offen offset:3072 sc1\n"
"buffer_store_dwordx4 v[160:163], v165, s[92:95], s75 offen offset:0 sc1\n"
"buffer_store_dwordx4 v[160:163], v165, s[92:95], s75 offen offset:1024 sc1\n"
"buffer_store_dwordx4 v[160:163], v165, s[92:95], s75 offen offset:2048 sc1\n"
"buffer_store_dwordx4 v[160:163], v165, s[92:95], s75 offen offset:3072 sc1\n"
"s_add_u32 s75, s75, 8192\n"
"buffer_store_dwordx4 v[160:163], v164, s[92:95], s75 offen offset:0 sc1\n"
"buffer_store_dwordx4 v[160:163], v164, s[92:95], s75 offen offset:1024 sc1\n"
"buffer_store_dwordx4 v[160:163], v164, s[92:95], s75 offen offset:2048 sc1\n"
"buffer_store_dwordx4 v[160:163], v164, s[92:95], s75 offen offset:3072 sc1\n"
"buffer_store_dwordx4 v[160:163], v165, s[92:95], s75 offen offset:0 sc1\n"
"ds_read_b64_tr_b16 a[240:241], v5 offset:12288\n"
"ds_read_b64_tr_b16 a[242:243], v5 offset:18432\n"
"ds_read_b64_tr_b16 a[244:245], v6 offset:12288\n"
"ds_read_b64_tr_b16 a[246:247], v6 offset:18432\n"
"ds_read_b64_tr_b16 a[248:249], v7 offset:12288\n"
"ds_read_b64_tr_b16 a[250:251], v7 offset:18432\n"
"ds_read_b64_tr_b16 a[252:253], v8 offset:12288\n"
"ds_read_b64_tr_b16 a[254:255], v8 offset:18432\n"
"ds_read_b64_tr_b16 a[224:225], v5 offset:12416\n"
"ds_read_b64_tr_b16 a[226:227], v5 offset:18560\n"
"v_exp_f32 v108, v108\n"
"v_exp_f32 v109, v109\n"
"v_exp_f32 v110, v110\n"
"v_exp_f32 v111, v111\n"
"v_exp_f32 v112, v112\n"
"v_exp_f32 v113, v113\n"
"v_exp_f32 v114, v114\n"
"v_exp_f32 v115, v115\n"
"v_exp_f32 v116, v116\n"
"v_exp_f32 v117, v117\n"
"v_exp_f32 v118, v118\n"
"v_exp_f32 v119, v119\n"
"s_nop 0\n"
"v_cvt_pk_bf16_f32 v128, v96, v97\n"
"v_cvt_pk_bf16_f32 v129, v98, v99\n"
"v_cvt_pk_bf16_f32 v130, v100, v101\n"
"v_cvt_pk_bf16_f32 v131, v102, v103\n"
"v_cvt_pk_bf16_f32 v132, v104, v105\n"
"v_cvt_pk_bf16_f32 v133, v106, v107\n"
"v_cvt_pk_bf16_f32 v134, v108, v109\n"
"v_cvt_pk_bf16_f32 v135, v110, v111\n"
"v_cvt_pk_bf16_f32 v136, v112, v113\n"
"v_cvt_pk_bf16_f32 v137, v114, v115\n"
"v_cvt_pk_bf16_f32 v138, v116, v117\n"
"v_cvt_pk_bf16_f32 v139, v118, v119\n"
"s_waitcnt lgkmcnt(4)\n"
"v_mfma_f32_16x16x32_bf16 a[0:3], a[240:243], v[128:131], a[0:3]\n"
"ds_read_b64_tr_b16 a[228:229], v6 offset:12416\n"
"ds_read_b64_tr_b16 a[230:231], v6 offset:18560\n"
"v_mfma_f32_16x16x32_bf16 a[4:7], a[240:243], v[132:135], a[4:7]\n"
"v_add_f32 v39, v39, v108\n"
"v_add_f32 v40, v40, v109\n"
"v_mfma_f32_16x16x32_bf16 a[8:11], a[240:243], v[136:139], a[8:11]\n"
"v_add_f32 v39, v39, v110\n"
"v_add_f32 v40, v40, v111\n"
"v_mfma_f32_16x16x32_bf16 a[16:19], a[244:247], v[128:131], a[16:19]\n"
"ds_read_b64_tr_b16 a[232:233], v7 offset:12416\n"
"ds_read_b64_tr_b16 a[234:235], v7 offset:18560\n"
"v_mfma_f32_16x16x32_bf16 a[20:23], a[244:247], v[132:135], a[20:23]\n"
"v_add_f32 v41, v41, v112\n"
"v_add_f32 v42, v42, v113\n"
"v_mfma_f32_16x16x32_bf16 a[24:27], a[244:247], v[136:139], a[24:27]\n"
"v_add_f32 v41, v41, v114\n"
"v_add_f32 v42, v42, v115\n"
"v_mfma_f32_16x16x32_bf16 a[32:35], a[248:251], v[128:131], a[32:35]\n"
"ds_read_b64_tr_b16 a[236:237], v8 offset:12416\n"
"ds_read_b64_tr_b16 a[238:239], v8 offset:18560\n"
"v_mfma_f32_16x16x32_bf16 a[36:39], a[248:251], v[132:135], a[36:39]\n"
"v_add_f32 v41, v41, v116\n"
"v_add_f32 v42, v42, v117\n"
"v_mfma_f32_16x16x32_bf16 a[40:43], a[248:251], v[136:139], a[40:43]\n"
"v_add_f32 v41, v41, v118\n"
"s_waitcnt lgkmcnt(4)\n"
"v_mfma_f32_16x16x32_bf16 a[48:51], a[252:255], v[128:131], a[48:51]\n"
"ds_read_b64_tr_b16 a[240:241], v5 offset:12544\n"
"ds_read_b64_tr_b16 a[242:243], v5 offset:18688\n"
"v_mfma_f32_16x16x32_bf16 a[52:55], a[252:255], v[132:135], a[52:55]\n"
"v_add_f32 v42, v42, v119\n"
"v_add_f32 v37, v37, v38\n"
"v_mfma_f32_16x16x32_bf16 a[56:59], a[252:255], v[136:139], a[56:59]\n"
"v_mov_b32 v55, v37\n"
"v_add_f32 v39, v39, v40\n"
"v_mfma_f32_16x16x32_bf16 a[64:67], a[224:227], v[128:131], a[64:67]\n"
"ds_read_b64_tr_b16 a[244:245], v6 offset:12544\n"
"ds_read_b64_tr_b16 a[246:247], v6 offset:18688\n"
"v_mfma_f32_16x16x32_bf16 a[68:71], a[224:227], v[132:135], a[68:71]\n"
"v_permlane32_swap_b32 v55, v37\n"
"v_mov_b32 v56, v39\n"
"v_mfma_f32_16x16x32_bf16 a[72:75], a[224:227], v[136:139], a[72:75]\n"
"v_add_f32 v37, v37, v55\n"
"v_mov_b32 v55, v37\n"
"v_mfma_f32_16x16x32_bf16 a[80:83], a[228:231], v[128:131], a[80:83]\n"
"ds_read_b64_tr_b16 a[248:249], v7 offset:12544\n"
"ds_read_b64_tr_b16 a[250:251], v7 offset:18688\n"
"v_mfma_f32_16x16x32_bf16 a[84:87], a[228:231], v[132:135], a[84:87]\n"
"v_permlane16_swap_b32 v55, v37\n"
"v_permlane32_swap_b32 v56, v39\n"
"v_mfma_f32_16x16x32_bf16 a[88:91], a[228:231], v[136:139], a[88:91]\n"
"v_add_f32 v37, v37, v55\n"
"s_waitcnt lgkmcnt(4)\n"
"v_mfma_f32_16x16x32_bf16 a[96:99], a[232:235], v[128:131], a[96:99]\n"
"ds_read_b64_tr_b16 a[252:253], v8 offset:12544\n"
"ds_read_b64_tr_b16 a[254:255], v8 offset:18688\n"
"v_mfma_f32_16x16x32_bf16 a[100:103], a[232:235], v[132:135], a[100:103]\n"
"v_rcp_f32 v49, v37\n"
"v_mfma_f32_16x16x32_bf16 a[104:107], a[232:235], v[136:139], a[104:107]\n"
"v_add_f32 v39, v39, v56\n"
"v_mov_b32 v56, v39\n"
"v_mfma_f32_16x16x32_bf16 a[112:115], a[236:239], v[128:131], a[112:115]\n"
"v_add_f32 v41, v41, v42\n"
"v_mov_b32 v57, v41\n"
"v_mfma_f32_16x16x32_bf16 a[116:119], a[236:239], v[132:135], a[116:119]\n"
"v_permlane16_swap_b32 v56, v39\n"
"v_mfma_f32_16x16x32_bf16 a[120:123], a[236:239], v[136:139], a[120:123]\n"
"v_add_f32 v39, v39, v56\n"
"v_permlane32_swap_b32 v57, v41\n"
"v_mfma_f32_16x16x32_bf16 a[128:131], a[240:243], v[128:131], a[128:131]\n"
"v_rcp_f32 v50, v39\n"
"v_mfma_f32_16x16x32_bf16 a[132:135], a[240:243], v[132:135], a[132:135]\n"
"v_add_f32 v41, v41, v57\n"
"v_mov_b32 v57, v41\n"
"v_mfma_f32_16x16x32_bf16 a[136:139], a[240:243], v[136:139], a[136:139]\n"
"s_waitcnt lgkmcnt(0)\n"
"v_mfma_f32_16x16x32_bf16 a[144:147], a[244:247], v[128:131], a[144:147]\n"
"v_permlane16_swap_b32 v57, v41\n"
"v_mfma_f32_16x16x32_bf16 a[148:151], a[244:247], v[132:135], a[148:151]\n"
"v_add_f32 v41, v41, v57\n"
"v_mfma_f32_16x16x32_bf16 a[152:155], a[244:247], v[136:139], a[152:155]\n"
"v_rcp_f32 v51, v41\n"
"v_mfma_f32_16x16x32_bf16 a[160:163], a[248:251], v[128:131], a[160:163]\n"
"v_mfma_f32_16x16x32_bf16 a[164:167], a[248:251], v[132:135], a[164:167]\n"
"v_mfma_f32_16x16x32_bf16 a[168:171], a[248:251], v[136:139], a[168:171]\n"
"v_mfma_f32_16x16x32_bf16 a[176:179], a[252:255], v[128:131], a[176:179]\n"
"v_mfma_f32_16x16x32_bf16 a[180:183], a[252:255], v[132:135], a[180:183]\n"
"v_mfma_f32_16x16x32_bf16 a[184:187], a[252:255], v[136:139], a[184:187]\n"
"v_lshlrev_b32 v1, 4, v0\n"
"s_nop 7\n"
"v_accvgpr_read_b32 v68, a0\n"
"v_accvgpr_read_b32 v69, a1\n"
"v_accvgpr_read_b32 v70, a2\n"
"v_accvgpr_read_b32 v71, a3\n"
"v_accvgpr_read_b32 v72, a16\n"
"v_accvgpr_read_b32 v73, a17\n"
"v_accvgpr_read_b32 v74, a18\n"
"v_accvgpr_read_b32 v75, a19\n"
"v_pk_mul_f32 v[68:69], v[68:69], v[48:49] op_sel:[0,1] op_sel_hi:[1,1]\n"
"v_pk_mul_f32 v[70:71], v[70:71], v[48:49] op_sel:[0,1] op_sel_hi:[1,1]\n"
"v_pk_mul_f32 v[72:73], v[72:73], v[48:49] op_sel:[0,1] op_sel_hi:[1,1]\n"
"v_pk_mul_f32 v[74:75], v[74:75], v[48:49] op_sel:[0,1] op_sel_hi:[1,1]\n"
"v_cvt_pk_bf16_f32 v64, v68, v69\n"
"v_cvt_pk_bf16_f32 v65, v70, v71\n"
"v_cvt_pk_bf16_f32 v66, v72, v73\n"
"v_cvt_pk_bf16_f32 v67, v74, v75\n"
"buffer_store_dwordx4 v[64:67], v1, s[52:55], s68 offen offset:0\n"
"v_accvgpr_read_b32 v84, a4\n"
"v_accvgpr_read_b32 v85, a5\n"
"v_accvgpr_read_b32 v86, a6\n"
"v_accvgpr_read_b32 v87, a7\n"
"v_accvgpr_read_b32 v88, a20\n"
"v_accvgpr_read_b32 v89, a21\n"
"v_accvgpr_read_b32 v90, a22\n"
"v_accvgpr_read_b32 v91, a23\n"
"v_pk_mul_f32 v[84:85], v[84:85], v[50:51] op_sel:[0,0] op_sel_hi:[1,0]\n"
"v_pk_mul_f32 v[86:87], v[86:87], v[50:51] op_sel:[0,0] op_sel_hi:[1,0]\n"
"v_pk_mul_f32 v[88:89], v[88:89], v[50:51] op_sel:[0,0] op_sel_hi:[1,0]\n"
"v_pk_mul_f32 v[90:91], v[90:91], v[50:51] op_sel:[0,0] op_sel_hi:[1,0]\n"
"v_cvt_pk_bf16_f32 v80, v84, v85\n"
"v_cvt_pk_bf16_f32 v81, v86, v87\n"
"v_cvt_pk_bf16_f32 v82, v88, v89\n"
"v_cvt_pk_bf16_f32 v83, v90, v91\n"
"buffer_store_dwordx4 v[80:83], v1, s[52:55], s68 offen offset:1024\n"
"v_accvgpr_read_b32 v100, a8\n"
"v_accvgpr_read_b32 v101, a9\n"
"v_accvgpr_read_b32 v102, a10\n"
"v_accvgpr_read_b32 v103, a11\n"
"v_accvgpr_read_b32 v104, a24\n"
"v_accvgpr_read_b32 v105, a25\n"
"v_accvgpr_read_b32 v106, a26\n"
"v_accvgpr_read_b32 v107, a27\n"
"v_pk_mul_f32 v[100:101], v[100:101], v[50:51] op_sel:[0,1] op_sel_hi:[1,1]\n"
"v_pk_mul_f32 v[102:103], v[102:103], v[50:51] op_sel:[0,1] op_sel_hi:[1,1]\n"
"v_pk_mul_f32 v[104:105], v[104:105], v[50:51] op_sel:[0,1] op_sel_hi:[1,1]\n"
"v_pk_mul_f32 v[106:107], v[106:107], v[50:51] op_sel:[0,1] op_sel_hi:[1,1]\n"
"v_cvt_pk_bf16_f32 v96, v100, v101\n"
"v_cvt_pk_bf16_f32 v97, v102, v103\n"
"v_cvt_pk_bf16_f32 v98, v104, v105\n"
"v_cvt_pk_bf16_f32 v99, v106, v107\n"
"buffer_store_dwordx4 v[96:99], v1, s[52:55], s68 offen offset:2048\n"
"s_add_u32 s68, s68, 122880\n"
"v_accvgpr_read_b32 v116, a32\n"
"v_accvgpr_read_b32 v117, a33\n"
"v_accvgpr_read_b32 v118, a34\n"
"v_accvgpr_read_b32 v119, a35\n"
"v_accvgpr_read_b32 v120, a48\n"
"v_accvgpr_read_b32 v121, a49\n"
"v_accvgpr_read_b32 v122, a50\n"
"v_accvgpr_read_b32 v123, a51\n"
"v_pk_mul_f32 v[116:117], v[116:117], v[48:49] op_sel:[0,1] op_sel_hi:[1,1]\n"
"v_pk_mul_f32 v[118:119], v[118:119], v[48:49] op_sel:[0,1] op_sel_hi:[1,1]\n"
"v_pk_mul_f32 v[120:121], v[120:121], v[48:49] op_sel:[0,1] op_sel_hi:[1,1]\n"
"v_pk_mul_f32 v[122:123], v[122:123], v[48:49] op_sel:[0,1] op_sel_hi:[1,1]\n"
"v_cvt_pk_bf16_f32 v112, v116, v117\n"
"v_cvt_pk_bf16_f32 v113, v118, v119\n"
"v_cvt_pk_bf16_f32 v114, v120, v121\n"
"v_cvt_pk_bf16_f32 v115, v122, v123\n"
"buffer_store_dwordx4 v[112:115], v1, s[52:55], s68 offen offset:0\n"
"v_accvgpr_read_b32 v68, a36\n"
"v_accvgpr_read_b32 v69, a37\n"
"v_accvgpr_read_b32 v70, a38\n"
"v_accvgpr_read_b32 v71, a39\n"
"v_accvgpr_read_b32 v72, a52\n"
"v_accvgpr_read_b32 v73, a53\n"
"v_accvgpr_read_b32 v74, a54\n"
"v_accvgpr_read_b32 v75, a55\n"
"v_pk_mul_f32 v[68:69], v[68:69], v[50:51] op_sel:[0,0] op_sel_hi:[1,0]\n"
"v_pk_mul_f32 v[70:71], v[70:71], v[50:51] op_sel:[0,0] op_sel_hi:[1,0]\n"
"v_pk_mul_f32 v[72:73], v[72:73], v[50:51] op_sel:[0,0] op_sel_hi:[1,0]\n"
"v_pk_mul_f32 v[74:75], v[74:75], v[50:51] op_sel:[0,0] op_sel_hi:[1,0]\n"
"v_cvt_pk_bf16_f32 v64, v68, v69\n"
"v_cvt_pk_bf16_f32 v65, v70, v71\n"
"v_cvt_pk_bf16_f32 v66, v72, v73\n"
"v_cvt_pk_bf16_f32 v67, v74, v75\n"
"buffer_store_dwordx4 v[64:67], v1, s[52:55], s68 offen offset:1024\n"
"v_accvgpr_read_b32 v84, a40\n"
"v_accvgpr_read_b32 v85, a41\n"
"v_accvgpr_read_b32 v86, a42\n"
"v_accvgpr_read_b32 v87, a43\n"
"v_accvgpr_read_b32 v88, a56\n"
"v_accvgpr_read_b32 v89, a57\n"
"v_accvgpr_read_b32 v90, a58\n"
"v_accvgpr_read_b32 v91, a59\n"
"v_pk_mul_f32 v[84:85], v[84:85], v[50:51] op_sel:[0,1] op_sel_hi:[1,1]\n"
"v_pk_mul_f32 v[86:87], v[86:87], v[50:51] op_sel:[0,1] op_sel_hi:[1,1]\n"
"v_pk_mul_f32 v[88:89], v[88:89], v[50:51] op_sel:[0,1] op_sel_hi:[1,1]\n"
"v_pk_mul_f32 v[90:91], v[90:91], v[50:51] op_sel:[0,1] op_sel_hi:[1,1]\n"
"v_cvt_pk_bf16_f32 v80, v84, v85\n"
"v_cvt_pk_bf16_f32 v81, v86, v87\n"
"v_cvt_pk_bf16_f32 v82, v88, v89\n"
"v_cvt_pk_bf16_f32 v83, v90, v91\n"
"buffer_store_dwordx4 v[80:83], v1, s[52:55], s68 offen offset:2048\n"
"s_add_u32 s68, s68, 122880\n"
"v_accvgpr_read_b32 v100, a64\n"
"v_accvgpr_read_b32 v101, a65\n"
"v_accvgpr_read_b32 v102, a66\n"
"v_accvgpr_read_b32 v103, a67\n"
"v_accvgpr_read_b32 v104, a80\n"
"v_accvgpr_read_b32 v105, a81\n"
"v_accvgpr_read_b32 v106, a82\n"
"v_accvgpr_read_b32 v107, a83\n"
"v_pk_mul_f32 v[100:101], v[100:101], v[48:49] op_sel:[0,1] op_sel_hi:[1,1]\n"
"v_pk_mul_f32 v[102:103], v[102:103], v[48:49] op_sel:[0,1] op_sel_hi:[1,1]\n"
"v_pk_mul_f32 v[104:105], v[104:105], v[48:49] op_sel:[0,1] op_sel_hi:[1,1]\n"
"v_pk_mul_f32 v[106:107], v[106:107], v[48:49] op_sel:[0,1] op_sel_hi:[1,1]\n"
"v_cvt_pk_bf16_f32 v96, v100, v101\n"
"v_cvt_pk_bf16_f32 v97, v102, v103\n"
"v_cvt_pk_bf16_f32 v98, v104, v105\n"
"v_cvt_pk_bf16_f32 v99, v106, v107\n"
"buffer_store_dwordx4 v[96:99], v1, s[52:55], s68 offen offset:0\n"
"v_accvgpr_read_b32 v116, a68\n"
"v_accvgpr_read_b32 v117, a69\n"
"v_accvgpr_read_b32 v118, a70\n"
"v_accvgpr_read_b32 v119, a71\n"
"v_accvgpr_read_b32 v120, a84\n"
"v_accvgpr_read_b32 v121, a85\n"
"v_accvgpr_read_b32 v122, a86\n"
"v_accvgpr_read_b32 v123, a87\n"
"v_pk_mul_f32 v[116:117], v[116:117], v[50:51] op_sel:[0,0] op_sel_hi:[1,0]\n"
"v_pk_mul_f32 v[118:119], v[118:119], v[50:51] op_sel:[0,0] op_sel_hi:[1,0]\n"
"v_pk_mul_f32 v[120:121], v[120:121], v[50:51] op_sel:[0,0] op_sel_hi:[1,0]\n"
"v_pk_mul_f32 v[122:123], v[122:123], v[50:51] op_sel:[0,0] op_sel_hi:[1,0]\n"
"v_cvt_pk_bf16_f32 v112, v116, v117\n"
"v_cvt_pk_bf16_f32 v113, v118, v119\n"
"v_cvt_pk_bf16_f32 v114, v120, v121\n"
"v_cvt_pk_bf16_f32 v115, v122, v123\n"
"buffer_store_dwordx4 v[112:115], v1, s[52:55], s68 offen offset:1024\n"
"v_accvgpr_read_b32 v68, a72\n"
"v_accvgpr_read_b32 v69, a73\n"
"v_accvgpr_read_b32 v70, a74\n"
"v_accvgpr_read_b32 v71, a75\n"
"v_accvgpr_read_b32 v72, a88\n"
"v_accvgpr_read_b32 v73, a89\n"
"v_accvgpr_read_b32 v74, a90\n"
"v_accvgpr_read_b32 v75, a91\n"
"v_pk_mul_f32 v[68:69], v[68:69], v[50:51] op_sel:[0,1] op_sel_hi:[1,1]\n"
"v_pk_mul_f32 v[70:71], v[70:71], v[50:51] op_sel:[0,1] op_sel_hi:[1,1]\n"
"v_pk_mul_f32 v[72:73], v[72:73], v[50:51] op_sel:[0,1] op_sel_hi:[1,1]\n"
"v_pk_mul_f32 v[74:75], v[74:75], v[50:51] op_sel:[0,1] op_sel_hi:[1,1]\n"
"v_cvt_pk_bf16_f32 v64, v68, v69\n"
"v_cvt_pk_bf16_f32 v65, v70, v71\n"
"v_cvt_pk_bf16_f32 v66, v72, v73\n"
"v_cvt_pk_bf16_f32 v67, v74, v75\n"
"buffer_store_dwordx4 v[64:67], v1, s[52:55], s68 offen offset:2048\n"
"s_add_u32 s68, s68, 122880\n"
"v_accvgpr_read_b32 v84, a96\n"
"v_accvgpr_read_b32 v85, a97\n"
"v_accvgpr_read_b32 v86, a98\n"
"v_accvgpr_read_b32 v87, a99\n"
"v_accvgpr_read_b32 v88, a112\n"
"v_accvgpr_read_b32 v89, a113\n"
"v_accvgpr_read_b32 v90, a114\n"
"v_accvgpr_read_b32 v91, a115\n"
"v_pk_mul_f32 v[84:85], v[84:85], v[48:49] op_sel:[0,1] op_sel_hi:[1,1]\n"
"v_pk_mul_f32 v[86:87], v[86:87], v[48:49] op_sel:[0,1] op_sel_hi:[1,1]\n"
"v_pk_mul_f32 v[88:89], v[88:89], v[48:49] op_sel:[0,1] op_sel_hi:[1,1]\n"
"v_pk_mul_f32 v[90:91], v[90:91], v[48:49] op_sel:[0,1] op_sel_hi:[1,1]\n"
"v_cvt_pk_bf16_f32 v80, v84, v85\n"
"v_cvt_pk_bf16_f32 v81, v86, v87\n"
"v_cvt_pk_bf16_f32 v82, v88, v89\n"
"v_cvt_pk_bf16_f32 v83, v90, v91\n"
"buffer_store_dwordx4 v[80:83], v1, s[52:55], s68 offen offset:0\n"
"v_accvgpr_read_b32 v100, a100\n"
"v_accvgpr_read_b32 v101, a101\n"
"v_accvgpr_read_b32 v102, a102\n"
"v_accvgpr_read_b32 v103, a103\n"
"v_accvgpr_read_b32 v104, a116\n"
"v_accvgpr_read_b32 v105, a117\n"
"v_accvgpr_read_b32 v106, a118\n"
"v_accvgpr_read_b32 v107, a119\n"
"v_pk_mul_f32 v[100:101], v[100:101], v[50:51] op_sel:[0,0] op_sel_hi:[1,0]\n"
"v_pk_mul_f32 v[102:103], v[102:103], v[50:51] op_sel:[0,0] op_sel_hi:[1,0]\n"
"v_pk_mul_f32 v[104:105], v[104:105], v[50:51] op_sel:[0,0] op_sel_hi:[1,0]\n"
"v_pk_mul_f32 v[106:107], v[106:107], v[50:51] op_sel:[0,0] op_sel_hi:[1,0]\n"
"v_cvt_pk_bf16_f32 v96, v100, v101\n"
"v_cvt_pk_bf16_f32 v97, v102, v103\n"
"v_cvt_pk_bf16_f32 v98, v104, v105\n"
"v_cvt_pk_bf16_f32 v99, v106, v107\n"
"buffer_store_dwordx4 v[96:99], v1, s[52:55], s68 offen offset:1024\n"
"v_accvgpr_read_b32 v116, a104\n"
"v_accvgpr_read_b32 v117, a105\n"
"v_accvgpr_read_b32 v118, a106\n"
"v_accvgpr_read_b32 v119, a107\n"
"v_accvgpr_read_b32 v120, a120\n"
"v_accvgpr_read_b32 v121, a121\n"
"v_accvgpr_read_b32 v122, a122\n"
"v_accvgpr_read_b32 v123, a123\n"
"v_pk_mul_f32 v[116:117], v[116:117], v[50:51] op_sel:[0,1] op_sel_hi:[1,1]\n"
"v_pk_mul_f32 v[118:119], v[118:119], v[50:51] op_sel:[0,1] op_sel_hi:[1,1]\n"
"v_pk_mul_f32 v[120:121], v[120:121], v[50:51] op_sel:[0,1] op_sel_hi:[1,1]\n"
"v_pk_mul_f32 v[122:123], v[122:123], v[50:51] op_sel:[0,1] op_sel_hi:[1,1]\n"
"v_cvt_pk_bf16_f32 v112, v116, v117\n"
"v_cvt_pk_bf16_f32 v113, v118, v119\n"
"v_cvt_pk_bf16_f32 v114, v120, v121\n"
"v_cvt_pk_bf16_f32 v115, v122, v123\n"
"buffer_store_dwordx4 v[112:115], v1, s[52:55], s68 offen offset:2048\n"
"s_add_u32 s68, s68, 122880\n"
"v_accvgpr_read_b32 v68, a128\n"
"v_accvgpr_read_b32 v69, a129\n"
"v_accvgpr_read_b32 v70, a130\n"
"v_accvgpr_read_b32 v71, a131\n"
"v_accvgpr_read_b32 v72, a144\n"
"v_accvgpr_read_b32 v73, a145\n"
"v_accvgpr_read_b32 v74, a146\n"
"v_accvgpr_read_b32 v75, a147\n"
"v_pk_mul_f32 v[68:69], v[68:69], v[48:49] op_sel:[0,1] op_sel_hi:[1,1]\n"
"v_pk_mul_f32 v[70:71], v[70:71], v[48:49] op_sel:[0,1] op_sel_hi:[1,1]\n"
"v_pk_mul_f32 v[72:73], v[72:73], v[48:49] op_sel:[0,1] op_sel_hi:[1,1]\n"
"v_pk_mul_f32 v[74:75], v[74:75], v[48:49] op_sel:[0,1] op_sel_hi:[1,1]\n"
"v_cvt_pk_bf16_f32 v64, v68, v69\n"
"v_cvt_pk_bf16_f32 v65, v70, v71\n"
"v_cvt_pk_bf16_f32 v66, v72, v73\n"
"v_cvt_pk_bf16_f32 v67, v74, v75\n"
"buffer_store_dwordx4 v[64:67], v1, s[52:55], s68 offen offset:0\n"
"v_accvgpr_read_b32 v84, a132\n"
"v_accvgpr_read_b32 v85, a133\n"
"v_accvgpr_read_b32 v86, a134\n"
"v_accvgpr_read_b32 v87, a135\n"
"v_accvgpr_read_b32 v88, a148\n"
"v_accvgpr_read_b32 v89, a149\n"
"v_accvgpr_read_b32 v90, a150\n"
"v_accvgpr_read_b32 v91, a151\n"
"v_pk_mul_f32 v[84:85], v[84:85], v[50:51] op_sel:[0,0] op_sel_hi:[1,0]\n"
"v_pk_mul_f32 v[86:87], v[86:87], v[50:51] op_sel:[0,0] op_sel_hi:[1,0]\n"
"v_pk_mul_f32 v[88:89], v[88:89], v[50:51] op_sel:[0,0] op_sel_hi:[1,0]\n"
"v_pk_mul_f32 v[90:91], v[90:91], v[50:51] op_sel:[0,0] op_sel_hi:[1,0]\n"
"v_cvt_pk_bf16_f32 v80, v84, v85\n"
"v_cvt_pk_bf16_f32 v81, v86, v87\n"
"v_cvt_pk_bf16_f32 v82, v88, v89\n"
"v_cvt_pk_bf16_f32 v83, v90, v91\n"
"buffer_store_dwordx4 v[80:83], v1, s[52:55], s68 offen offset:1024\n"
"v_accvgpr_read_b32 v100, a136\n"
"v_accvgpr_read_b32 v101, a137\n"
"v_accvgpr_read_b32 v102, a138\n"
"v_accvgpr_read_b32 v103, a139\n"
"v_accvgpr_read_b32 v104, a152\n"
"v_accvgpr_read_b32 v105, a153\n"
"v_accvgpr_read_b32 v106, a154\n"
"v_accvgpr_read_b32 v107, a155\n"
"v_pk_mul_f32 v[100:101], v[100:101], v[50:51] op_sel:[0,1] op_sel_hi:[1,1]\n"
"v_pk_mul_f32 v[102:103], v[102:103], v[50:51] op_sel:[0,1] op_sel_hi:[1,1]\n"
"v_pk_mul_f32 v[104:105], v[104:105], v[50:51] op_sel:[0,1] op_sel_hi:[1,1]\n"
"v_pk_mul_f32 v[106:107], v[106:107], v[50:51] op_sel:[0,1] op_sel_hi:[1,1]\n"
"v_cvt_pk_bf16_f32 v96, v100, v101\n"
"v_cvt_pk_bf16_f32 v97, v102, v103\n"
"v_cvt_pk_bf16_f32 v98, v104, v105\n"
"v_cvt_pk_bf16_f32 v99, v106, v107\n"
"buffer_store_dwordx4 v[96:99], v1, s[52:55], s68 offen offset:2048\n"
"s_add_u32 s68, s68, 122880\n"
"v_accvgpr_read_b32 v116, a160\n"
"v_accvgpr_read_b32 v117, a161\n"
"v_accvgpr_read_b32 v118, a162\n"
"v_accvgpr_read_b32 v119, a163\n"
"v_accvgpr_read_b32 v120, a176\n"
"v_accvgpr_read_b32 v121, a177\n"
"v_accvgpr_read_b32 v122, a178\n"
"v_accvgpr_read_b32 v123, a179\n"
"v_pk_mul_f32 v[116:117], v[116:117], v[48:49] op_sel:[0,1] op_sel_hi:[1,1]\n"
"v_pk_mul_f32 v[118:119], v[118:119], v[48:49] op_sel:[0,1] op_sel_hi:[1,1]\n"
"v_pk_mul_f32 v[120:121], v[120:121], v[48:49] op_sel:[0,1] op_sel_hi:[1,1]\n"
"v_pk_mul_f32 v[122:123], v[122:123], v[48:49] op_sel:[0,1] op_sel_hi:[1,1]\n"
"v_cvt_pk_bf16_f32 v112, v116, v117\n"
"v_cvt_pk_bf16_f32 v113, v118, v119\n"
"v_cvt_pk_bf16_f32 v114, v120, v121\n"
"v_cvt_pk_bf16_f32 v115, v122, v123\n"
"buffer_store_dwordx4 v[112:115], v1, s[52:55], s68 offen offset:0\n"
"v_accvgpr_read_b32 v68, a164\n"
"v_accvgpr_read_b32 v69, a165\n"
"v_accvgpr_read_b32 v70, a166\n"
"v_accvgpr_read_b32 v71, a167\n"
"v_accvgpr_read_b32 v72, a180\n"
"v_accvgpr_read_b32 v73, a181\n"
"v_accvgpr_read_b32 v74, a182\n"
"v_accvgpr_read_b32 v75, a183\n"
"v_pk_mul_f32 v[68:69], v[68:69], v[50:51] op_sel:[0,0] op_sel_hi:[1,0]\n"
"v_pk_mul_f32 v[70:71], v[70:71], v[50:51] op_sel:[0,0] op_sel_hi:[1,0]\n"
"v_pk_mul_f32 v[72:73], v[72:73], v[50:51] op_sel:[0,0] op_sel_hi:[1,0]\n"
"v_pk_mul_f32 v[74:75], v[74:75], v[50:51] op_sel:[0,0] op_sel_hi:[1,0]\n"
"v_cvt_pk_bf16_f32 v64, v68, v69\n"
"v_cvt_pk_bf16_f32 v65, v70, v71\n"
"v_cvt_pk_bf16_f32 v66, v72, v73\n"
"v_cvt_pk_bf16_f32 v67, v74, v75\n"
"buffer_store_dwordx4 v[64:67], v1, s[52:55], s68 offen offset:1024\n"
"v_accvgpr_read_b32 v84, a168\n"
"v_accvgpr_read_b32 v85, a169\n"
"v_accvgpr_read_b32 v86, a170\n"
"v_accvgpr_read_b32 v87, a171\n"
"v_accvgpr_read_b32 v88, a184\n"
"v_accvgpr_read_b32 v89, a185\n"
"v_accvgpr_read_b32 v90, a186\n"
"v_accvgpr_read_b32 v91, a187\n"
"v_pk_mul_f32 v[84:85], v[84:85], v[50:51] op_sel:[0,1] op_sel_hi:[1,1]\n"
"v_pk_mul_f32 v[86:87], v[86:87], v[50:51] op_sel:[0,1] op_sel_hi:[1,1]\n"
"v_pk_mul_f32 v[88:89], v[88:89], v[50:51] op_sel:[0,1] op_sel_hi:[1,1]\n"
"v_pk_mul_f32 v[90:91], v[90:91], v[50:51] op_sel:[0,1] op_sel_hi:[1,1]\n"
"v_cvt_pk_bf16_f32 v80, v84, v85\n"
"v_cvt_pk_bf16_f32 v81, v86, v87\n"
"v_cvt_pk_bf16_f32 v82, v88, v89\n"
"v_cvt_pk_bf16_f32 v83, v90, v91\n"
"buffer_store_dwordx4 v[80:83], v1, s[52:55], s68 offen offset:2048\n"
"s_branch K1_END\n"
"K1B_RS0:\n"
"s_mov_b32 s65, 0\n"
"s_branch K1B_RSX1\n"
"K1B_RO0:\n"
"s_mov_b32 s65, 0\n"
"s_branch K1B_ROX\n"
"K1B_RS1:\n"
"s_mov_b32 s65, 1\n"
"s_branch K1B_RSX0\n"
"K1B_RO1:\n"
"s_mov_b32 s65, 1\n"
"s_branch K1B_ROX\n"
"K1B_RS2:\n"
"s_mov_b32 s65, 2\n"
"s_branch K1B_RSX1\n"
"K1B_RO2:\n"
"s_mov_b32 s65, 2\n"
"s_branch K1B_ROX\n"
"K1B_RS3:\n"
"s_mov_b32 s65, 3\n"
"s_branch K1B_RSX0\n"
"K1B_RO3:\n"
"s_mov_b32 s65, 3\n"
"s_branch K1B_ROX\n"
"K1B_RS4:\n"
"s_mov_b32 s65, 4\n"
"s_branch K1B_RSX1\n"
"K1B_RO4:\n"
"s_mov_b32 s65, 4\n"
"s_branch K1B_ROX\n"
"K1B_RS5:\n"
"s_mov_b32 s65, 5\n"
"s_branch K1B_RSX0\n"
"K1B_RO5:\n"
"s_mov_b32 s65, 5\n"
"s_branch K1B_ROX\n"
"K1B_RS6:\n"
"s_mov_b32 s65, 6\n"
"s_branch K1B_RSX1\n"
"K1B_RO6:\n"
"s_mov_b32 s65, 6\n"
"s_branch K1B_ROX\n"
"K1B_RSX0:\n"
"v_max3_f32 v55, v64, v65, v66\n"
"v_max3_f32 v55, v55, v67, v68\n"
"v_max3_f32 v55, v55, v69, v70\n"
"v_max_f32 v55, v55, v71\n"
"v_mov_b32 v59, v55\n"
"s_nop 1\n"
"v_permlane32_swap_b32 v59, v55\n"
"s_nop 0\n"
"v_max_f32 v55, v55, v59\n"
"v_mov_b32 v59, v55\n"
"s_nop 1\n"
"v_permlane16_swap_b32 v59, v55\n"
"s_nop 0\n"
"v_max_f32 v55, v55, v59\n"
"v_max_f32 v55, 0, v55\n"
"v_sub_f32 v64, v64, v55\n"
"v_sub_f32 v65, v65, v55\n"
"v_sub_f32 v66, v66, v55\n"
"v_sub_f32 v67, v67, v55\n"
"v_sub_f32 v68, v68, v55\n"
"v_sub_f32 v69, v69, v55\n"
"v_sub_f32 v70, v70, v55\n"
"v_sub_f32 v71, v71, v55\n"
"v_sub_f32 v144, v144, v55\n"
"v_sub_f32 v145, v145, v55\n"
"v_sub_f32 v146, v146, v55\n"
"v_sub_f32 v147, v147, v55\n"
"v_sub_f32 v55, 0, v55\n"
"v_exp_f32 v49, v55\n"
"s_nop 0\n"
"v_mul_f32 v37, v37, v49\n"
"v_mul_f32 v38, v38, v49\n"
"v_max3_f32 v56, v72, v73, v74\n"
"v_max3_f32 v56, v56, v75, v76\n"
"v_max3_f32 v56, v56, v77, v78\n"
"v_max_f32 v56, v56, v79\n"
"v_mov_b32 v59, v56\n"
"s_nop 1\n"
"v_permlane32_swap_b32 v59, v56\n"
"s_nop 0\n"
"v_max_f32 v56, v56, v59\n"
"v_mov_b32 v59, v56\n"
"s_nop 1\n"
"v_permlane16_swap_b32 v59, v56\n"
"s_nop 0\n"
"v_max_f32 v56, v56, v59\n"
"v_max_f32 v56, 0, v56\n"
"v_sub_f32 v72, v72, v56\n"
"v_sub_f32 v73, v73, v56\n"
"v_sub_f32 v74, v74, v56\n"
"v_sub_f32 v75, v75, v56\n"
"v_sub_f32 v76, v76, v56\n"
"v_sub_f32 v77, v77, v56\n"
"v_sub_f32 v78, v78, v56\n"
"v_sub_f32 v79, v79, v56\n"
"v_sub_f32 v148, v148, v56\n"
"v_sub_f32 v149, v149, v56\n"
"v_sub_f32 v150, v150, v56\n"
"v_sub_f32 v151, v151, v56\n"
"v_sub_f32 v56, 0, v56\n"
"v_exp_f32 v50, v56\n"
"s_nop 0\n"
"v_mul_f32 v39, v39, v50\n"
"v_mul_f32 v40, v40, v50\n"
"v_max3_f32 v57, v80, v81, v82\n"
"v_max3_f32 v57, v57, v83, v84\n"
"v_max3_f32 v57, v57, v85, v86\n"
"v_max_f32 v57, v57, v87\n"
"v_mov_b32 v59, v57\n"
"s_nop 1\n"
"v_permlane32_swap_b32 v59, v57\n"
"s_nop 0\n"
"v_max_f32 v57, v57, v59\n"
"v_mov_b32 v59, v57\n"
"s_nop 1\n"
"v_permlane16_swap_b32 v59, v57\n"
"s_nop 0\n"
"v_max_f32 v57, v57, v59\n"
"v_max_f32 v57, 0, v57\n"
"v_sub_f32 v80, v80, v57\n"
"v_sub_f32 v81, v81, v57\n"
"v_sub_f32 v82, v82, v57\n"
"v_sub_f32 v83, v83, v57\n"
"v_sub_f32 v84, v84, v57\n"
"v_sub_f32 v85, v85, v57\n"
"v_sub_f32 v86, v86, v57\n"
"v_sub_f32 v87, v87, v57\n"
"v_sub_f32 v152, v152, v57\n"
"v_sub_f32 v153, v153, v57\n"
"v_sub_f32 v154, v154, v57\n"
"v_sub_f32 v155, v155, v57\n"
"v_sub_f32 v57, 0, v57\n"
"v_exp_f32 v51, v57\n"
"s_nop 0\n"
"v_mul_f32 v41, v41, v51\n"
"v_mul_f32 v42, v42, v51\n"
"s_mov_b64 vcc, -1\n"
"s_nop 1\n"
"s_cmp_eq_u32 s65, 1\n"
"s_cbranch_scc1 K1B_RSb1\n"
"s_cmp_eq_u32 s65, 3\n"
"s_cbranch_scc1 K1B_RSb3\n"
"s_branch K1B_RSb5\n"
"K1B_RSX1:\n"
"v_max3_f32 v55, v96, v97, v98\n"
"v_max3_f32 v55, v55, v99, v100\n"
"v_max3_f32 v55, v55, v101, v102\n"
"v_max_f32 v55, v55, v103\n"
"v_mov_b32 v59, v55\n"
"s_nop 1\n"
"v_permlane32_swap_b32 v59, v55\n"
"s_nop 0\n"
"v_max_f32 v55, v55, v59\n"
"v_mov_b32 v59, v55\n"
"s_nop 1\n"
"v_permlane16_swap_b32 v59, v55\n"
"s_nop 0\n"
"v_max_f32 v55, v55, v59\n"
"v_max_f32 v55, 0, v55\n"
"v_sub_f32 v96, v96, v55\n"
"v_sub_f32 v97, v97, v55\n"
"v_sub_f32 v98, v98, v55\n"
"v_sub_f32 v99, v99, v55\n"
"v_sub_f32 v100, v100, v55\n"
"v_sub_f32 v101, v101, v55\n"
"v_sub_f32 v102, v102, v55\n"
"v_sub_f32 v103, v103, v55\n"
"v_sub_f32 v144, v144, v55\n"
"v_sub_f32 v145, v145, v55\n"
"v_sub_f32 v146, v146, v55\n"
"v_sub_f32 v147, v147, v55\n"
"v_sub_f32 v55, 0, v55\n"
"v_exp_f32 v49, v55\n"
"s_nop 0\n"
"v_mul_f32 v37, v37, v49\n"
"v_mul_f32 v38, v38, v49\n"
"v_max3_f32 v56, v104, v105, v106\n"
"v_max3_f32 v56, v56, v107, v108\n"
"v_max3_f32 v56, v56, v109, v110\n"
"v_max_f32 v56, v56, v111\n"
"v_mov_b32 v59, v56\n"
"s_nop 1\n"
"v_permlane32_swap_b32 v59, v56\n"
"s_nop 0\n"
"v_max_f32 v56, v56, v59\n"
"v_mov_b32 v59, v56\n"
"s_nop 1\n"
"v_permlane16_swap_b32 v59, v56\n"
"s_nop 0\n"
"v_max_f32 v56, v56, v59\n"
"v_max_f32 v56, 0, v56\n"
"v_sub_f32 v104, v104, v56\n"
"v_sub_f32 v105, v105, v56\n"
"v_sub_f32 v106, v106, v56\n"
"v_sub_f32 v107, v107, v56\n"
"v_sub_f32 v108, v108, v56\n"
"v_sub_f32 v109, v109, v56\n"
"v_sub_f32 v110, v110, v56\n"
"v_sub_f32 v111, v111, v56\n"
"v_sub_f32 v148, v148, v56\n"
"v_sub_f32 v149, v149, v56\n"
"v_sub_f32 v150, v150, v56\n"
"v_sub_f32 v151, v151, v56\n"
"v_sub_f32 v56, 0, v56\n"
"v_exp_f32 v50, v56\n"
"s_nop 0\n"
"v_mul_f32 v39, v39, v50\n"
"v_mul_f32 v40, v40, v50\n"
"v_max3_f32 v57, v112, v113, v114\n"
"v_max3_f32 v57, v57, v115, v116\n"
"v_max3_f32 v57, v57, v117, v118\n"
"v_max_f32 v57, v57, v119\n"
"v_mov_b32 v59, v57\n"
"s_nop 1\n"
"v_permlane32_swap_b32 v59, v57\n"
"s_nop 0\n"
"v_max_f32 v57, v57, v59\n"
"v_mov_b32 v59, v57\n"
"s_nop 1\n"
"v_permlane16_swap_b32 v59, v57\n"
"s_nop 0\n"
"v_max_f32 v57, v57, v59\n"
"v_max_f32 v57, 0, v57\n"
"v_sub_f32 v112, v112, v57\n"
"v_sub_f32 v113, v113, v57\n"
"v_sub_f32 v114, v114, v57\n"
"v_sub_f32 v115, v115, v57\n"
"v_sub_f32 v116, v116, v57\n"
"v_sub_f32 v117, v117, v57\n"
"v_sub_f32 v118, v118, v57\n"
"v_sub_f32 v119, v119, v57\n"
"v_sub_f32 v152, v152, v57\n"
"v_sub_f32 v153, v153, v57\n"
"v_sub_f32 v154, v154, v57\n"
"v_sub_f32 v155, v155, v57\n"
"v_sub_f32 v57, 0, v57\n"
"v_exp_f32 v51, v57\n"
"s_nop 0\n"
"v_mul_f32 v41, v41, v51\n"
"v_mul_f32 v42, v42, v51\n"
"s_mov_b64 vcc, -1\n"
"s_nop 1\n"
"s_cmp_eq_u32 s65, 0\n"
"s_cbranch_scc1 K1B_RSb0\n"
"s_cmp_eq_u32 s65, 2\n"
"s_cbranch_scc1 K1B_RSb2\n"
"s_cmp_eq_u32 s65, 4\n"
"s_cbranch_scc1 K1B_RSb4\n"
"s_branch K1B_RSb6\n"
"K1B_ROX:\n"
"s_nop 7\n"
"v_accvgpr_read_b32 v55, a0\n"
"v_accvgpr_read_b32 v56, a1\n"
"v_accvgpr_read_b32 v57, a2\n"
"v_accvgpr_read_b32 v58, a3\n"
"v_mul_f32 v55, v55, v49\n"
"v_mul_f32 v56, v56, v49\n"
"v_mul_f32 v57, v57, v49\n"
"v_mul_f32 v58, v58, v49\n"
"v_accvgpr_write_b32 a0, v55\n"
"v_accvgpr_write_b32 a1, v56\n"
"v_accvgpr_write_b32 a2, v57\n"
"v_accvgpr_write_b32 a3, v58\n"
"v_accvgpr_read_b32 v55, a4\n"
"v_accvgpr_read_b32 v56, a5\n"
"v_accvgpr_read_b32 v57, a6\n"
"v_accvgpr_read_b32 v58, a7\n"
"v_mul_f32 v55, v55, v50\n"
"v_mul_f32 v56, v56, v50\n"
"v_mul_f32 v57, v57, v50\n"
"v_mul_f32 v58, v58, v50\n"
"v_accvgpr_write_b32 a4, v55\n"
"v_accvgpr_write_b32 a5, v56\n"
"v_accvgpr_write_b32 a6, v57\n"
"v_accvgpr_write_b32 a7, v58\n"
"v_accvgpr_read_b32 v55, a8\n"
"v_accvgpr_read_b32 v56, a9\n"
"v_accvgpr_read_b32 v57, a10\n"
"v_accvgpr_read_b32 v58, a11\n"
"v_mul_f32 v55, v55, v51\n"
"v_mul_f32 v56, v56, v51\n"
"v_mul_f32 v57, v57, v51\n"
"v_mul_f32 v58, v58, v51\n"
"v_accvgpr_write_b32 a8, v55\n"
"v_accvgpr_write_b32 a9, v56\n"
"v_accvgpr_write_b32 a10, v57\n"
"v_accvgpr_write_b32 a11, v58\n"
"v_accvgpr_read_b32 v55, a16\n"
"v_accvgpr_read_b32 v56, a17\n"
"v_accvgpr_read_b32 v57, a18\n"
"v_accvgpr_read_b32 v58, a19\n"
"v_mul_f32 v55, v55, v49\n"
"v_mul_f32 v56, v56, v49\n"
"v_mul_f32 v57, v57, v49\n"
"v_mul_f32 v58, v58, v49\n"
"v_accvgpr_write_b32 a16, v55\n"
"v_accvgpr_write_b32 a17, v56\n"
"v_accvgpr_write_b32 a18, v57\n"
"v_accvgpr_write_b32 a19, v58\n"
"v_accvgpr_read_b32 v55, a20\n"
"v_accvgpr_read_b32 v56, a21\n"
"v_accvgpr_read_b32 v57, a22\n"
"v_accvgpr_read_b32 v58, a23\n"
"v_mul_f32 v55, v55, v50\n"
"v_mul_f32 v56, v56, v50\n"
"v_mul_f32 v57, v57, v50\n"
"v_mul_f32 v58, v58, v50\n"
"v_accvgpr_write_b32 a20, v55\n"
"v_accvgpr_write_b32 a21, v56\n"
"v_accvgpr_write_b32 a22, v57\n"
"v_accvgpr_write_b32 a23, v58\n"
"v_accvgpr_read_b32 v55, a24\n"
"v_accvgpr_read_b32 v56, a25\n"
"v_accvgpr_read_b32 v57, a26\n"
"v_accvgpr_read_b32 v58, a27\n"
"v_mul_f32 v55, v55, v51\n"
"v_mul_f32 v56, v56, v51\n"
"v_mul_f32 v57, v57, v51\n"
"v_mul_f32 v58, v58, v51\n"
"v_accvgpr_write_b32 a24, v55\n"
"v_accvgpr_write_b32 a25, v56\n"
"v_accvgpr_write_b32 a26, v57\n"
"v_accvgpr_write_b32 a27, v58\n"
"v_accvgpr_read_b32 v55, a32\n"
"v_accvgpr_read_b32 v56, a33\n"
"v_accvgpr_read_b32 v57, a34\n"
"v_accvgpr_read_b32 v58, a35\n"
"v_mul_f32 v55, v55, v49\n"
"v_mul_f32 v56, v56, v49\n"
"v_mul_f32 v57, v57, v49\n"
"v_mul_f32 v58, v58, v49\n"
"v_accvgpr_write_b32 a32, v55\n"
"v_accvgpr_write_b32 a33, v56\n"
"v_accvgpr_write_b32 a34, v57\n"
"v_accvgpr_write_b32 a35, v58\n"
"v_accvgpr_read_b32 v55, a36\n"
"v_accvgpr_read_b32 v56, a37\n"
"v_accvgpr_read_b32 v57, a38\n"
"v_accvgpr_read_b32 v58, a39\n"
"v_mul_f32 v55, v55, v50\n"
"v_mul_f32 v56, v56, v50\n"
"v_mul_f32 v57, v57, v50\n"
"v_mul_f32 v58, v58, v50\n"
"v_accvgpr_write_b32 a36, v55\n"
"v_accvgpr_write_b32 a37, v56\n"
"v_accvgpr_write_b32 a38, v57\n"
"v_accvgpr_write_b32 a39, v58\n"
"v_accvgpr_read_b32 v55, a40\n"
"v_accvgpr_read_b32 v56, a41\n"
"v_accvgpr_read_b32 v57, a42\n"
"v_accvgpr_read_b32 v58, a43\n"
"v_mul_f32 v55, v55, v51\n"
"v_mul_f32 v56, v56, v51\n"
"v_mul_f32 v57, v57, v51\n"
"v_mul_f32 v58, v58, v51\n"
"v_accvgpr_write_b32 a40, v55\n"
"v_accvgpr_write_b32 a41, v56\n"
"v_accvgpr_write_b32 a42, v57\n"
"v_accvgpr_write_b32 a43, v58\n"
"v_accvgpr_read_b32 v55, a48\n"
"v_accvgpr_read_b32 v56, a49\n"
"v_accvgpr_read_b32 v57, a50\n"
"v_accvgpr_read_b32 v58, a51\n"
"v_mul_f32 v55, v55, v49\n"
"v_mul_f32 v56, v56, v49\n"
"v_mul_f32 v57, v57, v49\n"
"v_mul_f32 v58, v58, v49\n"
"v_accvgpr_write_b32 a48, v55\n"
"v_accvgpr_write_b32 a49, v56\n"
"v_accvgpr_write_b32 a50, v57\n"
"v_accvgpr_write_b32 a51, v58\n"
"v_accvgpr_read_b32 v55, a52\n"
"v_accvgpr_read_b32 v56, a53\n"
"v_accvgpr_read_b32 v57, a54\n"
"v_accvgpr_read_b32 v58, a55\n"
"v_mul_f32 v55, v55, v50\n"
"v_mul_f32 v56, v56, v50\n"
"v_mul_f32 v57, v57, v50\n"
"v_mul_f32 v58, v58, v50\n"
"v_accvgpr_write_b32 a52, v55\n"
"v_accvgpr_write_b32 a53, v56\n"
"v_accvgpr_write_b32 a54, v57\n"
"v_accvgpr_write_b32 a55, v58\n"
"v_accvgpr_read_b32 v55, a56\n"
"v_accvgpr_read_b32 v56, a57\n"
"v_accvgpr_read_b32 v57, a58\n"
"v_accvgpr_read_b32 v58, a59\n"
"v_mul_f32 v55, v55, v51\n"
"v_mul_f32 v56, v56, v51\n"
"v_mul_f32 v57, v57, v51\n"
"v_mul_f32 v58, v58, v51\n"
"v_accvgpr_write_b32 a56, v55\n"
"v_accvgpr_write_b32 a57, v56\n"
"v_accvgpr_write_b32 a58, v57\n"
"v_accvgpr_write_b32 a59, v58\n"
"v_accvgpr_read_b32 v55, a64\n"
"v_accvgpr_read_b32 v56, a65\n"
"v_accvgpr_read_b32 v57, a66\n"
"v_accvgpr_read_b32 v58, a67\n"
"v_mul_f32 v55, v55, v49\n"
"v_mul_f32 v56, v56, v49\n"
"v_mul_f32 v57, v57, v49\n"
"v_mul_f32 v58, v58, v49\n"
"v_accvgpr_write_b32 a64, v55\n"
"v_accvgpr_write_b32 a65, v56\n"
"v_accvgpr_write_b32 a66, v57\n"
"v_accvgpr_write_b32 a67, v58\n"
"v_accvgpr_read_b32 v55, a68\n"
"v_accvgpr_read_b32 v56, a69\n"
"v_accvgpr_read_b32 v57, a70\n"
"v_accvgpr_read_b32 v58, a71\n"
"v_mul_f32 v55, v55, v50\n"
"v_mul_f32 v56, v56, v50\n"
"v_mul_f32 v57, v57, v50\n"
"v_mul_f32 v58, v58, v50\n"
"v_accvgpr_write_b32 a68, v55\n"
"v_accvgpr_write_b32 a69, v56\n"
"v_accvgpr_write_b32 a70, v57\n"
"v_accvgpr_write_b32 a71, v58\n"
"v_accvgpr_read_b32 v55, a72\n"
"v_accvgpr_read_b32 v56, a73\n"
"v_accvgpr_read_b32 v57, a74\n"
"v_accvgpr_read_b32 v58, a75\n"
"v_mul_f32 v55, v55, v51\n"
"v_mul_f32 v56, v56, v51\n"
"v_mul_f32 v57, v57, v51\n"
"v_mul_f32 v58, v58, v51\n"
"v_accvgpr_write_b32 a72, v55\n"
"v_accvgpr_write_b32 a73, v56\n"
"v_accvgpr_write_b32 a74, v57\n"
"v_accvgpr_write_b32 a75, v58\n"
"v_accvgpr_read_b32 v55, a80\n"
"v_accvgpr_read_b32 v56, a81\n"
"v_accvgpr_read_b32 v57, a82\n"
"v_accvgpr_read_b32 v58, a83\n"
"v_mul_f32 v55, v55, v49\n"
"v_mul_f32 v56, v56, v49\n"
"v_mul_f32 v57, v57, v49\n"
"v_mul_f32 v58, v58, v49\n"
"v_accvgpr_write_b32 a80, v55\n"
"v_accvgpr_write_b32 a81, v56\n"
"v_accvgpr_write_b32 a82, v57\n"
"v_accvgpr_write_b32 a83, v58\n"
"v_accvgpr_read_b32 v55, a84\n"
"v_accvgpr_read_b32 v56, a85\n"
"v_accvgpr_read_b32 v57, a86\n"
"v_accvgpr_read_b32 v58, a87\n"
"v_mul_f32 v55, v55, v50\n"
"v_mul_f32 v56, v56, v50\n"
"v_mul_f32 v57, v57, v50\n"
"v_mul_f32 v58, v58, v50\n"
"v_accvgpr_write_b32 a84, v55\n"
"v_accvgpr_write_b32 a85, v56\n"
"v_accvgpr_write_b32 a86, v57\n"
"v_accvgpr_write_b32 a87, v58\n"
"v_accvgpr_read_b32 v55, a88\n"
"v_accvgpr_read_b32 v56, a89\n"
"v_accvgpr_read_b32 v57, a90\n"
"v_accvgpr_read_b32 v58, a91\n"
"v_mul_f32 v55, v55, v51\n"
"v_mul_f32 v56, v56, v51\n"
"v_mul_f32 v57, v57, v51\n"
"v_mul_f32 v58, v58, v51\n"
"v_accvgpr_write_b32 a88, v55\n"
"v_accvgpr_write_b32 a89, v56\n"
"v_accvgpr_write_b32 a90, v57\n"
"v_accvgpr_write_b32 a91, v58\n"
"v_accvgpr_read_b32 v55, a96\n"
"v_accvgpr_read_b32 v56, a97\n"
"v_accvgpr_read_b32 v57, a98\n"
"v_accvgpr_read_b32 v58, a99\n"
"v_mul_f32 v55, v55, v49\n"
"v_mul_f32 v56, v56, v49\n"
"v_mul_f32 v57, v57, v49\n"
"v_mul_f32 v58, v58, v49\n"
"v_accvgpr_write_b32 a96, v55\n"
"v_accvgpr_write_b32 a97, v56\n"
"v_accvgpr_write_b32 a98, v57\n"
"v_accvgpr_write_b32 a99, v58\n"
"v_accvgpr_read_b32 v55, a100\n"
"v_accvgpr_read_b32 v56, a101\n"
"v_accvgpr_read_b32 v57, a102\n"
"v_accvgpr_read_b32 v58, a103\n"
"v_mul_f32 v55, v55, v50\n"
"v_mul_f32 v56, v56, v50\n"
"v_mul_f32 v57, v57, v50\n"
"v_mul_f32 v58, v58, v50\n"
"v_accvgpr_write_b32 a100, v55\n"
"v_accvgpr_write_b32 a101, v56\n"
"v_accvgpr_write_b32 a102, v57\n"
"v_accvgpr_write_b32 a103, v58\n"
"v_accvgpr_read_b32 v55, a104\n"
"v_accvgpr_read_b32 v56, a105\n"
"v_accvgpr_read_b32 v57, a106\n"
"v_accvgpr_read_b32 v58, a107\n"
"v_mul_f32 v55, v55, v51\n"
"v_mul_f32 v56, v56, v51\n"
"v_mul_f32 v57, v57, v51\n"
"v_mul_f32 v58, v58, v51\n"
"v_accvgpr_write_b32 a104, v55\n"
"v_accvgpr_write_b32 a105, v56\n"
"v_accvgpr_write_b32 a106, v57\n"
"v_accvgpr_write_b32 a107, v58\n"
"v_accvgpr_read_b32 v55, a112\n"
"v_accvgpr_read_b32 v56, a113\n"
"v_accvgpr_read_b32 v57, a114\n"
"v_accvgpr_read_b32 v58, a115\n"
"v_mul_f32 v55, v55, v49\n"
"v_mul_f32 v56, v56, v49\n"
"v_mul_f32 v57, v57, v49\n"
"v_mul_f32 v58, v58, v49\n"
"v_accvgpr_write_b32 a112, v55\n"
"v_accvgpr_write_b32 a113, v56\n"
"v_accvgpr_write_b32 a114, v57\n"
"v_accvgpr_write_b32 a115, v58\n"
"v_accvgpr_read_b32 v55, a116\n"
"v_accvgpr_read_b32 v56, a117\n"
"v_accvgpr_read_b32 v57, a118\n"
"v_accvgpr_read_b32 v58, a119\n"
"v_mul_f32 v55, v55, v50\n"
"v_mul_f32 v56, v56, v50\n"
"v_mul_f32 v57, v57, v50\n"
"v_mul_f32 v58, v58, v50\n"
"v_accvgpr_write_b32 a116, v55\n"
"v_accvgpr_write_b32 a117, v56\n"
"v_accvgpr_write_b32 a118, v57\n"
"v_accvgpr_write_b32 a119, v58\n"
"v_accvgpr_read_b32 v55, a120\n"
"v_accvgpr_read_b32 v56, a121\n"
"v_accvgpr_read_b32 v57, a122\n"
"v_accvgpr_read_b32 v58, a123\n"
"v_mul_f32 v55, v55, v51\n"
"v_mul_f32 v56, v56, v51\n"
"v_mul_f32 v57, v57, v51\n"
"v_mul_f32 v58, v58, v51\n"
"v_accvgpr_write_b32 a120, v55\n"
"v_accvgpr_write_b32 a121, v56\n"
"v_accvgpr_write_b32 a122, v57\n"
"v_accvgpr_write_b32 a123, v58\n"
"v_accvgpr_read_b32 v55, a128\n"
"v_accvgpr_read_b32 v56, a129\n"
"v_accvgpr_read_b32 v57, a130\n"
"v_accvgpr_read_b32 v58, a131\n"
"v_mul_f32 v55, v55, v49\n"
"v_mul_f32 v56, v56, v49\n"
"v_mul_f32 v57, v57, v49\n"
"v_mul_f32 v58, v58, v49\n"
"v_accvgpr_write_b32 a128, v55\n"
"v_accvgpr_write_b32 a129, v56\n"
"v_accvgpr_write_b32 a130, v57\n"
"v_accvgpr_write_b32 a131, v58\n"
"v_accvgpr_read_b32 v55, a132\n"
"v_accvgpr_read_b32 v56, a133\n"
"v_accvgpr_read_b32 v57, a134\n"
"v_accvgpr_read_b32 v58, a135\n"
"v_mul_f32 v55, v55, v50\n"
"v_mul_f32 v56, v56, v50\n"
"v_mul_f32 v57, v57, v50\n"
"v_mul_f32 v58, v58, v50\n"
"v_accvgpr_write_b32 a132, v55\n"
"v_accvgpr_write_b32 a133, v56\n"
"v_accvgpr_write_b32 a134, v57\n"
"v_accvgpr_write_b32 a135, v58\n"
"v_accvgpr_read_b32 v55, a136\n"
"v_accvgpr_read_b32 v56, a137\n"
"v_accvgpr_read_b32 v57, a138\n"
"v_accvgpr_read_b32 v58, a139\n"
"v_mul_f32 v55, v55, v51\n"
"v_mul_f32 v56, v56, v51\n"
"v_mul_f32 v57, v57, v51\n"
"v_mul_f32 v58, v58, v51\n"
"v_accvgpr_write_b32 a136, v55\n"
"v_accvgpr_write_b32 a137, v56\n"
"v_accvgpr_write_b32 a138, v57\n"
"v_accvgpr_write_b32 a139, v58\n"
"v_accvgpr_read_b32 v55, a144\n"
"v_accvgpr_read_b32 v56, a145\n"
"v_accvgpr_read_b32 v57, a146\n"
"v_accvgpr_read_b32 v58, a147\n"
"v_mul_f32 v55, v55, v49\n"
"v_mul_f32 v56, v56, v49\n"
"v_mul_f32 v57, v57, v49\n"
"v_mul_f32 v58, v58, v49\n"
"v_accvgpr_write_b32 a144, v55\n"
"v_accvgpr_write_b32 a145, v56\n"
"v_accvgpr_write_b32 a146, v57\n"
"v_accvgpr_write_b32 a147, v58\n"
"v_accvgpr_read_b32 v55, a148\n"
"v_accvgpr_read_b32 v56, a149\n"
"v_accvgpr_read_b32 v57, a150\n"
"v_accvgpr_read_b32 v58, a151\n"
"v_mul_f32 v55, v55, v50\n"
"v_mul_f32 v56, v56, v50\n"
"v_mul_f32 v57, v57, v50\n"
"v_mul_f32 v58, v58, v50\n"
"v_accvgpr_write_b32 a148, v55\n"
"v_accvgpr_write_b32 a149, v56\n"
"v_accvgpr_write_b32 a150, v57\n"
"v_accvgpr_write_b32 a151, v58\n"
"v_accvgpr_read_b32 v55, a152\n"
"v_accvgpr_read_b32 v56, a153\n"
"v_accvgpr_read_b32 v57, a154\n"
"v_accvgpr_read_b32 v58, a155\n"
"v_mul_f32 v55, v55, v51\n"
"v_mul_f32 v56, v56, v51\n"
"v_mul_f32 v57, v57, v51\n"
"v_mul_f32 v58, v58, v51\n"
"v_accvgpr_write_b32 a152, v55\n"
"v_accvgpr_write_b32 a153, v56\n"
"v_accvgpr_write_b32 a154, v57\n"
"v_accvgpr_write_b32 a155, v58\n"
"v_accvgpr_read_b32 v55, a160\n"
"v_accvgpr_read_b32 v56, a161\n"
"v_accvgpr_read_b32 v57, a162\n"
"v_accvgpr_read_b32 v58, a163\n"
"v_mul_f32 v55, v55, v49\n"
"v_mul_f32 v56, v56, v49\n"
"v_mul_f32 v57, v57, v49\n"
"v_mul_f32 v58, v58, v49\n"
"v_accvgpr_write_b32 a160, v55\n"
"v_accvgpr_write_b32 a161, v56\n"
"v_accvgpr_write_b32 a162, v57\n"
"v_accvgpr_write_b32 a163, v58\n"
"v_accvgpr_read_b32 v55, a164\n"
"v_accvgpr_read_b32 v56, a165\n"
"v_accvgpr_read_b32 v57, a166\n"
"v_accvgpr_read_b32 v58, a167\n"
"v_mul_f32 v55, v55, v50\n"
"v_mul_f32 v56, v56, v50\n"
"v_mul_f32 v57, v57, v50\n"
"v_mul_f32 v58, v58, v50\n"
"v_accvgpr_write_b32 a164, v55\n"
"v_accvgpr_write_b32 a165, v56\n"
"v_accvgpr_write_b32 a166, v57\n"
"v_accvgpr_write_b32 a167, v58\n"
"v_accvgpr_read_b32 v55, a168\n"
"v_accvgpr_read_b32 v56, a169\n"
"v_accvgpr_read_b32 v57, a170\n"
"v_accvgpr_read_b32 v58, a171\n"
"v_mul_f32 v55, v55, v51\n"
"v_mul_f32 v56, v56, v51\n"
"v_mul_f32 v57, v57, v51\n"
"v_mul_f32 v58, v58, v51\n"
"v_accvgpr_write_b32 a168, v55\n"
"v_accvgpr_write_b32 a169, v56\n"
"v_accvgpr_write_b32 a170, v57\n"
"v_accvgpr_write_b32 a171, v58\n"
"v_accvgpr_read_b32 v55, a176\n"
"v_accvgpr_read_b32 v56, a177\n"
"v_accvgpr_read_b32 v57, a178\n"
"v_accvgpr_read_b32 v58, a179\n"
"v_mul_f32 v55, v55, v49\n"
"v_mul_f32 v56, v56, v49\n"
"v_mul_f32 v57, v57, v49\n"
"v_mul_f32 v58, v58, v49\n"
"v_accvgpr_write_b32 a176, v55\n"
"v_accvgpr_write_b32 a177, v56\n"
"v_accvgpr_write_b32 a178, v57\n"
"v_accvgpr_write_b32 a179, v58\n"
"v_accvgpr_read_b32 v55, a180\n"
"v_accvgpr_read_b32 v56, a181\n"
"v_accvgpr_read_b32 v57, a182\n"
"v_accvgpr_read_b32 v58, a183\n"
"v_mul_f32 v55, v55, v50\n"
"v_mul_f32 v56, v56, v50\n"
"v_mul_f32 v57, v57, v50\n"
"v_mul_f32 v58, v58, v50\n"
"v_accvgpr_write_b32 a180, v55\n"
"v_accvgpr_write_b32 a181, v56\n"
"v_accvgpr_write_b32 a182, v57\n"
"v_accvgpr_write_b32 a183, v58\n"
"v_accvgpr_read_b32 v55, a184\n"
"v_accvgpr_read_b32 v56, a185\n"
"v_accvgpr_read_b32 v57, a186\n"
"v_accvgpr_read_b32 v58, a187\n"
"v_mul_f32 v55, v55, v51\n"
"v_mul_f32 v56, v56, v51\n"
"v_mul_f32 v57, v57, v51\n"
"v_mul_f32 v58, v58, v51\n"
"v_accvgpr_write_b32 a184, v55\n"
"v_accvgpr_write_b32 a185, v56\n"
"v_accvgpr_write_b32 a186, v57\n"
"v_accvgpr_write_b32 a187, v58\n"
"s_nop 1\n"
"s_cmp_eq_u32 s65, 0\n"
"s_cbranch_scc1 K1B_ROb0\n"
"s_cmp_eq_u32 s65, 1\n"
"s_cbranch_scc1 K1B_ROb1\n"
"s_cmp_eq_u32 s65, 2\n"
"s_cbranch_scc1 K1B_ROb2\n"
"s_cmp_eq_u32 s65, 3\n"
"s_cbranch_scc1 K1B_ROb3\n"
"s_cmp_eq_u32 s65, 4\n"
"s_cbranch_scc1 K1B_ROb4\n"
"s_cmp_eq_u32 s65, 5\n"
"s_cbranch_scc1 K1B_ROb5\n"
"s_branch K1B_ROb6\n"
"K1_END:\n"
  : [tid] "+v"(tid)
  : [bid] "s"(bid), [xlo] "s"((unsigned)(size_t)x), [xhi] "s"((unsigned)((size_t)x >> 32)), [klo] "s"((unsigned)(size_t)kc), [khi] "s"((unsigned)((size_t)kc >> 32)), [vlo] "s"((unsigned)(size_t)vc), [vhi] "s"((unsigned)((size_t)vc >> 32)), [wlo] "s"((unsigned)(size_t)wq), [whi] "s"((unsigned)((size_t)wq >> 32)), [plo] "s"((unsigned)(size_t)wo), [phi] "s"((unsigned)((size_t)wo >> 32)), [olo] "s"((unsigned)(size_t)o), [ohi] "s"((unsigned)((size_t)o >> 32))
  : "memory", "vcc", "scc", "s36","s37","s38","s39","s40","s41","s42","s43","s44","s45","s46","s47","s48","s49","s50","s51","s52","s53","s54","s55","s56","s57","s58","s59","s60","s61","s62","s63","s64","s65","s66","s67","s68","s69","s70","s71","s72","s73","s74","s75","s76","s77","s78","s79","s80","s81","s82","s83","s84","s85","s86","s87","s88","s89","s90","s91","s92","s93","s94","s95","s96","s97","s98","s99", "v1","v2","v3","v4","v5","v6","v7","v8","v9","v10","v11","v12","v13","v14","v15","v16","v17","v18","v19","v20","v21","v22","v23","v24","v25","v26","v27","v28","v29","v30","v31","v32","v33","v34","v35","v36","v37","v38","v39","v40","v41","v42","v43","v44","v45","v46","v47","v48","v49","v50","v51","v52","v53","v54","v55","v56","v57","v58","v59","v60","v61","v62","v63","v64","v65","v66","v67","v68","v69","v70","v71","v72","v73","v74","v75","v76","v77","v78","v79","v80","v81","v82","v83","v84","v85","v86","v87","v88","v89","v90","v91","v92","v93","v94","v95","v96","v97","v98","v99","v100","v101","v102","v103","v104","v105","v106","v107","v108","v109","v110","v111","v112","v113","v114","v115","v116","v117","v118","v119","v120","v121","v122","v123","v124","v125","v126","v127","v128","v129","v130","v131","v132","v133","v134","v135","v136","v137","v138","v139","v140","v141","v142","v143","v144","v145","v146","v147","v148","v149","v150","v151","v152","v153","v154","v155","v156","v157","v158","v159","v160","v161","v162","v163","v164","v165","v166","v167","v168","v169","v170","v171","v172","v173","v174","v175","v176","v177","v178","v179","v180","v181","v182","v183","v184","v185","v186","v187","v188","v189","v190","v191","v192","v193","v194","v195","v196","v197","v198","v199","v200","v201","v202","v203","v204","v205","v206","v207","v208","v209","v210","v211","v212","v213","v214","v215","v216","v217","v218","v219","v220","v221","v222","v223","v224","v225","v226","v227","v228","v229","v230","v231","v232","v233","v234","v235","v236","v237","v238","v239","v240","v241","v242","v243","v244","v245","v246","v247","v248","v249","v250","v251","v252","v253","v254","v255", "a0","a1","a2","a3","a4","a5","a6","a7","a8","a9","a10","a11","a12","a13","a14","a15","a16","a17","a18","a19","a20","a21","a22","a23","a24","a25","a26","a27","a28","a29","a30","a31","a32","a33","a34","a35","a36","a37","a38","a39","a40","a41","a42","a43","a44","a45","a46","a47","a48","a49","a50","a51","a52","a53","a54","a55","a56","a57","a58","a59","a60","a61","a62","a63","a64","a65","a66","a67","a68","a69","a70","a71","a72","a73","a74","a75","a76","a77","a78","a79","a80","a81","a82","a83","a84","a85","a86","a87","a88","a89","a90","a91","a92","a93","a94","a95","a96","a97","a98","a99","a100","a101","a102","a103","a104","a105","a106","a107","a108","a109","a110","a111","a112","a113","a114","a115","a116","a117","a118","a119","a120","a121","a122","a123","a124","a125","a126","a127","a128","a129","a130","a131","a132","a133","a134","a135","a136","a137","a138","a139","a140","a141","a142","a143","a144","a145","a146","a147","a148","a149","a150","a151","a152","a153","a154","a155","a156","a157","a158","a159","a160","a161","a162","a163","a164","a165","a166","a167","a168","a169","a170","a171","a172","a173","a174","a175","a176","a177","a178","a179","a180","a181","a182","a183","a184","a185","a186","a187","a188","a189","a190","a191","a192","a193","a194","a195","a196","a197","a198","a199","a200","a201","a202","a203","a204","a205","a206","a207","a208","a209","a210","a211","a212","a213","a214","a215","a216","a217","a218","a219","a220","a221","a222","a223","a224","a225","a226","a227","a228","a229","a230","a231","a232","a233","a234","a235","a236","a237","a238","a239","a240","a241","a242","a243","a244","a245","a246","a247","a248","a249","a250","a251","a252","a253","a254","a255");
}
extern "C" __global__ __launch_bounds__(256, 1) void k2_oproj(const void* o, const void* wo, void* y, void* dbg) {
  unsigned tid = threadIdx.x; unsigned bid = blockIdx.x;
  asm volatile(
"K2_CODE_BEGIN:\n"
"v_lshrrev_b32 v12, 6, %[tid]\n"
"v_and_b32 v0, 63, %[tid]\n"
"s_and_b32 s60, %[bid], 7\n"
"v_readfirstlane_b32 s56, v12\n"
"s_lshr_b32 s61, %[bid], 3\n"
"s_lshr_b32 s63, s60, 1\n"
"s_and_b32 s60, s60, 1\n"
"s_mul_i32 s60, s60, 30\n"
"s_add_u32 s67, s60, s61\n"
"s_lshr_b32 s64, s67, 2\n"
"s_and_b32 s65, s67, 3\n"
"s_mov_b32 s36, %[olo]\n"
"s_mov_b32 s37, %[ohi]\n"
"s_mov_b32 s38, 23592960\n"
"s_mov_b32 s39, 0x20000\n"
"s_mov_b32 s40, %[wlo]\n"
"s_mov_b32 s41, %[whi]\n"
"s_mov_b32 s42, 7864320\n"
"s_mov_b32 s43, 0x20000\n"
"s_mul_i32 s57, s63, 5760\n"
"s_lshl_b32 s60, s64, 3\n"
"s_add_u32 s57, s57, s60\n"
"s_lshl_b32 s60, s56, 1\n"
"s_add_u32 s57, s57, s60\n"
"s_lshl_b32 s57, s57, 10\n"
"s_mul_i32 s58, s63, 1966080\n"
"s_lshr_b32 s60, s56, 1\n"
"s_mul_i32 s61, s60, 40960\n"
"s_add_u32 s58, s58, s61\n"
"s_mul_i32 s61, s65, 320\n"
"s_add_u32 s58, s58, s61\n"
"s_mul_i32 s66, s60, 10240\n"
"s_and_b32 s60, s56, 1\n"
"s_mul_i32 s61, s60, 5120\n"
"s_add_u32 s66, s66, s61\n"
"s_mul_i32 s61, s60, 320\n"
"v_lshlrev_b32 v1, 4, v0\n"
"v_add_u32 v13, s61, v0\n"
"v_mul_u32_u24 v15, 3277, v13\n"
"v_lshrrev_b32 v15, 16, v15\n"
"v_mul_u32_u24 v16, 20, v15\n"
"v_sub_u32 v16, v13, v16\n"
"v_bfe_u32 v17, v15, 2, 1\n"
"v_lshlrev_b32 v17, 1, v17\n"
"v_xor_b32 v16, v16, v17\n"
"v_mul_u32_u24 v15, 1280, v15\n"
"v_lshl_add_u32 v2, v16, 4, v15\n"
"v_add_u32 v14, 64, v13\n"
"v_mul_u32_u24 v15, 3277, v14\n"
"v_lshrrev_b32 v15, 16, v15\n"
"v_mul_u32_u24 v16, 20, v15\n"
"v_sub_u32 v16, v14, v16\n"
"v_bfe_u32 v17, v15, 2, 1\n"
"v_lshlrev_b32 v17, 1, v17\n"
"v_xor_b32 v16, v16, v17\n"
"v_mul_u32_u24 v15, 1280, v15\n"
"v_lshl_add_u32 v3, v16, 4, v15\n"
"v_subrev_u32 v3, 1024, v3\n"
"v_add_u32 v14, 128, v13\n"
"v_mul_u32_u24 v15, 3277, v14\n"
"v_lshrrev_b32 v15, 16, v15\n"
"v_mul_u32_u24 v16, 20, v15\n"
"v_sub_u32 v16, v14, v16\n"
"v_bfe_u32 v17, v15, 2, 1\n"
"v_lshlrev_b32 v17, 1, v17\n"
"v_xor_b32 v16, v16, v17\n"
"v_mul_u32_u24 v15, 1280, v15\n"
"v_lshl_add_u32 v4, v16, 4, v15\n"
"v_subrev_u32 v4, 2048, v4\n"
"v_add_u32 v14, 192, v13\n"
"v_mul_u32_u24 v15, 3277, v14\n"
"v_lshrrev_b32 v15, 16, v15\n"
"v_mul_u32_u24 v16, 20, v15\n"
"v_sub_u32 v16, v14, v16\n"
"v_bfe_u32 v17, v15, 2, 1\n"
"v_lshlrev_b32 v17, 1, v17\n"
"v_xor_b32 v16, v16, v17\n"
"v_mul_u32_u24 v15, 1280, v15\n"
"v_lshl_add_u32 v5, v16, 4, v15\n"
"v_subrev_u32 v5, 3072, v5\n"
"v_add_u32 v14, 256, v13\n"
"v_mul_u32_u24 v15, 3277, v14\n"
"v_lshrrev_b32 v15, 16, v15\n"
"v_mul_u32_u24 v16, 20, v15\n"
"v_sub_u32 v16, v14, v16\n"
"v_bfe_u32 v17, v15, 2, 1\n"
"v_lshlrev_b32 v17, 1, v17\n"
"v_xor_b32 v16, v16, v17\n"
"v_mul_u32_u24 v15, 1280, v15\n"
"v_lshl_add_u32 v6, v16, 4, v15\n"
"s_add_u32 s60, s63, 1\n"
"s_mul_i32 s38, s60, 5898240\n"
"s_mul_i32 s42, s60, 1966080\n"
"buffer_load_dwordx4 v[32:35], v1, s[36:39], s57 offen\n"
"buffer_load_dwordx4 v[36:39], v1, s[36:39], s57 offen offset:1024\n"
"s_add_u32 s57, s57, 122880\n"
"s_add_u32 m0, s66, 0\n"
"s_nop 0\n"
"buffer_load_dwordx4 v2, s[40:43], s58 offen offset:0 lds\n"
"buffer_load_dwordx4 v3, s[40:43], s58 offen offset:1024 lds\n"
"buffer_load_dwordx4 v4, s[40:43], s58 offen offset:2048 lds\n"
"buffer_load_dwordx4 v5, s[40:43], s58 offen offset:3072 lds\n"
"s_add_u32 m0, m0, 4096\n"
"s_nop 0\n"
"buffer_load_dwordx4 v6, s[40:43], s58 offen lds\n"
"s_add_u32 s58, s58, 81920\n"
"buffer_load_dwordx4 v[40:43], v1, s[36:39], s57 offen\n"
"buffer_load_dwordx4 v[44:47], v1, s[36:39], s57 offen offset:1024\n"
"s_add_u32 s57, s57, 122880\n"
"buffer_load_dwordx4 v[48:51], v1, s[36:39], s57 offen\n"
"buffer_load_dwordx4 v[52:55], v1, s[36:39], s57 offen offset:1024\n"
"s_add_u32 s57, s57, 122880\n"
"s_add_u32 m0, s66, 20480\n"
"s_nop 0\n"
"buffer_load_dwordx4 v2, s[40:43], s58 offen offset:0 lds\n"
"buffer_load_dwordx4 v3, s[40:43], s58 offen offset:1024 lds\n"
"buffer_load_dwordx4 v4, s[40:43], s58 offen offset:2048 lds\n"
"buffer_load_dwordx4 v5, s[40:43], s58 offen offset:3072 lds\n"
"s_add_u32 m0, m0, 4096\n"
"s_nop 0\n"
"buffer_load_dwordx4 v6, s[40:43], s58 offen lds\n"
"s_add_u32 s58, s58, 81920\n"
"buffer_load_dwordx4 v[56:59], v1, s[36:39], s57 offen\n"
"buffer_load_dwordx4 v[60:63], v1, s[36:39], s57 offen offset:1024\n"
"s_add_u32 s57, s57, 122880\n"
"buffer_load_dwordx4 v[64:67], v1, s[36:39], s57 offen\n"
"buffer_load_dwordx4 v[68:71], v1, s[36:39], s57 offen offset:1024\n"
"s_add_u32 s57, s57, 122880\n"
"s_add_u32 m0, s66, 40960\n"
"s_nop 0\n"
"buffer_load_dwordx4 v2, s[40:43], s58 offen offset:0 lds\n"
"buffer_load_dwordx4 v3, s[40:43], s58 offen offset:1024 lds\n"
"buffer_load_dwordx4 v4, s[40:43], s58 offen offset:2048 lds\n"
"buffer_load_dwordx4 v5, s[40:43], s58 offen offset:3072 lds\n"
"s_add_u32 m0, m0, 4096\n"
"s_nop 0\n"
"buffer_load_dwordx4 v6, s[40:43], s58 offen lds\n"
"s_add_u32 s58, s58, 81920\n"
"buffer_load_dwordx4 v[72:75], v1, s[36:39], s57 offen\n"
"buffer_load_dwordx4 v[76:79], v1, s[36:39], s57 offen offset:1024\n"
"s_add_u32 s57, s57, 122880\n"
"buffer_load_dwordx4 v[80:83], v1, s[36:39], s57 offen\n"
"buffer_load_dwordx4 v[84:87], v1, s[36:39], s57 offen offset:1024\n"
"s_add_u32 s57, s57, 122880\n"
"s_add_u32 m0, s66, 61440\n"
"s_nop 0\n"
"buffer_load_dwordx4 v2, s[40:43], s58 offen offset:0 lds\n"
"buffer_load_dwordx4 v3, s[40:43], s58 offen offset:1024 lds\n"
"v_accvgpr_write_b32 a0, 0\n"
"v_accvgpr_write_b32 a1, 0\n"
"v_accvgpr_write_b32 a2, 0\n"
"v_accvgpr_write_b32 a3, 0\n"
"v_accvgpr_write_b32 a4, 0\n"
"v_accvgpr_write_b32 a5, 0\n"
"v_accvgpr_write_b32 a6, 0\n"
"v_accvgpr_write_b32 a7, 0\n"
"v_accvgpr_write_b32 a8, 0\n"
"v_accvgpr_write_b32 a9, 0\n"
"v_accvgpr_write_b32 a10, 0\n"
"v_accvgpr_write_b32 a11, 0\n"
"v_accvgpr_write_b32 a12, 0\n"
"v_accvgpr_write_b32 a13, 0\n"
"v_accvgpr_write_b32 a14, 0\n"
"v_accvgpr_write_b32 a15, 0\n"
"v_accvgpr_write_b32 a16, 0\n"
"v_accvgpr_write_b32 a17, 0\n"
"v_accvgpr_write_b32 a18, 0\n"
"v_accvgpr_write_b32 a19, 0\n"
"v_accvgpr_write_b32 a20, 0\n"
"v_accvgpr_write_b32 a21, 0\n"
"v_accvgpr_write_b32 a22, 0\n"
"v_accvgpr_write_b32 a23, 0\n"
"v_accvgpr_write_b32 a24, 0\n"
"v_accvgpr_write_b32 a25, 0\n"
"v_accvgpr_write_b32 a26, 0\n"
"v_accvgpr_write_b32 a27, 0\n"
"v_accvgpr_write_b32 a28, 0\n"
"v_accvgpr_write_b32 a29, 0\n"
"v_accvgpr_write_b32 a30, 0\n"
"v_accvgpr_write_b32 a31, 0\n"
"v_accvgpr_write_b32 a32, 0\n"
"v_accvgpr_write_b32 a33, 0\n"
"v_accvgpr_write_b32 a34, 0\n"
"v_accvgpr_write_b32 a35, 0\n"
"v_accvgpr_write_b32 a36, 0\n"
"v_accvgpr_write_b32 a37, 0\n"
"v_accvgpr_write_b32 a38, 0\n"
"v_accvgpr_write_b32 a39, 0\n"
"v_accvgpr_write_b32 a40, 0\n"
"v_accvgpr_write_b32 a41, 0\n"
"v_accvgpr_write_b32 a42, 0\n"
"v_accvgpr_write_b32 a43, 0\n"
"v_accvgpr_write_b32 a44, 0\n"
"v_accvgpr_write_b32 a45, 0\n"
"v_accvgpr_write_b32 a46, 0\n"
"v_accvgpr_write_b32 a47, 0\n"
"v_accvgpr_write_b32 a48, 0\n"
"v_accvgpr_write_b32 a49, 0\n"
"v_accvgpr_write_b32 a50, 0\n"
"v_accvgpr_write_b32 a51, 0\n"
"v_accvgpr_write_b32 a52, 0\n"
"v_accvgpr_write_b32 a53, 0\n"
"v_accvgpr_write_b32 a54, 0\n"
"v_accvgpr_write_b32 a55, 0\n"
"v_accvgpr_write_b32 a56, 0\n"
"v_accvgpr_write_b32 a57, 0\n"
"v_accvgpr_write_b32 a58, 0\n"
"v_accvgpr_write_b32 a59, 0\n"
"v_accvgpr_write_b32 a60, 0\n"
"v_accvgpr_write_b32 a61, 0\n"
"v_accvgpr_write_b32 a62, 0\n"
"v_accvgpr_write_b32 a63, 0\n"
"v_accvgpr_write_b32 a64, 0\n"
"v_accvgpr_write_b32 a65, 0\n"
"v_accvgpr_write_b32 a66, 0\n"
"v_accvgpr_write_b32 a67, 0\n"
"v_accvgpr_write_b32 a68, 0\n"
"v_accvgpr_write_b32 a69, 0\n"
"v_accvgpr_write_b32 a70, 0\n"
"v_accvgpr_write_b32 a71, 0\n"
"v_accvgpr_write_b32 a72, 0\n"
"v_accvgpr_write_b32 a73, 0\n"
"v_accvgpr_write_b32 a74, 0\n"
"v_accvgpr_write_b32 a75, 0\n"
"v_accvgpr_write_b32 a76, 0\n"
"v_accvgpr_write_b32 a77, 0\n"
"v_accvgpr_write_b32 a78, 0\n"
"v_accvgpr_write_b32 a79, 0\n"
"v_lshrrev_b32 v12, 4, v0\n"
"v_bfe_u32 v13, v0, 2, 2\n"
"v_lshl_add_u32 v13, v12, 2, v13\n"
"v_mul_u32_u24 v13, 320, v13\n"
"v_and_b32 v14, 3, v0\n"
"v_lshl_add_u32 v13, v14, 3, v13\n"
"v_and_b32 v12, 1, v12\n"
"v_lshlrev_b32 v12, 5, v12\n"
"v_add_u32 v8, v13, v12\n"
"v_sub_u32 v9, v13, v12\n"
"v_add_u32 v10, 61440, v8\n"
"v_add_u32 v11, 61440, v9\n"
"s_add_u32 s48, %[olo], 50331648\n"
"s_addc_u32 s49, %[ohi], 0\n"
"s_mov_b32 s50, 7372800\n"
"s_mov_b32 s51, 0x20000\n"
"s_mov_b32 s44, %[ylo]\n"
"s_mov_b32 s45, %[yhi]\n"
"s_mov_b32 s46, 2457600\n"
"s_mov_b32 s47, 0x20000\n"
"s_lshl_b32 s60, s67, 7\n"
"s_add_u32 s60, s60, 41943040\n"
"s_add_u32 s70, %[olo], s60\n"
"s_addc_u32 s71, %[ohi], 0\n"
"s_waitcnt vmcnt(24)\n"
"s_barrier\n"
"ds_read_b64_tr_b16 a[80:81], v8 offset:0\n"
"ds_read_b64_tr_b16 a[82:83], v8 offset:5120\n"
"ds_read_b64_tr_b16 a[84:85], v9 offset:32\n"
"ds_read_b64_tr_b16 a[86:87], v9 offset:5152\n"
"ds_read_b64_tr_b16 a[88:89], v8 offset:64\n"
"ds_read_b64_tr_b16 a[90:91], v8 offset:5184\n"
"ds_read_b64_tr_b16 a[92:93], v9 offset:96\n"
"ds_read_b64_tr_b16 a[94:95], v9 offset:5216\n"
"ds_read_b64_tr_b16 a[96:97], v8 offset:128\n"
"ds_read_b64_tr_b16 a[98:99], v8 offset:5248\n"
"s_cmp_eq_u32 s56, 1\n"
"s_cbranch_scc1 K2_W1\n"
"s_cmp_eq_u32 s56, 2\n"
"s_cbranch_scc1 K2_W2\n"
"s_cmp_eq_u32 s56, 3\n"
"s_cbranch_scc1 K2_W3\n"
"s_mov_b32 s59, 4\n"
"K2_W0L:\n"
"s_waitcnt lgkmcnt(4)\n"
"v_mfma_f32_16x16x32_bf16 a[0:3], a[80:83], v[32:35], a[0:3]\n"
"buffer_load_dwordx4 v4, s[40:43], s58 offen offset:2048 lds\n"
"ds_read_b64_tr_b16 a[100:101], v9 offset:160\n"
"v_mfma_f32_16x16x32_bf16 a[4:7], a[80:83], v[36:39], a[4:7]\n"
"ds_read_b64_tr_b16 a[102:103], v9 offset:5280\n"
"v_mfma_f32_16x16x32_bf16 a[8:11], a[84:87], v[32:35], a[8:11]\n"
"ds_read_b64_tr_b16 a[104:105], v8 offset:192\n"
"ds_read_b64_tr_b16 a[106:107], v8 offset:5312\n"
"v_mfma_f32_16x16x32_bf16 a[12:15], a[84:87], v[36:39], a[12:15]\n"
"v_mfma_f32_16x16x32_bf16 a[16:19], a[88:91], v[32:35], a[16:19]\n"
"buffer_load_dwordx4 v5, s[40:43], s58 offen offset:3072 lds\n"
"ds_read_b64_tr_b16 a[108:109], v9 offset:224\n"
"v_mfma_f32_16x16x32_bf16 a[20:23], a[88:91], v[36:39], a[20:23]\n"
"ds_read_b64_tr_b16 a[110:111], v9 offset:5344\n"
"s_waitcnt lgkmcnt(4)\n"
"v_mfma_f32_16x16x32_bf16 a[24:27], a[92:95], v[32:35], a[24:27]\n"
"s_add_u32 m0, m0, 4096\n"
"ds_read_b64_tr_b16 a[80:81], v8 offset:256\n"
"v_mfma_f32_16x16x32_bf16 a[28:31], a[92:95], v[36:39], a[28:31]\n"
"ds_read_b64_tr_b16 a[82:83], v8 offset:5376\n"
"v_mfma_f32_16x16x32_bf16 a[32:35], a[96:99], v[32:35], a[32:35]\n"
"buffer_load_dwordx4 v6, s[40:43], s58 offen lds\n"
"ds_read_b64_tr_b16 a[84:85], v9 offset:288\n"
"v_mfma_f32_16x16x32_bf16 a[36:39], a[96:99], v[36:39], a[36:39]\n"
"ds_read_b64_tr_b16 a[86:87], v9 offset:5408\n"
"s_add_u32 s58, s58, 81920\n"
"v_mfma_f32_16x16x32_bf16 a[40:43], a[100:103], v[32:35], a[40:43]\n"
"ds_read_b64_tr_b16 a[88:89], v8 offset:10240\n"
"ds_read_b64_tr_b16 a[90:91], v8 offset:15360\n"
"v_mfma_f32_16x16x32_bf16 a[44:47], a[100:103], v[36:39], a[44:47]\n"
"s_waitcnt lgkmcnt(4)\n"
"v_mfma_f32_16x16x32_bf16 a[48:51], a[104:107], v[32:35], a[48:51]\n"
"buffer_load_dwordx4 v[88:91], v1, s[36:39], s57 offen\n"
"ds_read_b64_tr_b16 a[92:93], v9 offset:10272\n"
"v_mfma_f32_16x16x32_bf16 a[52:55], a[104:107], v[36:39], a[52:55]\n"
"ds_read_b64_tr_b16 a[94:95], v9 offset:15392\n"
"v_mfma_f32_16x16x32_bf16 a[56:59], a[108:111], v[32:35], a[56:59]\n"
"ds_read_b64_tr_b16 a[96:97], v8 offset:10304\n"
"ds_read_b64_tr_b16 a[98:99], v8 offset:15424\n"
"v_mfma_f32_16x16x32_bf16 a[60:63], a[108:111], v[36:39], a[60:63]\n"
"v_mfma_f32_16x16x32_bf16 a[64:67], a[80:83], v[32:35], a[64:67]\n"
"buffer_load_dwordx4 v[92:95], v1, s[36:39], s57 offen offset:1024\n"
"ds_read_b64_tr_b16 a[100:101], v9 offset:10336\n"
"v_mfma_f32_16x16x32_bf16 a[68:71], a[80:83], v[36:39], a[68:71]\n"
"ds_read_b64_tr_b16 a[102:103], v9 offset:15456\n"
"s_waitcnt lgkmcnt(8)\n"
"v_mfma_f32_16x16x32_bf16 a[72:75], a[84:87], v[32:35], a[72:75]\n"
"ds_read_b64_tr_b16 a[104:105], v8 offset:10368\n"
"ds_read_b64_tr_b16 a[106:107], v8 offset:15488\n"
"v_mfma_f32_16x16x32_bf16 a[76:79], a[84:87], v[36:39], a[76:79]\n"
"s_add_u32 s57, s57, 122880\n"
"s_waitcnt vmcnt(27) lgkmcnt(4)\n"
"v_mfma_f32_16x16x32_bf16 a[0:3], a[88:91], v[40:43], a[0:3]\n"
"buffer_load_dwordx4 v[96:99], v1, s[36:39], s57 offen\n"
"ds_read_b64_tr_b16 a[108:109], v9 offset:10400\n"
"v_mfma_f32_16x16x32_bf16 a[4:7], a[88:91], v[44:47], a[4:7]\n"
"ds_read_b64_tr_b16 a[110:111], v9 offset:15520\n"
"v_mfma_f32_16x16x32_bf16 a[8:11], a[92:95], v[40:43], a[8:11]\n"
"ds_read_b64_tr_b16 a[80:81], v8 offset:10432\n"
"ds_read_b64_tr_b16 a[82:83], v8 offset:15552\n"
"v_mfma_f32_16x16x32_bf16 a[12:15], a[92:95], v[44:47], a[12:15]\n"
"v_mfma_f32_16x16x32_bf16 a[16:19], a[96:99], v[40:43], a[16:19]\n"
"buffer_load_dwordx4 v[100:103], v1, s[36:39], s57 offen offset:1024\n"
"ds_read_b64_tr_b16 a[84:85], v9 offset:10464\n"
"v_mfma_f32_16x16x32_bf16 a[20:23], a[96:99], v[44:47], a[20:23]\n"
"ds_read_b64_tr_b16 a[86:87], v9 offset:15584\n"
"s_waitcnt lgkmcnt(4)\n"
"v_mfma_f32_16x16x32_bf16 a[24:27], a[100:103], v[40:43], a[24:27]\n"
"ds_read_b64_tr_b16 a[88:89], v8 offset:10496\n"
"ds_read_b64_tr_b16 a[90:91], v8 offset:15616\n"
"v_mfma_f32_16x16x32_bf16 a[28:31], a[100:103], v[44:47], a[28:31]\n"
"s_add_u32 s57, s57, 122880\n"
"v_mfma_f32_16x16x32_bf16 a[32:35], a[104:107], v[40:43], a[32:35]\n"
"ds_read_b64_tr_b16 a[92:93], v9 offset:10528\n"
"ds_read_b64_tr_b16 a[94:95], v9 offset:15648\n"
"v_mfma_f32_16x16x32_bf16 a[36:39], a[104:107], v[44:47], a[36:39]\n"
"s_waitcnt vmcnt(22) lgkmcnt(0)\n"
"s_barrier\n"
"v_mfma_f32_16x16x32_bf16 a[40:43], a[108:111], v[40:43], a[40:43]\n"
"s_add_u32 m0, s66, 81920\n"
"ds_read_b64_tr_b16 a[96:97], v8 offset:20480\n"
"v_mfma_f32_16x16x32_bf16 a[44:47], a[108:111], v[44:47], a[44:47]\n"
"ds_read_b64_tr_b16 a[98:99], v8 offset:25600\n"
"v_mfma_f32_16x16x32_bf16 a[48:51], a[80:83], v[40:43], a[48:51]\n"
"buffer_load_dwordx4 v2, s[40:43], s58 offen offset:0 lds\n"
"ds_read_b64_tr_b16 a[100:101], v9 offset:20512\n"
"v_mfma_f32_16x16x32_bf16 a[52:55], a[80:83], v[44:47], a[52:55]\n"
"ds_read_b64_tr_b16 a[102:103], v9 offset:25632\n"
"v_mfma_f32_16x16x32_bf16 a[56:59], a[84:87], v[40:43], a[56:59]\n"
"ds_read_b64_tr_b16 a[104:105], v8 offset:20544\n"
"ds_read_b64_tr_b16 a[106:107], v8 offset:25664\n"
"v_mfma_f32_16x16x32_bf16 a[60:63], a[84:87], v[44:47], a[60:63]\n"
"v_mfma_f32_16x16x32_bf16 a[64:67], a[88:91], v[40:43], a[64:67]\n"
"buffer_load_dwordx4 v3, s[40:43], s58 offen offset:1024 lds\n"
"ds_read_b64_tr_b16 a[108:109], v9 offset:20576\n"
"v_mfma_f32_16x16x32_bf16 a[68:71], a[88:91], v[44:47], a[68:71]\n"
"ds_read_b64_tr_b16 a[110:111], v9 offset:25696\n"
"v_mfma_f32_16x16x32_bf16 a[72:75], a[92:95], v[40:43], a[72:75]\n"
"ds_read_b64_tr_b16 a[80:81], v8 offset:20608\n"
"ds_read_b64_tr_b16 a[82:83], v8 offset:25728\n"
"v_mfma_f32_16x16x32_bf16 a[76:79], a[92:95], v[44:47], a[76:79]\n"
"s_waitcnt lgkmcnt(4)\n"
"v_mfma_f32_16x16x32_bf16 a[0:3], a[96:99], v[48:51], a[0:3]\n"
"buffer_load_dwordx4 v4, s[40:43], s58 offen offset:2048 lds\n"
"ds_read_b64_tr_b16 a[84:85], v9 offset:20640\n"
"v_mfma_f32_16x16x32_bf16 a[4:7], a[96:99], v[52:55], a[4:7]\n"
"ds_read_b64_tr_b16 a[86:87], v9 offset:25760\n"
"v_mfma_f32_16x16x32_bf16 a[8:11], a[100:103], v[48:51], a[8:11]\n"
"ds_read_b64_tr_b16 a[88:89], v8 offset:20672\n"
"ds_read_b64_tr_b16 a[90:91], v8 offset:25792\n"
"v_mfma_f32_16x16x32_bf16 a[12:15], a[100:103], v[52:55], a[12:15]\n"
"v_mfma_f32_16x16x32_bf16 a[16:19], a[104:107], v[48:51], a[16:19]\n"
"buffer_load_dwordx4 v5, s[40:43], s58 offen offset:3072 lds\n"
"ds_read_b64_tr_b16 a[92:93], v9 offset:20704\n"
"v_mfma_f32_16x16x32_bf16 a[20:23], a[104:107], v[52:55], a[20:23]\n"
"ds_read_b64_tr_b16 a[94:95], v9 offset:25824\n"
"s_waitcnt lgkmcnt(4)\n"
"v_mfma_f32_16x16x32_bf16 a[24:27], a[108:111], v[48:51], a[24:27]\n"
"s_add_u32 m0, m0, 4096\n"
"ds_read_b64_tr_b16 a[96:97], v8 offset:20736\n"
"v_mfma_f32_16x16x32_bf16 a[28:31], a[108:111], v[52:55], a[28:31]\n"
"ds_read_b64_tr_b16 a[98:99], v8 offset:25856\n"
"v_mfma_f32_16x16x32_bf16 a[32:35], a[80:83], v[48:51], a[32:35]\n"
"buffer_load_dwordx4 v6, s[40:43], s58 offen lds\n"
"ds_read_b64_tr_b16 a[100:101], v9 offset:20768\n"
"v_mfma_f32_16x16x32_bf16 a[36:39], a[80:83], v[52:55], a[36:39]\n"
"ds_read_b64_tr_b16 a[102:103], v9 offset:25888\n"
"s_add_u32 s58, s58, 81920\n"
"v_mfma_f32_16x16x32_bf16 a[40:43], a[84:87], v[48:51], a[40:43]\n"
"ds_read_b64_tr_b16 a[104:105], v8 offset:30720\n"
"ds_read_b64_tr_b16 a[106:107], v8 offset:35840\n"
"v_mfma_f32_16x16x32_bf16 a[44:47], a[84:87], v[52:55], a[44:47]\n"
"s_waitcnt lgkmcnt(4)\n"
"v_mfma_f32_16x16x32_bf16 a[48:51], a[88:91], v[48:51], a[48:51]\n"
"buffer_load_dwordx4 v[104:107], v1, s[36:39], s57 offen\n"
"ds_read_b64_tr_b16 a[108:109], v9 offset:30752\n"
"v_mfma_f32_16x16x32_bf16 a[52:55], a[88:91], v[52:55], a[52:55]\n"
"ds_read_b64_tr_b16 a[110:111], v9 offset:35872\n"
"v_mfma_f32_16x16x32_bf16 a[56:59], a[92:95], v[48:51], a[56:59]\n"
"ds_read_b64_tr_b16 a[80:81], v8 offset:30784\n"
"ds_read_b64_tr_b16 a[82:83], v8 offset:35904\n"
"v_mfma_f32_16x16x32_bf16 a[60:63], a[92:95], v[52:55], a[60:63]\n"
"v_mfma_f32_16x16x32_bf16 a[64:67], a[96:99], v[48:51], a[64:67]\n"
"buffer_load_dwordx4 v[108:111], v1, s[36:39], s57 offen offset:1024\n"
"ds_read_b64_tr_b16 a[84:85], v9 offset:30816\n"
"v_mfma_f32_16x16x32_bf16 a[68:71], a[96:99], v[52:55], a[68:71]\n"
"ds_read_b64_tr_b16 a[86:87], v9 offset:35936\n"
"s_waitcnt lgkmcnt(8)\n"
"v_mfma_f32_16x16x32_bf16 a[72:75], a[100:103], v[48:51], a[72:75]\n"
"ds_read_b64_tr_b16 a[88:89], v8 offset:30848\n"
"ds_read_b64_tr_b16 a[90:91], v8 offset:35968\n"
"v_mfma_f32_16x16x32_bf16 a[76:79], a[100:103], v[52:55], a[76:79]\n"
"s_add_u32 s57, s57, 122880\n"
"s_waitcnt vmcnt(27) lgkmcnt(4)\n"
"v_mfma_f32_16x16x32_bf16 a[0:3], a[104:107], v[56:59], a[0:3]\n"
"buffer_load_dwordx4 v[112:115], v1, s[36:39], s57 offen\n"
"ds_read_b64_tr_b16 a[92:93], v9 offset:30880\n"
"v_mfma_f32_16x16x32_bf16 a[4:7], a[104:107], v[60:63], a[4:7]\n"
"ds_read_b64_tr_b16 a[94:95], v9 offset:36000\n"
"v_mfma_f32_16x16x32_bf16 a[8:11], a[108:111], v[56:59], a[8:11]\n"
"ds_read_b64_tr_b16 a[96:97], v8 offset:30912\n"
"ds_read_b64_tr_b16 a[98:99], v8 offset:36032\n"
"v_mfma_f32_16x16x32_bf16 a[12:15], a[108:111], v[60:63], a[12:15]\n"
"v_mfma_f32_16x16x32_bf16 a[16:19], a[80:83], v[56:59], a[16:19]\n"
"buffer_load_dwordx4 v[116:119], v1, s[36:39], s57 offen offset:1024\n"
"ds_read_b64_tr_b16 a[100:101], v9 offset:30944\n"
"v_mfma_f32_16x16x32_bf16 a[20:23], a[80:83], v[60:63], a[20:23]\n"
"ds_read_b64_tr_b16 a[102:103], v9 offset:36064\n"
"s_waitcnt lgkmcnt(4)\n"
"v_mfma_f32_16x16x32_bf16 a[24:27], a[84:87], v[56:59], a[24:27]\n"
"ds_read_b64_tr_b16 a[104:105], v8 offset:30976\n"
"ds_read_b64_tr_b16 a[106:107], v8 offset:36096\n"
"v_mfma_f32_16x16x32_bf16 a[28:31], a[84:87], v[60:63], a[28:31]\n"
"s_add_u32 s57, s57, 122880\n"
"v_mfma_f32_16x16x32_bf16 a[32:35], a[88:91], v[56:59], a[32:35]\n"
"ds_read_b64_tr_b16 a[108:109], v9 offset:31008\n"
"ds_read_b64_tr_b16 a[110:111], v9 offset:36128\n"
"v_mfma_f32_16x16x32_bf16 a[36:39], a[88:91], v[60:63], a[36:39]\n"
"s_waitcnt vmcnt(22) lgkmcnt(0)\n"
"s_barrier\n"
"v_mfma_f32_16x16x32_bf16 a[40:43], a[92:95], v[56:59], a[40:43]\n"
"s_add_u32 m0, s66, 102400\n"
"ds_read_b64_tr_b16 a[80:81], v8 offset:40960\n"
"v_mfma_f32_16x16x32_bf16 a[44:47], a[92:95], v[60:63], a[44:47]\n"
"ds_read_b64_tr_b16 a[82:83], v8 offset:46080\n"
"v_mfma_f32_16x16x32_bf16 a[48:51], a[96:99], v[56:59], a[48:51]\n"
"buffer_load_dwordx4 v2, s[40:43], s58 offen offset:0 lds\n"
"ds_read_b64_tr_b16 a[84:85], v9 offset:40992\n"
"v_mfma_f32_16x16x32_bf16 a[52:55], a[96:99], v[60:63], a[52:55]\n"
"ds_read_b64_tr_b16 a[86:87], v9 offset:46112\n"
"v_mfma_f32_16x16x32_bf16 a[56:59], a[100:103], v[56:59], a[56:59]\n"
"ds_read_b64_tr_b16 a[88:89], v8 offset:41024\n"
"ds_read_b64_tr_b16 a[90:91], v8 offset:46144\n"
"v_mfma_f32_16x16x32_bf16 a[60:63], a[100:103], v[60:63], a[60:63]\n"
"v_mfma_f32_16x16x32_bf16 a[64:67], a[104:107], v[56:59], a[64:67]\n"
"buffer_load_dwordx4 v3, s[40:43], s58 offen offset:1024 lds\n"
"ds_read_b64_tr_b16 a[92:93], v9 offset:41056\n"
"v_mfma_f32_16x16x32_bf16 a[68:71], a[104:107], v[60:63], a[68:71]\n"
"ds_read_b64_tr_b16 a[94:95], v9 offset:46176\n"
"v_mfma_f32_16x16x32_bf16 a[72:75], a[108:111], v[56:59], a[72:75]\n"
"ds_read_b64_tr_b16 a[96:97], v8 offset:41088\n"
"ds_read_b64_tr_b16 a[98:99], v8 offset:46208\n"
"v_mfma_f32_16x16x32_bf16 a[76:79], a[108:111], v[60:63], a[76:79]\n"
"s_waitcnt lgkmcnt(4)\n"
"v_mfma_f32_16x16x32_bf16 a[0:3], a[80:83], v[64:67], a[0:3]\n"
"buffer_load_dwordx4 v4, s[40:43], s58 offen offset:2048 lds\n"
"ds_read_b64_tr_b16 a[100:101], v9 offset:41120\n"
"v_mfma_f32_16x16x32_bf16 a[4:7], a[80:83], v[68:71], a[4:7]\n"
"ds_read_b64_tr_b16 a[102:103], v9 offset:46240\n"
"v_mfma_f32_16x16x32_bf16 a[8:11], a[84:87], v[64:67], a[8:11]\n"
"ds_read_b64_tr_b16 a[104:105], v8 offset:41152\n"
"ds_read_b64_tr_b16 a[106:107], v8 offset:46272\n"
"v_mfma_f32_16x16x32_bf16 a[12:15], a[84:87], v[68:71], a[12:15]\n"
"v_mfma_f32_16x16x32_bf16 a[16:19], a[88:91], v[64:67], a[16:19]\n"
"buffer_load_dwordx4 v5, s[40:43], s58 offen offset:3072 lds\n"
"ds_read_b64_tr_b16 a[108:109], v9 offset:41184\n"
"v_mfma_f32_16x16x32_bf16 a[20:23], a[88:91], v[68:71], a[20:23]\n"
"ds_read_b64_tr_b16 a[110:111], v9 offset:46304\n"
"s_waitcnt lgkmcnt(4)\n"
"v_mfma_f32_16x16x32_bf16 a[24:27], a[92:95], v[64:67], a[24:27]\n"
"s_add_u32 m0, m0, 4096\n"
"ds_read_b64_tr_b16 a[80:81], v8 offset:41216\n"
"v_mfma_f32_16x16x32_bf16 a[28:31], a[92:95], v[68:71], a[28:31]\n"
"ds_read_b64_tr_b16 a[82:83], v8 offset:46336\n"
"v_mfma_f32_16x16x32_bf16 a[32:35], a[96:99], v[64:67], a[32:35]\n"
"buffer_load_dwordx4 v6, s[40:43], s58 offen lds\n"
"ds_read_b64_tr_b16 a[84:85], v9 offset:41248\n"
"v_mfma_f32_16x16x32_bf16 a[36:39], a[96:99], v[68:71], a[36:39]\n"
"ds_read_b64_tr_b16 a[86:87], v9 offset:46368\n"
"s_add_u32 s58, s58, 81920\n"
"v_mfma_f32_16x16x32_bf16 a[40:43], a[100:103], v[64:67], a[40:43]\n"
"ds_read_b64_tr_b16 a[88:89], v8 offset:51200\n"
"ds_read_b64_tr_b16 a[90:91], v8 offset:56320\n"
"v_mfma_f32_16x16x32_bf16 a[44:47], a[100:103], v[68:71], a[44:47]\n"
"s_waitcnt lgkmcnt(4)\n"
"v_mfma_f32_16x16x32_bf16 a[48:51], a[104:107], v[64:67], a[48:51]\n"
"buffer_load_dwordx4 v[120:123], v1, s[36:39], s57 offen\n"
"ds_read_b64_tr_b16 a[92:93], v9 offset:51232\n"
"v_mfma_f32_16x16x32_bf16 a[52:55], a[104:107], v[68:71], a[52:55]\n"
"ds_read_b64_tr_b16 a[94:95], v9 offset:56352\n"
"v_mfma_f32_16x16x32_bf16 a[56:59], a[108:111], v[64:67], a[56:59]\n"
"ds_read_b64_tr_b16 a[96:97], v8 offset:51264\n"
"ds_read_b64_tr_b16 a[98:99], v8 offset:56384\n"
"v_mfma_f32_16x16x32_bf16 a[60:63], a[108:111], v[68:71], a[60:63]\n"
"v_mfma_f32_16x16x32_bf16 a[64:67], a[80:83], v[64:67], a[64:67]\n"
"buffer_load_dwordx4 v[124:127], v1, s[36:39], s57 offen offset:1024\n"
"ds_read_b64_tr_b16 a[100:101], v9 offset:51296\n"
"v_mfma_f32_16x16x32_bf16 a[68:71], a[80:83], v[68:71], a[68:71]\n"
"ds_read_b64_tr_b16 a[102:103], v9 offset:56416\n"
"s_waitcnt lgkmcnt(8)\n"
"v_mfma_f32_16x16x32_bf16 a[72:75], a[84:87], v[64:67], a[72:75]\n"
"ds_read_b64_tr_b16 a[104:105], v8 offset:51328\n"
"ds_read_b64_tr_b16 a[106:107], v8 offset:56448\n"
"v_mfma_f32_16x16x32_bf16 a[76:79], a[84:87], v[68:71], a[76:79]\n"
"s_add_u32 s57, s57, 122880\n"
"s_waitcnt vmcnt(27) lgkmcnt(4)\n"
"v_mfma_f32_16x16x32_bf16 a[0:3], a[88:91], v[72:75], a[0:3]\n"
"buffer_load_dwordx4 v[32:35], v1, s[36:39], s57 offen\n"
"ds_read_b64_tr_b16 a[108:109], v9 offset:51360\n"
"v_mfma_f32_16x16x32_bf16 a[4:7], a[88:91], v[76:79], a[4:7]\n"
"ds_read_b64_tr_b16 a[110:111], v9 offset:56480\n"
"v_mfma_f32_16x16x32_bf16 a[8:11], a[92:95], v[72:75], a[8:11]\n"
"ds_read_b64_tr_b16 a[80:81], v8 offset:51392\n"
"ds_read_b64_tr_b16 a[82:83], v8 offset:56512\n"
"v_mfma_f32_16x16x32_bf16 a[12:15], a[92:95], v[76:79], a[12:15]\n"
"v_mfma_f32_16x16x32_bf16 a[16:19], a[96:99], v[72:75], a[16:19]\n"
"buffer_load_dwordx4 v[36:39], v1, s[36:39], s57 offen offset:1024\n"
"ds_read_b64_tr_b16 a[84:85], v9 offset:51424\n"
"v_mfma_f32_16x16x32_bf16 a[20:23], a[96:99], v[76:79], a[20:23]\n"
"ds_read_b64_tr_b16 a[86:87], v9 offset:56544\n"
"s_waitcnt lgkmcnt(4)\n"
"v_mfma_f32_16x16x32_bf16 a[24:27], a[100:103], v[72:75], a[24:27]\n"
"ds_read_b64_tr_b16 a[88:89], v8 offset:51456\n"
"ds_read_b64_tr_b16 a[90:91], v8 offset:56576\n"
"v_mfma_f32_16x16x32_bf16 a[28:31], a[100:103], v[76:79], a[28:31]\n"
"s_add_u32 s57, s57, 122880\n"
"v_mfma_f32_16x16x32_bf16 a[32:35], a[104:107], v[72:75], a[32:35]\n"
"ds_read_b64_tr_b16 a[92:93], v9 offset:51488\n"
"ds_read_b64_tr_b16 a[94:95], v9 offset:56608\n"
"v_mfma_f32_16x16x32_bf16 a[36:39], a[104:107], v[76:79], a[36:39]\n"
"s_waitcnt vmcnt(22) lgkmcnt(0)\n"
"s_barrier\n"
"v_mfma_f32_16x16x32_bf16 a[40:43], a[108:111], v[72:75], a[40:43]\n"
"s_add_u32 m0, s66, 0\n"
"ds_read_b64_tr_b16 a[96:97], v10 offset:0\n"
"v_mfma_f32_16x16x32_bf16 a[44:47], a[108:111], v[76:79], a[44:47]\n"
"ds_read_b64_tr_b16 a[98:99], v10 offset:5120\n"
"v_mfma_f32_16x16x32_bf16 a[48:51], a[80:83], v[72:75], a[48:51]\n"
"buffer_load_dwordx4 v2, s[40:43], s58 offen offset:0 lds\n"
"ds_read_b64_tr_b16 a[100:101], v11 offset:32\n"
"v_mfma_f32_16x16x32_bf16 a[52:55], a[80:83], v[76:79], a[52:55]\n"
"ds_read_b64_tr_b16 a[102:103], v11 offset:5152\n"
"v_mfma_f32_16x16x32_bf16 a[56:59], a[84:87], v[72:75], a[56:59]\n"
"ds_read_b64_tr_b16 a[104:105], v10 offset:64\n"
"ds_read_b64_tr_b16 a[106:107], v10 offset:5184\n"
"v_mfma_f32_16x16x32_bf16 a[60:63], a[84:87], v[76:79], a[60:63]\n"
"v_mfma_f32_16x16x32_bf16 a[64:67], a[88:91], v[72:75], a[64:67]\n"
"buffer_load_dwordx4 v3, s[40:43], s58 offen offset:1024 lds\n"
"ds_read_b64_tr_b16 a[108:109], v11 offset:96\n"
"v_mfma_f32_16x16x32_bf16 a[68:71], a[88:91], v[76:79], a[68:71]\n"
"ds_read_b64_tr_b16 a[110:111], v11 offset:5216\n"
"v_mfma_f32_16x16x32_bf16 a[72:75], a[92:95], v[72:75], a[72:75]\n"
"ds_read_b64_tr_b16 a[80:81], v10 offset:128\n"
"ds_read_b64_tr_b16 a[82:83], v10 offset:5248\n"
"v_mfma_f32_16x16x32_bf16 a[76:79], a[92:95], v[76:79], a[76:79]\n"
"s_waitcnt lgkmcnt(4)\n"
"v_mfma_f32_16x16x32_bf16 a[0:3], a[96:99], v[80:83], a[0:3]\n"
"buffer_load_dwordx4 v4, s[40:43], s58 offen offset:2048 lds\n"
"ds_read_b64_tr_b16 a[84:85], v11 offset:160\n"
"v_mfma_f32_16x16x32_bf16 a[4:7], a[96:99], v[84:87], a[4:7]\n"
"ds_read_b64_tr_b16 a[86:87], v11 offset:5280\n"
"v_mfma_f32_16x16x32_bf16 a[8:11], a[100:103], v[80:83], a[8:11]\n"
"ds_read_b64_tr_b16 a[88:89], v10 offset:192\n"
"ds_read_b64_tr_b16 a[90:91], v10 offset:5312\n"
"v_mfma_f32_16x16x32_bf16 a[12:15], a[100:103], v[84:87], a[12:15]\n"
"v_mfma_f32_16x16x32_bf16 a[16:19], a[104:107], v[80:83], a[16:19]\n"
"buffer_load_dwordx4 v5, s[40:43], s58 offen offset:3072 lds\n"
"ds_read_b64_tr_b16 a[92:93], v11 offset:224\n"
"v_mfma_f32_16x16x32_bf16 a[20:23], a[104:107], v[84:87], a[20:23]\n"
"ds_read_b64_tr_b16 a[94:95], v11 offset:5344\n"
"s_waitcnt lgkmcnt(4)\n"
"v_mfma_f32_16x16x32_bf16 a[24:27], a[108:111], v[80:83], a[24:27]\n"
"s_add_u32 m0, m0, 4096\n"
"ds_read_b64_tr_b16 a[96:97], v10 offset:256\n"
"v_mfma_f32_16x16x32_bf16 a[28:31], a[108:111], v[84:87], a[28:31]\n"
"ds_read_b64_tr_b16 a[98:99], v10 offset:5376\n"
"v_mfma_f32_16x16x32_bf16 a[32:35], a[80:83], v[80:83], a[32:35]\n"
"buffer_load_dwordx4 v6, s[40:43], s58 offen lds\n"
"ds_read_b64_tr_b16 a[100:101], v11 offset:288\n"
"v_mfma_f32_16x16x32_bf16 a[36:39], a[80:83], v[84:87], a[36:39]\n"
"ds_read_b64_tr_b16 a[102:103], v11 offset:5408\n"
"s_add_u32 s58, s58, 81920\n"
"v_mfma_f32_16x16x32_bf16 a[40:43], a[84:87], v[80:83], a[40:43]\n"
"ds_read_b64_tr_b16 a[104:105], v10 offset:10240\n"
"ds_read_b64_tr_b16 a[106:107], v10 offset:15360\n"
"v_mfma_f32_16x16x32_bf16 a[44:47], a[84:87], v[84:87], a[44:47]\n"
"s_waitcnt lgkmcnt(4)\n"
"v_mfma_f32_16x16x32_bf16 a[48:51], a[88:91], v[80:83], a[48:51]\n"
"buffer_load_dwordx4 v[40:43], v1, s[36:39], s57 offen\n"
"ds_read_b64_tr_b16 a[108:109], v11 offset:10272\n"
"v_mfma_f32_16x16x32_bf16 a[52:55], a[88:91], v[84:87], a[52:55]\n"
"ds_read_b64_tr_b16 a[110:111], v11 offset:15392\n"
"v_mfma_f32_16x16x32_bf16 a[56:59], a[92:95], v[80:83], a[56:59]\n"
"ds_read_b64_tr_b16 a[80:81], v10 offset:10304\n"
"ds_read_b64_tr_b16 a[82:83], v10 offset:15424\n"
"v_mfma_f32_16x16x32_bf16 a[60:63], a[92:95], v[84:87], a[60:63]\n"
"v_mfma_f32_16x16x32_bf16 a[64:67], a[96:99], v[80:83], a[64:67]\n"
"buffer_load_dwordx4 v[44:47], v1, s[36:39], s57 offen offset:1024\n"
"ds_read_b64_tr_b16 a[84:85], v11 offset:10336\n"
"v_mfma_f32_16x16x32_bf16 a[68:71], a[96:99], v[84:87], a[68:71]\n"
"ds_read_b64_tr_b16 a[86:87], v11 offset:15456\n"
"s_waitcnt lgkmcnt(8)\n"
"v_mfma_f32_16x16x32_bf16 a[72:75], a[100:103], v[80:83], a[72:75]\n"
"ds_read_b64_tr_b16 a[88:89], v10 offset:10368\n"
"ds_read_b64_tr_b16 a[90:91], v10 offset:15488\n"
"v_mfma_f32_16x16x32_bf16 a[76:79], a[100:103], v[84:87], a[76:79]\n"
"s_add_u32 s57, s57, 122880\n"
"s_waitcnt vmcnt(27) lgkmcnt(4)\n"
"v_mfma_f32_16x16x32_bf16 a[0:3], a[104:107], v[88:91], a[0:3]\n"
"buffer_load_dwordx4 v[48:51], v1, s[36:39], s57 offen\n"
"ds_read_b64_tr_b16 a[92:93], v11 offset:10400\n"
"v_mfma_f32_16x16x32_bf16 a[4:7], a[104:107], v[92:95], a[4:7]\n"
"ds_read_b64_tr_b16 a[94:95], v11 offset:15520\n"
"v_mfma_f32_16x16x32_bf16 a[8:11], a[108:111], v[88:91], a[8:11]\n"
"ds_read_b64_tr_b16 a[96:97], v10 offset:10432\n"
"ds_read_b64_tr_b16 a[98:99], v10 offset:15552\n"
"v_mfma_f32_16x16x32_bf16 a[12:15], a[108:111], v[92:95], a[12:15]\n"
"v_mfma_f32_16x16x32_bf16 a[16:19], a[80:83], v[88:91], a[16:19]\n"
"buffer_load_dwordx4 v[52:55], v1, s[36:39], s57 offen offset:1024\n"
"ds_read_b64_tr_b16 a[100:101], v11 offset:10464\n"
"v_mfma_f32_16x16x32_bf16 a[20:23], a[80:83], v[92:95], a[20:23]\n"
"ds_read_b64_tr_b16 a[102:103], v11 offset:15584\n"
"s_waitcnt lgkmcnt(4)\n"
"v_mfma_f32_16x16x32_bf16 a[24:27], a[84:87], v[88:91], a[24:27]\n"
"ds_read_b64_tr_b16 a[104:105], v10 offset:10496\n"
"ds_read_b64_tr_b16 a[106:107], v10 offset:15616\n"
"v_mfma_f32_16x16x32_bf16 a[28:31], a[84:87], v[92:95], a[28:31]\n"
"s_add_u32 s57, s57, 122880\n"
"v_mfma_f32_16x16x32_bf16 a[32:35], a[88:91], v[88:91], a[32:35]\n"
"ds_read_b64_tr_b16 a[108:109], v11 offset:10528\n"
"ds_read_b64_tr_b16 a[110:111], v11 offset:15648\n"
"v_mfma_f32_16x16x32_bf16 a[36:39], a[88:91], v[92:95], a[36:39]\n"
"s_waitcnt vmcnt(22) lgkmcnt(0)\n"
"s_barrier\n"
"v_mfma_f32_16x16x32_bf16 a[40:43], a[92:95], v[88:91], a[40:43]\n"
"s_add_u32 m0, s66, 20480\n"
"ds_read_b64_tr_b16 a[80:81], v10 offset:20480\n"
"v_mfma_f32_16x16x32_bf16 a[44:47], a[92:95], v[92:95], a[44:47]\n"
"ds_read_b64_tr_b16 a[82:83], v10 offset:25600\n"
"v_mfma_f32_16x16x32_bf16 a[48:51], a[96:99], v[88:91], a[48:51]\n"
"buffer_load_dwordx4 v2, s[40:43], s58 offen offset:0 lds\n"
"ds_read_b64_tr_b16 a[84:85], v11 offset:20512\n"
"v_mfma_f32_16x16x32_bf16 a[52:55], a[96:99], v[92:95], a[52:55]\n"
"ds_read_b64_tr_b16 a[86:87], v11 offset:25632\n"
"v_mfma_f32_16x16x32_bf16 a[56:59], a[100:103], v[88:91], a[56:59]\n"
"ds_read_b64_tr_b16 a[88:89], v10 offset:20544\n"
"ds_read_b64_tr_b16 a[90:91], v10 offset:25664\n"
"v_mfma_f32_16x16x32_bf16 a[60:63], a[100:103], v[92:95], a[60:63]\n"
"v_mfma_f32_16x16x32_bf16 a[64:67], a[104:107], v[88:91], a[64:67]\n"
"buffer_load_dwordx4 v3, s[40:43], s58 offen offset:1024 lds\n"
"ds_read_b64_tr_b16 a[92:93], v11 offset:20576\n"
"v_mfma_f32_16x16x32_bf16 a[68:71], a[104:107], v[92:95], a[68:71]\n"
"ds_read_b64_tr_b16 a[94:95], v11 offset:25696\n"
"v_mfma_f32_16x16x32_bf16 a[72:75], a[108:111], v[88:91], a[72:75]\n"
"ds_read_b64_tr_b16 a[96:97], v10 offset:20608\n"
"ds_read_b64_tr_b16 a[98:99], v10 offset:25728\n"
"v_mfma_f32_16x16x32_bf16 a[76:79], a[108:111], v[92:95], a[76:79]\n"
"s_waitcnt lgkmcnt(4)\n"
"v_mfma_f32_16x16x32_bf16 a[0:3], a[80:83], v[96:99], a[0:3]\n"
"buffer_load_dwordx4 v4, s[40:43], s58 offen offset:2048 lds\n"
"ds_read_b64_tr_b16 a[100:101], v11 offset:20640\n"
"v_mfma_f32_16x16x32_bf16 a[4:7], a[80:83], v[100:103], a[4:7]\n"
"ds_read_b64_tr_b16 a[102:103], v11 offset:25760\n"
"v_mfma_f32_16x16x32_bf16 a[8:11], a[84:87], v[96:99], a[8:11]\n"
"ds_read_b64_tr_b16 a[104:105], v10 offset:20672\n"
"ds_read_b64_tr_b16 a[106:107], v10 offset:25792\n"
"v_mfma_f32_16x16x32_bf16 a[12:15], a[84:87], v[100:103], a[12:15]\n"
"v_mfma_f32_16x16x32_bf16 a[16:19], a[88:91], v[96:99], a[16:19]\n"
"buffer_load_dwordx4 v5, s[40:43], s58 offen offset:3072 lds\n"
"ds_read_b64_tr_b16 a[108:109], v11 offset:20704\n"
"v_mfma_f32_16x16x32_bf16 a[20:23], a[88:91], v[100:103], a[20:23]\n"
"ds_read_b64_tr_b16 a[110:111], v11 offset:25824\n"
"s_waitcnt lgkmcnt(4)\n"
"v_mfma_f32_16x16x32_bf16 a[24:27], a[92:95], v[96:99], a[24:27]\n"
"s_add_u32 m0, m0, 4096\n"
"ds_read_b64_tr_b16 a[80:81], v10 offset:20736\n"
"v_mfma_f32_16x16x32_bf16 a[28:31], a[92:95], v[100:103], a[28:31]\n"
"ds_read_b64_tr_b16 a[82:83], v10 offset:25856\n"
"v_mfma_f32_16x16x32_bf16 a[32:35], a[96:99], v[96:99], a[32:35]\n"
"buffer_load_dwordx4 v6, s[40:43], s58 offen lds\n"
"ds_read_b64_tr_b16 a[84:85], v11 offset:20768\n"
"v_mfma_f32_16x16x32_bf16 a[36:39], a[96:99], v[100:103], a[36:39]\n"
"ds_read_b64_tr_b16 a[86:87], v11 offset:25888\n"
"s_add_u32 s58, s58, 81920\n"
"v_mfma_f32_16x16x32_bf16 a[40:43], a[100:103], v[96:99], a[40:43]\n"
"ds_read_b64_tr_b16 a[88:89], v10 offset:30720\n"
"ds_read_b64_tr_b16 a[90:91], v10 offset:35840\n"
"v_mfma_f32_16x16x32_bf16 a[44:47], a[100:103], v[100:103], a[44:47]\n"
"s_waitcnt lgkmcnt(4)\n"
"v_mfma_f32_16x16x32_bf16 a[48:51], a[104:107], v[96:99], a[48:51]\n"
"buffer_load_dwordx4 v[56:59], v1, s[36:39], s57 offen\n"
"ds_read_b64_tr_b16 a[92:93], v11 offset:30752\n"
"v_mfma_f32_16x16x32_bf16 a[52:55], a[104:107], v[100:103], a[52:55]\n"
"ds_read_b64_tr_b16 a[94:95], v11 offset:35872\n"
"v_mfma_f32_16x16x32_bf16 a[56:59], a[108:111], v[96:99], a[56:59]\n"
"ds_read_b64_tr_b16 a[96:97], v10 offset:30784\n"
"ds_read_b64_tr_b16 a[98:99], v10 offset:35904\n"
"v_mfma_f32_16x16x32_bf16 a[60:63], a[108:111], v[100:103], a[60:63]\n"
"v_mfma_f32_16x16x32_bf16 a[64:67], a[80:83], v[96:99], a[64:67]\n"
"buffer_load_dwordx4 v[60:63], v1, s[36:39], s57 offen offset:1024\n"
"ds_read_b64_tr_b16 a[100:101], v11 offset:30816\n"
"v_mfma_f32_16x16x32_bf16 a[68:71], a[80:83], v[100:103], a[68:71]\n"
"ds_read_b64_tr_b16 a[102:103], v11 offset:35936\n"
"s_waitcnt lgkmcnt(8)\n"
"v_mfma_f32_16x16x32_bf16 a[72:75], a[84:87], v[96:99], a[72:75]\n"
"ds_read_b64_tr_b16 a[104:105], v10 offset:30848\n"
"ds_read_b64_tr_b16 a[106:107], v10 offset:35968\n"
"v_mfma_f32_16x16x32_bf16 a[76:79], a[84:87], v[100:103], a[76:79]\n"
"s_add_u32 s57, s57, 122880\n"
"s_waitcnt vmcnt(27) lgkmcnt(4)\n"
"v_mfma_f32_16x16x32_bf16 a[0:3], a[88:91], v[104:107], a[0:3]\n"
"buffer_load_dwordx4 v[64:67], v1, s[36:39], s57 offen\n"
"ds_read_b64_tr_b16 a[108:109], v11 offset:30880\n"
"v_mfma_f32_16x16x32_bf16 a[4:7], a[88:91], v[108:111], a[4:7]\n"
"ds_read_b64_tr_b16 a[110:111], v11 offset:36000\n"
"v_mfma_f32_16x16x32_bf16 a[8:11], a[92:95], v[104:107], a[8:11]\n"
"ds_read_b64_tr_b16 a[80:81], v10 offset:30912\n"
"ds_read_b64_tr_b16 a[82:83], v10 offset:36032\n"
"v_mfma_f32_16x16x32_bf16 a[12:15], a[92:95], v[108:111], a[12:15]\n"
"v_mfma_f32_16x16x32_bf16 a[16:19], a[96:99], v[104:107], a[16:19]\n"
"buffer_load_dwordx4 v[68:71], v1, s[36:39], s57 offen offset:1024\n"
"ds_read_b64_tr_b16 a[84:85], v11 offset:30944\n"
"v_mfma_f32_16x16x32_bf16 a[20:23], a[96:99], v[108:111], a[20:23]\n"
"ds_read_b64_tr_b16 a[86:87], v11 offset:36064\n"
"s_waitcnt lgkmcnt(4)\n"
"v_mfma_f32_16x16x32_bf16 a[24:27], a[100:103], v[104:107], a[24:27]\n"
"ds_read_b64_tr_b16 a[88:89], v10 offset:30976\n"
"ds_read_b64_tr_b16 a[90:91], v10 offset:36096\n"
"v_mfma_f32_16x16x32_bf16 a[28:31], a[100:103], v[108:111], a[28:31]\n"
"s_add_u32 s57, s57, 122880\n"
"v_mfma_f32_16x16x32_bf16 a[32:35], a[104:107], v[104:107], a[32:35]\n"
"ds_read_b64_tr_b16 a[92:93], v11 offset:31008\n"
"ds_read_b64_tr_b16 a[94:95], v11 offset:36128\n"
"v_mfma_f32_16x16x32_bf16 a[36:39], a[104:107], v[108:111], a[36:39]\n"
"s_waitcnt vmcnt(22) lgkmcnt(0)\n"
"s_barrier\n"
"v_mfma_f32_16x16x32_bf16 a[40:43], a[108:111], v[104:107], a[40:43]\n"
"s_add_u32 m0, s66, 40960\n"
"ds_read_b64_tr_b16 a[96:97], v10 offset:40960\n"
"v_mfma_f32_16x16x32_bf16 a[44:47], a[108:111], v[108:111], a[44:47]\n"
"ds_read_b64_tr_b16 a[98:99], v10 offset:46080\n"
"v_mfma_f32_16x16x32_bf16 a[48:51], a[80:83], v[104:107], a[48:51]\n"
"buffer_load_dwordx4 v2, s[40:43], s58 offen offset:0 lds\n"
"ds_read_b64_tr_b16 a[100:101], v11 offset:40992\n"
"v_mfma_f32_16x16x32_bf16 a[52:55], a[80:83], v[108:111], a[52:55]\n"
"ds_read_b64_tr_b16 a[102:103], v11 offset:46112\n"
"v_mfma_f32_16x16x32_bf16 a[56:59], a[84:87], v[104:107], a[56:59]\n"
"ds_read_b64_tr_b16 a[104:105], v10 offset:41024\n"
"ds_read_b64_tr_b16 a[106:107], v10 offset:46144\n"
"v_mfma_f32_16x16x32_bf16 a[60:63], a[84:87], v[108:111], a[60:63]\n"
"v_mfma_f32_16x16x32_bf16 a[64:67], a[88:91], v[104:107], a[64:67]\n"
"buffer_load_dwordx4 v3, s[40:43], s58 offen offset:1024 lds\n"
"ds_read_b64_tr_b16 a[108:109], v11 offset:41056\n"
"v_mfma_f32_16x16x32_bf16 a[68:71], a[88:91], v[108:111], a[68:71]\n"
"ds_read_b64_tr_b16 a[110:111], v11 offset:46176\n"
"v_mfma_f32_16x16x32_bf16 a[72:75], a[92:95], v[104:107], a[72:75]\n"
"ds_read_b64_tr_b16 a[80:81], v10 offset:41088\n"
"ds_read_b64_tr_b16 a[82:83], v10 offset:46208\n"
"v_mfma_f32_16x16x32_bf16 a[76:79], a[92:95], v[108:111], a[76:79]\n"
"s_waitcnt lgkmcnt(4)\n"
"v_mfma_f32_16x16x32_bf16 a[0:3], a[96:99], v[112:115], a[0:3]\n"
"buffer_load_dwordx4 v4, s[40:43], s58 offen offset:2048 lds\n"
"ds_read_b64_tr_b16 a[84:85], v11 offset:41120\n"
"v_mfma_f32_16x16x32_bf16 a[4:7], a[96:99], v[116:119], a[4:7]\n"
"ds_read_b64_tr_b16 a[86:87], v11 offset:46240\n"
"v_mfma_f32_16x16x32_bf16 a[8:11], a[100:103], v[112:115], a[8:11]\n"
"ds_read_b64_tr_b16 a[88:89], v10 offset:41152\n"
"ds_read_b64_tr_b16 a[90:91], v10 offset:46272\n"
"v_mfma_f32_16x16x32_bf16 a[12:15], a[100:103], v[116:119], a[12:15]\n"
"v_mfma_f32_16x16x32_bf16 a[16:19], a[104:107], v[112:115], a[16:19]\n"
"buffer_load_dwordx4 v5, s[40:43], s58 offen offset:3072 lds\n"
"ds_read_b64_tr_b16 a[92:93], v11 offset:41184\n"
"v_mfma_f32_16x16x32_bf16 a[20:23], a[104:107], v[116:119], a[20:23]\n"
"ds_read_b64_tr_b16 a[94:95], v11 offset:46304\n"
"s_waitcnt lgkmcnt(4)\n"
"v_mfma_f32_16x16x32_bf16 a[24:27], a[108:111], v[112:115], a[24:27]\n"
"s_add_u32 m0, m0, 4096\n"
"ds_read_b64_tr_b16 a[96:97], v10 offset:41216\n"
"v_mfma_f32_16x16x32_bf16 a[28:31], a[108:111], v[116:119], a[28:31]\n"
"ds_read_b64_tr_b16 a[98:99], v10 offset:46336\n"
"v_mfma_f32_16x16x32_bf16 a[32:35], a[80:83], v[112:115], a[32:35]\n"
"buffer_load_dwordx4 v6, s[40:43], s58 offen lds\n"
"ds_read_b64_tr_b16 a[100:101], v11 offset:41248\n"
"v_mfma_f32_16x16x32_bf16 a[36:39], a[80:83], v[116:119], a[36:39]\n"
"ds_read_b64_tr_b16 a[102:103], v11 offset:46368\n"
"s_add_u32 s58, s58, 81920\n"
"v_mfma_f32_16x16x32_bf16 a[40:43], a[84:87], v[112:115], a[40:43]\n"
"ds_read_b64_tr_b16 a[104:105], v10 offset:51200\n"
"ds_read_b64_tr_b16 a[106:107], v10 offset:56320\n"
"v_mfma_f32_16x16x32_bf16 a[44:47], a[84:87], v[116:119], a[44:47]\n"
"s_waitcnt lgkmcnt(4)\n"
"v_mfma_f32_16x16x32_bf16 a[48:51], a[88:91], v[112:115], a[48:51]\n"
"buffer_load_dwordx4 v[72:75], v1, s[36:39], s57 offen\n"
"ds_read_b64_tr_b16 a[108:109], v11 offset:51232\n"
"v_mfma_f32_16x16x32_bf16 a[52:55], a[88:91], v[116:119], a[52:55]\n"
"ds_read_b64_tr_b16 a[110:111], v11 offset:56352\n"
"v_mfma_f32_16x16x32_bf16 a[56:59], a[92:95], v[112:115], a[56:59]\n"
"ds_read_b64_tr_b16 a[80:81], v10 offset:51264\n"
"ds_read_b64_tr_b16 a[82:83], v10 offset:56384\n"
"v_mfma_f32_16x16x32_bf16 a[60:63], a[92:95], v[116:119], a[60:63]\n"
"v_mfma_f32_16x16x32_bf16 a[64:67], a[96:99], v[112:115], a[64:67]\n"
"buffer_load_dwordx4 v[76:79], v1, s[36:39], s57 offen offset:1024\n"
"ds_read_b64_tr_b16 a[84:85], v11 offset:51296\n"
"v_mfma_f32_16x16x32_bf16 a[68:71], a[96:99], v[116:119], a[68:71]\n"
"ds_read_b64_tr_b16 a[86:87], v11 offset:56416\n"
"s_waitcnt lgkmcnt(8)\n"
"v_mfma_f32_16x16x32_bf16 a[72:75], a[100:103], v[112:115], a[72:75]\n"
"ds_read_b64_tr_b16 a[88:89], v10 offset:51328\n"
"ds_read_b64_tr_b16 a[90:91], v10 offset:56448\n"
"v_mfma_f32_16x16x32_bf16 a[76:79], a[100:103], v[116:119], a[76:79]\n"
"s_add_u32 s57, s57, 122880\n"
"s_waitcnt vmcnt(27) lgkmcnt(4)\n"
"v_mfma_f32_16x16x32_bf16 a[0:3], a[104:107], v[120:123], a[0:3]\n"
"buffer_load_dwordx4 v[80:83], v1, s[36:39], s57 offen\n"
"ds_read_b64_tr_b16 a[92:93], v11 offset:51360\n"
"v_mfma_f32_16x16x32_bf16 a[4:7], a[104:107], v[124:127], a[4:7]\n"
"ds_read_b64_tr_b16 a[94:95], v11 offset:56480\n"
"v_mfma_f32_16x16x32_bf16 a[8:11], a[108:111], v[120:123], a[8:11]\n"
"ds_read_b64_tr_b16 a[96:97], v10 offset:51392\n"
"ds_read_b64_tr_b16 a[98:99], v10 offset:56512\n"
"v_mfma_f32_16x16x32_bf16 a[12:15], a[108:111], v[124:127], a[12:15]\n"
"v_mfma_f32_16x16x32_bf16 a[16:19], a[80:83], v[120:123], a[16:19]\n"
"buffer_load_dwordx4 v[84:87], v1, s[36:39], s57 offen offset:1024\n"
"ds_read_b64_tr_b16 a[100:101], v11 offset:51424\n"
"v_mfma_f32_16x16x32_bf16 a[20:23], a[80:83], v[124:127], a[20:23]\n"
"ds_read_b64_tr_b16 a[102:103], v11 offset:56544\n"
"s_waitcnt lgkmcnt(4)\n"
"v_mfma_f32_16x16x32_bf16 a[24:27], a[84:87], v[120:123], a[24:27]\n"
"ds_read_b64_tr_b16 a[104:105], v10 offset:51456\n"
"ds_read_b64_tr_b16 a[106:107], v10 offset:56576\n"
"v_mfma_f32_16x16x32_bf16 a[28:31], a[84:87], v[124:127], a[28:31]\n"
"s_add_u32 s57, s57, 122880\n"
"v_mfma_f32_16x16x32_bf16 a[32:35], a[88:91], v[120:123], a[32:35]\n"
"ds_read_b64_tr_b16 a[108:109], v11 offset:51488\n"
"ds_read_b64_tr_b16 a[110:111], v11 offset:56608\n"
"v_mfma_f32_16x16x32_bf16 a[36:39], a[88:91], v[124:127], a[36:39]\n"
"s_waitcnt vmcnt(22) lgkmcnt(0)\n"
"s_barrier\n"
"v_mfma_f32_16x16x32_bf16 a[40:43], a[92:95], v[120:123], a[40:43]\n"
"s_add_u32 m0, s66, 61440\n"
"ds_read_b64_tr_b16 a[80:81], v8 offset:0\n"
"v_mfma_f32_16x16x32_bf16 a[44:47], a[92:95], v[124:127], a[44:47]\n"
"ds_read_b64_tr_b16 a[82:83], v8 offset:5120\n"
"v_mfma_f32_16x16x32_bf16 a[48:51], a[96:99], v[120:123], a[48:51]\n"
"buffer_load_dwordx4 v2, s[40:43], s58 offen offset:0 lds\n"
"ds_read_b64_tr_b16 a[84:85], v9 offset:32\n"
"v_mfma_f32_16x16x32_bf16 a[52:55], a[96:99], v[124:127], a[52:55]\n"
"ds_read_b64_tr_b16 a[86:87], v9 offset:5152\n"
"v_mfma_f32_16x16x32_bf16 a[56:59], a[100:103], v[120:123], a[56:59]\n"
"ds_read_b64_tr_b16 a[88:89], v8 offset:64\n"
"ds_read_b64_tr_b16 a[90:91], v8 offset:5184\n"
"v_mfma_f32_16x16x32_bf16 a[60:63], a[100:103], v[124:127], a[60:63]\n"
"v_mfma_f32_16x16x32_bf16 a[64:67], a[104:107], v[120:123], a[64:67]\n"
"buffer_load_dwordx4 v3, s[40:43], s58 offen offset:1024 lds\n"
"ds_read_b64_tr_b16 a[92:93], v9 offset:96\n"
"v_mfma_f32_16x16x32_bf16 a[68:71], a[104:107], v[124:127], a[68:71]\n"
"ds_read_b64_tr_b16 a[94:95], v9 offset:5216\n"
"v_mfma_f32_16x16x32_bf16 a[72:75], a[108:111], v[120:123], a[72:75]\n"
"ds_read_b64_tr_b16 a[96:97], v8 offset:128\n"
"ds_read_b64_tr_b16 a[98:99], v8 offset:5248\n"
"v_mfma_f32_16x16x32_bf16 a[76:79], a[108:111], v[124:127], a[76:79]\n"
"s_sub_u32 s59, s59, 1\n"
"s_cmp_lg_u32 s59, 0\n"
"s_cbranch_scc1 K2_W0L\n"
"s_branch K2_DONE\n"
"K2_W1:\n"
"s_mov_b32 s59, 4\n"
"K2_W1L:\n"
"s_waitcnt lgkmcnt(4)\n"
"v_mfma_f32_16x16x32_bf16 a[0:3], a[80:83], v[32:35], a[0:3]\n"
"ds_read_b64_tr_b16 a[100:101], v9 offset:160\n"
"ds_read_b64_tr_b16 a[102:103], v9 offset:5280\n"
"v_mfma_f32_16x16x32_bf16 a[4:7], a[80:83], v[36:39], a[4:7]\n"
"buffer_load_dwordx4 v4, s[40:43], s58 offen offset:2048 lds\n"
"v_mfma_f32_16x16x32_bf16 a[8:11], a[84:87], v[32:35], a[8:11]\n"
"ds_read_b64_tr_b16 a[104:105], v8 offset:192\n"
"ds_read_b64_tr_b16 a[106:107], v8 offset:5312\n"
"v_mfma_f32_16x16x32_bf16 a[12:15], a[84:87], v[36:39], a[12:15]\n"
"v_mfma_f32_16x16x32_bf16 a[16:19], a[88:91], v[32:35], a[16:19]\n"
"ds_read_b64_tr_b16 a[108:109], v9 offset:224\n"
"ds_read_b64_tr_b16 a[110:111], v9 offset:5344\n"
"v_mfma_f32_16x16x32_bf16 a[20:23], a[88:91], v[36:39], a[20:23]\n"
"buffer_load_dwordx4 v5, s[40:43], s58 offen offset:3072 lds\n"
"s_waitcnt lgkmcnt(4)\n"
"v_mfma_f32_16x16x32_bf16 a[24:27], a[92:95], v[32:35], a[24:27]\n"
"ds_read_b64_tr_b16 a[80:81], v8 offset:256\n"
"ds_read_b64_tr_b16 a[82:83], v8 offset:5376\n"
"v_mfma_f32_16x16x32_bf16 a[28:31], a[92:95], v[36:39], a[28:31]\n"
"s_add_u32 m0, m0, 4096\n"
"v_mfma_f32_16x16x32_bf16 a[32:35], a[96:99], v[32:35], a[32:35]\n"
"ds_read_b64_tr_b16 a[84:85], v9 offset:288\n"
"ds_read_b64_tr_b16 a[86:87], v9 offset:5408\n"
"v_mfma_f32_16x16x32_bf16 a[36:39], a[96:99], v[36:39], a[36:39]\n"
"buffer_load_dwordx4 v6, s[40:43], s58 offen lds\n"
"s_add_u32 s58, s58, 81920\n"
"v_mfma_f32_16x16x32_bf16 a[40:43], a[100:103], v[32:35], a[40:43]\n"
"ds_read_b64_tr_b16 a[88:89], v8 offset:10240\n"
"ds_read_b64_tr_b16 a[90:91], v8 offset:15360\n"
"v_mfma_f32_16x16x32_bf16 a[44:47], a[100:103], v[36:39], a[44:47]\n"
"s_waitcnt lgkmcnt(4)\n"
"v_mfma_f32_16x16x32_bf16 a[48:51], a[104:107], v[32:35], a[48:51]\n"
"ds_read_b64_tr_b16 a[92:93], v9 offset:10272\n"
"ds_read_b64_tr_b16 a[94:95], v9 offset:15392\n"
"v_mfma_f32_16x16x32_bf16 a[52:55], a[104:107], v[36:39], a[52:55]\n"
"buffer_load_dwordx4 v[88:91], v1, s[36:39], s57 offen\n"
"v_mfma_f32_16x16x32_bf16 a[56:59], a[108:111], v[32:35], a[56:59]\n"
"ds_read_b64_tr_b16 a[96:97], v8 offset:10304\n"
"ds_read_b64_tr_b16 a[98:99], v8 offset:15424\n"
"v_mfma_f32_16x16x32_bf16 a[60:63], a[108:111], v[36:39], a[60:63]\n"
"v_mfma_f32_16x16x32_bf16 a[64:67], a[80:83], v[32:35], a[64:67]\n"
"ds_read_b64_tr_b16 a[100:101], v9 offset:10336\n"
"ds_read_b64_tr_b16 a[102:103], v9 offset:15456\n"
"v_mfma_f32_16x16x32_bf16 a[68:71], a[80:83], v[36:39], a[68:71]\n"
"buffer_load_dwordx4 v[92:95], v1, s[36:39], s57 offen offset:1024\n"
"s_waitcnt lgkmcnt(8)\n"
"v_mfma_f32_16x16x32_bf16 a[72:75], a[84:87], v[32:35], a[72:75]\n"
"ds_read_b64_tr_b16 a[104:105], v8 offset:10368\n"
"ds_read_b64_tr_b16 a[106:107], v8 offset:15488\n"
"v_mfma_f32_16x16x32_bf16 a[76:79], a[84:87], v[36:39], a[76:79]\n"
"s_add_u32 s57, s57, 122880\n"
"s_waitcnt vmcnt(27) lgkmcnt(4)\n"
"v_mfma_f32_16x16x32_bf16 a[0:3], a[88:91], v[40:43], a[0:3]\n"
"ds_read_b64_tr_b16 a[108:109], v9 offset:10400\n"
"ds_read_b64_tr_b16 a[110:111], v9 offset:15520\n"
"v_mfma_f32_16x16x32_bf16 a[4:7], a[88:91], v[44:47], a[4:7]\n"
"buffer_load_dwordx4 v[96:99], v1, s[36:39], s57 offen\n"
"v_mfma_f32_16x16x32_bf16 a[8:11], a[92:95], v[40:43], a[8:11]\n"
"ds_read_b64_tr_b16 a[80:81], v8 offset:10432\n"
"ds_read_b64_tr_b16 a[82:83], v8 offset:15552\n"
"v_mfma_f32_16x16x32_bf16 a[12:15], a[92:95], v[44:47], a[12:15]\n"
"v_mfma_f32_16x16x32_bf16 a[16:19], a[96:99], v[40:43], a[16:19]\n"
"ds_read_b64_tr_b16 a[84:85], v9 offset:10464\n"
"ds_read_b64_tr_b16 a[86:87], v9 offset:15584\n"
"v_mfma_f32_16x16x32_bf16 a[20:23], a[96:99], v[44:47], a[20:23]\n"
"buffer_load_dwordx4 v[100:103], v1, s[36:39], s57 offen offset:1024\n"
"s_waitcnt lgkmcnt(4)\n"
"v_mfma_f32_16x16x32_bf16 a[24:27], a[100:103], v[40:43], a[24:27]\n"
"ds_read_b64_tr_b16 a[88:89], v8 offset:10496\n"
"ds_read_b64_tr_b16 a[90:91], v8 offset:15616\n"
"v_mfma_f32_16x16x32_bf16 a[28:31], a[100:103], v[44:47], a[28:31]\n"
"s_add_u32 s57, s57, 122880\n"
"v_mfma_f32_16x16x32_bf16 a[32:35], a[104:107], v[40:43], a[32:35]\n"
"ds_read_b64_tr_b16 a[92:93], v9 offset:10528\n"
"ds_read_b64_tr_b16 a[94:95], v9 offset:15648\n"
"v_mfma_f32_16x16x32_bf16 a[36:39], a[104:107], v[44:47], a[36:39]\n"
"s_waitcnt vmcnt(22) lgkmcnt(0)\n"
"s_barrier\n"
"v_mfma_f32_16x16x32_bf16 a[40:43], a[108:111], v[40:43], a[40:43]\n"
"ds_read_b64_tr_b16 a[96:97], v8 offset:20480\n"
"ds_read_b64_tr_b16 a[98:99], v8 offset:25600\n"
"v_mfma_f32_16x16x32_bf16 a[44:47], a[108:111], v[44:47], a[44:47]\n"
"s_add_u32 m0, s66, 81920\n"
"v_mfma_f32_16x16x32_bf16 a[48:51], a[80:83], v[40:43], a[48:51]\n"
"ds_read_b64_tr_b16 a[100:101], v9 offset:20512\n"
"ds_read_b64_tr_b16 a[102:103], v9 offset:25632\n"
"v_mfma_f32_16x16x32_bf16 a[52:55], a[80:83], v[44:47], a[52:55]\n"
"buffer_load_dwordx4 v2, s[40:43], s58 offen offset:0 lds\n"
"v_mfma_f32_16x16x32_bf16 a[56:59], a[84:87], v[40:43], a[56:59]\n"
"ds_read_b64_tr_b16 a[104:105], v8 offset:20544\n"
"ds_read_b64_tr_b16 a[106:107], v8 offset:25664\n"
"v_mfma_f32_16x16x32_bf16 a[60:63], a[84:87], v[44:47], a[60:63]\n"
"v_mfma_f32_16x16x32_bf16 a[64:67], a[88:91], v[40:43], a[64:67]\n"
"ds_read_b64_tr_b16 a[108:109], v9 offset:20576\n"
"ds_read_b64_tr_b16 a[110:111], v9 offset:25696\n"
"v_mfma_f32_16x16x32_bf16 a[68:71], a[88:91], v[44:47], a[68:71]\n"
"buffer_load_dwordx4 v3, s[40:43], s58 offen offset:1024 lds\n"
"v_mfma_f32_16x16x32_bf16 a[72:75], a[92:95], v[40:43], a[72:75]\n"
"ds_read_b64_tr_b16 a[80:81], v8 offset:20608\n"
"ds_read_b64_tr_b16 a[82:83], v8 offset:25728\n"
"v_mfma_f32_16x16x32_bf16 a[76:79], a[92:95], v[44:47], a[76:79]\n"
"s_waitcnt lgkmcnt(4)\n"
"v_mfma_f32_16x16x32_bf16 a[0:3], a[96:99], v[48:51], a[0:3]\n"
"ds_read_b64_tr_b16 a[84:85], v9 offset:20640\n"
"ds_read_b64_tr_b16 a[86:87], v9 offset:25760\n"
"v_mfma_f32_16x16x32_bf16 a[4:7], a[96:99], v[52:55], a[4:7]\n"
"buffer_load_dwordx4 v4, s[40:43], s58 offen offset:2048 lds\n"
"v_mfma_f32_16x16x32_bf16 a[8:11], a[100:103], v[48:51], a[8:11]\n"
"ds_read_b64_tr_b16 a[88:89], v8 offset:20672\n"
"ds_read_b64_tr_b16 a[90:91], v8 offset:25792\n"
"v_mfma_f32_16x16x32_bf16 a[12:15], a[100:103], v[52:55], a[12:15]\n"
"v_mfma_f32_16x16x32_bf16 a[16:19], a[104:107], v[48:51], a[16:19]\n"
"ds_read_b64_tr_b16 a[92:93], v9 offset:20704\n"
"ds_read_b64_tr_b16 a[94:95], v9 offset:25824\n"
"v_mfma_f32_16x16x32_bf16 a[20:23], a[104:107], v[52:55], a[20:23]\n"
"buffer_load_dwordx4 v5, s[40:43], s58 offen offset:3072 lds\n"
"s_waitcnt lgkmcnt(4)\n"
"v_mfma_f32_16x16x32_bf16 a[24:27], a[108:111], v[48:51], a[24:27]\n"
"ds_read_b64_tr_b16 a[96:97], v8 offset:20736\n"
"ds_read_b64_tr_b16 a[98:99], v8 offset:25856\n"
"v_mfma_f32_16x16x32_bf16 a[28:31], a[108:111], v[52:55], a[28:31]\n"
"s_add_u32 m0, m0, 4096\n"
"v_mfma_f32_16x16x32_bf16 a[32:35], a[80:83], v[48:51], a[32:35]\n"
"ds_read_b64_tr_b16 a[100:101], v9 offset:20768\n"
"ds_read_b64_tr_b16 a[102:103], v9 offset:25888\n"
"v_mfma_f32_16x16x32_bf16 a[36:39], a[80:83], v[52:55], a[36:39]\n"
"buffer_load_dwordx4 v6, s[40:43], s58 offen lds\n"
"s_add_u32 s58, s58, 81920\n"
"v_mfma_f32_16x16x32_bf16 a[40:43], a[84:87], v[48:51], a[40:43]\n"
"ds_read_b64_tr_b16 a[104:105], v8 offset:30720\n"
"ds_read_b64_tr_b16 a[106:107], v8 offset:35840\n"
"v_mfma_f32_16x16x32_bf16 a[44:47], a[84:87], v[52:55], a[44:47]\n"
"s_waitcnt lgkmcnt(4)\n"
"v_mfma_f32_16x16x32_bf16 a[48:51], a[88:91], v[48:51], a[48:51]\n"
"ds_read_b64_tr_b16 a[108:109], v9 offset:30752\n"
"ds_read_b64_tr_b16 a[110:111], v9 offset:35872\n"
"v_mfma_f32_16x16x32_bf16 a[52:55], a[88:91], v[52:55], a[52:55]\n"
"buffer_load_dwordx4 v[104:107], v1, s[36:39], s57 offen\n"
"v_mfma_f32_16x16x32_bf16 a[56:59], a[92:95], v[48:51], a[56:59]\n"
"ds_read_b64_tr_b16 a[80:81], v8 offset:30784\n"
"ds_read_b64_tr_b16 a[82:83], v8 offset:35904\n"
"v_mfma_f32_16x16x32_bf16 a[60:63], a[92:95], v[52:55], a[60:63]\n"
"v_mfma_f32_16x16x32_bf16 a[64:67], a[96:99], v[48:51], a[64:67]\n"
"ds_read_b64_tr_b16 a[84:85], v9 offset:30816\n"
"ds_read_b64_tr_b16 a[86:87], v9 offset:35936\n"
"v_mfma_f32_16x16x32_bf16 a[68:71], a[96:99], v[52:55], a[68:71]\n"
"buffer_load_dwordx4 v[108:111], v1, s[36:39], s57 offen offset:1024\n"
"s_waitcnt lgkmcnt(8)\n"
"v_mfma_f32_16x16x32_bf16 a[72:75], a[100:103], v[48:51], a[72:75]\n"
"ds_read_b64_tr_b16 a[88:89], v8 offset:30848\n"
"ds_read_b64_tr_b16 a[90:91], v8 offset:35968\n"
"v_mfma_f32_16x16x32_bf16 a[76:79], a[100:103], v[52:55], a[76:79]\n"
"s_add_u32 s57, s57, 122880\n"
"s_waitcnt vmcnt(27) lgkmcnt(4)\n"
"v_mfma_f32_16x16x32_bf16 a[0:3], a[104:107], v[56:59], a[0:3]\n"
"ds_read_b64_tr_b16 a[92:93], v9 offset:30880\n"
"ds_read_b64_tr_b16 a[94:95], v9 offset:36000\n"
"v_mfma_f32_16x16x32_bf16 a[4:7], a[104:107], v[60:63], a[4:7]\n"
"buffer_load_dwordx4 v[112:115], v1, s[36:39], s57 offen\n"
"v_mfma_f32_16x16x32_bf16 a[8:11], a[108:111], v[56:59], a[8:11]\n"
"ds_read_b64_tr_b16 a[96:97], v8 offset:30912\n"
"ds_read_b64_tr_b16 a[98:99], v8 offset:36032\n"
"v_mfma_f32_16x16x32_bf16 a[12:15], a[108:111], v[60:63], a[12:15]\n"
"v_mfma_f32_16x16x32_bf16 a[16:19], a[80:83], v[56:59], a[16:19]\n"
"ds_read_b64_tr_b16 a[100:101], v9 offset:30944\n"
"ds_read_b64_tr_b16 a[102:103], v9 offset:36064\n"
"v_mfma_f32_16x16x32_bf16 a[20:23], a[80:83], v[60:63], a[20:23]\n"
"buffer_load_dwordx4 v[116:119], v1, s[36:39], s57 offen offset:1024\n"
"s_waitcnt lgkmcnt(4)\n"
"v_mfma_f32_16x16x32_bf16 a[24:27], a[84:87], v[56:59], a[24:27]\n"
"ds_read_b64_tr_b16 a[104:105], v8 offset:30976\n"
"ds_read_b64_tr_b16 a[106:107], v8 offset:36096\n"
"v_mfma_f32_16x16x32_bf16 a[28:31], a[84:87], v[60:63], a[28:31]\n"
"s_add_u32 s57, s57, 122880\n"
"v_mfma_f32_16x16x32_bf16 a[32:35], a[88:91], v[56:59], a[32:35]\n"
"ds_read_b64_tr_b16 a[108:109], v9 offset:31008\n"
"ds_read_b64_tr_b16 a[110:111], v9 offset:36128\n"
"v_mfma_f32_16x16x32_bf16 a[36:39], a[88:91], v[60:63], a[36:39]\n"
"s_waitcnt vmcnt(22) lgkmcnt(0)\n"
"s_barrier\n"
"v_mfma_f32_16x16x32_bf16 a[40:43], a[92:95], v[56:59], a[40:43]\n"
"ds_read_b64_tr_b16 a[80:81], v8 offset:40960\n"
"ds_read_b64_tr_b16 a[82:83], v8 offset:46080\n"
"v_mfma_f32_16x16x32_bf16 a[44:47], a[92:95], v[60:63], a[44:47]\n"
"s_add_u32 m0, s66, 102400\n"
"v_mfma_f32_16x16x32_bf16 a[48:51], a[96:99], v[56:59], a[48:51]\n"
"ds_read_b64_tr_b16 a[84:85], v9 offset:40992\n"
"ds_read_b64_tr_b16 a[86:87], v9 offset:46112\n"
"v_mfma_f32_16x16x32_bf16 a[52:55], a[96:99], v[60:63], a[52:55]\n"
"buffer_load_dwordx4 v2, s[40:43], s58 offen offset:0 lds\n"
"v_mfma_f32_16x16x32_bf16 a[56:59], a[100:103], v[56:59], a[56:59]\n"
"ds_read_b64_tr_b16 a[88:89], v8 offset:41024\n"
"ds_read_b64_tr_b16 a[90:91], v8 offset:46144\n"
"v_mfma_f32_16x16x32_bf16 a[60:63], a[100:103], v[60:63], a[60:63]\n"
"v_mfma_f32_16x16x32_bf16 a[64:67], a[104:107], v[56:59], a[64:67]\n"
"ds_read_b64_tr_b16 a[92:93], v9 offset:41056\n"
"ds_read_b64_tr_b16 a[94:95], v9 offset:46176\n"
"v_mfma_f32_16x16x32_bf16 a[68:71], a[104:107], v[60:63], a[68:71]\n"
"buffer_load_dwordx4 v3, s[40:43], s58 offen offset:1024 lds\n"
"v_mfma_f32_16x16x32_bf16 a[72:75], a[108:111], v[56:59], a[72:75]\n"
"ds_read_b64_tr_b16 a[96:97], v8 offset:41088\n"
"ds_read_b64_tr_b16 a[98:99], v8 offset:46208\n"
"v_mfma_f32_16x16x32_bf16 a[76:79], a[108:111], v[60:63], a[76:79]\n"
"s_waitcnt lgkmcnt(4)\n"
"v_mfma_f32_16x16x32_bf16 a[0:3], a[80:83], v[64:67], a[0:3]\n"
"ds_read_b64_tr_b16 a[100:101], v9 offset:41120\n"
"ds_read_b64_tr_b16 a[102:103], v9 offset:46240\n"
"v_mfma_f32_16x16x32_bf16 a[4:7], a[80:83], v[68:71], a[4:7]\n"
"buffer_load_dwordx4 v4, s[40:43], s58 offen offset:2048 lds\n"
"v_mfma_f32_16x16x32_bf16 a[8:11], a[84:87], v[64:67], a[8:11]\n"
"ds_read_b64_tr_b16 a[104:105], v8 offset:41152\n"
"ds_read_b64_tr_b16 a[106:107], v8 offset:46272\n"
"v_mfma_f32_16x16x32_bf16 a[12:15], a[84:87], v[68:71], a[12:15]\n"
"v_mfma_f32_16x16x32_bf16 a[16:19], a[88:91], v[64:67], a[16:19]\n"
"ds_read_b64_tr_b16 a[108:109], v9 offset:41184\n"
"ds_read_b64_tr_b16 a[110:111], v9 offset:46304\n"
"v_mfma_f32_16x16x32_bf16 a[20:23], a[88:91], v[68:71], a[20:23]\n"
"buffer_load_dwordx4 v5, s[40:43], s58 offen offset:3072 lds\n"
"s_waitcnt lgkmcnt(4)\n"
"v_mfma_f32_16x16x32_bf16 a[24:27], a[92:95], v[64:67], a[24:27]\n"
"ds_read_b64_tr_b16 a[80:81], v8 offset:41216\n"
"ds_read_b64_tr_b16 a[82:83], v8 offset:46336\n"
"v_mfma_f32_16x16x32_bf16 a[28:31], a[92:95], v[68:71], a[28:31]\n"
"s_add_u32 m0, m0, 4096\n"
"v_mfma_f32_16x16x32_bf16 a[32:35], a[96:99], v[64:67], a[32:35]\n"
"ds_read_b64_tr_b16 a[84:85], v9 offset:41248\n"
"ds_read_b64_tr_b16 a[86:87], v9 offset:46368\n"
"v_mfma_f32_16x16x32_bf16 a[36:39], a[96:99], v[68:71], a[36:39]\n"
"buffer_load_dwordx4 v6, s[40:43], s58 offen lds\n"
"s_add_u32 s58, s58, 81920\n"
"v_mfma_f32_16x16x32_bf16 a[40:43], a[100:103], v[64:67], a[40:43]\n"
"ds_read_b64_tr_b16 a[88:89], v8 offset:51200\n"
"ds_read_b64_tr_b16 a[90:91], v8 offset:56320\n"
"v_mfma_f32_16x16x32_bf16 a[44:47], a[100:103], v[68:71], a[44:47]\n"
"s_waitcnt lgkmcnt(4)\n"
"v_mfma_f32_16x16x32_bf16 a[48:51], a[104:107], v[64:67], a[48:51]\n"
"ds_read_b64_tr_b16 a[92:93], v9 offset:51232\n"
"ds_read_b64_tr_b16 a[94:95], v9 offset:56352\n"
"v_mfma_f32_16x16x32_bf16 a[52:55], a[104:107], v[68:71], a[52:55]\n"
"buffer_load_dwordx4 v[120:123], v1, s[36:39], s57 offen\n"
"v_mfma_f32_16x16x32_bf16 a[56:59], a[108:111], v[64:67], a[56:59]\n"
"ds_read_b64_tr_b16 a[96:97], v8 offset:51264\n"
"ds_read_b64_tr_b16 a[98:99], v8 offset:56384\n"
"v_mfma_f32_16x16x32_bf16 a[60:63], a[108:111], v[68:71], a[60:63]\n"
"v_mfma_f32_16x16x32_bf16 a[64:67], a[80:83], v[64:67], a[64:67]\n"
"ds_read_b64_tr_b16 a[100:101], v9 offset:51296\n"
"ds_read_b64_tr_b16 a[102:103], v9 offset:56416\n"
"v_mfma_f32_16x16x32_bf16 a[68:71], a[80:83], v[68:71], a[68:71]\n"
"buffer_load_dwordx4 v[124:127], v1, s[36:39], s57 offen offset:1024\n"
"s_waitcnt lgkmcnt(8)\n"
"v_mfma_f32_16x16x32_bf16 a[72:75], a[84:87], v[64:67], a[72:75]\n"
"ds_read_b64_tr_b16 a[104:105], v8 offset:51328\n"
"ds_read_b64_tr_b16 a[106:107], v8 offset:56448\n"
"v_mfma_f32_16x16x32_bf16 a[76:79], a[84:87], v[68:71], a[76:79]\n"
"s_add_u32 s57, s57, 122880\n"
"s_waitcnt vmcnt(27) lgkmcnt(4)\n"
"v_mfma_f32_16x16x32_bf16 a[0:3], a[88:91], v[72:75], a[0:3]\n"
"ds_read_b64_tr_b16 a[108:109], v9 offset:51360\n"
"ds_read_b64_tr_b16 a[110:111], v9 offset:56480\n"
"v_mfma_f32_16x16x32_bf16 a[4:7], a[88:91], v[76:79], a[4:7]\n"
"buffer_load_dwordx4 v[32:35], v1, s[36:39], s57 offen\n"
"v_mfma_f32_16x16x32_bf16 a[8:11], a[92:95], v[72:75], a[8:11]\n"
"ds_read_b64_tr_b16 a[80:81], v8 offset:51392\n"
"ds_read_b64_tr_b16 a[82:83], v8 offset:56512\n"
"v_mfma_f32_16x16x32_bf16 a[12:15], a[92:95], v[76:79], a[12:15]\n"
"v_mfma_f32_16x16x32_bf16 a[16:19], a[96:99], v[72:75], a[16:19]\n"
"ds_read_b64_tr_b16 a[84:85], v9 offset:51424\n"
"ds_read_b64_tr_b16 a[86:87], v9 offset:56544\n"
"v_mfma_f32_16x16x32_bf16 a[20:23], a[96:99], v[76:79], a[20:23]\n"
"buffer_load_dwordx4 v[36:39], v1, s[36:39], s57 offen offset:1024\n"
"s_waitcnt lgkmcnt(4)\n"
"v_mfma_f32_16x16x32_bf16 a[24:27], a[100:103], v[72:75], a[24:27]\n"
"ds_read_b64_tr_b16 a[88:89], v8 offset:51456\n"
"ds_read_b64_tr_b16 a[90:91], v8 offset:56576\n"
"v_mfma_f32_16x16x32_bf16 a[28:31], a[100:103], v[76:79], a[28:31]\n"
"s_add_u32 s57, s57, 122880\n"
"v_mfma_f32_16x16x32_bf16 a[32:35], a[104:107], v[72:75], a[32:35]\n"
"ds_read_b64_tr_b16 a[92:93], v9 offset:51488\n"
"ds_read_b64_tr_b16 a[94:95], v9 offset:56608\n"
"v_mfma_f32_16x16x32_bf16 a[36:39], a[104:107], v[76:79], a[36:39]\n"
"s_waitcnt vmcnt(22) lgkmcnt(0)\n"
"s_barrier\n"
"v_mfma_f32_16x16x32_bf16 a[40:43], a[108:111], v[72:75], a[40:43]\n"
"ds_read_b64_tr_b16 a[96:97], v10 offset:0\n"
"ds_read_b64_tr_b16 a[98:99], v10 offset:5120\n"
"v_mfma_f32_16x16x32_bf16 a[44:47], a[108:111], v[76:79], a[44:47]\n"
"s_add_u32 m0, s66, 0\n"
"v_mfma_f32_16x16x32_bf16 a[48:51], a[80:83], v[72:75], a[48:51]\n"
"ds_read_b64_tr_b16 a[100:101], v11 offset:32\n"
"ds_read_b64_tr_b16 a[102:103], v11 offset:5152\n"
"v_mfma_f32_16x16x32_bf16 a[52:55], a[80:83], v[76:79], a[52:55]\n"
"buffer_load_dwordx4 v2, s[40:43], s58 offen offset:0 lds\n"
"v_mfma_f32_16x16x32_bf16 a[56:59], a[84:87], v[72:75], a[56:59]\n"
"ds_read_b64_tr_b16 a[104:105], v10 offset:64\n"
"ds_read_b64_tr_b16 a[106:107], v10 offset:5184\n"
"v_mfma_f32_16x16x32_bf16 a[60:63], a[84:87], v[76:79], a[60:63]\n"
"v_mfma_f32_16x16x32_bf16 a[64:67], a[88:91], v[72:75], a[64:67]\n"
"ds_read_b64_tr_b16 a[108:109], v11 offset:96\n"
"ds_read_b64_tr_b16 a[110:111], v11 offset:5216\n"
"v_mfma_f32_16x16x32_bf16 a[68:71], a[88:91], v[76:79], a[68:71]\n"
"buffer_load_dwordx4 v3, s[40:43], s58 offen offset:1024 lds\n"
"v_mfma_f32_16x16x32_bf16 a[72:75], a[92:95], v[72:75], a[72:75]\n"
"ds_read_b64_tr_b16 a[80:81], v10 offset:128\n"
"ds_read_b64_tr_b16 a[82:83], v10 offset:5248\n"
"v_mfma_f32_16x16x32_bf16 a[76:79], a[92:95], v[76:79], a[76:79]\n"
"s_waitcnt lgkmcnt(4)\n"
"v_mfma_f32_16x16x32_bf16 a[0:3], a[96:99], v[80:83], a[0:3]\n"
"ds_read_b64_tr_b16 a[84:85], v11 offset:160\n"
"ds_read_b64_tr_b16 a[86:87], v11 offset:5280\n"
"v_mfma_f32_16x16x32_bf16 a[4:7], a[96:99], v[84:87], a[4:7]\n"
"buffer_load_dwordx4 v4, s[40:43], s58 offen offset:2048 lds\n"
"v_mfma_f32_16x16x32_bf16 a[8:11], a[100:103], v[80:83], a[8:11]\n"
"ds_read_b64_tr_b16 a[88:89], v10 offset:192\n"
"ds_read_b64_tr_b16 a[90:91], v10 offset:5312\n"
"v_mfma_f32_16x16x32_bf16 a[12:15], a[100:103], v[84:87], a[12:15]\n"
"v_mfma_f32_16x16x32_bf16 a[16:19], a[104:107], v[80:83], a[16:19]\n"
"ds_read_b64_tr_b16 a[92:93], v11 offset:224\n"
"ds_read_b64_tr_b16 a[94:95], v11 offset:5344\n"
"v_mfma_f32_16x16x32_bf16 a[20:23], a[104:107], v[84:87], a[20:23]\n"
"buffer_load_dwordx4 v5, s[40:43], s58 offen offset:3072 lds\n"
"s_waitcnt lgkmcnt(4)\n"
"v_mfma_f32_16x16x32_bf16 a[24:27], a[108:111], v[80:83], a[24:27]\n"
"ds_read_b64_tr_b16 a[96:97], v10 offset:256\n"
"ds_read_b64_tr_b16 a[98:99], v10 offset:5376\n"
"v_mfma_f32_16x16x32_bf16 a[28:31], a[108:111], v[84:87], a[28:31]\n"
"s_add_u32 m0, m0, 4096\n"
"v_mfma_f32_16x16x32_bf16 a[32:35], a[80:83], v[80:83], a[32:35]\n"
"ds_read_b64_tr_b16 a[100:101], v11 offset:288\n"
"ds_read_b64_tr_b16 a[102:103], v11 offset:5408\n"
"v_mfma_f32_16x16x32_bf16 a[36:39], a[80:83], v[84:87], a[36:39]\n"
"buffer_load_dwordx4 v6, s[40:43], s58 offen lds\n"
"s_add_u32 s58, s58, 81920\n"
"v_mfma_f32_16x16x32_bf16 a[40:43], a[84:87], v[80:83], a[40:43]\n"
"ds_read_b64_tr_b16 a[104:105], v10 offset:10240\n"
"ds_read_b64_tr_b16 a[106:107], v10 offset:15360\n"
"v_mfma_f32_16x16x32_bf16 a[44:47], a[84:87], v[84:87], a[44:47]\n"
"s_waitcnt lgkmcnt(4)\n"
"v_mfma_f32_16x16x32_bf16 a[48:51], a[88:91], v[80:83], a[48:51]\n"
"ds_read_b64_tr_b16 a[108:109], v11 offset:10272\n"
"ds_read_b64_tr_b16 a[110:111], v11 offset:15392\n"
"v_mfma_f32_16x16x32_bf16 a[52:55], a[88:91], v[84:87], a[52:55]\n"
"buffer_load_dwordx4 v[40:43], v1, s[36:39], s57 offen\n"
"v_mfma_f32_16x16x32_bf16 a[56:59], a[92:95], v[80:83], a[56:59]\n"
"ds_read_b64_tr_b16 a[80:81], v10 offset:10304\n"
"ds_read_b64_tr_b16 a[82:83], v10 offset:15424\n"
"v_mfma_f32_16x16x32_bf16 a[60:63], a[92:95], v[84:87], a[60:63]\n"
"v_mfma_f32_16x16x32_bf16 a[64:67], a[96:99], v[80:83], a[64:67]\n"
"ds_read_b64_tr_b16 a[84:85], v11 offset:10336\n"
"ds_read_b64_tr_b16 a[86:87], v11 offset:15456\n"
"v_mfma_f32_16x16x32_bf16 a[68:71], a[96:99], v[84:87], a[68:71]\n"
"buffer_load_dwordx4 v[44:47], v1, s[36:39], s57 offen offset:1024\n"
"s_waitcnt lgkmcnt(8)\n"
"v_mfma_f32_16x16x32_bf16 a[72:75], a[100:103], v[80:83], a[72:75]\n"
"ds_read_b64_tr_b16 a[88:89], v10 offset:10368\n"
"ds_read_b64_tr_b16 a[90:91], v10 offset:15488\n"
"v_mfma_f32_16x16x32_bf16 a[76:79], a[100:103], v[84:87], a[76:79]\n"
"s_add_u32 s57, s57, 122880\n"
"s_waitcnt vmcnt(27) lgkmcnt(4)\n"
"v_mfma_f32_16x16x32_bf16 a[0:3], a[104:107], v[88:91], a[0:3]\n"
"ds_read_b64_tr_b16 a[92:93], v11 offset:10400\n"
"ds_read_b64_tr_b16 a[94:95], v11 offset:15520\n"
"v_mfma_f32_16x16x32_bf16 a[4:7], a[104:107], v[92:95], a[4:7]\n"
"buffer_load_dwordx4 v[48:51], v1, s[36:39], s57 offen\n"
"v_mfma_f32_16x16x32_bf16 a[8:11], a[108:111], v[88:91], a[8:11]\n"
"ds_read_b64_tr_b16 a[96:97], v10 offset:10432\n"
"ds_read_b64_tr_b16 a[98:99], v10 offset:15552\n"
"v_mfma_f32_16x16x32_bf16 a[12:15], a[108:111], v[92:95], a[12:15]\n"
"v_mfma_f32_16x16x32_bf16 a[16:19], a[80:83], v[88:91], a[16:19]\n"
"ds_read_b64_tr_b16 a[100:101], v11 offset:10464\n"
"ds_read_b64_tr_b16 a[102:103], v11 offset:15584\n"
"v_mfma_f32_16x16x32_bf16 a[20:23], a[80:83], v[92:95], a[20:23]\n"
"buffer_load_dwordx4 v[52:55], v1, s[36:39], s57 offen offset:1024\n"
"s_waitcnt lgkmcnt(4)\n"
"v_mfma_f32_16x16x32_bf16 a[24:27], a[84:87], v[88:91], a[24:27]\n"
"ds_read_b64_tr_b16 a[104:105], v10 offset:10496\n"
"ds_read_b64_tr_b16 a[106:107], v10 offset:15616\n"
"v_mfma_f32_16x16x32_bf16 a[28:31], a[84:87], v[92:95], a[28:31]\n"
"s_add_u32 s57, s57, 122880\n"
"v_mfma_f32_16x16x32_bf16 a[32:35], a[88:91], v[88:91], a[32:35]\n"
"ds_read_b64_tr_b16 a[108:109], v11 offset:10528\n"
"ds_read_b64_tr_b16 a[110:111], v11 offset:15648\n"
"v_mfma_f32_16x16x32_bf16 a[36:39], a[88:91], v[92:95], a[36:39]\n"
"s_waitcnt vmcnt(22) lgkmcnt(0)\n"
"s_barrier\n"
"v_mfma_f32_16x16x32_bf16 a[40:43], a[92:95], v[88:91], a[40:43]\n"
"ds_read_b64_tr_b16 a[80:81], v10 offset:20480\n"
"ds_read_b64_tr_b16 a[82:83], v10 offset:25600\n"
"v_mfma_f32_16x16x32_bf16 a[44:47], a[92:95], v[92:95], a[44:47]\n"
"s_add_u32 m0, s66, 20480\n"
"v_mfma_f32_16x16x32_bf16 a[48:51], a[96:99], v[88:91], a[48:51]\n"
"ds_read_b64_tr_b16 a[84:85], v11 offset:20512\n"
"ds_read_b64_tr_b16 a[86:87], v11 offset:25632\n"
"v_mfma_f32_16x16x32_bf16 a[52:55], a[96:99], v[92:95], a[52:55]\n"
"buffer_load_dwordx4 v2, s[40:43], s58 offen offset:0 lds\n"
"v_mfma_f32_16x16x32_bf16 a[56:59], a[100:103], v[88:91], a[56:59]\n"
"ds_read_b64_tr_b16 a[88:89], v10 offset:20544\n"
"ds_read_b64_tr_b16 a[90:91], v10 offset:25664\n"
"v_mfma_f32_16x16x32_bf16 a[60:63], a[100:103], v[92:95], a[60:63]\n"
"v_mfma_f32_16x16x32_bf16 a[64:67], a[104:107], v[88:91], a[64:67]\n"
"ds_read_b64_tr_b16 a[92:93], v11 offset:20576\n"
"ds_read_b64_tr_b16 a[94:95], v11 offset:25696\n"
"v_mfma_f32_16x16x32_bf16 a[68:71], a[104:107], v[92:95], a[68:71]\n"
"buffer_load_dwordx4 v3, s[40:43], s58 offen offset:1024 lds\n"
"v_mfma_f32_16x16x32_bf16 a[72:75], a[108:111], v[88:91], a[72:75]\n"
"ds_read_b64_tr_b16 a[96:97], v10 offset:20608\n"
"ds_read_b64_tr_b16 a[98:99], v10 offset:25728\n"
"v_mfma_f32_16x16x32_bf16 a[76:79], a[108:111], v[92:95], a[76:79]\n"
"s_waitcnt lgkmcnt(4)\n"
"v_mfma_f32_16x16x32_bf16 a[0:3], a[80:83], v[96:99], a[0:3]\n"
"ds_read_b64_tr_b16 a[100:101], v11 offset:20640\n"
"ds_read_b64_tr_b16 a[102:103], v11 offset:25760\n"
"v_mfma_f32_16x16x32_bf16 a[4:7], a[80:83], v[100:103], a[4:7]\n"
"buffer_load_dwordx4 v4, s[40:43], s58 offen offset:2048 lds\n"
"v_mfma_f32_16x16x32_bf16 a[8:11], a[84:87], v[96:99], a[8:11]\n"
"ds_read_b64_tr_b16 a[104:105], v10 offset:20672\n"
"ds_read_b64_tr_b16 a[106:107], v10 offset:25792\n"
"v_mfma_f32_16x16x32_bf16 a[12:15], a[84:87], v[100:103], a[12:15]\n"
"v_mfma_f32_16x16x32_bf16 a[16:19], a[88:91], v[96:99], a[16:19]\n"
"ds_read_b64_tr_b16 a[108:109], v11 offset:20704\n"
"ds_read_b64_tr_b16 a[110:111], v11 offset:25824\n"
"v_mfma_f32_16x16x32_bf16 a[20:23], a[88:91], v[100:103], a[20:23]\n"
"buffer_load_dwordx4 v5, s[40:43], s58 offen offset:3072 lds\n"
"s_waitcnt lgkmcnt(4)\n"
"v_mfma_f32_16x16x32_bf16 a[24:27], a[92:95], v[96:99], a[24:27]\n"
"ds_read_b64_tr_b16 a[80:81], v10 offset:20736\n"
"ds_read_b64_tr_b16 a[82:83], v10 offset:25856\n"
"v_mfma_f32_16x16x32_bf16 a[28:31], a[92:95], v[100:103], a[28:31]\n"
"s_add_u32 m0, m0, 4096\n"
"v_mfma_f32_16x16x32_bf16 a[32:35], a[96:99], v[96:99], a[32:35]\n"
"ds_read_b64_tr_b16 a[84:85], v11 offset:20768\n"
"ds_read_b64_tr_b16 a[86:87], v11 offset:25888\n"
"v_mfma_f32_16x16x32_bf16 a[36:39], a[96:99], v[100:103], a[36:39]\n"
"buffer_load_dwordx4 v6, s[40:43], s58 offen lds\n"
"s_add_u32 s58, s58, 81920\n"
"v_mfma_f32_16x16x32_bf16 a[40:43], a[100:103], v[96:99], a[40:43]\n"
"ds_read_b64_tr_b16 a[88:89], v10 offset:30720\n"
"ds_read_b64_tr_b16 a[90:91], v10 offset:35840\n"
"v_mfma_f32_16x16x32_bf16 a[44:47], a[100:103], v[100:103], a[44:47]\n"
"s_waitcnt lgkmcnt(4)\n"
"v_mfma_f32_16x16x32_bf16 a[48:51], a[104:107], v[96:99], a[48:51]\n"
"ds_read_b64_tr_b16 a[92:93], v11 offset:30752\n"
"ds_read_b64_tr_b16 a[94:95], v11 offset:35872\n"
"v_mfma_f32_16x16x32_bf16 a[52:55], a[104:107], v[100:103], a[52:55]\n"
"buffer_load_dwordx4 v[56:59], v1, s[36:39], s57 offen\n"
"v_mfma_f32_16x16x32_bf16 a[56:59], a[108:111], v[96:99], a[56:59]\n"
"ds_read_b64_tr_b16 a[96:97], v10 offset:30784\n"
"ds_read_b64_tr_b16 a[98:99], v10 offset:35904\n"
"v_mfma_f32_16x16x32_bf16 a[60:63], a[108:111], v[100:103], a[60:63]\n"
"v_mfma_f32_16x16x32_bf16 a[64:67], a[80:83], v[96:99], a[64:67]\n"
"ds_read_b64_tr_b16 a[100:101], v11 offset:30816\n"
"ds_read_b64_tr_b16 a[102:103], v11 offset:35936\n"
"v_mfma_f32_16x16x32_bf16 a[68:71], a[80:83], v[100:103], a[68:71]\n"
"buffer_load_dwordx4 v[60:63], v1, s[36:39], s57 offen offset:1024\n"
"s_waitcnt lgkmcnt(8)\n"
"v_mfma_f32_16x16x32_bf16 a[72:75], a[84:87], v[96:99], a[72:75]\n"
"ds_read_b64_tr_b16 a[104:105], v10 offset:30848\n"
"ds_read_b64_tr_b16 a[106:107], v10 offset:35968\n"
"v_mfma_f32_16x16x32_bf16 a[76:79], a[84:87], v[100:103], a[76:79]\n"
"s_add_u32 s57, s57, 122880\n"
"s_waitcnt vmcnt(27) lgkmcnt(4)\n"
"v_mfma_f32_16x16x32_bf16 a[0:3], a[88:91], v[104:107], a[0:3]\n"
"ds_read_b64_tr_b16 a[108:109], v11 offset:30880\n"
"ds_read_b64_tr_b16 a[110:111], v11 offset:36000\n"
"v_mfma_f32_16x16x32_bf16 a[4:7], a[88:91], v[108:111], a[4:7]\n"
"buffer_load_dwordx4 v[64:67], v1, s[36:39], s57 offen\n"
"v_mfma_f32_16x16x32_bf16 a[8:11], a[92:95], v[104:107], a[8:11]\n"
"ds_read_b64_tr_b16 a[80:81], v10 offset:30912\n"
"ds_read_b64_tr_b16 a[82:83], v10 offset:36032\n"
"v_mfma_f32_16x16x32_bf16 a[12:15], a[92:95], v[108:111], a[12:15]\n"
"v_mfma_f32_16x16x32_bf16 a[16:19], a[96:99], v[104:107], a[16:19]\n"
"ds_read_b64_tr_b16 a[84:85], v11 offset:30944\n"
"ds_read_b64_tr_b16 a[86:87], v11 offset:36064\n"
"v_mfma_f32_16x16x32_bf16 a[20:23], a[96:99], v[108:111], a[20:23]\n"
"buffer_load_dwordx4 v[68:71], v1, s[36:39], s57 offen offset:1024\n"
"s_waitcnt lgkmcnt(4)\n"
"v_mfma_f32_16x16x32_bf16 a[24:27], a[100:103], v[104:107], a[24:27]\n"
"ds_read_b64_tr_b16 a[88:89], v10 offset:30976\n"
"ds_read_b64_tr_b16 a[90:91], v10 offset:36096\n"
"v_mfma_f32_16x16x32_bf16 a[28:31], a[100:103], v[108:111], a[28:31]\n"
"s_add_u32 s57, s57, 122880\n"
"v_mfma_f32_16x16x32_bf16 a[32:35], a[104:107], v[104:107], a[32:35]\n"
"ds_read_b64_tr_b16 a[92:93], v11 offset:31008\n"
"ds_read_b64_tr_b16 a[94:95], v11 offset:36128\n"
"v_mfma_f32_16x16x32_bf16 a[36:39], a[104:107], v[108:111], a[36:39]\n"
"s_waitcnt vmcnt(22) lgkmcnt(0)\n"
"s_barrier\n"
"v_mfma_f32_16x16x32_bf16 a[40:43], a[108:111], v[104:107], a[40:43]\n"
"ds_read_b64_tr_b16 a[96:97], v10 offset:40960\n"
"ds_read_b64_tr_b16 a[98:99], v10 offset:46080\n"
"v_mfma_f32_16x16x32_bf16 a[44:47], a[108:111], v[108:111], a[44:47]\n"
"s_add_u32 m0, s66, 40960\n"
"v_mfma_f32_16x16x32_bf16 a[48:51], a[80:83], v[104:107], a[48:51]\n"
"ds_read_b64_tr_b16 a[100:101], v11 offset:40992\n"
"ds_read_b64_tr_b16 a[102:103], v11 offset:46112\n"
"v_mfma_f32_16x16x32_bf16 a[52:55], a[80:83], v[108:111], a[52:55]\n"
"buffer_load_dwordx4 v2, s[40:43], s58 offen offset:0 lds\n"
"v_mfma_f32_16x16x32_bf16 a[56:59], a[84:87], v[104:107], a[56:59]\n"
"ds_read_b64_tr_b16 a[104:105], v10 offset:41024\n"
"ds_read_b64_tr_b16 a[106:107], v10 offset:46144\n"
"v_mfma_f32_16x16x32_bf16 a[60:63], a[84:87], v[108:111], a[60:63]\n"
"v_mfma_f32_16x16x32_bf16 a[64:67], a[88:91], v[104:107], a[64:67]\n"
"ds_read_b64_tr_b16 a[108:109], v11 offset:41056\n"
"ds_read_b64_tr_b16 a[110:111], v11 offset:46176\n"
"v_mfma_f32_16x16x32_bf16 a[68:71], a[88:91], v[108:111], a[68:71]\n"
"buffer_load_dwordx4 v3, s[40:43], s58 offen offset:1024 lds\n"
"v_mfma_f32_16x16x32_bf16 a[72:75], a[92:95], v[104:107], a[72:75]\n"
"ds_read_b64_tr_b16 a[80:81], v10 offset:41088\n"
"ds_read_b64_tr_b16 a[82:83], v10 offset:46208\n"
"v_mfma_f32_16x16x32_bf16 a[76:79], a[92:95], v[108:111], a[76:79]\n"
"s_waitcnt lgkmcnt(4)\n"
"v_mfma_f32_16x16x32_bf16 a[0:3], a[96:99], v[112:115], a[0:3]\n"
"ds_read_b64_tr_b16 a[84:85], v11 offset:41120\n"
"ds_read_b64_tr_b16 a[86:87], v11 offset:46240\n"
"v_mfma_f32_16x16x32_bf16 a[4:7], a[96:99], v[116:119], a[4:7]\n"
"buffer_load_dwordx4 v4, s[40:43], s58 offen offset:2048 lds\n"
"v_mfma_f32_16x16x32_bf16 a[8:11], a[100:103], v[112:115], a[8:11]\n"
"ds_read_b64_tr_b16 a[88:89], v10 offset:41152\n"
"ds_read_b64_tr_b16 a[90:91], v10 offset:46272\n"
"v_mfma_f32_16x16x32_bf16 a[12:15], a[100:103], v[116:119], a[12:15]\n"
"v_mfma_f32_16x16x32_bf16 a[16:19], a[104:107], v[112:115], a[16:19]\n"
"ds_read_b64_tr_b16 a[92:93], v11 offset:41184\n"
"ds_read_b64_tr_b16 a[94:95], v11 offset:46304\n"
"v_mfma_f32_16x16x32_bf16 a[20:23], a[104:107], v[116:119], a[20:23]\n"
"buffer_load_dwordx4 v5, s[40:43], s58 offen offset:3072 lds\n"
"s_waitcnt lgkmcnt(4)\n"
"v_mfma_f32_16x16x32_bf16 a[24:27], a[108:111], v[112:115], a[24:27]\n"
"ds_read_b64_tr_b16 a[96:97], v10 offset:41216\n"
"ds_read_b64_tr_b16 a[98:99], v10 offset:46336\n"
"v_mfma_f32_16x16x32_bf16 a[28:31], a[108:111], v[116:119], a[28:31]\n"
"s_add_u32 m0, m0, 4096\n"
"v_mfma_f32_16x16x32_bf16 a[32:35], a[80:83], v[112:115], a[32:35]\n"
"ds_read_b64_tr_b16 a[100:101], v11 offset:41248\n"
"ds_read_b64_tr_b16 a[102:103], v11 offset:46368\n"
"v_mfma_f32_16x16x32_bf16 a[36:39], a[80:83], v[116:119], a[36:39]\n"
"buffer_load_dwordx4 v6, s[40:43], s58 offen lds\n"
"s_add_u32 s58, s58, 81920\n"
"v_mfma_f32_16x16x32_bf16 a[40:43], a[84:87], v[112:115], a[40:43]\n"
"ds_read_b64_tr_b16 a[104:105], v10 offset:51200\n"
"ds_read_b64_tr_b16 a[106:107], v10 offset:56320\n"
"v_mfma_f32_16x16x32_bf16 a[44:47], a[84:87], v[116:119], a[44:47]\n"
"s_waitcnt lgkmcnt(4)\n"
"v_mfma_f32_16x16x32_bf16 a[48:51], a[88:91], v[112:115], a[48:51]\n"
"ds_read_b64_tr_b16 a[108:109], v11 offset:51232\n"
"ds_read_b64_tr_b16 a[110:111], v11 offset:56352\n"
"v_mfma_f32_16x16x32_bf16 a[52:55], a[88:91], v[116:119], a[52:55]\n"
"buffer_load_dwordx4 v[72:75], v1, s[36:39], s57 offen\n"
"v_mfma_f32_16x16x32_bf16 a[56:59], a[92:95], v[112:115], a[56:59]\n"
"ds_read_b64_tr_b16 a[80:81], v10 offset:51264\n"
"ds_read_b64_tr_b16 a[82:83], v10 offset:56384\n"
"v_mfma_f32_16x16x32_bf16 a[60:63], a[92:95], v[116:119], a[60:63]\n"
"v_mfma_f32_16x16x32_bf16 a[64:67], a[96:99], v[112:115], a[64:67]\n"
"ds_read_b64_tr_b16 a[84:85], v11 offset:51296\n"
"ds_read_b64_tr_b16 a[86:87], v11 offset:56416\n"
"v_mfma_f32_16x16x32_bf16 a[68:71], a[96:99], v[116:119], a[68:71]\n"
"buffer_load_dwordx4 v[76:79], v1, s[36:39], s57 offen offset:1024\n"
"s_waitcnt lgkmcnt(8)\n"
"v_mfma_f32_16x16x32_bf16 a[72:75], a[100:103], v[112:115], a[72:75]\n"
"ds_read_b64_tr_b16 a[88:89], v10 offset:51328\n"
"ds_read_b64_tr_b16 a[90:91], v10 offset:56448\n"
"v_mfma_f32_16x16x32_bf16 a[76:79], a[100:103], v[116:119], a[76:79]\n"
"s_add_u32 s57, s57, 122880\n"
"s_waitcnt vmcnt(27) lgkmcnt(4)\n"
"v_mfma_f32_16x16x32_bf16 a[0:3], a[104:107], v[120:123], a[0:3]\n"
"ds_read_b64_tr_b16 a[92:93], v11 offset:51360\n"
"ds_read_b64_tr_b16 a[94:95], v11 offset:56480\n"
"v_mfma_f32_16x16x32_bf16 a[4:7], a[104:107], v[124:127], a[4:7]\n"
"buffer_load_dwordx4 v[80:83], v1, s[36:39], s57 offen\n"
"v_mfma_f32_16x16x32_bf16 a[8:11], a[108:111], v[120:123], a[8:11]\n"
"ds_read_b64_tr_b16 a[96:97], v10 offset:51392\n"
"ds_read_b64_tr_b16 a[98:99], v10 offset:56512\n"
"v_mfma_f32_16x16x32_bf16 a[12:15], a[108:111], v[124:127], a[12:15]\n"
"v_mfma_f32_16x16x32_bf16 a[16:19], a[80:83], v[120:123], a[16:19]\n"
"ds_read_b64_tr_b16 a[100:101], v11 offset:51424\n"
"ds_read_b64_tr_b16 a[102:103], v11 offset:56544\n"
"v_mfma_f32_16x16x32_bf16 a[20:23], a[80:83], v[124:127], a[20:23]\n"
"buffer_load_dwordx4 v[84:87], v1, s[36:39], s57 offen offset:1024\n"
"s_waitcnt lgkmcnt(4)\n"
"v_mfma_f32_16x16x32_bf16 a[24:27], a[84:87], v[120:123], a[24:27]\n"
"ds_read_b64_tr_b16 a[104:105], v10 offset:51456\n"
"ds_read_b64_tr_b16 a[106:107], v10 offset:56576\n"
"v_mfma_f32_16x16x32_bf16 a[28:31], a[84:87], v[124:127], a[28:31]\n"
"s_add_u32 s57, s57, 122880\n"
"v_mfma_f32_16x16x32_bf16 a[32:35], a[88:91], v[120:123], a[32:35]\n"
"ds_read_b64_tr_b16 a[108:109], v11 offset:51488\n"
"ds_read_b64_tr_b16 a[110:111], v11 offset:56608\n"
"v_mfma_f32_16x16x32_bf16 a[36:39], a[88:91], v[124:127], a[36:39]\n"
"s_waitcnt vmcnt(22) lgkmcnt(0)\n"
"s_barrier\n"
"v_mfma_f32_16x16x32_bf16 a[40:43], a[92:95], v[120:123], a[40:43]\n"
"ds_read_b64_tr_b16 a[80:81], v8 offset:0\n"
"ds_read_b64_tr_b16 a[82:83], v8 offset:5120\n"
"v_mfma_f32_16x16x32_bf16 a[44:47], a[92:95], v[124:127], a[44:47]\n"
"s_add_u32 m0, s66, 61440\n"
"v_mfma_f32_16x16x32_bf16 a[48:51], a[96:99], v[120:123], a[48:51]\n"
"ds_read_b64_tr_b16 a[84:85], v9 offset:32\n"
"ds_read_b64_tr_b16 a[86:87], v9 offset:5152\n"
"v_mfma_f32_16x16x32_bf16 a[52:55], a[96:99], v[124:127], a[52:55]\n"
"buffer_load_dwordx4 v2, s[40:43], s58 offen offset:0 lds\n"
"v_mfma_f32_16x16x32_bf16 a[56:59], a[100:103], v[120:123], a[56:59]\n"
"ds_read_b64_tr_b16 a[88:89], v8 offset:64\n"
"ds_read_b64_tr_b16 a[90:91], v8 offset:5184\n"
"v_mfma_f32_16x16x32_bf16 a[60:63], a[100:103], v[124:127], a[60:63]\n"
"v_mfma_f32_16x16x32_bf16 a[64:67], a[104:107], v[120:123], a[64:67]\n"
"ds_read_b64_tr_b16 a[92:93], v9 offset:96\n"
"ds_read_b64_tr_b16 a[94:95], v9 offset:5216\n"
"v_mfma_f32_16x16x32_bf16 a[68:71], a[104:107], v[124:127], a[68:71]\n"
"buffer_load_dwordx4 v3, s[40:43], s58 offen offset:1024 lds\n"
"v_mfma_f32_16x16x32_bf16 a[72:75], a[108:111], v[120:123], a[72:75]\n"
"ds_read_b64_tr_b16 a[96:97], v8 offset:128\n"
"ds_read_b64_tr_b16 a[98:99], v8 offset:5248\n"
"v_mfma_f32_16x16x32_bf16 a[76:79], a[108:111], v[124:127], a[76:79]\n"
"s_sub_u32 s59, s59, 1\n"
"s_cmp_lg_u32 s59, 0\n"
"s_cbranch_scc1 K2_W1L\n"
"s_branch K2_DONE\n"
"K2_W2:\n"
"s_mov_b32 s59, 4\n"
"K2_W2L:\n"
"s_waitcnt lgkmcnt(4)\n"
"v_mfma_f32_16x16x32_bf16 a[0:3], a[80:83], v[32:35], a[0:3]\n"
"ds_read_b64_tr_b16 a[100:101], v9 offset:160\n"
"ds_read_b64_tr_b16 a[102:103], v9 offset:5280\n"
"v_mfma_f32_16x16x32_bf16 a[4:7], a[80:83], v[36:39], a[4:7]\n"
"v_mfma_f32_16x16x32_bf16 a[8:11], a[84:87], v[32:35], a[8:11]\n"
"buffer_load_dwordx4 v4, s[40:43], s58 offen offset:2048 lds\n"
"ds_read_b64_tr_b16 a[104:105], v8 offset:192\n"
"v_mfma_f32_16x16x32_bf16 a[12:15], a[84:87], v[36:39], a[12:15]\n"
"ds_read_b64_tr_b16 a[106:107], v8 offset:5312\n"
"v_mfma_f32_16x16x32_bf16 a[16:19], a[88:91], v[32:35], a[16:19]\n"
"ds_read_b64_tr_b16 a[108:109], v9 offset:224\n"
"ds_read_b64_tr_b16 a[110:111], v9 offset:5344\n"
"v_mfma_f32_16x16x32_bf16 a[20:23], a[88:91], v[36:39], a[20:23]\n"
"s_waitcnt lgkmcnt(4)\n"
"v_mfma_f32_16x16x32_bf16 a[24:27], a[92:95], v[32:35], a[24:27]\n"
"buffer_load_dwordx4 v5, s[40:43], s58 offen offset:3072 lds\n"
"ds_read_b64_tr_b16 a[80:81], v8 offset:256\n"
"v_mfma_f32_16x16x32_bf16 a[28:31], a[92:95], v[36:39], a[28:31]\n"
"ds_read_b64_tr_b16 a[82:83], v8 offset:5376\n"
"v_mfma_f32_16x16x32_bf16 a[32:35], a[96:99], v[32:35], a[32:35]\n"
"s_add_u32 m0, m0, 4096\n"
"ds_read_b64_tr_b16 a[84:85], v9 offset:288\n"
"v_mfma_f32_16x16x32_bf16 a[36:39], a[96:99], v[36:39], a[36:39]\n"
"ds_read_b64_tr_b16 a[86:87], v9 offset:5408\n"
"v_mfma_f32_16x16x32_bf16 a[40:43], a[100:103], v[32:35], a[40:43]\n"
"buffer_load_dwordx4 v6, s[40:43], s58 offen lds\n"
"ds_read_b64_tr_b16 a[88:89], v8 offset:10240\n"
"v_mfma_f32_16x16x32_bf16 a[44:47], a[100:103], v[36:39], a[44:47]\n"
"ds_read_b64_tr_b16 a[90:91], v8 offset:15360\n"
"s_waitcnt lgkmcnt(4)\n"
"v_mfma_f32_16x16x32_bf16 a[48:51], a[104:107], v[32:35], a[48:51]\n"
"ds_read_b64_tr_b16 a[92:93], v9 offset:10272\n"
"ds_read_b64_tr_b16 a[94:95], v9 offset:15392\n"
"v_mfma_f32_16x16x32_bf16 a[52:55], a[104:107], v[36:39], a[52:55]\n"
"s_add_u32 s58, s58, 81920\n"
"v_mfma_f32_16x16x32_bf16 a[56:59], a[108:111], v[32:35], a[56:59]\n"
"buffer_load_dwordx4 v[88:91], v1, s[36:39], s57 offen\n"
"ds_read_b64_tr_b16 a[96:97], v8 offset:10304\n"
"v_mfma_f32_16x16x32_bf16 a[60:63], a[108:111], v[36:39], a[60:63]\n"
"ds_read_b64_tr_b16 a[98:99], v8 offset:15424\n"
"v_mfma_f32_16x16x32_bf16 a[64:67], a[80:83], v[32:35], a[64:67]\n"
"ds_read_b64_tr_b16 a[100:101], v9 offset:10336\n"
"ds_read_b64_tr_b16 a[102:103], v9 offset:15456\n"
"v_mfma_f32_16x16x32_bf16 a[68:71], a[80:83], v[36:39], a[68:71]\n"
"s_waitcnt lgkmcnt(8)\n"
"v_mfma_f32_16x16x32_bf16 a[72:75], a[84:87], v[32:35], a[72:75]\n"
"ds_read_b64_tr_b16 a[104:105], v8 offset:10368\n"
"ds_read_b64_tr_b16 a[106:107], v8 offset:15488\n"
"v_mfma_f32_16x16x32_bf16 a[76:79], a[84:87], v[36:39], a[76:79]\n"
"buffer_load_dwordx4 v[92:95], v1, s[36:39], s57 offen offset:1024\n"
"s_add_u32 s57, s57, 122880\n"
"s_waitcnt vmcnt(27) lgkmcnt(4)\n"
"v_mfma_f32_16x16x32_bf16 a[0:3], a[88:91], v[40:43], a[0:3]\n"
"ds_read_b64_tr_b16 a[108:109], v9 offset:10400\n"
"ds_read_b64_tr_b16 a[110:111], v9 offset:15520\n"
"v_mfma_f32_16x16x32_bf16 a[4:7], a[88:91], v[44:47], a[4:7]\n"
"v_mfma_f32_16x16x32_bf16 a[8:11], a[92:95], v[40:43], a[8:11]\n"
"buffer_load_dwordx4 v[96:99], v1, s[36:39], s57 offen\n"
"ds_read_b64_tr_b16 a[80:81], v8 offset:10432\n"
"v_mfma_f32_16x16x32_bf16 a[12:15], a[92:95], v[44:47], a[12:15]\n"
"ds_read_b64_tr_b16 a[82:83], v8 offset:15552\n"
"v_mfma_f32_16x16x32_bf16 a[16:19], a[96:99], v[40:43], a[16:19]\n"
"ds_read_b64_tr_b16 a[84:85], v9 offset:10464\n"
"ds_read_b64_tr_b16 a[86:87], v9 offset:15584\n"
"v_mfma_f32_16x16x32_bf16 a[20:23], a[96:99], v[44:47], a[20:23]\n"
"s_waitcnt lgkmcnt(4)\n"
"v_mfma_f32_16x16x32_bf16 a[24:27], a[100:103], v[40:43], a[24:27]\n"
"buffer_load_dwordx4 v[100:103], v1, s[36:39], s57 offen offset:1024\n"
"ds_read_b64_tr_b16 a[88:89], v8 offset:10496\n"
"v_mfma_f32_16x16x32_bf16 a[28:31], a[100:103], v[44:47], a[28:31]\n"
"ds_read_b64_tr_b16 a[90:91], v8 offset:15616\n"
"s_add_u32 s57, s57, 122880\n"
"v_mfma_f32_16x16x32_bf16 a[32:35], a[104:107], v[40:43], a[32:35]\n"
"ds_read_b64_tr_b16 a[92:93], v9 offset:10528\n"
"ds_read_b64_tr_b16 a[94:95], v9 offset:15648\n"
"v_mfma_f32_16x16x32_bf16 a[36:39], a[104:107], v[44:47], a[36:39]\n"
"s_waitcnt vmcnt(22) lgkmcnt(0)\n"
"s_barrier\n"
"v_mfma_f32_16x16x32_bf16 a[40:43], a[108:111], v[40:43], a[40:43]\n"
"ds_read_b64_tr_b16 a[96:97], v8 offset:20480\n"
"ds_read_b64_tr_b16 a[98:99], v8 offset:25600\n"
"v_mfma_f32_16x16x32_bf16 a[44:47], a[108:111], v[44:47], a[44:47]\n"
"v_mfma_f32_16x16x32_bf16 a[48:51], a[80:83], v[40:43], a[48:51]\n"
"s_add_u32 m0, s66, 81920\n"
"ds_read_b64_tr_b16 a[100:101], v9 offset:20512\n"
"v_mfma_f32_16x16x32_bf16 a[52:55], a[80:83], v[44:47], a[52:55]\n"
"ds_read_b64_tr_b16 a[102:103], v9 offset:25632\n"
"v_mfma_f32_16x16x32_bf16 a[56:59], a[84:87], v[40:43], a[56:59]\n"
"buffer_load_dwordx4 v2, s[40:43], s58 offen offset:0 lds\n"
"ds_read_b64_tr_b16 a[104:105], v8 offset:20544\n"
"v_mfma_f32_16x16x32_bf16 a[60:63], a[84:87], v[44:47], a[60:63]\n"
"ds_read_b64_tr_b16 a[106:107], v8 offset:25664\n"
"v_mfma_f32_16x16x32_bf16 a[64:67], a[88:91], v[40:43], a[64:67]\n"
"ds_read_b64_tr_b16 a[108:109], v9 offset:20576\n"
"ds_read_b64_tr_b16 a[110:111], v9 offset:25696\n"
"v_mfma_f32_16x16x32_bf16 a[68:71], a[88:91], v[44:47], a[68:71]\n"
"v_mfma_f32_16x16x32_bf16 a[72:75], a[92:95], v[40:43], a[72:75]\n"
"ds_read_b64_tr_b16 a[80:81], v8 offset:20608\n"
"ds_read_b64_tr_b16 a[82:83], v8 offset:25728\n"
"v_mfma_f32_16x16x32_bf16 a[76:79], a[92:95], v[44:47], a[76:79]\n"
"buffer_load_dwordx4 v3, s[40:43], s58 offen offset:1024 lds\n"
"s_waitcnt lgkmcnt(4)\n"
"v_mfma_f32_16x16x32_bf16 a[0:3], a[96:99], v[48:51], a[0:3]\n"
"ds_read_b64_tr_b16 a[84:85], v9 offset:20640\n"
"ds_read_b64_tr_b16 a[86:87], v9 offset:25760\n"
"v_mfma_f32_16x16x32_bf16 a[4:7], a[96:99], v[52:55], a[4:7]\n"
"v_mfma_f32_16x16x32_bf16 a[8:11], a[100:103], v[48:51], a[8:11]\n"
"buffer_load_dwordx4 v4, s[40:43], s58 offen offset:2048 lds\n"
"ds_read_b64_tr_b16 a[88:89], v8 offset:20672\n"
"v_mfma_f32_16x16x32_bf16 a[12:15], a[100:103], v[52:55], a[12:15]\n"
"ds_read_b64_tr_b16 a[90:91], v8 offset:25792\n"
"v_mfma_f32_16x16x32_bf16 a[16:19], a[104:107], v[48:51], a[16:19]\n"
"ds_read_b64_tr_b16 a[92:93], v9 offset:20704\n"
"ds_read_b64_tr_b16 a[94:95], v9 offset:25824\n"
"v_mfma_f32_16x16x32_bf16 a[20:23], a[104:107], v[52:55], a[20:23]\n"
"s_waitcnt lgkmcnt(4)\n"
"v_mfma_f32_16x16x32_bf16 a[24:27], a[108:111], v[48:51], a[24:27]\n"
"buffer_load_dwordx4 v5, s[40:43], s58 offen offset:3072 lds\n"
"ds_read_b64_tr_b16 a[96:97], v8 offset:20736\n"
"v_mfma_f32_16x16x32_bf16 a[28:31], a[108:111], v[52:55], a[28:31]\n"
"ds_read_b64_tr_b16 a[98:99], v8 offset:25856\n"
"v_mfma_f32_16x16x32_bf16 a[32:35], a[80:83], v[48:51], a[32:35]\n"
"s_add_u32 m0, m0, 4096\n"
"ds_read_b64_tr_b16 a[100:101], v9 offset:20768\n"
"v_mfma_f32_16x16x32_bf16 a[36:39], a[80:83], v[52:55], a[36:39]\n"
"ds_read_b64_tr_b16 a[102:103], v9 offset:25888\n"
"v_mfma_f32_16x16x32_bf16 a[40:43], a[84:87], v[48:51], a[40:43]\n"
"buffer_load_dwordx4 v6, s[40:43], s58 offen lds\n"
"ds_read_b64_tr_b16 a[104:105], v8 offset:30720\n"
"v_mfma_f32_16x16x32_bf16 a[44:47], a[84:87], v[52:55], a[44:47]\n"
"ds_read_b64_tr_b16 a[106:107], v8 offset:35840\n"
"s_waitcnt lgkmcnt(4)\n"
"v_mfma_f32_16x16x32_bf16 a[48:51], a[88:91], v[48:51], a[48:51]\n"
"ds_read_b64_tr_b16 a[108:109], v9 offset:30752\n"
"ds_read_b64_tr_b16 a[110:111], v9 offset:35872\n"
"v_mfma_f32_16x16x32_bf16 a[52:55], a[88:91], v[52:55], a[52:55]\n"
"s_add_u32 s58, s58, 81920\n"
"v_mfma_f32_16x16x32_bf16 a[56:59], a[92:95], v[48:51], a[56:59]\n"
"buffer_load_dwordx4 v[104:107], v1, s[36:39], s57 offen\n"
"ds_read_b64_tr_b16 a[80:81], v8 offset:30784\n"
"v_mfma_f32_16x16x32_bf16 a[60:63], a[92:95], v[52:55], a[60:63]\n"
"ds_read_b64_tr_b16 a[82:83], v8 offset:35904\n"
"v_mfma_f32_16x16x32_bf16 a[64:67], a[96:99], v[48:51], a[64:67]\n"
"ds_read_b64_tr_b16 a[84:85], v9 offset:30816\n"
"ds_read_b64_tr_b16 a[86:87], v9 offset:35936\n"
"v_mfma_f32_16x16x32_bf16 a[68:71], a[96:99], v[52:55], a[68:71]\n"
"s_waitcnt lgkmcnt(8)\n"
"v_mfma_f32_16x16x32_bf16 a[72:75], a[100:103], v[48:51], a[72:75]\n"
"ds_read_b64_tr_b16 a[88:89], v8 offset:30848\n"
"ds_read_b64_tr_b16 a[90:91], v8 offset:35968\n"
"v_mfma_f32_16x16x32_bf16 a[76:79], a[100:103], v[52:55], a[76:79]\n"
"buffer_load_dwordx4 v[108:111], v1, s[36:39], s57 offen offset:1024\n"
"s_add_u32 s57, s57, 122880\n"
"s_waitcnt vmcnt(27) lgkmcnt(4)\n"
"v_mfma_f32_16x16x32_bf16 a[0:3], a[104:107], v[56:59], a[0:3]\n"
"ds_read_b64_tr_b16 a[92:93], v9 offset:30880\n"
"ds_read_b64_tr_b16 a[94:95], v9 offset:36000\n"
"v_mfma_f32_16x16x32_bf16 a[4:7], a[104:107], v[60:63], a[4:7]\n"
"v_mfma_f32_16x16x32_bf16 a[8:11], a[108:111], v[56:59], a[8:11]\n"
"buffer_load_dwordx4 v[112:115], v1, s[36:39], s57 offen\n"
"ds_read_b64_tr_b16 a[96:97], v8 offset:30912\n"
"v_mfma_f32_16x16x32_bf16 a[12:15], a[108:111], v[60:63], a[12:15]\n"
"ds_read_b64_tr_b16 a[98:99], v8 offset:36032\n"
"v_mfma_f32_16x16x32_bf16 a[16:19], a[80:83], v[56:59], a[16:19]\n"
"ds_read_b64_tr_b16 a[100:101], v9 offset:30944\n"
"ds_read_b64_tr_b16 a[102:103], v9 offset:36064\n"
"v_mfma_f32_16x16x32_bf16 a[20:23], a[80:83], v[60:63], a[20:23]\n"
"s_waitcnt lgkmcnt(4)\n"
"v_mfma_f32_16x16x32_bf16 a[24:27], a[84:87], v[56:59], a[24:27]\n"
"buffer_load_dwordx4 v[116:119], v1, s[36:39], s57 offen offset:1024\n"
"ds_read_b64_tr_b16 a[104:105], v8 offset:30976\n"
"v_mfma_f32_16x16x32_bf16 a[28:31], a[84:87], v[60:63], a[28:31]\n"
"ds_read_b64_tr_b16 a[106:107], v8 offset:36096\n"
"s_add_u32 s57, s57, 122880\n"
"v_mfma_f32_16x16x32_bf16 a[32:35], a[88:91], v[56:59], a[32:35]\n"
"ds_read_b64_tr_b16 a[108:109], v9 offset:31008\n"
"ds_read_b64_tr_b16 a[110:111], v9 offset:36128\n"
"v_mfma_f32_16x16x32_bf16 a[36:39], a[88:91], v[60:63], a[36:39]\n"
"s_waitcnt vmcnt(22) lgkmcnt(0)\n"
"s_barrier\n"
"v_mfma_f32_16x16x32_bf16 a[40:43], a[92:95], v[56:59], a[40:43]\n"
"ds_read_b64_tr_b16 a[80:81], v8 offset:40960\n"
"ds_read_b64_tr_b16 a[82:83], v8 offset:46080\n"
"v_mfma_f32_16x16x32_bf16 a[44:47], a[92:95], v[60:63], a[44:47]\n"
"v_mfma_f32_16x16x32_bf16 a[48:51], a[96:99], v[56:59], a[48:51]\n"
"s_add_u32 m0, s66, 102400\n"
"ds_read_b64_tr_b16 a[84:85], v9 offset:40992\n"
"v_mfma_f32_16x16x32_bf16 a[52:55], a[96:99], v[60:63], a[52:55]\n"
"ds_read_b64_tr_b16 a[86:87], v9 offset:46112\n"
"v_mfma_f32_16x16x32_bf16 a[56:59], a[100:103], v[56:59], a[56:59]\n"
"buffer_load_dwordx4 v2, s[40:43], s58 offen offset:0 lds\n"
"ds_read_b64_tr_b16 a[88:89], v8 offset:41024\n"
"v_mfma_f32_16x16x32_bf16 a[60:63], a[100:103], v[60:63], a[60:63]\n"
"ds_read_b64_tr_b16 a[90:91], v8 offset:46144\n"
"v_mfma_f32_16x16x32_bf16 a[64:67], a[104:107], v[56:59], a[64:67]\n"
"ds_read_b64_tr_b16 a[92:93], v9 offset:41056\n"
"ds_read_b64_tr_b16 a[94:95], v9 offset:46176\n"
"v_mfma_f32_16x16x32_bf16 a[68:71], a[104:107], v[60:63], a[68:71]\n"
"v_mfma_f32_16x16x32_bf16 a[72:75], a[108:111], v[56:59], a[72:75]\n"
"ds_read_b64_tr_b16 a[96:97], v8 offset:41088\n"
"ds_read_b64_tr_b16 a[98:99], v8 offset:46208\n"
"v_mfma_f32_16x16x32_bf16 a[76:79], a[108:111], v[60:63], a[76:79]\n"
"buffer_load_dwordx4 v3, s[40:43], s58 offen offset:1024 lds\n"
"s_waitcnt lgkmcnt(4)\n"
"v_mfma_f32_16x16x32_bf16 a[0:3], a[80:83], v[64:67], a[0:3]\n"
"ds_read_b64_tr_b16 a[100:101], v9 offset:41120\n"
"ds_read_b64_tr_b16 a[102:103], v9 offset:46240\n"
"v_mfma_f32_16x16x32_bf16 a[4:7], a[80:83], v[68:71], a[4:7]\n"
"v_mfma_f32_16x16x32_bf16 a[8:11], a[84:87], v[64:67], a[8:11]\n"
"buffer_load_dwordx4 v4, s[40:43], s58 offen offset:2048 lds\n"
"ds_read_b64_tr_b16 a[104:105], v8 offset:41152\n"
"v_mfma_f32_16x16x32_bf16 a[12:15], a[84:87], v[68:71], a[12:15]\n"
"ds_read_b64_tr_b16 a[106:107], v8 offset:46272\n"
"v_mfma_f32_16x16x32_bf16 a[16:19], a[88:91], v[64:67], a[16:19]\n"
"ds_read_b64_tr_b16 a[108:109], v9 offset:41184\n"
"ds_read_b64_tr_b16 a[110:111], v9 offset:46304\n"
"v_mfma_f32_16x16x32_bf16 a[20:23], a[88:91], v[68:71], a[20:23]\n"
"s_waitcnt lgkmcnt(4)\n"
"v_mfma_f32_16x16x32_bf16 a[24:27], a[92:95], v[64:67], a[24:27]\n"
"buffer_load_dwordx4 v5, s[40:43], s58 offen offset:3072 lds\n"
"ds_read_b64_tr_b16 a[80:81], v8 offset:41216\n"
"v_mfma_f32_16x16x32_bf16 a[28:31], a[92:95], v[68:71], a[28:31]\n"
"ds_read_b64_tr_b16 a[82:83], v8 offset:46336\n"
"v_mfma_f32_16x16x32_bf16 a[32:35], a[96:99], v[64:67], a[32:35]\n"
"s_add_u32 m0, m0, 4096\n"
"ds_read_b64_tr_b16 a[84:85], v9 offset:41248\n"
"v_mfma_f32_16x16x32_bf16 a[36:39], a[96:99], v[68:71], a[36:39]\n"
"ds_read_b64_tr_b16 a[86:87], v9 offset:46368\n"
"v_mfma_f32_16x16x32_bf16 a[40:43], a[100:103], v[64:67], a[40:43]\n"
"buffer_load_dwordx4 v6, s[40:43], s58 offen lds\n"
"ds_read_b64_tr_b16 a[88:89], v8 offset:51200\n"
"v_mfma_f32_16x16x32_bf16 a[44:47], a[100:103], v[68:71], a[44:47]\n"
"ds_read_b64_tr_b16 a[90:91], v8 offset:56320\n"
"s_waitcnt lgkmcnt(4)\n"
"v_mfma_f32_16x16x32_bf16 a[48:51], a[104:107], v[64:67], a[48:51]\n"
"ds_read_b64_tr_b16 a[92:93], v9 offset:51232\n"
"ds_read_b64_tr_b16 a[94:95], v9 offset:56352\n"
"v_mfma_f32_16x16x32_bf16 a[52:55], a[104:107], v[68:71], a[52:55]\n"
"s_add_u32 s58, s58, 81920\n"
"v_mfma_f32_16x16x32_bf16 a[56:59], a[108:111], v[64:67], a[56:59]\n"
"buffer_load_dwordx4 v[120:123], v1, s[36:39], s57 offen\n"
"ds_read_b64_tr_b16 a[96:97], v8 offset:51264\n"
"v_mfma_f32_16x16x32_bf16 a[60:63], a[108:111], v[68:71], a[60:63]\n"
"ds_read_b64_tr_b16 a[98:99], v8 offset:56384\n"
"v_mfma_f32_16x16x32_bf16 a[64:67], a[80:83], v[64:67], a[64:67]\n"
"ds_read_b64_tr_b16 a[100:101], v9 offset:51296\n"
"ds_read_b64_tr_b16 a[102:103], v9 offset:56416\n"
"v_mfma_f32_16x16x32_bf16 a[68:71], a[80:83], v[68:71], a[68:71]\n"
"s_waitcnt lgkmcnt(8)\n"
"v_mfma_f32_16x16x32_bf16 a[72:75], a[84:87], v[64:67], a[72:75]\n"
"ds_read_b64_tr_b16 a[104:105], v8 offset:51328\n"
"ds_read_b64_tr_b16 a[106:107], v8 offset:56448\n"
"v_mfma_f32_16x16x32_bf16 a[76:79], a[84:87], v[68:71], a[76:79]\n"
"buffer_load_dwordx4 v[124:127], v1, s[36:39], s57 offen offset:1024\n"
"s_add_u32 s57, s57, 122880\n"
"s_waitcnt vmcnt(27) lgkmcnt(4)\n"
"v_mfma_f32_16x16x32_bf16 a[0:3], a[88:91], v[72:75], a[0:3]\n"
"ds_read_b64_tr_b16 a[108:109], v9 offset:51360\n"
"ds_read_b64_tr_b16 a[110:111], v9 offset:56480\n"
"v_mfma_f32_16x16x32_bf16 a[4:7], a[88:91], v[76:79], a[4:7]\n"
"v_mfma_f32_16x16x32_bf16 a[8:11], a[92:95], v[72:75], a[8:11]\n"
"buffer_load_dwordx4 v[32:35], v1, s[36:39], s57 offen\n"
"ds_read_b64_tr_b16 a[80:81], v8 offset:51392\n"
"v_mfma_f32_16x16x32_bf16 a[12:15], a[92:95], v[76:79], a[12:15]\n"
"ds_read_b64_tr_b16 a[82:83], v8 offset:56512\n"
"v_mfma_f32_16x16x32_bf16 a[16:19], a[96:99], v[72:75], a[16:19]\n"
"ds_read_b64_tr_b16 a[84:85], v9 offset:51424\n"
"ds_read_b64_tr_b16 a[86:87], v9 offset:56544\n"
"v_mfma_f32_16x16x32_bf16 a[20:23], a[96:99], v[76:79], a[20:23]\n"
"s_waitcnt lgkmcnt(4)\n"
"v_mfma_f32_16x16x32_bf16 a[24:27], a[100:103], v[72:75], a[24:27]\n"
"buffer_load_dwordx4 v[36:39], v1, s[36:39], s57 offen offset:1024\n"
"ds_read_b64_tr_b16 a[88:89], v8 offset:51456\n"
"v_mfma_f32_16x16x32_bf16 a[28:31], a[100:103], v[76:79], a[28:31]\n"
"ds_read_b64_tr_b16 a[90:91], v8 offset:56576\n"
"s_add_u32 s57, s57, 122880\n"
"v_mfma_f32_16x16x32_bf16 a[32:35], a[104:107], v[72:75], a[32:35]\n"
"ds_read_b64_tr_b16 a[92:93], v9 offset:51488\n"
"ds_read_b64_tr_b16 a[94:95], v9 offset:56608\n"
"v_mfma_f32_16x16x32_bf16 a[36:39], a[104:107], v[76:79], a[36:39]\n"
"s_waitcnt vmcnt(22) lgkmcnt(0)\n"
"s_barrier\n"
"v_mfma_f32_16x16x32_bf16 a[40:43], a[108:111], v[72:75], a[40:43]\n"
"ds_read_b64_tr_b16 a[96:97], v10 offset:0\n"
"ds_read_b64_tr_b16 a[98:99], v10 offset:5120\n"
"v_mfma_f32_16x16x32_bf16 a[44:47], a[108:111], v[76:79], a[44:47]\n"
"v_mfma_f32_16x16x32_bf16 a[48:51], a[80:83], v[72:75], a[48:51]\n"
"s_add_u32 m0, s66, 0\n"
"ds_read_b64_tr_b16 a[100:101], v11 offset:32\n"
"v_mfma_f32_16x16x32_bf16 a[52:55], a[80:83], v[76:79], a[52:55]\n"
"ds_read_b64_tr_b16 a[102:103], v11 offset:5152\n"
"v_mfma_f32_16x16x32_bf16 a[56:59], a[84:87], v[72:75], a[56:59]\n"
"buffer_load_dwordx4 v2, s[40:43], s58 offen offset:0 lds\n"
"ds_read_b64_tr_b16 a[104:105], v10 offset:64\n"
"v_mfma_f32_16x16x32_bf16 a[60:63], a[84:87], v[76:79], a[60:63]\n"
"ds_read_b64_tr_b16 a[106:107], v10 offset:5184\n"
"v_mfma_f32_16x16x32_bf16 a[64:67], a[88:91], v[72:75], a[64:67]\n"
"ds_read_b64_tr_b16 a[108:109], v11 offset:96\n"
"ds_read_b64_tr_b16 a[110:111], v11 offset:5216\n"
"v_mfma_f32_16x16x32_bf16 a[68:71], a[88:91], v[76:79], a[68:71]\n"
"v_mfma_f32_16x16x32_bf16 a[72:75], a[92:95], v[72:75], a[72:75]\n"
"ds_read_b64_tr_b16 a[80:81], v10 offset:128\n"
"ds_read_b64_tr_b16 a[82:83], v10 offset:5248\n"
"v_mfma_f32_16x16x32_bf16 a[76:79], a[92:95], v[76:79], a[76:79]\n"
"buffer_load_dwordx4 v3, s[40:43], s58 offen offset:1024 lds\n"
"s_waitcnt lgkmcnt(4)\n"
"v_mfma_f32_16x16x32_bf16 a[0:3], a[96:99], v[80:83], a[0:3]\n"
"ds_read_b64_tr_b16 a[84:85], v11 offset:160\n"
"ds_read_b64_tr_b16 a[86:87], v11 offset:5280\n"
"v_mfma_f32_16x16x32_bf16 a[4:7], a[96:99], v[84:87], a[4:7]\n"
"v_mfma_f32_16x16x32_bf16 a[8:11], a[100:103], v[80:83], a[8:11]\n"
"buffer_load_dwordx4 v4, s[40:43], s58 offen offset:2048 lds\n"
"ds_read_b64_tr_b16 a[88:89], v10 offset:192\n"
"v_mfma_f32_16x16x32_bf16 a[12:15], a[100:103], v[84:87], a[12:15]\n"
"ds_read_b64_tr_b16 a[90:91], v10 offset:5312\n"
"v_mfma_f32_16x16x32_bf16 a[16:19], a[104:107], v[80:83], a[16:19]\n"
"ds_read_b64_tr_b16 a[92:93], v11 offset:224\n"
"ds_read_b64_tr_b16 a[94:95], v11 offset:5344\n"
"v_mfma_f32_16x16x32_bf16 a[20:23], a[104:107], v[84:87], a[20:23]\n"
"s_waitcnt lgkmcnt(4)\n"
"v_mfma_f32_16x16x32_bf16 a[24:27], a[108:111], v[80:83], a[24:27]\n"
"buffer_load_dwordx4 v5, s[40:43], s58 offen offset:3072 lds\n"
"ds_read_b64_tr_b16 a[96:97], v10 offset:256\n"
"v_mfma_f32_16x16x32_bf16 a[28:31], a[108:111], v[84:87], a[28:31]\n"
"ds_read_b64_tr_b16 a[98:99], v10 offset:5376\n"
"v_mfma_f32_16x16x32_bf16 a[32:35], a[80:83], v[80:83], a[32:35]\n"
"s_add_u32 m0, m0, 4096\n"
"ds_read_b64_tr_b16 a[100:101], v11 offset:288\n"
"v_mfma_f32_16x16x32_bf16 a[36:39], a[80:83], v[84:87], a[36:39]\n"
"ds_read_b64_tr_b16 a[102:103], v11 offset:5408\n"
"v_mfma_f32_16x16x32_bf16 a[40:43], a[84:87], v[80:83], a[40:43]\n"
"buffer_load_dwordx4 v6, s[40:43], s58 offen lds\n"
"ds_read_b64_tr_b16 a[104:105], v10 offset:10240\n"
"v_mfma_f32_16x16x32_bf16 a[44:47], a[84:87], v[84:87], a[44:47]\n"
"ds_read_b64_tr_b16 a[106:107], v10 offset:15360\n"
"s_waitcnt lgkmcnt(4)\n"
"v_mfma_f32_16x16x32_bf16 a[48:51], a[88:91], v[80:83], a[48:51]\n"
"ds_read_b64_tr_b16 a[108:109], v11 offset:10272\n"
"ds_read_b64_tr_b16 a[110:111], v11 offset:15392\n"
"v_mfma_f32_16x16x32_bf16 a[52:55], a[88:91], v[84:87], a[52:55]\n"
"s_add_u32 s58, s58, 81920\n"
"v_mfma_f32_16x16x32_bf16 a[56:59], a[92:95], v[80:83], a[56:59]\n"
"buffer_load_dwordx4 v[40:43], v1, s[36:39], s57 offen\n"
"ds_read_b64_tr_b16 a[80:81], v10 offset:10304\n"
"v_mfma_f32_16x16x32_bf16 a[60:63], a[92:95], v[84:87], a[60:63]\n"
"ds_read_b64_tr_b16 a[82:83], v10 offset:15424\n"
"v_mfma_f32_16x16x32_bf16 a[64:67], a[96:99], v[80:83], a[64:67]\n"
"ds_read_b64_tr_b16 a[84:85], v11 offset:10336\n"
"ds_read_b64_tr_b16 a[86:87], v11 offset:15456\n"
"v_mfma_f32_16x16x32_bf16 a[68:71], a[96:99], v[84:87], a[68:71]\n"
"s_waitcnt lgkmcnt(8)\n"
"v_mfma_f32_16x16x32_bf16 a[72:75], a[100:103], v[80:83], a[72:75]\n"
"ds_read_b64_tr_b16 a[88:89], v10 offset:10368\n"
"ds_read_b64_tr_b16 a[90:91], v10 offset:15488\n"
"v_mfma_f32_16x16x32_bf16 a[76:79], a[100:103], v[84:87], a[76:79]\n"
"buffer_load_dwordx4 v[44:47], v1, s[36:39], s57 offen offset:1024\n"
"s_add_u32 s57, s57, 122880\n"
"s_waitcnt vmcnt(27) lgkmcnt(4)\n"
"v_mfma_f32_16x16x32_bf16 a[0:3], a[104:107], v[88:91], a[0:3]\n"
"ds_read_b64_tr_b16 a[92:93], v11 offset:10400\n"
"ds_read_b64_tr_b16 a[94:95], v11 offset:15520\n"
"v_mfma_f32_16x16x32_bf16 a[4:7], a[104:107], v[92:95], a[4:7]\n"
"v_mfma_f32_16x16x32_bf16 a[8:11], a[108:111], v[88:91], a[8:11]\n"
"buffer_load_dwordx4 v[48:51], v1, s[36:39], s57 offen\n"
"ds_read_b64_tr_b16 a[96:97], v10 offset:10432\n"
"v_mfma_f32_16x16x32_bf16 a[12:15], a[108:111], v[92:95], a[12:15]\n"
"ds_read_b64_tr_b16 a[98:99], v10 offset:15552\n"
"v_mfma_f32_16x16x32_bf16 a[16:19], a[80:83], v[88:91], a[16:19]\n"
"ds_read_b64_tr_b16 a[100:101], v11 offset:10464\n"
"ds_read_b64_tr_b16 a[102:103], v11 offset:15584\n"
"v_mfma_f32_16x16x32_bf16 a[20:23], a[80:83], v[92:95], a[20:23]\n"
"s_waitcnt lgkmcnt(4)\n"
"v_mfma_f32_16x16x32_bf16 a[24:27], a[84:87], v[88:91], a[24:27]\n"
"buffer_load_dwordx4 v[52:55], v1, s[36:39], s57 offen offset:1024\n"
"ds_read_b64_tr_b16 a[104:105], v10 offset:10496\n"
"v_mfma_f32_16x16x32_bf16 a[28:31], a[84:87], v[92:95], a[28:31]\n"
"ds_read_b64_tr_b16 a[106:107], v10 offset:15616\n"
"s_add_u32 s57, s57, 122880\n"
"v_mfma_f32_16x16x32_bf16 a[32:35], a[88:91], v[88:91], a[32:35]\n"
"ds_read_b64_tr_b16 a[108:109], v11 offset:10528\n"
"ds_read_b64_tr_b16 a[110:111], v11 offset:15648\n"
"v_mfma_f32_16x16x32_bf16 a[36:39], a[88:91], v[92:95], a[36:39]\n"
"s_waitcnt vmcnt(22) lgkmcnt(0)\n"
"s_barrier\n"
"v_mfma_f32_16x16x32_bf16 a[40:43], a[92:95], v[88:91], a[40:43]\n"
"ds_read_b64_tr_b16 a[80:81], v10 offset:20480\n"
"ds_read_b64_tr_b16 a[82:83], v10 offset:25600\n"
"v_mfma_f32_16x16x32_bf16 a[44:47], a[92:95], v[92:95], a[44:47]\n"
"v_mfma_f32_16x16x32_bf16 a[48:51], a[96:99], v[88:91], a[48:51]\n"
"s_add_u32 m0, s66, 20480\n"
"ds_read_b64_tr_b16 a[84:85], v11 offset:20512\n"
"v_mfma_f32_16x16x32_bf16 a[52:55], a[96:99], v[92:95], a[52:55]\n"
"ds_read_b64_tr_b16 a[86:87], v11 offset:25632\n"
"v_mfma_f32_16x16x32_bf16 a[56:59], a[100:103], v[88:91], a[56:59]\n"
"buffer_load_dwordx4 v2, s[40:43], s58 offen offset:0 lds\n"
"ds_read_b64_tr_b16 a[88:89], v10 offset:20544\n"
"v_mfma_f32_16x16x32_bf16 a[60:63], a[100:103], v[92:95], a[60:63]\n"
"ds_read_b64_tr_b16 a[90:91], v10 offset:25664\n"
"v_mfma_f32_16x16x32_bf16 a[64:67], a[104:107], v[88:91], a[64:67]\n"
"ds_read_b64_tr_b16 a[92:93], v11 offset:20576\n"
"ds_read_b64_tr_b16 a[94:95], v11 offset:25696\n"
"v_mfma_f32_16x16x32_bf16 a[68:71], a[104:107], v[92:95], a[68:71]\n"
"v_mfma_f32_16x16x32_bf16 a[72:75], a[108:111], v[88:91], a[72:75]\n"
"ds_read_b64_tr_b16 a[96:97], v10 offset:20608\n"
"ds_read_b64_tr_b16 a[98:99], v10 offset:25728\n"
"v_mfma_f32_16x16x32_bf16 a[76:79], a[108:111], v[92:95], a[76:79]\n"
"buffer_load_dwordx4 v3, s[40:43], s58 offen offset:1024 lds\n"
"s_waitcnt lgkmcnt(4)\n"
"v_mfma_f32_16x16x32_bf16 a[0:3], a[80:83], v[96:99], a[0:3]\n"
"ds_read_b64_tr_b16 a[100:101], v11 offset:20640\n"
"ds_read_b64_tr_b16 a[102:103], v11 offset:25760\n"
"v_mfma_f32_16x16x32_bf16 a[4:7], a[80:83], v[100:103], a[4:7]\n"
"v_mfma_f32_16x16x32_bf16 a[8:11], a[84:87], v[96:99], a[8:11]\n"
"buffer_load_dwordx4 v4, s[40:43], s58 offen offset:2048 lds\n"
"ds_read_b64_tr_b16 a[104:105], v10 offset:20672\n"
"v_mfma_f32_16x16x32_bf16 a[12:15], a[84:87], v[100:103], a[12:15]\n"
"ds_read_b64_tr_b16 a[106:107], v10 offset:25792\n"
"v_mfma_f32_16x16x32_bf16 a[16:19], a[88:91], v[96:99], a[16:19]\n"
"ds_read_b64_tr_b16 a[108:109], v11 offset:20704\n"
"ds_read_b64_tr_b16 a[110:111], v11 offset:25824\n"
"v_mfma_f32_16x16x32_bf16 a[20:23], a[88:91], v[100:103], a[20:23]\n"
"s_waitcnt lgkmcnt(4)\n"
"v_mfma_f32_16x16x32_bf16 a[24:27], a[92:95], v[96:99], a[24:27]\n"
"buffer_load_dwordx4 v5, s[40:43], s58 offen offset:3072 lds\n"
"ds_read_b64_tr_b16 a[80:81], v10 offset:20736\n"
"v_mfma_f32_16x16x32_bf16 a[28:31], a[92:95], v[100:103], a[28:31]\n"
"ds_read_b64_tr_b16 a[82:83], v10 offset:25856\n"
"v_mfma_f32_16x16x32_bf16 a[32:35], a[96:99], v[96:99], a[32:35]\n"
"s_add_u32 m0, m0, 4096\n"
"ds_read_b64_tr_b16 a[84:85], v11 offset:20768\n"
"v_mfma_f32_16x16x32_bf16 a[36:39], a[96:99], v[100:103], a[36:39]\n"
"ds_read_b64_tr_b16 a[86:87], v11 offset:25888\n"
"v_mfma_f32_16x16x32_bf16 a[40:43], a[100:103], v[96:99], a[40:43]\n"
"buffer_load_dwordx4 v6, s[40:43], s58 offen lds\n"
"ds_read_b64_tr_b16 a[88:89], v10 offset:30720\n"
"v_mfma_f32_16x16x32_bf16 a[44:47], a[100:103], v[100:103], a[44:47]\n"
"ds_read_b64_tr_b16 a[90:91], v10 offset:35840\n"
"s_waitcnt lgkmcnt(4)\n"
"v_mfma_f32_16x16x32_bf16 a[48:51], a[104:107], v[96:99], a[48:51]\n"
"ds_read_b64_tr_b16 a[92:93], v11 offset:30752\n"
"ds_read_b64_tr_b16 a[94:95], v11 offset:35872\n"
"v_mfma_f32_16x16x32_bf16 a[52:55], a[104:107], v[100:103], a[52:55]\n"
"s_add_u32 s58, s58, 81920\n"
"v_mfma_f32_16x16x32_bf16 a[56:59], a[108:111], v[96:99], a[56:59]\n"
"buffer_load_dwordx4 v[56:59], v1, s[36:39], s57 offen\n"
"ds_read_b64_tr_b16 a[96:97], v10 offset:30784\n"
"v_mfma_f32_16x16x32_bf16 a[60:63], a[108:111], v[100:103], a[60:63]\n"
"ds_read_b64_tr_b16 a[98:99], v10 offset:35904\n"
"v_mfma_f32_16x16x32_bf16 a[64:67], a[80:83], v[96:99], a[64:67]\n"
"ds_read_b64_tr_b16 a[100:101], v11 offset:30816\n"
"ds_read_b64_tr_b16 a[102:103], v11 offset:35936\n"
"v_mfma_f32_16x16x32_bf16 a[68:71], a[80:83], v[100:103], a[68:71]\n"
"s_waitcnt lgkmcnt(8)\n"
"v_mfma_f32_16x16x32_bf16 a[72:75], a[84:87], v[96:99], a[72:75]\n"
"ds_read_b64_tr_b16 a[104:105], v10 offset:30848\n"
"ds_read_b64_tr_b16 a[106:107], v10 offset:35968\n"
"v_mfma_f32_16x16x32_bf16 a[76:79], a[84:87], v[100:103], a[76:79]\n"
"buffer_load_dwordx4 v[60:63], v1, s[36:39], s57 offen offset:1024\n"
"s_add_u32 s57, s57, 122880\n"
"s_waitcnt vmcnt(27) lgkmcnt(4)\n"
"v_mfma_f32_16x16x32_bf16 a[0:3], a[88:91], v[104:107], a[0:3]\n"
"ds_read_b64_tr_b16 a[108:109], v11 offset:30880\n"
"ds_read_b64_tr_b16 a[110:111], v11 offset:36000\n"
"v_mfma_f32_16x16x32_bf16 a[4:7], a[88:91], v[108:111], a[4:7]\n"
"v_mfma_f32_16x16x32_bf16 a[8:11], a[92:95], v[104:107], a[8:11]\n"
"buffer_load_dwordx4 v[64:67], v1, s[36:39], s57 offen\n"
"ds_read_b64_tr_b16 a[80:81], v10 offset:30912\n"
"v_mfma_f32_16x16x32_bf16 a[12:15], a[92:95], v[108:111], a[12:15]\n"
"ds_read_b64_tr_b16 a[82:83], v10 offset:36032\n"
"v_mfma_f32_16x16x32_bf16 a[16:19], a[96:99], v[104:107], a[16:19]\n"
"ds_read_b64_tr_b16 a[84:85], v11 offset:30944\n"
"ds_read_b64_tr_b16 a[86:87], v11 offset:36064\n"
"v_mfma_f32_16x16x32_bf16 a[20:23], a[96:99], v[108:111], a[20:23]\n"
"s_waitcnt lgkmcnt(4)\n"
"v_mfma_f32_16x16x32_bf16 a[24:27], a[100:103], v[104:107], a[24:27]\n"
"buffer_load_dwordx4 v[68:71], v1, s[36:39], s57 offen offset:1024\n"
"ds_read_b64_tr_b16 a[88:89], v10 offset:30976\n"
"v_mfma_f32_16x16x32_bf16 a[28:31], a[100:103], v[108:111], a[28:31]\n"
"ds_read_b64_tr_b16 a[90:91], v10 offset:36096\n"
"s_add_u32 s57, s57, 122880\n"
"v_mfma_f32_16x16x32_bf16 a[32:35], a[104:107], v[104:107], a[32:35]\n"
"ds_read_b64_tr_b16 a[92:93], v11 offset:31008\n"
"ds_read_b64_tr_b16 a[94:95], v11 offset:36128\n"
"v_mfma_f32_16x16x32_bf16 a[36:39], a[104:107], v[108:111], a[36:39]\n"
"s_waitcnt vmcnt(22) lgkmcnt(0)\n"
"s_barrier\n"
"v_mfma_f32_16x16x32_bf16 a[40:43], a[108:111], v[104:107], a[40:43]\n"
"ds_read_b64_tr_b16 a[96:97], v10 offset:40960\n"
"ds_read_b64_tr_b16 a[98:99], v10 offset:46080\n"
"v_mfma_f32_16x16x32_bf16 a[44:47], a[108:111], v[108:111], a[44:47]\n"
"v_mfma_f32_16x16x32_bf16 a[48:51], a[80:83], v[104:107], a[48:51]\n"
"s_add_u32 m0, s66, 40960\n"
"ds_read_b64_tr_b16 a[100:101], v11 offset:40992\n"
"v_mfma_f32_16x16x32_bf16 a[52:55], a[80:83], v[108:111], a[52:55]\n"
"ds_read_b64_tr_b16 a[102:103], v11 offset:46112\n"
"v_mfma_f32_16x16x32_bf16 a[56:59], a[84:87], v[104:107], a[56:59]\n"
"buffer_load_dwordx4 v2, s[40:43], s58 offen offset:0 lds\n"
"ds_read_b64_tr_b16 a[104:105], v10 offset:41024\n"
"v_mfma_f32_16x16x32_bf16 a[60:63], a[84:87], v[108:111], a[60:63]\n"
"ds_read_b64_tr_b16 a[106:107], v10 offset:46144\n"
"v_mfma_f32_16x16x32_bf16 a[64:67], a[88:91], v[104:107], a[64:67]\n"
"ds_read_b64_tr_b16 a[108:109], v11 offset:41056\n"
"ds_read_b64_tr_b16 a[110:111], v11 offset:46176\n"
"v_mfma_f32_16x16x32_bf16 a[68:71], a[88:91], v[108:111], a[68:71]\n"
"v_mfma_f32_16x16x32_bf16 a[72:75], a[92:95], v[104:107], a[72:75]\n"
"ds_read_b64_tr_b16 a[80:81], v10 offset:41088\n"
"ds_read_b64_tr_b16 a[82:83], v10 offset:46208\n"
"v_mfma_f32_16x16x32_bf16 a[76:79], a[92:95], v[108:111], a[76:79]\n"
"buffer_load_dwordx4 v3, s[40:43], s58 offen offset:1024 lds\n"
"s_waitcnt lgkmcnt(4)\n"
"v_mfma_f32_16x16x32_bf16 a[0:3], a[96:99], v[112:115], a[0:3]\n"
"ds_read_b64_tr_b16 a[84:85], v11 offset:41120\n"
"ds_read_b64_tr_b16 a[86:87], v11 offset:46240\n"
"v_mfma_f32_16x16x32_bf16 a[4:7], a[96:99], v[116:119], a[4:7]\n"
"v_mfma_f32_16x16x32_bf16 a[8:11], a[100:103], v[112:115], a[8:11]\n"
"buffer_load_dwordx4 v4, s[40:43], s58 offen offset:2048 lds\n"
"ds_read_b64_tr_b16 a[88:89], v10 offset:41152\n"
"v_mfma_f32_16x16x32_bf16 a[12:15], a[100:103], v[116:119], a[12:15]\n"
"ds_read_b64_tr_b16 a[90:91], v10 offset:46272\n"
"v_mfma_f32_16x16x32_bf16 a[16:19], a[104:107], v[112:115], a[16:19]\n"
"ds_read_b64_tr_b16 a[92:93], v11 offset:41184\n"
"ds_read_b64_tr_b16 a[94:95], v11 offset:46304\n"
"v_mfma_f32_16x16x32_bf16 a[20:23], a[104:107], v[116:119], a[20:23]\n"
"s_waitcnt lgkmcnt(4)\n"
"v_mfma_f32_16x16x32_bf16 a[24:27], a[108:111], v[112:115], a[24:27]\n"
"buffer_load_dwordx4 v5, s[40:43], s58 offen offset:3072 lds\n"
"ds_read_b64_tr_b16 a[96:97], v10 offset:41216\n"
"v_mfma_f32_16x16x32_bf16 a[28:31], a[108:111], v[116:119], a[28:31]\n"
"ds_read_b64_tr_b16 a[98:99], v10 offset:46336\n"
"v_mfma_f32_16x16x32_bf16 a[32:35], a[80:83], v[112:115], a[32:35]\n"
"s_add_u32 m0, m0, 4096\n"
"ds_read_b64_tr_b16 a[100:101], v11 offset:41248\n"
"v_mfma_f32_16x16x32_bf16 a[36:39], a[80:83], v[116:119], a[36:39]\n"
"ds_read_b64_tr_b16 a[102:103], v11 offset:46368\n"
"v_mfma_f32_16x16x32_bf16 a[40:43], a[84:87], v[112:115], a[40:43]\n"
"buffer_load_dwordx4 v6, s[40:43], s58 offen lds\n"
"ds_read_b64_tr_b16 a[104:105], v10 offset:51200\n"
"v_mfma_f32_16x16x32_bf16 a[44:47], a[84:87], v[116:119], a[44:47]\n"
"ds_read_b64_tr_b16 a[106:107], v10 offset:56320\n"
"s_waitcnt lgkmcnt(4)\n"
"v_mfma_f32_16x16x32_bf16 a[48:51], a[88:91], v[112:115], a[48:51]\n"
"ds_read_b64_tr_b16 a[108:109], v11 offset:51232\n"
"ds_read_b64_tr_b16 a[110:111], v11 offset:56352\n"
"v_mfma_f32_16x16x32_bf16 a[52:55], a[88:91], v[116:119], a[52:55]\n"
"s_add_u32 s58, s58, 81920\n"
"v_mfma_f32_16x16x32_bf16 a[56:59], a[92:95], v[112:115], a[56:59]\n"
"buffer_load_dwordx4 v[72:75], v1, s[36:39], s57 offen\n"
"ds_read_b64_tr_b16 a[80:81], v10 offset:51264\n"
"v_mfma_f32_16x16x32_bf16 a[60:63], a[92:95], v[116:119], a[60:63]\n"
"ds_read_b64_tr_b16 a[82:83], v10 offset:56384\n"
"v_mfma_f32_16x16x32_bf16 a[64:67], a[96:99], v[112:115], a[64:67]\n"
"ds_read_b64_tr_b16 a[84:85], v11 offset:51296\n"
"ds_read_b64_tr_b16 a[86:87], v11 offset:56416\n"
"v_mfma_f32_16x16x32_bf16 a[68:71], a[96:99], v[116:119], a[68:71]\n"
"s_waitcnt lgkmcnt(8)\n"
"v_mfma_f32_16x16x32_bf16 a[72:75], a[100:103], v[112:115], a[72:75]\n"
"ds_read_b64_tr_b16 a[88:89], v10 offset:51328\n"
"ds_read_b64_tr_b16 a[90:91], v10 offset:56448\n"
"v_mfma_f32_16x16x32_bf16 a[76:79], a[100:103], v[116:119], a[76:79]\n"
"buffer_load_dwordx4 v[76:79], v1, s[36:39], s57 offen offset:1024\n"
"s_add_u32 s57, s57, 122880\n"
"s_waitcnt vmcnt(27) lgkmcnt(4)\n"
"v_mfma_f32_16x16x32_bf16 a[0:3], a[104:107], v[120:123], a[0:3]\n"
"ds_read_b64_tr_b16 a[92:93], v11 offset:51360\n"
"ds_read_b64_tr_b16 a[94:95], v11 offset:56480\n"
"v_mfma_f32_16x16x32_bf16 a[4:7], a[104:107], v[124:127], a[4:7]\n"
"v_mfma_f32_16x16x32_bf16 a[8:11], a[108:111], v[120:123], a[8:11]\n"
"buffer_load_dwordx4 v[80:83], v1, s[36:39], s57 offen\n"
"ds_read_b64_tr_b16 a[96:97], v10 offset:51392\n"
"v_mfma_f32_16x16x32_bf16 a[12:15], a[108:111], v[124:127], a[12:15]\n"
"ds_read_b64_tr_b16 a[98:99], v10 offset:56512\n"
"v_mfma_f32_16x16x32_bf16 a[16:19], a[80:83], v[120:123], a[16:19]\n"
"ds_read_b64_tr_b16 a[100:101], v11 offset:51424\n"
"ds_read_b64_tr_b16 a[102:103], v11 offset:56544\n"
"v_mfma_f32_16x16x32_bf16 a[20:23], a[80:83], v[124:127], a[20:23]\n"
"s_waitcnt lgkmcnt(4)\n"
"v_mfma_f32_16x16x32_bf16 a[24:27], a[84:87], v[120:123], a[24:27]\n"
"buffer_load_dwordx4 v[84:87], v1, s[36:39], s57 offen offset:1024\n"
"ds_read_b64_tr_b16 a[104:105], v10 offset:51456\n"
"v_mfma_f32_16x16x32_bf16 a[28:31], a[84:87], v[124:127], a[28:31]\n"
"ds_read_b64_tr_b16 a[106:107], v10 offset:56576\n"
"s_add_u32 s57, s57, 122880\n"
"v_mfma_f32_16x16x32_bf16 a[32:35], a[88:91], v[120:123], a[32:35]\n"
"ds_read_b64_tr_b16 a[108:109], v11 offset:51488\n"
"ds_read_b64_tr_b16 a[110:111], v11 offset:56608\n"
"v_mfma_f32_16x16x32_bf16 a[36:39], a[88:91], v[124:127], a[36:39]\n"
"s_waitcnt vmcnt(22) lgkmcnt(0)\n"
"s_barrier\n"
"v_mfma_f32_16x16x32_bf16 a[40:43], a[92:95], v[120:123], a[40:43]\n"
"ds_read_b64_tr_b16 a[80:81], v8 offset:0\n"
"ds_read_b64_tr_b16 a[82:83], v8 offset:5120\n"
"v_mfma_f32_16x16x32_bf16 a[44:47], a[92:95], v[124:127], a[44:47]\n"
"v_mfma_f32_16x16x32_bf16 a[48:51], a[96:99], v[120:123], a[48:51]\n"
"s_add_u32 m0, s66, 61440\n"
"ds_read_b64_tr_b16 a[84:85], v9 offset:32\n"
"v_mfma_f32_16x16x32_bf16 a[52:55], a[96:99], v[124:127], a[52:55]\n"
"ds_read_b64_tr_b16 a[86:87], v9 offset:5152\n"
"v_mfma_f32_16x16x32_bf16 a[56:59], a[100:103], v[120:123], a[56:59]\n"
"buffer_load_dwordx4 v2, s[40:43], s58 offen offset:0 lds\n"
"ds_read_b64_tr_b16 a[88:89], v8 offset:64\n"
"v_mfma_f32_16x16x32_bf16 a[60:63], a[100:103], v[124:127], a[60:63]\n"
"ds_read_b64_tr_b16 a[90:91], v8 offset:5184\n"
"v_mfma_f32_16x16x32_bf16 a[64:67], a[104:107], v[120:123], a[64:67]\n"
"ds_read_b64_tr_b16 a[92:93], v9 offset:96\n"
"ds_read_b64_tr_b16 a[94:95], v9 offset:5216\n"
"v_mfma_f32_16x16x32_bf16 a[68:71], a[104:107], v[124:127], a[68:71]\n"
"v_mfma_f32_16x16x32_bf16 a[72:75], a[108:111], v[120:123], a[72:75]\n"
"ds_read_b64_tr_b16 a[96:97], v8 offset:128\n"
"ds_read_b64_tr_b16 a[98:99], v8 offset:5248\n"
"v_mfma_f32_16x16x32_bf16 a[76:79], a[108:111], v[124:127], a[76:79]\n"
"buffer_load_dwordx4 v3, s[40:43], s58 offen offset:1024 lds\n"
"s_sub_u32 s59, s59, 1\n"
"s_cmp_lg_u32 s59, 0\n"
"s_cbranch_scc1 K2_W2L\n"
"s_branch K2_DONE\n"
"K2_W3:\n"
"s_mov_b32 s59, 4\n"
"K2_W3L:\n"
"s_waitcnt lgkmcnt(4)\n"
"v_mfma_f32_16x16x32_bf16 a[0:3], a[80:83], v[32:35], a[0:3]\n"
"ds_read_b64_tr_b16 a[100:101], v9 offset:160\n"
"ds_read_b64_tr_b16 a[102:103], v9 offset:5280\n"
"v_mfma_f32_16x16x32_bf16 a[4:7], a[80:83], v[36:39], a[4:7]\n"
"v_mfma_f32_16x16x32_bf16 a[8:11], a[84:87], v[32:35], a[8:11]\n"
"ds_read_b64_tr_b16 a[104:105], v8 offset:192\n"
"ds_read_b64_tr_b16 a[106:107], v8 offset:5312\n"
"v_mfma_f32_16x16x32_bf16 a[12:15], a[84:87], v[36:39], a[12:15]\n"
"buffer_load_dwordx4 v4, s[40:43], s58 offen offset:2048 lds\n"
"v_mfma_f32_16x16x32_bf16 a[16:19], a[88:91], v[32:35], a[16:19]\n"
"ds_read_b64_tr_b16 a[108:109], v9 offset:224\n"
"ds_read_b64_tr_b16 a[110:111], v9 offset:5344\n"
"v_mfma_f32_16x16x32_bf16 a[20:23], a[88:91], v[36:39], a[20:23]\n"
"s_waitcnt lgkmcnt(4)\n"
"v_mfma_f32_16x16x32_bf16 a[24:27], a[92:95], v[32:35], a[24:27]\n"
"ds_read_b64_tr_b16 a[80:81], v8 offset:256\n"
"ds_read_b64_tr_b16 a[82:83], v8 offset:5376\n"
"v_mfma_f32_16x16x32_bf16 a[28:31], a[92:95], v[36:39], a[28:31]\n"
"buffer_load_dwordx4 v5, s[40:43], s58 offen offset:3072 lds\n"
"v_mfma_f32_16x16x32_bf16 a[32:35], a[96:99], v[32:35], a[32:35]\n"
"ds_read_b64_tr_b16 a[84:85], v9 offset:288\n"
"ds_read_b64_tr_b16 a[86:87], v9 offset:5408\n"
"v_mfma_f32_16x16x32_bf16 a[36:39], a[96:99], v[36:39], a[36:39]\n"
"s_add_u32 m0, m0, 4096\n"
"v_mfma_f32_16x16x32_bf16 a[40:43], a[100:103], v[32:35], a[40:43]\n"
"ds_read_b64_tr_b16 a[88:89], v8 offset:10240\n"
"ds_read_b64_tr_b16 a[90:91], v8 offset:15360\n"
"v_mfma_f32_16x16x32_bf16 a[44:47], a[100:103], v[36:39], a[44:47]\n"
"buffer_load_dwordx4 v6, s[40:43], s58 offen lds\n"
"s_waitcnt lgkmcnt(4)\n"
"v_mfma_f32_16x16x32_bf16 a[48:51], a[104:107], v[32:35], a[48:51]\n"
"ds_read_b64_tr_b16 a[92:93], v9 offset:10272\n"
"ds_read_b64_tr_b16 a[94:95], v9 offset:15392\n"
"v_mfma_f32_16x16x32_bf16 a[52:55], a[104:107], v[36:39], a[52:55]\n"
"s_add_u32 s58, s58, 81920\n"
"v_mfma_f32_16x16x32_bf16 a[56:59], a[108:111], v[32:35], a[56:59]\n"
"ds_read_b64_tr_b16 a[96:97], v8 offset:10304\n"
"ds_read_b64_tr_b16 a[98:99], v8 offset:15424\n"
"v_mfma_f32_16x16x32_bf16 a[60:63], a[108:111], v[36:39], a[60:63]\n"
"buffer_load_dwordx4 v[88:91], v1, s[36:39], s57 offen\n"
"v_mfma_f32_16x16x32_bf16 a[64:67], a[80:83], v[32:35], a[64:67]\n"
"ds_read_b64_tr_b16 a[100:101], v9 offset:10336\n"
"ds_read_b64_tr_b16 a[102:103], v9 offset:15456\n"
"v_mfma_f32_16x16x32_bf16 a[68:71], a[80:83], v[36:39], a[68:71]\n"
"s_waitcnt lgkmcnt(8)\n"
"v_mfma_f32_16x16x32_bf16 a[72:75], a[84:87], v[32:35], a[72:75]\n"
"ds_read_b64_tr_b16 a[104:105], v8 offset:10368\n"
"ds_read_b64_tr_b16 a[106:107], v8 offset:15488\n"
"v_mfma_f32_16x16x32_bf16 a[76:79], a[84:87], v[36:39], a[76:79]\n"
"buffer_load_dwordx4 v[92:95], v1, s[36:39], s57 offen offset:1024\n"
"s_add_u32 s57, s57, 122880\n"
"s_waitcnt vmcnt(27) lgkmcnt(4)\n"
"v_mfma_f32_16x16x32_bf16 a[0:3], a[88:91], v[40:43], a[0:3]\n"
"ds_read_b64_tr_b16 a[108:109], v9 offset:10400\n"
"ds_read_b64_tr_b16 a[110:111], v9 offset:15520\n"
"v_mfma_f32_16x16x32_bf16 a[4:7], a[88:91], v[44:47], a[4:7]\n"
"v_mfma_f32_16x16x32_bf16 a[8:11], a[92:95], v[40:43], a[8:11]\n"
"ds_read_b64_tr_b16 a[80:81], v8 offset:10432\n"
"ds_read_b64_tr_b16 a[82:83], v8 offset:15552\n"
"v_mfma_f32_16x16x32_bf16 a[12:15], a[92:95], v[44:47], a[12:15]\n"
"buffer_load_dwordx4 v[96:99], v1, s[36:39], s57 offen\n"
"v_mfma_f32_16x16x32_bf16 a[16:19], a[96:99], v[40:43], a[16:19]\n"
"ds_read_b64_tr_b16 a[84:85], v9 offset:10464\n"
"ds_read_b64_tr_b16 a[86:87], v9 offset:15584\n"
"v_mfma_f32_16x16x32_bf16 a[20:23], a[96:99], v[44:47], a[20:23]\n"
"s_waitcnt lgkmcnt(4)\n"
"v_mfma_f32_16x16x32_bf16 a[24:27], a[100:103], v[40:43], a[24:27]\n"
"ds_read_b64_tr_b16 a[88:89], v8 offset:10496\n"
"ds_read_b64_tr_b16 a[90:91], v8 offset:15616\n"
"v_mfma_f32_16x16x32_bf16 a[28:31], a[100:103], v[44:47], a[28:31]\n"
"buffer_load_dwordx4 v[100:103], v1, s[36:39], s57 offen offset:1024\n"
"s_add_u32 s57, s57, 122880\n"
"v_mfma_f32_16x16x32_bf16 a[32:35], a[104:107], v[40:43], a[32:35]\n"
"ds_read_b64_tr_b16 a[92:93], v9 offset:10528\n"
"ds_read_b64_tr_b16 a[94:95], v9 offset:15648\n"
"v_mfma_f32_16x16x32_bf16 a[36:39], a[104:107], v[44:47], a[36:39]\n"
"s_waitcnt vmcnt(22) lgkmcnt(0)\n"
"s_barrier\n"
"v_mfma_f32_16x16x32_bf16 a[40:43], a[108:111], v[40:43], a[40:43]\n"
"ds_read_b64_tr_b16 a[96:97], v8 offset:20480\n"
"ds_read_b64_tr_b16 a[98:99], v8 offset:25600\n"
"v_mfma_f32_16x16x32_bf16 a[44:47], a[108:111], v[44:47], a[44:47]\n"
"v_mfma_f32_16x16x32_bf16 a[48:51], a[80:83], v[40:43], a[48:51]\n"
"ds_read_b64_tr_b16 a[100:101], v9 offset:20512\n"
"ds_read_b64_tr_b16 a[102:103], v9 offset:25632\n"
"v_mfma_f32_16x16x32_bf16 a[52:55], a[80:83], v[44:47], a[52:55]\n"
"s_add_u32 m0, s66, 81920\n"
"v_mfma_f32_16x16x32_bf16 a[56:59], a[84:87], v[40:43], a[56:59]\n"
"ds_read_b64_tr_b16 a[104:105], v8 offset:20544\n"
"ds_read_b64_tr_b16 a[106:107], v8 offset:25664\n"
"v_mfma_f32_16x16x32_bf16 a[60:63], a[84:87], v[44:47], a[60:63]\n"
"buffer_load_dwordx4 v2, s[40:43], s58 offen offset:0 lds\n"
"v_mfma_f32_16x16x32_bf16 a[64:67], a[88:91], v[40:43], a[64:67]\n"
"ds_read_b64_tr_b16 a[108:109], v9 offset:20576\n"
"ds_read_b64_tr_b16 a[110:111], v9 offset:25696\n"
"v_mfma_f32_16x16x32_bf16 a[68:71], a[88:91], v[44:47], a[68:71]\n"
"v_mfma_f32_16x16x32_bf16 a[72:75], a[92:95], v[40:43], a[72:75]\n"
"ds_read_b64_tr_b16 a[80:81], v8 offset:20608\n"
"ds_read_b64_tr_b16 a[82:83], v8 offset:25728\n"
"v_mfma_f32_16x16x32_bf16 a[76:79], a[92:95], v[44:47], a[76:79]\n"
"buffer_load_dwordx4 v3, s[40:43], s58 offen offset:1024 lds\n"
"s_waitcnt lgkmcnt(4)\n"
"v_mfma_f32_16x16x32_bf16 a[0:3], a[96:99], v[48:51], a[0:3]\n"
"ds_read_b64_tr_b16 a[84:85], v9 offset:20640\n"
"ds_read_b64_tr_b16 a[86:87], v9 offset:25760\n"
"v_mfma_f32_16x16x32_bf16 a[4:7], a[96:99], v[52:55], a[4:7]\n"
"v_mfma_f32_16x16x32_bf16 a[8:11], a[100:103], v[48:51], a[8:11]\n"
"ds_read_b64_tr_b16 a[88:89], v8 offset:20672\n"
"ds_read_b64_tr_b16 a[90:91], v8 offset:25792\n"
"v_mfma_f32_16x16x32_bf16 a[12:15], a[100:103], v[52:55], a[12:15]\n"
"buffer_load_dwordx4 v4, s[40:43], s58 offen offset:2048 lds\n"
"v_mfma_f32_16x16x32_bf16 a[16:19], a[104:107], v[48:51], a[16:19]\n"
"ds_read_b64_tr_b16 a[92:93], v9 offset:20704\n"
"ds_read_b64_tr_b16 a[94:95], v9 offset:25824\n"
"v_mfma_f32_16x16x32_bf16 a[20:23], a[104:107], v[52:55], a[20:23]\n"
"s_waitcnt lgkmcnt(4)\n"
"v_mfma_f32_16x16x32_bf16 a[24:27], a[108:111], v[48:51], a[24:27]\n"
"ds_read_b64_tr_b16 a[96:97], v8 offset:20736\n"
"ds_read_b64_tr_b16 a[98:99], v8 offset:25856\n"
"v_mfma_f32_16x16x32_bf16 a[28:31], a[108:111], v[52:55], a[28:31]\n"
"buffer_load_dwordx4 v5, s[40:43], s58 offen offset:3072 lds\n"
"v_mfma_f32_16x16x32_bf16 a[32:35], a[80:83], v[48:51], a[32:35]\n"
"ds_read_b64_tr_b16 a[100:101], v9 offset:20768\n"
"ds_read_b64_tr_b16 a[102:103], v9 offset:25888\n"
"v_mfma_f32_16x16x32_bf16 a[36:39], a[80:83], v[52:55], a[36:39]\n"
"s_add_u32 m0, m0, 4096\n"
"v_mfma_f32_16x16x32_bf16 a[40:43], a[84:87], v[48:51], a[40:43]\n"
"ds_read_b64_tr_b16 a[104:105], v8 offset:30720\n"
"ds_read_b64_tr_b16 a[106:107], v8 offset:35840\n"
"v_mfma_f32_16x16x32_bf16 a[44:47], a[84:87], v[52:55], a[44:47]\n"
"buffer_load_dwordx4 v6, s[40:43], s58 offen lds\n"
"s_waitcnt lgkmcnt(4)\n"
"v_mfma_f32_16x16x32_bf16 a[48:51], a[88:91], v[48:51], a[48:51]\n"
"ds_read_b64_tr_b16 a[108:109], v9 offset:30752\n"
"ds_read_b64_tr_b16 a[110:111], v9 offset:35872\n"
"v_mfma_f32_16x16x32_bf16 a[52:55], a[88:91], v[52:55], a[52:55]\n"
"s_add_u32 s58, s58, 81920\n"
"v_mfma_f32_16x16x32_bf16 a[56:59], a[92:95], v[48:51], a[56:59]\n"
"ds_read_b64_tr_b16 a[80:81], v8 offset:30784\n"
"ds_read_b64_tr_b16 a[82:83], v8 offset:35904\n"
"v_mfma_f32_16x16x32_bf16 a[60:63], a[92:95], v[52:55], a[60:63]\n"
"buffer_load_dwordx4 v[104:107], v1, s[36:39], s57 offen\n"
"v_mfma_f32_16x16x32_bf16 a[64:67], a[96:99], v[48:51], a[64:67]\n"
"ds_read_b64_tr_b16 a[84:85], v9 offset:30816\n"
"ds_read_b64_tr_b16 a[86:87], v9 offset:35936\n"
"v_mfma_f32_16x16x32_bf16 a[68:71], a[96:99], v[52:55], a[68:71]\n"
"s_waitcnt lgkmcnt(8)\n"
"v_mfma_f32_16x16x32_bf16 a[72:75], a[100:103], v[48:51], a[72:75]\n"
"ds_read_b64_tr_b16 a[88:89], v8 offset:30848\n"
"ds_read_b64_tr_b16 a[90:91], v8 offset:35968\n"
"v_mfma_f32_16x16x32_bf16 a[76:79], a[100:103], v[52:55], a[76:79]\n"
"buffer_load_dwordx4 v[108:111], v1, s[36:39], s57 offen offset:1024\n"
"s_add_u32 s57, s57, 122880\n"
"s_waitcnt vmcnt(27) lgkmcnt(4)\n"
"v_mfma_f32_16x16x32_bf16 a[0:3], a[104:107], v[56:59], a[0:3]\n"
"ds_read_b64_tr_b16 a[92:93], v9 offset:30880\n"
"ds_read_b64_tr_b16 a[94:95], v9 offset:36000\n"
"v_mfma_f32_16x16x32_bf16 a[4:7], a[104:107], v[60:63], a[4:7]\n"
"v_mfma_f32_16x16x32_bf16 a[8:11], a[108:111], v[56:59], a[8:11]\n"
"ds_read_b64_tr_b16 a[96:97], v8 offset:30912\n"
"ds_read_b64_tr_b16 a[98:99], v8 offset:36032\n"
"v_mfma_f32_16x16x32_bf16 a[12:15], a[108:111], v[60:63], a[12:15]\n"
"buffer_load_dwordx4 v[112:115], v1, s[36:39], s57 offen\n"
"v_mfma_f32_16x16x32_bf16 a[16:19], a[80:83], v[56:59], a[16:19]\n"
"ds_read_b64_tr_b16 a[100:101], v9 offset:30944\n"
"ds_read_b64_tr_b16 a[102:103], v9 offset:36064\n"
"v_mfma_f32_16x16x32_bf16 a[20:23], a[80:83], v[60:63], a[20:23]\n"
"s_waitcnt lgkmcnt(4)\n"
"v_mfma_f32_16x16x32_bf16 a[24:27], a[84:87], v[56:59], a[24:27]\n"
"ds_read_b64_tr_b16 a[104:105], v8 offset:30976\n"
"ds_read_b64_tr_b16 a[106:107], v8 offset:36096\n"
"v_mfma_f32_16x16x32_bf16 a[28:31], a[84:87], v[60:63], a[28:31]\n"
"buffer_load_dwordx4 v[116:119], v1, s[36:39], s57 offen offset:1024\n"
"s_add_u32 s57, s57, 122880\n"
"v_mfma_f32_16x16x32_bf16 a[32:35], a[88:91], v[56:59], a[32:35]\n"
"ds_read_b64_tr_b16 a[108:109], v9 offset:31008\n"
"ds_read_b64_tr_b16 a[110:111], v9 offset:36128\n"
"v_mfma_f32_16x16x32_bf16 a[36:39], a[88:91], v[60:63], a[36:39]\n"
"s_waitcnt vmcnt(22) lgkmcnt(0)\n"
"s_barrier\n"
"v_mfma_f32_16x16x32_bf16 a[40:43], a[92:95], v[56:59], a[40:43]\n"
"ds_read_b64_tr_b16 a[80:81], v8 offset:40960\n"
"ds_read_b64_tr_b16 a[82:83], v8 offset:46080\n"
"v_mfma_f32_16x16x32_bf16 a[44:47], a[92:95], v[60:63], a[44:47]\n"
"v_mfma_f32_16x16x32_bf16 a[48:51], a[96:99], v[56:59], a[48:51]\n"
"ds_read_b64_tr_b16 a[84:85], v9 offset:40992\n"
"ds_read_b64_tr_b16 a[86:87], v9 offset:46112\n"
"v_mfma_f32_16x16x32_bf16 a[52:55], a[96:99], v[60:63], a[52:55]\n"
"s_add_u32 m0, s66, 102400\n"
"v_mfma_f32_16x16x32_bf16 a[56:59], a[100:103], v[56:59], a[56:59]\n"
"ds_read_b64_tr_b16 a[88:89], v8 offset:41024\n"
"ds_read_b64_tr_b16 a[90:91], v8 offset:46144\n"
"v_mfma_f32_16x16x32_bf16 a[60:63], a[100:103], v[60:63], a[60:63]\n"
"buffer_load_dwordx4 v2, s[40:43], s58 offen offset:0 lds\n"
"v_mfma_f32_16x16x32_bf16 a[64:67], a[104:107], v[56:59], a[64:67]\n"
"ds_read_b64_tr_b16 a[92:93], v9 offset:41056\n"
"ds_read_b64_tr_b16 a[94:95], v9 offset:46176\n"
"v_mfma_f32_16x16x32_bf16 a[68:71], a[104:107], v[60:63], a[68:71]\n"
"v_mfma_f32_16x16x32_bf16 a[72:75], a[108:111], v[56:59], a[72:75]\n"
"ds_read_b64_tr_b16 a[96:97], v8 offset:41088\n"
"ds_read_b64_tr_b16 a[98:99], v8 offset:46208\n"
"v_mfma_f32_16x16x32_bf16 a[76:79], a[108:111], v[60:63], a[76:79]\n"
"buffer_load_dwordx4 v3, s[40:43], s58 offen offset:1024 lds\n"
"s_waitcnt lgkmcnt(4)\n"
"v_mfma_f32_16x16x32_bf16 a[0:3], a[80:83], v[64:67], a[0:3]\n"
"ds_read_b64_tr_b16 a[100:101], v9 offset:41120\n"
"ds_read_b64_tr_b16 a[102:103], v9 offset:46240\n"
"v_mfma_f32_16x16x32_bf16 a[4:7], a[80:83], v[68:71], a[4:7]\n"
"v_mfma_f32_16x16x32_bf16 a[8:11], a[84:87], v[64:67], a[8:11]\n"
"ds_read_b64_tr_b16 a[104:105], v8 offset:41152\n"
"ds_read_b64_tr_b16 a[106:107], v8 offset:46272\n"
"v_mfma_f32_16x16x32_bf16 a[12:15], a[84:87], v[68:71], a[12:15]\n"
"buffer_load_dwordx4 v4, s[40:43], s58 offen offset:2048 lds\n"
"v_mfma_f32_16x16x32_bf16 a[16:19], a[88:91], v[64:67], a[16:19]\n"
"ds_read_b64_tr_b16 a[108:109], v9 offset:41184\n"
"ds_read_b64_tr_b16 a[110:111], v9 offset:46304\n"
"v_mfma_f32_16x16x32_bf16 a[20:23], a[88:91], v[68:71], a[20:23]\n"
"s_waitcnt lgkmcnt(4)\n"
"v_mfma_f32_16x16x32_bf16 a[24:27], a[92:95], v[64:67], a[24:27]\n"
"ds_read_b64_tr_b16 a[80:81], v8 offset:41216\n"
"ds_read_b64_tr_b16 a[82:83], v8 offset:46336\n"
"v_mfma_f32_16x16x32_bf16 a[28:31], a[92:95], v[68:71], a[28:31]\n"
"buffer_load_dwordx4 v5, s[40:43], s58 offen offset:3072 lds\n"
"v_mfma_f32_16x16x32_bf16 a[32:35], a[96:99], v[64:67], a[32:35]\n"
"ds_read_b64_tr_b16 a[84:85], v9 offset:41248\n"
"ds_read_b64_tr_b16 a[86:87], v9 offset:46368\n"
"v_mfma_f32_16x16x32_bf16 a[36:39], a[96:99], v[68:71], a[36:39]\n"
"s_add_u32 m0, m0, 4096\n"
"v_mfma_f32_16x16x32_bf16 a[40:43], a[100:103], v[64:67], a[40:43]\n"
"ds_read_b64_tr_b16 a[88:89], v8 offset:51200\n"
"ds_read_b64_tr_b16 a[90:91], v8 offset:56320\n"
"v_mfma_f32_16x16x32_bf16 a[44:47], a[100:103], v[68:71], a[44:47]\n"
"buffer_load_dwordx4 v6, s[40:43], s58 offen lds\n"
"s_waitcnt lgkmcnt(4)\n"
"v_mfma_f32_16x16x32_bf16 a[48:51], a[104:107], v[64:67], a[48:51]\n"
"ds_read_b64_tr_b16 a[92:93], v9 offset:51232\n"
"ds_read_b64_tr_b16 a[94:95], v9 offset:56352\n"
"v_mfma_f32_16x16x32_bf16 a[52:55], a[104:107], v[68:71], a[52:55]\n"
"s_add_u32 s58, s58, 81920\n"
"v_mfma_f32_16x16x32_bf16 a[56:59], a[108:111], v[64:67], a[56:59]\n"
"ds_read_b64_tr_b16 a[96:97], v8 offset:51264\n"
"ds_read_b64_tr_b16 a[98:99], v8 offset:56384\n"
"v_mfma_f32_16x16x32_bf16 a[60:63], a[108:111], v[68:71], a[60:63]\n"
"buffer_load_dwordx4 v[120:123], v1, s[36:39], s57 offen\n"
"v_mfma_f32_16x16x32_bf16 a[64:67], a[80:83], v[64:67], a[64:67]\n"
"ds_read_b64_tr_b16 a[100:101], v9 offset:51296\n"
"ds_read_b64_tr_b16 a[102:103], v9 offset:56416\n"
"v_mfma_f32_16x16x32_bf16 a[68:71], a[80:83], v[68:71], a[68:71]\n"
"s_waitcnt lgkmcnt(8)\n"
"v_mfma_f32_16x16x32_bf16 a[72:75], a[84:87], v[64:67], a[72:75]\n"
"ds_read_b64_tr_b16 a[104:105], v8 offset:51328\n"
"ds_read_b64_tr_b16 a[106:107], v8 offset:56448\n"
"v_mfma_f32_16x16x32_bf16 a[76:79], a[84:87], v[68:71], a[76:79]\n"
"buffer_load_dwordx4 v[124:127], v1, s[36:39], s57 offen offset:1024\n"
"s_add_u32 s57, s57, 122880\n"
"s_waitcnt vmcnt(27) lgkmcnt(4)\n"
"v_mfma_f32_16x16x32_bf16 a[0:3], a[88:91], v[72:75], a[0:3]\n"
"ds_read_b64_tr_b16 a[108:109], v9 offset:51360\n"
"ds_read_b64_tr_b16 a[110:111], v9 offset:56480\n"
"v_mfma_f32_16x16x32_bf16 a[4:7], a[88:91], v[76:79], a[4:7]\n"
"v_mfma_f32_16x16x32_bf16 a[8:11], a[92:95], v[72:75], a[8:11]\n"
"ds_read_b64_tr_b16 a[80:81], v8 offset:51392\n"
"ds_read_b64_tr_b16 a[82:83], v8 offset:56512\n"
"v_mfma_f32_16x16x32_bf16 a[12:15], a[92:95], v[76:79], a[12:15]\n"
"buffer_load_dwordx4 v[32:35], v1, s[36:39], s57 offen\n"
"v_mfma_f32_16x16x32_bf16 a[16:19], a[96:99], v[72:75], a[16:19]\n"
"ds_read_b64_tr_b16 a[84:85], v9 offset:51424\n"
"ds_read_b64_tr_b16 a[86:87], v9 offset:56544\n"
"v_mfma_f32_16x16x32_bf16 a[20:23], a[96:99], v[76:79], a[20:23]\n"
"s_waitcnt lgkmcnt(4)\n"
"v_mfma_f32_16x16x32_bf16 a[24:27], a[100:103], v[72:75], a[24:27]\n"
"ds_read_b64_tr_b16 a[88:89], v8 offset:51456\n"
"ds_read_b64_tr_b16 a[90:91], v8 offset:56576\n"
"v_mfma_f32_16x16x32_bf16 a[28:31], a[100:103], v[76:79], a[28:31]\n"
"buffer_load_dwordx4 v[36:39], v1, s[36:39], s57 offen offset:1024\n"
"s_add_u32 s57, s57, 122880\n"
"v_mfma_f32_16x16x32_bf16 a[32:35], a[104:107], v[72:75], a[32:35]\n"
"ds_read_b64_tr_b16 a[92:93], v9 offset:51488\n"
"ds_read_b64_tr_b16 a[94:95], v9 offset:56608\n"
"v_mfma_f32_16x16x32_bf16 a[36:39], a[104:107], v[76:79], a[36:39]\n"
"s_waitcnt vmcnt(22) lgkmcnt(0)\n"
"s_barrier\n"
"v_mfma_f32_16x16x32_bf16 a[40:43], a[108:111], v[72:75], a[40:43]\n"
"ds_read_b64_tr_b16 a[96:97], v10 offset:0\n"
"ds_read_b64_tr_b16 a[98:99], v10 offset:5120\n"
"v_mfma_f32_16x16x32_bf16 a[44:47], a[108:111], v[76:79], a[44:47]\n"
"v_mfma_f32_16x16x32_bf16 a[48:51], a[80:83], v[72:75], a[48:51]\n"
"ds_read_b64_tr_b16 a[100:101], v11 offset:32\n"
"ds_read_b64_tr_b16 a[102:103], v11 offset:5152\n"
"v_mfma_f32_16x16x32_bf16 a[52:55], a[80:83], v[76:79], a[52:55]\n"
"s_add_u32 m0, s66, 0\n"
"v_mfma_f32_16x16x32_bf16 a[56:59], a[84:87], v[72:75], a[56:59]\n"
"ds_read_b64_tr_b16 a[104:105], v10 offset:64\n"
"ds_read_b64_tr_b16 a[106:107], v10 offset:5184\n"
"v_mfma_f32_16x16x32_bf16 a[60:63], a[84:87], v[76:79], a[60:63]\n"
"buffer_load_dwordx4 v2, s[40:43], s58 offen offset:0 lds\n"
"v_mfma_f32_16x16x32_bf16 a[64:67], a[88:91], v[72:75], a[64:67]\n"
"ds_read_b64_tr_b16 a[108:109], v11 offset:96\n"
"ds_read_b64_tr_b16 a[110:111], v11 offset:5216\n"
"v_mfma_f32_16x16x32_bf16 a[68:71], a[88:91], v[76:79], a[68:71]\n"
"v_mfma_f32_16x16x32_bf16 a[72:75], a[92:95], v[72:75], a[72:75]\n"
"ds_read_b64_tr_b16 a[80:81], v10 offset:128\n"
"ds_read_b64_tr_b16 a[82:83], v10 offset:5248\n"
"v_mfma_f32_16x16x32_bf16 a[76:79], a[92:95], v[76:79], a[76:79]\n"
"buffer_load_dwordx4 v3, s[40:43], s58 offen offset:1024 lds\n"
"s_waitcnt lgkmcnt(4)\n"
"v_mfma_f32_16x16x32_bf16 a[0:3], a[96:99], v[80:83], a[0:3]\n"
"ds_read_b64_tr_b16 a[84:85], v11 offset:160\n"
"ds_read_b64_tr_b16 a[86:87], v11 offset:5280\n"
"v_mfma_f32_16x16x32_bf16 a[4:7], a[96:99], v[84:87], a[4:7]\n"
"v_mfma_f32_16x16x32_bf16 a[8:11], a[100:103], v[80:83], a[8:11]\n"
"ds_read_b64_tr_b16 a[88:89], v10 offset:192\n"
"ds_read_b64_tr_b16 a[90:91], v10 offset:5312\n"
"v_mfma_f32_16x16x32_bf16 a[12:15], a[100:103], v[84:87], a[12:15]\n"
"buffer_load_dwordx4 v4, s[40:43], s58 offen offset:2048 lds\n"
"v_mfma_f32_16x16x32_bf16 a[16:19], a[104:107], v[80:83], a[16:19]\n"
"ds_read_b64_tr_b16 a[92:93], v11 offset:224\n"
"ds_read_b64_tr_b16 a[94:95], v11 offset:5344\n"
"v_mfma_f32_16x16x32_bf16 a[20:23], a[104:107], v[84:87], a[20:23]\n"
"s_waitcnt lgkmcnt(4)\n"
"v_mfma_f32_16x16x32_bf16 a[24:27], a[108:111], v[80:83], a[24:27]\n"
"ds_read_b64_tr_b16 a[96:97], v10 offset:256\n"
"ds_read_b64_tr_b16 a[98:99], v10 offset:5376\n"
"v_mfma_f32_16x16x32_bf16 a[28:31], a[108:111], v[84:87], a[28:31]\n"
"buffer_load_dwordx4 v5, s[40:43], s58 offen offset:3072 lds\n"
"v_mfma_f32_16x16x32_bf16 a[32:35], a[80:83], v[80:83], a[32:35]\n"
"ds_read_b64_tr_b16 a[100:101], v11 offset:288\n"
"ds_read_b64_tr_b16 a[102:103], v11 offset:5408\n"
"v_mfma_f32_16x16x32_bf16 a[36:39], a[80:83], v[84:87], a[36:39]\n"
"s_add_u32 m0, m0, 4096\n"
"v_mfma_f32_16x16x32_bf16 a[40:43], a[84:87], v[80:83], a[40:43]\n"
"ds_read_b64_tr_b16 a[104:105], v10 offset:10240\n"
"ds_read_b64_tr_b16 a[106:107], v10 offset:15360\n"
"v_mfma_f32_16x16x32_bf16 a[44:47], a[84:87], v[84:87], a[44:47]\n"
"buffer_load_dwordx4 v6, s[40:43], s58 offen lds\n"
"s_waitcnt lgkmcnt(4)\n"
"v_mfma_f32_16x16x32_bf16 a[48:51], a[88:91], v[80:83], a[48:51]\n"
"ds_read_b64_tr_b16 a[108:109], v11 offset:10272\n"
"ds_read_b64_tr_b16 a[110:111], v11 offset:15392\n"
"v_mfma_f32_16x16x32_bf16 a[52:55], a[88:91], v[84:87], a[52:55]\n"
"s_add_u32 s58, s58, 81920\n"
"v_mfma_f32_16x16x32_bf16 a[56:59], a[92:95], v[80:83], a[56:59]\n"
"ds_read_b64_tr_b16 a[80:81], v10 offset:10304\n"
"ds_read_b64_tr_b16 a[82:83], v10 offset:15424\n"
"v_mfma_f32_16x16x32_bf16 a[60:63], a[92:95], v[84:87], a[60:63]\n"
"buffer_load_dwordx4 v[40:43], v1, s[36:39], s57 offen\n"
"v_mfma_f32_16x16x32_bf16 a[64:67], a[96:99], v[80:83], a[64:67]\n"
"ds_read_b64_tr_b16 a[84:85], v11 offset:10336\n"
"ds_read_b64_tr_b16 a[86:87], v11 offset:15456\n"
"v_mfma_f32_16x16x32_bf16 a[68:71], a[96:99], v[84:87], a[68:71]\n"
"s_waitcnt lgkmcnt(8)\n"
"v_mfma_f32_16x16x32_bf16 a[72:75], a[100:103], v[80:83], a[72:75]\n"
"ds_read_b64_tr_b16 a[88:89], v10 offset:10368\n"
"ds_read_b64_tr_b16 a[90:91], v10 offset:15488\n"
"v_mfma_f32_16x16x32_bf16 a[76:79], a[100:103], v[84:87], a[76:79]\n"
"buffer_load_dwordx4 v[44:47], v1, s[36:39], s57 offen offset:1024\n"
"s_add_u32 s57, s57, 122880\n"
"s_waitcnt vmcnt(27) lgkmcnt(4)\n"
"v_mfma_f32_16x16x32_bf16 a[0:3], a[104:107], v[88:91], a[0:3]\n"
"ds_read_b64_tr_b16 a[92:93], v11 offset:10400\n"
"ds_read_b64_tr_b16 a[94:95], v11 offset:15520\n"
"v_mfma_f32_16x16x32_bf16 a[4:7], a[104:107], v[92:95], a[4:7]\n"
"v_mfma_f32_16x16x32_bf16 a[8:11], a[108:111], v[88:91], a[8:11]\n"
"ds_read_b64_tr_b16 a[96:97], v10 offset:10432\n"
"ds_read_b64_tr_b16 a[98:99], v10 offset:15552\n"
"v_mfma_f32_16x16x32_bf16 a[12:15], a[108:111], v[92:95], a[12:15]\n"
"buffer_load_dwordx4 v[48:51], v1, s[36:39], s57 offen\n"
"v_mfma_f32_16x16x32_bf16 a[16:19], a[80:83], v[88:91], a[16:19]\n"
"ds_read_b64_tr_b16 a[100:101], v11 offset:10464\n"
"ds_read_b64_tr_b16 a[102:103], v11 offset:15584\n"
"v_mfma_f32_16x16x32_bf16 a[20:23], a[80:83], v[92:95], a[20:23]\n"
"s_waitcnt lgkmcnt(4)\n"
"v_mfma_f32_16x16x32_bf16 a[24:27], a[84:87], v[88:91], a[24:27]\n"
"ds_read_b64_tr_b16 a[104:105], v10 offset:10496\n"
"ds_read_b64_tr_b16 a[106:107], v10 offset:15616\n"
"v_mfma_f32_16x16x32_bf16 a[28:31], a[84:87], v[92:95], a[28:31]\n"
"buffer_load_dwordx4 v[52:55], v1, s[36:39], s57 offen offset:1024\n"
"s_add_u32 s57, s57, 122880\n"
"v_mfma_f32_16x16x32_bf16 a[32:35], a[88:91], v[88:91], a[32:35]\n"
"ds_read_b64_tr_b16 a[108:109], v11 offset:10528\n"
"ds_read_b64_tr_b16 a[110:111], v11 offset:15648\n"
"v_mfma_f32_16x16x32_bf16 a[36:39], a[88:91], v[92:95], a[36:39]\n"
"s_waitcnt vmcnt(22) lgkmcnt(0)\n"
"s_barrier\n"
"v_mfma_f32_16x16x32_bf16 a[40:43], a[92:95], v[88:91], a[40:43]\n"
"ds_read_b64_tr_b16 a[80:81], v10 offset:20480\n"
"ds_read_b64_tr_b16 a[82:83], v10 offset:25600\n"
"v_mfma_f32_16x16x32_bf16 a[44:47], a[92:95], v[92:95], a[44:47]\n"
"v_mfma_f32_16x16x32_bf16 a[48:51], a[96:99], v[88:91], a[48:51]\n"
"ds_read_b64_tr_b16 a[84:85], v11 offset:20512\n"
"ds_read_b64_tr_b16 a[86:87], v11 offset:25632\n"
"v_mfma_f32_16x16x32_bf16 a[52:55], a[96:99], v[92:95], a[52:55]\n"
"s_add_u32 m0, s66, 20480\n"
"v_mfma_f32_16x16x32_bf16 a[56:59], a[100:103], v[88:91], a[56:59]\n"
"ds_read_b64_tr_b16 a[88:89], v10 offset:20544\n"
"ds_read_b64_tr_b16 a[90:91], v10 offset:25664\n"
"v_mfma_f32_16x16x32_bf16 a[60:63], a[100:103], v[92:95], a[60:63]\n"
"buffer_load_dwordx4 v2, s[40:43], s58 offen offset:0 lds\n"
"v_mfma_f32_16x16x32_bf16 a[64:67], a[104:107], v[88:91], a[64:67]\n"
"ds_read_b64_tr_b16 a[92:93], v11 offset:20576\n"
"ds_read_b64_tr_b16 a[94:95], v11 offset:25696\n"
"v_mfma_f32_16x16x32_bf16 a[68:71], a[104:107], v[92:95], a[68:71]\n"
"v_mfma_f32_16x16x32_bf16 a[72:75], a[108:111], v[88:91], a[72:75]\n"
"ds_read_b64_tr_b16 a[96:97], v10 offset:20608\n"
"ds_read_b64_tr_b16 a[98:99], v10 offset:25728\n"
"v_mfma_f32_16x16x32_bf16 a[76:79], a[108:111], v[92:95], a[76:79]\n"
"buffer_load_dwordx4 v3, s[40:43], s58 offen offset:1024 lds\n"
"s_waitcnt lgkmcnt(4)\n"
"v_mfma_f32_16x16x32_bf16 a[0:3], a[80:83], v[96:99], a[0:3]\n"
"ds_read_b64_tr_b16 a[100:101], v11 offset:20640\n"
"ds_read_b64_tr_b16 a[102:103], v11 offset:25760\n"
"v_mfma_f32_16x16x32_bf16 a[4:7], a[80:83], v[100:103], a[4:7]\n"
"v_mfma_f32_16x16x32_bf16 a[8:11], a[84:87], v[96:99], a[8:11]\n"
"ds_read_b64_tr_b16 a[104:105], v10 offset:20672\n"
"ds_read_b64_tr_b16 a[106:107], v10 offset:25792\n"
"v_mfma_f32_16x16x32_bf16 a[12:15], a[84:87], v[100:103], a[12:15]\n"
"buffer_load_dwordx4 v4, s[40:43], s58 offen offset:2048 lds\n"
"v_mfma_f32_16x16x32_bf16 a[16:19], a[88:91], v[96:99], a[16:19]\n"
"ds_read_b64_tr_b16 a[108:109], v11 offset:20704\n"
"ds_read_b64_tr_b16 a[110:111], v11 offset:25824\n"
"v_mfma_f32_16x16x32_bf16 a[20:23], a[88:91], v[100:103], a[20:23]\n"
"s_waitcnt lgkmcnt(4)\n"
"v_mfma_f32_16x16x32_bf16 a[24:27], a[92:95], v[96:99], a[24:27]\n"
"ds_read_b64_tr_b16 a[80:81], v10 offset:20736\n"
"ds_read_b64_tr_b16 a[82:83], v10 offset:25856\n"
"v_mfma_f32_16x16x32_bf16 a[28:31], a[92:95], v[100:103], a[28:31]\n"
"buffer_load_dwordx4 v5, s[40:43], s58 offen offset:3072 lds\n"
"v_mfma_f32_16x16x32_bf16 a[32:35], a[96:99], v[96:99], a[32:35]\n"
"ds_read_b64_tr_b16 a[84:85], v11 offset:20768\n"
"ds_read_b64_tr_b16 a[86:87], v11 offset:25888\n"
"v_mfma_f32_16x16x32_bf16 a[36:39], a[96:99], v[100:103], a[36:39]\n"
"s_add_u32 m0, m0, 4096\n"
"v_mfma_f32_16x16x32_bf16 a[40:43], a[100:103], v[96:99], a[40:43]\n"
"ds_read_b64_tr_b16 a[88:89], v10 offset:30720\n"
"ds_read_b64_tr_b16 a[90:91], v10 offset:35840\n"
"v_mfma_f32_16x16x32_bf16 a[44:47], a[100:103], v[100:103], a[44:47]\n"
"buffer_load_dwordx4 v6, s[40:43], s58 offen lds\n"
"s_waitcnt lgkmcnt(4)\n"
"v_mfma_f32_16x16x32_bf16 a[48:51], a[104:107], v[96:99], a[48:51]\n"
"ds_read_b64_tr_b16 a[92:93], v11 offset:30752\n"
"ds_read_b64_tr_b16 a[94:95], v11 offset:35872\n"
"v_mfma_f32_16x16x32_bf16 a[52:55], a[104:107], v[100:103], a[52:55]\n"
"s_add_u32 s58, s58, 81920\n"
"v_mfma_f32_16x16x32_bf16 a[56:59], a[108:111], v[96:99], a[56:59]\n"
"ds_read_b64_tr_b16 a[96:97], v10 offset:30784\n"
"ds_read_b64_tr_b16 a[98:99], v10 offset:35904\n"
"v_mfma_f32_16x16x32_bf16 a[60:63], a[108:111], v[100:103], a[60:63]\n"
"buffer_load_dwordx4 v[56:59], v1, s[36:39], s57 offen\n"
"v_mfma_f32_16x16x32_bf16 a[64:67], a[80:83], v[96:99], a[64:67]\n"
"ds_read_b64_tr_b16 a[100:101], v11 offset:30816\n"
"ds_read_b64_tr_b16 a[102:103], v11 offset:35936\n"
"v_mfma_f32_16x16x32_bf16 a[68:71], a[80:83], v[100:103], a[68:71]\n"
"s_waitcnt lgkmcnt(8)\n"
"v_mfma_f32_16x16x32_bf16 a[72:75], a[84:87], v[96:99], a[72:75]\n"
"ds_read_b64_tr_b16 a[104:105], v10 offset:30848\n"
"ds_read_b64_tr_b16 a[106:107], v10 offset:35968\n"
"v_mfma_f32_16x16x32_bf16 a[76:79], a[84:87], v[100:103], a[76:79]\n"
"buffer_load_dwordx4 v[60:63], v1, s[36:39], s57 offen offset:1024\n"
"s_add_u32 s57, s57, 122880\n"
"s_waitcnt vmcnt(27) lgkmcnt(4)\n"
"v_mfma_f32_16x16x32_bf16 a[0:3], a[88:91], v[104:107], a[0:3]\n"
"ds_read_b64_tr_b16 a[108:109], v11 offset:30880\n"
"ds_read_b64_tr_b16 a[110:111], v11 offset:36000\n"
"v_mfma_f32_16x16x32_bf16 a[4:7], a[88:91], v[108:111], a[4:7]\n"
"v_mfma_f32_16x16x32_bf16 a[8:11], a[92:95], v[104:107], a[8:11]\n"
"ds_read_b64_tr_b16 a[80:81], v10 offset:30912\n"
"ds_read_b64_tr_b16 a[82:83], v10 offset:36032\n"
"v_mfma_f32_16x16x32_bf16 a[12:15], a[92:95], v[108:111], a[12:15]\n"
"buffer_load_dwordx4 v[64:67], v1, s[36:39], s57 offen\n"
"v_mfma_f32_16x16x32_bf16 a[16:19], a[96:99], v[104:107], a[16:19]\n"
"ds_read_b64_tr_b16 a[84:85], v11 offset:30944\n"
"ds_read_b64_tr_b16 a[86:87], v11 offset:36064\n"
"v_mfma_f32_16x16x32_bf16 a[20:23], a[96:99], v[108:111], a[20:23]\n"
"s_waitcnt lgkmcnt(4)\n"
"v_mfma_f32_16x16x32_bf16 a[24:27], a[100:103], v[104:107], a[24:27]\n"
"ds_read_b64_tr_b16 a[88:89], v10 offset:30976\n"
"ds_read_b64_tr_b16 a[90:91], v10 offset:36096\n"
"v_mfma_f32_16x16x32_bf16 a[28:31], a[100:103], v[108:111], a[28:31]\n"
"buffer_load_dwordx4 v[68:71], v1, s[36:39], s57 offen offset:1024\n"
"s_add_u32 s57, s57, 122880\n"
"v_mfma_f32_16x16x32_bf16 a[32:35], a[104:107], v[104:107], a[32:35]\n"
"ds_read_b64_tr_b16 a[92:93], v11 offset:31008\n"
"ds_read_b64_tr_b16 a[94:95], v11 offset:36128\n"
"v_mfma_f32_16x16x32_bf16 a[36:39], a[104:107], v[108:111], a[36:39]\n"
"s_waitcnt vmcnt(22) lgkmcnt(0)\n"
"s_barrier\n"
"v_mfma_f32_16x16x32_bf16 a[40:43], a[108:111], v[104:107], a[40:43]\n"
"ds_read_b64_tr_b16 a[96:97], v10 offset:40960\n"
"ds_read_b64_tr_b16 a[98:99], v10 offset:46080\n"
"v_mfma_f32_16x16x32_bf16 a[44:47], a[108:111], v[108:111], a[44:47]\n"
"v_mfma_f32_16x16x32_bf16 a[48:51], a[80:83], v[104:107], a[48:51]\n"
"ds_read_b64_tr_b16 a[100:101], v11 offset:40992\n"
"ds_read_b64_tr_b16 a[102:103], v11 offset:46112\n"
"v_mfma_f32_16x16x32_bf16 a[52:55], a[80:83], v[108:111], a[52:55]\n"
"s_add_u32 m0, s66, 40960\n"
"v_mfma_f32_16x16x32_bf16 a[56:59], a[84:87], v[104:107], a[56:59]\n"
"ds_read_b64_tr_b16 a[104:105], v10 offset:41024\n"
"ds_read_b64_tr_b16 a[106:107], v10 offset:46144\n"
"v_mfma_f32_16x16x32_bf16 a[60:63], a[84:87], v[108:111], a[60:63]\n"
"buffer_load_dwordx4 v2, s[40:43], s58 offen offset:0 lds\n"
"v_mfma_f32_16x16x32_bf16 a[64:67], a[88:91], v[104:107], a[64:67]\n"
"ds_read_b64_tr_b16 a[108:109], v11 offset:41056\n"
"ds_read_b64_tr_b16 a[110:111], v11 offset:46176\n"
"v_mfma_f32_16x16x32_bf16 a[68:71], a[88:91], v[108:111], a[68:71]\n"
"v_mfma_f32_16x16x32_bf16 a[72:75], a[92:95], v[104:107], a[72:75]\n"
"ds_read_b64_tr_b16 a[80:81], v10 offset:41088\n"
"ds_read_b64_tr_b16 a[82:83], v10 offset:46208\n"
"v_mfma_f32_16x16x32_bf16 a[76:79], a[92:95], v[108:111], a[76:79]\n"
"buffer_load_dwordx4 v3, s[40:43], s58 offen offset:1024 lds\n"
"s_waitcnt lgkmcnt(4)\n"
"v_mfma_f32_16x16x32_bf16 a[0:3], a[96:99], v[112:115], a[0:3]\n"
"ds_read_b64_tr_b16 a[84:85], v11 offset:41120\n"
"ds_read_b64_tr_b16 a[86:87], v11 offset:46240\n"
"v_mfma_f32_16x16x32_bf16 a[4:7], a[96:99], v[116:119], a[4:7]\n"
"v_mfma_f32_16x16x32_bf16 a[8:11], a[100:103], v[112:115], a[8:11]\n"
"ds_read_b64_tr_b16 a[88:89], v10 offset:41152\n"
"ds_read_b64_tr_b16 a[90:91], v10 offset:46272\n"
"v_mfma_f32_16x16x32_bf16 a[12:15], a[100:103], v[116:119], a[12:15]\n"
"buffer_load_dwordx4 v4, s[40:43], s58 offen offset:2048 lds\n"
"v_mfma_f32_16x16x32_bf16 a[16:19], a[104:107], v[112:115], a[16:19]\n"
"ds_read_b64_tr_b16 a[92:93], v11 offset:41184\n"
"ds_read_b64_tr_b16 a[94:95], v11 offset:46304\n"
"v_mfma_f32_16x16x32_bf16 a[20:23], a[104:107], v[116:119], a[20:23]\n"
"s_waitcnt lgkmcnt(4)\n"
"v_mfma_f32_16x16x32_bf16 a[24:27], a[108:111], v[112:115], a[24:27]\n"
"ds_read_b64_tr_b16 a[96:97], v10 offset:41216\n"
"ds_read_b64_tr_b16 a[98:99], v10 offset:46336\n"
"v_mfma_f32_16x16x32_bf16 a[28:31], a[108:111], v[116:119], a[28:31]\n"
"buffer_load_dwordx4 v5, s[40:43], s58 offen offset:3072 lds\n"
"v_mfma_f32_16x16x32_bf16 a[32:35], a[80:83], v[112:115], a[32:35]\n"
"ds_read_b64_tr_b16 a[100:101], v11 offset:41248\n"
"ds_read_b64_tr_b16 a[102:103], v11 offset:46368\n"
"v_mfma_f32_16x16x32_bf16 a[36:39], a[80:83], v[116:119], a[36:39]\n"
"s_add_u32 m0, m0, 4096\n"
"v_mfma_f32_16x16x32_bf16 a[40:43], a[84:87], v[112:115], a[40:43]\n"
"ds_read_b64_tr_b16 a[104:105], v10 offset:51200\n"
"ds_read_b64_tr_b16 a[106:107], v10 offset:56320\n"
"v_mfma_f32_16x16x32_bf16 a[44:47], a[84:87], v[116:119], a[44:47]\n"
"buffer_load_dwordx4 v6, s[40:43], s58 offen lds\n"
"s_waitcnt lgkmcnt(4)\n"
"v_mfma_f32_16x16x32_bf16 a[48:51], a[88:91], v[112:115], a[48:51]\n"
"ds_read_b64_tr_b16 a[108:109], v11 offset:51232\n"
"ds_read_b64_tr_b16 a[110:111], v11 offset:56352\n"
"v_mfma_f32_16x16x32_bf16 a[52:55], a[88:91], v[116:119], a[52:55]\n"
"s_add_u32 s58, s58, 81920\n"
"v_mfma_f32_16x16x32_bf16 a[56:59], a[92:95], v[112:115], a[56:59]\n"
"ds_read_b64_tr_b16 a[80:81], v10 offset:51264\n"
"ds_read_b64_tr_b16 a[82:83], v10 offset:56384\n"
"v_mfma_f32_16x16x32_bf16 a[60:63], a[92:95], v[116:119], a[60:63]\n"
"buffer_load_dwordx4 v[72:75], v1, s[36:39], s57 offen\n"
"v_mfma_f32_16x16x32_bf16 a[64:67], a[96:99], v[112:115], a[64:67]\n"
"ds_read_b64_tr_b16 a[84:85], v11 offset:51296\n"
"ds_read_b64_tr_b16 a[86:87], v11 offset:56416\n"
"v_mfma_f32_16x16x32_bf16 a[68:71], a[96:99], v[116:119], a[68:71]\n"
"s_waitcnt lgkmcnt(8)\n"
"v_mfma_f32_16x16x32_bf16 a[72:75], a[100:103], v[112:115], a[72:75]\n"
"ds_read_b64_tr_b16 a[88:89], v10 offset:51328\n"
"ds_read_b64_tr_b16 a[90:91], v10 offset:56448\n"
"v_mfma_f32_16x16x32_bf16 a[76:79], a[100:103], v[116:119], a[76:79]\n"
"buffer_load_dwordx4 v[76:79], v1, s[36:39], s57 offen offset:1024\n"
"s_add_u32 s57, s57, 122880\n"
"s_waitcnt vmcnt(27) lgkmcnt(4)\n"
"v_mfma_f32_16x16x32_bf16 a[0:3], a[104:107], v[120:123], a[0:3]\n"
"ds_read_b64_tr_b16 a[92:93], v11 offset:51360\n"
"ds_read_b64_tr_b16 a[94:95], v11 offset:56480\n"
"v_mfma_f32_16x16x32_bf16 a[4:7], a[104:107], v[124:127], a[4:7]\n"
"v_mfma_f32_16x16x32_bf16 a[8:11], a[108:111], v[120:123], a[8:11]\n"
"ds_read_b64_tr_b16 a[96:97], v10 offset:51392\n"
"ds_read_b64_tr_b16 a[98:99], v10 offset:56512\n"
"v_mfma_f32_16x16x32_bf16 a[12:15], a[108:111], v[124:127], a[12:15]\n"
"buffer_load_dwordx4 v[80:83], v1, s[36:39], s57 offen\n"
"v_mfma_f32_16x16x32_bf16 a[16:19], a[80:83], v[120:123], a[16:19]\n"
"ds_read_b64_tr_b16 a[100:101], v11 offset:51424\n"
"ds_read_b64_tr_b16 a[102:103], v11 offset:56544\n"
"v_mfma_f32_16x16x32_bf16 a[20:23], a[80:83], v[124:127], a[20:23]\n"
"s_waitcnt lgkmcnt(4)\n"
"v_mfma_f32_16x16x32_bf16 a[24:27], a[84:87], v[120:123], a[24:27]\n"
"ds_read_b64_tr_b16 a[104:105], v10 offset:51456\n"
"ds_read_b64_tr_b16 a[106:107], v10 offset:56576\n"
"v_mfma_f32_16x16x32_bf16 a[28:31], a[84:87], v[124:127], a[28:31]\n"
"buffer_load_dwordx4 v[84:87], v1, s[36:39], s57 offen offset:1024\n"
"s_add_u32 s57, s57, 122880\n"
"v_mfma_f32_16x16x32_bf16 a[32:35], a[88:91], v[120:123], a[32:35]\n"
"ds_read_b64_tr_b16 a[108:109], v11 offset:51488\n"
"ds_read_b64_tr_b16 a[110:111], v11 offset:56608\n"
"v_mfma_f32_16x16x32_bf16 a[36:39], a[88:91], v[124:127], a[36:39]\n"
"s_waitcnt vmcnt(22) lgkmcnt(0)\n"
"s_barrier\n"
"v_mfma_f32_16x16x32_bf16 a[40:43], a[92:95], v[120:123], a[40:43]\n"
"ds_read_b64_tr_b16 a[80:81], v8 offset:0\n"
"ds_read_b64_tr_b16 a[82:83], v8 offset:5120\n"
"v_mfma_f32_16x16x32_bf16 a[44:47], a[92:95], v[124:127], a[44:47]\n"
"v_mfma_f32_16x16x32_bf16 a[48:51], a[96:99], v[120:123], a[48:51]\n"
"ds_read_b64_tr_b16 a[84:85], v9 offset:32\n"
"ds_read_b64_tr_b16 a[86:87], v9 offset:5152\n"
"v_mfma_f32_16x16x32_bf16 a[52:55], a[96:99], v[124:127], a[52:55]\n"
"s_add_u32 m0, s66, 61440\n"
"v_mfma_f32_16x16x32_bf16 a[56:59], a[100:103], v[120:123], a[56:59]\n"
"ds_read_b64_tr_b16 a[88:89], v8 offset:64\n"
"ds_read_b64_tr_b16 a[90:91], v8 offset:5184\n"
"v_mfma_f32_16x16x32_bf16 a[60:63], a[100:103], v[124:127], a[60:63]\n"
"buffer_load_dwordx4 v2, s[40:43], s58 offen offset:0 lds\n"
"v_mfma_f32_16x16x32_bf16 a[64:67], a[104:107], v[120:123], a[64:67]\n"
"ds_read_b64_tr_b16 a[92:93], v9 offset:96\n"
"ds_read_b64_tr_b16 a[94:95], v9 offset:5216\n"
"v_mfma_f32_16x16x32_bf16 a[68:71], a[104:107], v[124:127], a[68:71]\n"
"v_mfma_f32_16x16x32_bf16 a[72:75], a[108:111], v[120:123], a[72:75]\n"
"ds_read_b64_tr_b16 a[96:97], v8 offset:128\n"
"ds_read_b64_tr_b16 a[98:99], v8 offset:5248\n"
"v_mfma_f32_16x16x32_bf16 a[76:79], a[108:111], v[124:127], a[76:79]\n"
"buffer_load_dwordx4 v3, s[40:43], s58 offen offset:1024 lds\n"
"s_sub_u32 s59, s59, 1\n"
"s_cmp_lg_u32 s59, 0\n"
"s_cbranch_scc1 K2_W3L\n"
"K2_DONE:\n"
"s_waitcnt vmcnt(0)\n"
"s_waitcnt lgkmcnt(0)\n"
"s_nop 7\n"
"s_lshl_b32 s60, s67, 2\n"
"s_add_u32 s60, s60, s63\n"
"s_mul_i32 s60, s60, 3\n"
"s_sub_u32 s61, s56, s63\n"
"s_sub_u32 s61, s61, 1\n"
"s_and_b32 s61, s61, 3\n"
"s_add_u32 s61, s61, s60\n"
"s_mul_i32 s80, s61, 10240\n"
"s_cmp_eq_u32 s56, s63\n"
"s_cbranch_scc1 K2_OWN\n"
"v_accvgpr_read_b32 v36, a0\n"
"v_accvgpr_read_b32 v37, a1\n"
"v_accvgpr_read_b32 v38, a2\n"
"v_accvgpr_read_b32 v39, a3\n"
"v_accvgpr_read_b32 v40, a4\n"
"v_accvgpr_read_b32 v41, a5\n"
"v_accvgpr_read_b32 v42, a6\n"
"v_accvgpr_read_b32 v43, a7\n"
"v_cvt_pk_bf16_f32 v32, v36, v37\n"
"v_cvt_pk_bf16_f32 v33, v38, v39\n"
"v_cvt_pk_bf16_f32 v34, v40, v41\n"
"v_cvt_pk_bf16_f32 v35, v42, v43\n"
"buffer_store_dwordx4 v[32:35], v1, s[48:51], s80 offen offset:0 sc1\n"
"v_accvgpr_read_b32 v48, a8\n"
"v_accvgpr_read_b32 v49, a9\n"
"v_accvgpr_read_b32 v50, a10\n"
"v_accvgpr_read_b32 v51, a11\n"
"v_accvgpr_read_b32 v52, a12\n"
"v_accvgpr_read_b32 v53, a13\n"
"v_accvgpr_read_b32 v54, a14\n"
"v_accvgpr_read_b32 v55, a15\n"
"v_cvt_pk_bf16_f32 v44, v48, v49\n"
"v_cvt_pk_bf16_f32 v45, v50, v51\n"
"v_cvt_pk_bf16_f32 v46, v52, v53\n"
"v_cvt_pk_bf16_f32 v47, v54, v55\n"
"buffer_store_dwordx4 v[44:47], v1, s[48:51], s80 offen offset:1024 sc1\n"
"v_accvgpr_read_b32 v60, a16\n"
"v_accvgpr_read_b32 v61, a17\n"
"v_accvgpr_read_b32 v62, a18\n"
"v_accvgpr_read_b32 v63, a19\n"
"v_accvgpr_read_b32 v64, a20\n"
"v_accvgpr_read_b32 v65, a21\n"
"v_accvgpr_read_b32 v66, a22\n"
"v_accvgpr_read_b32 v67, a23\n"
"v_cvt_pk_bf16_f32 v56, v60, v61\n"
"v_cvt_pk_bf16_f32 v57, v62, v63\n"
"v_cvt_pk_bf16_f32 v58, v64, v65\n"
"v_cvt_pk_bf16_f32 v59, v66, v67\n"
"buffer_store_dwordx4 v[56:59], v1, s[48:51], s80 offen offset:2048 sc1\n"
"v_accvgpr_read_b32 v72, a24\n"
"v_accvgpr_read_b32 v73, a25\n"
"v_accvgpr_read_b32 v74, a26\n"
"v_accvgpr_read_b32 v75, a27\n"
"v_accvgpr_read_b32 v76, a28\n"
"v_accvgpr_read_b32 v77, a29\n"
"v_accvgpr_read_b32 v78, a30\n"
"v_accvgpr_read_b32 v79, a31\n"
"v_cvt_pk_bf16_f32 v68, v72, v73\n"
"v_cvt_pk_bf16_f32 v69, v74, v75\n"
"v_cvt_pk_bf16_f32 v70, v76, v77\n"
"v_cvt_pk_bf16_f32 v71, v78, v79\n"
"buffer_store_dwordx4 v[68:71], v1, s[48:51], s80 offen offset:3072 sc1\n"
"s_add_u32 s80, s80, 4096\n"
"v_accvgpr_read_b32 v84, a32\n"
"v_accvgpr_read_b32 v85, a33\n"
"v_accvgpr_read_b32 v86, a34\n"
"v_accvgpr_read_b32 v87, a35\n"
"v_accvgpr_read_b32 v88, a36\n"
"v_accvgpr_read_b32 v89, a37\n"
"v_accvgpr_read_b32 v90, a38\n"
"v_accvgpr_read_b32 v91, a39\n"
"v_cvt_pk_bf16_f32 v80, v84, v85\n"
"v_cvt_pk_bf16_f32 v81, v86, v87\n"
"v_cvt_pk_bf16_f32 v82, v88, v89\n"
"v_cvt_pk_bf16_f32 v83, v90, v91\n"
"buffer_store_dwordx4 v[80:83], v1, s[48:51], s80 offen offset:0 sc1\n"
"v_accvgpr_read_b32 v96, a40\n"
"v_accvgpr_read_b32 v97, a41\n"
"v_accvgpr_read_b32 v98, a42\n"
"v_accvgpr_read_b32 v99, a43\n"
"v_accvgpr_read_b32 v100, a44\n"
"v_accvgpr_read_b32 v101, a45\n"
"v_accvgpr_read_b32 v102, a46\n"
"v_accvgpr_read_b32 v103, a47\n"
"v_cvt_pk_bf16_f32 v92, v96, v97\n"
"v_cvt_pk_bf16_f32 v93, v98, v99\n"
"v_cvt_pk_bf16_f32 v94, v100, v101\n"
"v_cvt_pk_bf16_f32 v95, v102, v103\n"
"buffer_store_dwordx4 v[92:95], v1, s[48:51], s80 offen offset:1024 sc1\n"
"v_accvgpr_read_b32 v36, a48\n"
"v_accvgpr_read_b32 v37, a49\n"
"v_accvgpr_read_b32 v38, a50\n"
"v_accvgpr_read_b32 v39, a51\n"
"v_accvgpr_read_b32 v40, a52\n"
"v_accvgpr_read_b32 v41, a53\n"
"v_accvgpr_read_b32 v42, a54\n"
"v_accvgpr_read_b32 v43, a55\n"
"v_cvt_pk_bf16_f32 v32, v36, v37\n"
"v_cvt_pk_bf16_f32 v33, v38, v39\n"
"v_cvt_pk_bf16_f32 v34, v40, v41\n"
"v_cvt_pk_bf16_f32 v35, v42, v43\n"
"buffer_store_dwordx4 v[32:35], v1, s[48:51], s80 offen offset:2048 sc1\n"
"v_accvgpr_read_b32 v48, a56\n"
"v_accvgpr_read_b32 v49, a57\n"
"v_accvgpr_read_b32 v50, a58\n"
"v_accvgpr_read_b32 v51, a59\n"
"v_accvgpr_read_b32 v52, a60\n"
"v_accvgpr_read_b32 v53, a61\n"
"v_accvgpr_read_b32 v54, a62\n"
"v_accvgpr_read_b32 v55, a63\n"
"v_cvt_pk_bf16_f32 v44, v48, v49\n"
"v_cvt_pk_bf16_f32 v45, v50, v51\n"
"v_cvt_pk_bf16_f32 v46, v52, v53\n"
"v_cvt_pk_bf16_f32 v47, v54, v55\n"
"buffer_store_dwordx4 v[44:47], v1, s[48:51], s80 offen offset:3072 sc1\n"
"s_add_u32 s80, s80, 4096\n"
"v_accvgpr_read_b32 v60, a64\n"
"v_accvgpr_read_b32 v61, a65\n"
"v_accvgpr_read_b32 v62, a66\n"
"v_accvgpr_read_b32 v63, a67\n"
"v_accvgpr_read_b32 v64, a68\n"
"v_accvgpr_read_b32 v65, a69\n"
"v_accvgpr_read_b32 v66, a70\n"
"v_accvgpr_read_b32 v67, a71\n"
"v_cvt_pk_bf16_f32 v56, v60, v61\n"
"v_cvt_pk_bf16_f32 v57, v62, v63\n"
"v_cvt_pk_bf16_f32 v58, v64, v65\n"
"v_cvt_pk_bf16_f32 v59, v66, v67\n"
"buffer_store_dwordx4 v[56:59], v1, s[48:51], s80 offen offset:0 sc1\n"
"v_accvgpr_read_b32 v72, a72\n"
"v_accvgpr_read_b32 v73, a73\n"
"v_accvgpr_read_b32 v74, a74\n"
"v_accvgpr_read_b32 v75, a75\n"
"v_accvgpr_read_b32 v76, a76\n"
"v_accvgpr_read_b32 v77, a77\n"
"v_accvgpr_read_b32 v78, a78\n"
"v_accvgpr_read_b32 v79, a79\n"
"v_cvt_pk_bf16_f32 v68, v72, v73\n"
"v_cvt_pk_bf16_f32 v69, v74, v75\n"
"v_cvt_pk_bf16_f32 v70, v76, v77\n"
"v_cvt_pk_bf16_f32 v71, v78, v79\n"
"buffer_store_dwordx4 v[68:71], v1, s[48:51], s80 offen offset:1024 sc1\n"
"s_nop 1\n"
"s_branch K2_JOIN\n"
"K2_OWN:\n"
"v_add_u32 v12, 122880, v1\n"
"ds_write_b128 v12, a[0:3] offset:0\n"
"ds_write_b128 v12, a[4:7] offset:1024\n"
"ds_write_b128 v12, a[8:11] offset:2048\n"
"ds_write_b128 v12, a[12:15] offset:3072\n"
"ds_write_b128 v12, a[16:19] offset:4096\n"
"ds_write_b128 v12, a[20:23] offset:5120\n"
"ds_write_b128 v12, a[24:27] offset:6144\n"
"ds_write_b128 v12, a[28:31] offset:7168\n"
"ds_write_b128 v12, a[32:35] offset:8192\n"
"ds_write_b128 v12, a[36:39] offset:9216\n"
"ds_write_b128 v12, a[40:43] offset:10240\n"
"ds_write_b128 v12, a[44:47] offset:11264\n"
"ds_write_b128 v12, a[48:51] offset:12288\n"
"ds_write_b128 v12, a[52:55] offset:13312\n"
"s_waitcnt lgkmcnt(0)\n"
"ds_write_b128 v12, a[56:59] offset:14336\n"
"ds_write_b128 v12, a[60:63] offset:15360\n"
"ds_write_b128 v12, a[64:67] offset:16384\n"
"ds_write_b128 v12, a[68:71] offset:17408\n"
"ds_write_b128 v12, a[72:75] offset:18432\n"
"ds_write_b128 v12, a[76:79] offset:19456\n"
"s_waitcnt lgkmcnt(0)\n"
"K2_JOIN:\n"
"s_barrier\n"
"s_lshl_b32 s61, s56, 10\n"
"v_add_u32 v13, 4096, v1\n"
"v_add_u32 v11, 8192, v1\n"
"s_cmp_ge_u32 s56, 2\n"
"s_cselect_b32 s59, 0x7fffff00, 0\n"
"v_add_u32 v11, s59, v11\n"
"s_add_u32 s60, s63, 1\n"
"s_and_b32 s60, s60, 3\n"
"s_lshl_b32 s62, s67, 2\n"
"s_add_u32 s60, s60, s62\n"
"s_mul_i32 s60, s60, 3\n"
"s_add_u32 s60, s60, 2\n"
"s_mul_i32 s60, s60, 10240\n"
"s_add_u32 s81, s60, s61\n"
"s_add_u32 s60, s63, 2\n"
"s_and_b32 s60, s60, 3\n"
"s_lshl_b32 s62, s67, 2\n"
"s_add_u32 s60, s60, s62\n"
"s_mul_i32 s60, s60, 3\n"
"s_add_u32 s60, s60, 1\n"
"s_mul_i32 s60, s60, 10240\n"
"s_add_u32 s82, s60, s61\n"
"s_add_u32 s60, s63, 3\n"
"s_and_b32 s60, s60, 3\n"
"s_lshl_b32 s62, s67, 2\n"
"s_add_u32 s60, s60, s62\n"
"s_mul_i32 s60, s60, 3\n"
"s_add_u32 s60, s60, 0\n"
"s_mul_i32 s60, s60, 10240\n"
"s_add_u32 s83, s60, s61\n"
"s_lshl_b32 s61, s56, 11\n"
"v_add_u32 v12, 122880, v1\n"
"v_add_u32 v12, s61, v12\n"
"ds_read_b128 v[32:35], v12 offset:0\n"
"ds_read_b128 v[36:39], v12 offset:1024\n"
"ds_read_b128 v[52:55], v12 offset:8192\n"
"ds_read_b128 v[56:59], v12 offset:9216\n"
"ds_read_b128 v[72:75], v12 offset:16384\n"
"ds_read_b128 v[76:79], v12 offset:17408\n"
"s_mov_b32 s59, 0x8000\n"
"K2_POLL:\n"
"buffer_load_dwordx4 v[40:43], v1, s[48:51], s81 offen sc1\n"
"buffer_load_dwordx4 v[60:63], v13, s[48:51], s81 offen sc1\n"
"buffer_load_dwordx4 v[80:83], v11, s[48:51], s81 offen sc1\n"
"buffer_load_dwordx4 v[44:47], v1, s[48:51], s82 offen sc1\n"
"buffer_load_dwordx4 v[64:67], v13, s[48:51], s82 offen sc1\n"
"buffer_load_dwordx4 v[84:87], v11, s[48:51], s82 offen sc1\n"
"buffer_load_dwordx4 v[48:51], v1, s[48:51], s83 offen sc1\n"
"buffer_load_dwordx4 v[68:71], v13, s[48:51], s83 offen sc1\n"
"buffer_load_dwordx4 v[88:91], v11, s[48:51], s83 offen sc1\n"
"s_waitcnt vmcnt(0)\n"
"v_max3_u32 v16, v40, v41, v42\n"
"v_max3_u32 v17, v43, v44, v45\n"
"v_max3_u32 v16, v16, v46, v47\n"
"v_max3_u32 v17, v17, v48, v49\n"
"v_max3_u32 v16, v16, v50, v51\n"
"v_max3_u32 v17, v17, v60, v61\n"
"v_max3_u32 v16, v16, v62, v63\n"
"v_max3_u32 v17, v17, v64, v65\n"
"v_max3_u32 v16, v16, v66, v67\n"
"v_max3_u32 v17, v17, v68, v69\n"
"v_max3_u32 v16, v16, v70, v71\n"
"v_max3_u32 v17, v17, v80, v81\n"
"v_max3_u32 v16, v16, v82, v83\n"
"v_max3_u32 v17, v17, v84, v85\n"
"v_max3_u32 v16, v16, v86, v87\n"
"v_max3_u32 v17, v17, v88, v89\n"
"v_max3_u32 v16, v16, v90, v91\n"
"v_max_u32 v16, v16, v17\n"
"v_cmp_eq_u32 vcc, -1, v16\n"
"s_nop 0\n"
"s_cbranch_vccz K2_GOT\n"
"s_sub_u32 s59, s59, 1\n"
"s_cmp_lg_u32 s59, 0\n"
"s_cbranch_scc1 K2_POLL\n"
"K2_GOT:\n"
"v_and_b32 v14, 15, v0\n"
"v_mul_u32_u24 v14, 1280, v14\n"
"v_lshrrev_b32 v15, 4, v0\n"
"v_lshl_add_u32 v14, v15, 3, v14\n"
"s_lshl_b32 s60, s64, 3\n"
"s_lshl_b32 s61, s63, 1\n"
"s_add_u32 s60, s60, s61\n"
"s_mul_i32 s60, s60, 20480\n"
"s_mul_i32 s61, s65, 320\n"
"s_add_u32 s60, s60, s61\n"
"s_lshl_b32 s61, s56, 5\n"
"s_add_u32 s60, s60, s61\n"
"s_mov_b32 s72, 0x00003f80\n"
"s_mov_b32 s73, 0x3f800000\n"
"v_add_u32 v15, 20480, v14\n"
"s_waitcnt lgkmcnt(0)\n"
"v_dot2c_f32_bf16 v32, s72, v40\n"
"v_dot2c_f32_bf16 v33, s73, v40\n"
"v_dot2c_f32_bf16 v34, s72, v41\n"
"v_dot2c_f32_bf16 v35, s73, v41\n"
"v_dot2c_f32_bf16 v36, s72, v42\n"
"v_dot2c_f32_bf16 v37, s73, v42\n"
"v_dot2c_f32_bf16 v38, s72, v43\n"
"v_dot2c_f32_bf16 v39, s73, v43\n"
"v_dot2c_f32_bf16 v32, s72, v44\n"
"v_dot2c_f32_bf16 v33, s73, v44\n"
"v_dot2c_f32_bf16 v34, s72, v45\n"
"v_dot2c_f32_bf16 v35, s73, v45\n"
"v_dot2c_f32_bf16 v36, s72, v46\n"
"v_dot2c_f32_bf16 v37, s73, v46\n"
"v_dot2c_f32_bf16 v38, s72, v47\n"
"v_dot2c_f32_bf16 v39, s73, v47\n"
"v_dot2c_f32_bf16 v32, s72, v48\n"
"v_dot2c_f32_bf16 v33, s73, v48\n"
"v_dot2c_f32_bf16 v34, s72, v49\n"
"v_dot2c_f32_bf16 v35, s73, v49\n"
"v_dot2c_f32_bf16 v36, s72, v50\n"
"v_dot2c_f32_bf16 v37, s73, v50\n"
"v_dot2c_f32_bf16 v38, s72, v51\n"
"v_dot2c_f32_bf16 v39, s73, v51\n"
"s_nop 2\n"
"v_cvt_pk_bf16_f32 v40, v32, v33\n"
"v_cvt_pk_bf16_f32 v41, v34, v35\n"
"v_cvt_pk_bf16_f32 v42, v36, v37\n"
"v_cvt_pk_bf16_f32 v43, v38, v39\n"
"buffer_store_dwordx2 v[40:41], v14, s[44:47], s60 offen\n"
"buffer_store_dwordx2 v[42:43], v15, s[44:47], s60 offen\n"
"s_add_u32 s60, s60, 128\n"
"v_dot2c_f32_bf16 v52, s72, v60\n"
"v_dot2c_f32_bf16 v53, s73, v60\n"
"v_dot2c_f32_bf16 v54, s72, v61\n"
"v_dot2c_f32_bf16 v55, s73, v61\n"
"v_dot2c_f32_bf16 v56, s72, v62\n"
"v_dot2c_f32_bf16 v57, s73, v62\n"
"v_dot2c_f32_bf16 v58, s72, v63\n"
"v_dot2c_f32_bf16 v59, s73, v63\n"
"v_dot2c_f32_bf16 v52, s72, v64\n"
"v_dot2c_f32_bf16 v53, s73, v64\n"
"v_dot2c_f32_bf16 v54, s72, v65\n"
"v_dot2c_f32_bf16 v55, s73, v65\n"
"v_dot2c_f32_bf16 v56, s72, v66\n"
"v_dot2c_f32_bf16 v57, s73, v66\n"
"v_dot2c_f32_bf16 v58, s72, v67\n"
"v_dot2c_f32_bf16 v59, s73, v67\n"
"v_dot2c_f32_bf16 v52, s72, v68\n"
"v_dot2c_f32_bf16 v53, s73, v68\n"
"v_dot2c_f32_bf16 v54, s72, v69\n"
"v_dot2c_f32_bf16 v55, s73, v69\n"
"v_dot2c_f32_bf16 v56, s72, v70\n"
"v_dot2c_f32_bf16 v57, s73, v70\n"
"v_dot2c_f32_bf16 v58, s72, v71\n"
"v_dot2c_f32_bf16 v59, s73, v71\n"
"s_nop 2\n"
"v_cvt_pk_bf16_f32 v60, v52, v53\n"
"v_cvt_pk_bf16_f32 v61, v54, v55\n"
"v_cvt_pk_bf16_f32 v62, v56, v57\n"
"v_cvt_pk_bf16_f32 v63, v58, v59\n"
"buffer_store_dwordx2 v[60:61], v14, s[44:47], s60 offen\n"
"buffer_store_dwordx2 v[62:63], v15, s[44:47], s60 offen\n"
"s_add_u32 s60, s60, 128\n"
"s_cmp_ge_u32 s56, 2\n"
"s_cbranch_scc1 K2_FIN\n"
"v_dot2c_f32_bf16 v72, s72, v80\n"
"v_dot2c_f32_bf16 v73, s73, v80\n"
"v_dot2c_f32_bf16 v74, s72, v81\n"
"v_dot2c_f32_bf16 v75, s73, v81\n"
"v_dot2c_f32_bf16 v76, s72, v82\n"
"v_dot2c_f32_bf16 v77, s73, v82\n"
"v_dot2c_f32_bf16 v78, s72, v83\n"
"v_dot2c_f32_bf16 v79, s73, v83\n"
"v_dot2c_f32_bf16 v72, s72, v84\n"
"v_dot2c_f32_bf16 v73, s73, v84\n"
"v_dot2c_f32_bf16 v74, s72, v85\n"
"v_dot2c_f32_bf16 v75, s73, v85\n"
"v_dot2c_f32_bf16 v76, s72, v86\n"
"v_dot2c_f32_bf16 v77, s73, v86\n"
"v_dot2c_f32_bf16 v78, s72, v87\n"
"v_dot2c_f32_bf16 v79, s73, v87\n"
"v_dot2c_f32_bf16 v72, s72, v88\n"
"v_dot2c_f32_bf16 v73, s73, v88\n"
"v_dot2c_f32_bf16 v74, s72, v89\n"
"v_dot2c_f32_bf16 v75, s73, v89\n"
"v_dot2c_f32_bf16 v76, s72, v90\n"
"v_dot2c_f32_bf16 v77, s73, v90\n"
"v_dot2c_f32_bf16 v78, s72, v91\n"
"v_dot2c_f32_bf16 v79, s73, v91\n"
"s_nop 2\n"
"v_cvt_pk_bf16_f32 v80, v72, v73\n"
"v_cvt_pk_bf16_f32 v81, v74, v75\n"
"v_cvt_pk_bf16_f32 v82, v76, v77\n"
"v_cvt_pk_bf16_f32 v83, v78, v79\n"
"buffer_store_dwordx2 v[80:81], v14, s[44:47], s60 offen\n"
"buffer_store_dwordx2 v[82:83], v15, s[44:47], s60 offen\n"
"s_add_u32 s60, s60, 128\n"
"K2_FIN:\n"
"K2_CODE_END:\n"
  : [tid] "+v"(tid)
  : [bid] "s"(bid), [olo] "s"((unsigned)(size_t)o), [ohi] "s"((unsigned)((size_t)o >> 32)), [wlo] "s"((unsigned)(size_t)wo), [whi] "s"((unsigned)((size_t)wo >> 32)), [ylo] "s"((unsigned)(size_t)y), [yhi] "s"((unsigned)((size_t)y >> 32))
  : "memory", "vcc", "scc", "s36","s37","s38","s39","s40","s41","s42","s43","s44","s45","s46","s47","s48","s49","s50","s51","s52","s53","s54","s55","s56","s57","s58","s59","s60","s61","s62","s63","s64","s65","s66","s67","s68","s69","s70","s71","s72","s73","s74","s75","s76","s77","s78","s79","s80","s81","s82","s83","s84","s85","s86","s87","s88","s89","s90","s91","s92","s93","s94","s95", "v1","v2","v3","v4","v5","v6","v7","v8","v9","v10","v11","v12","v13","v14","v15","v16","v17","v18","v19","v20","v21","v22","v23","v24","v25","v26","v27","v28","v29","v30","v31","v32","v33","v34","v35","v36","v37","v38","v39","v40","v41","v42","v43","v44","v45","v46","v47","v48","v49","v50","v51","v52","v53","v54","v55","v56","v57","v58","v59","v60","v61","v62","v63","v64","v65","v66","v67","v68","v69","v70","v71","v72","v73","v74","v75","v76","v77","v78","v79","v80","v81","v82","v83","v84","v85","v86","v87","v88","v89","v90","v91","v92","v93","v94","v95","v96","v97","v98","v99","v100","v101","v102","v103","v104","v105","v106","v107","v108","v109","v110","v111","v112","v113","v114","v115","v116","v117","v118","v119","v120","v121","v122","v123","v124","v125","v126","v127", "a0","a1","a2","a3","a4","a5","a6","a7","a8","a9","a10","a11","a12","a13","a14","a15","a16","a17","a18","a19","a20","a21","a22","a23","a24","a25","a26","a27","a28","a29","a30","a31","a32","a33","a34","a35","a36","a37","a38","a39","a40","a41","a42","a43","a44","a45","a46","a47","a48","a49","a50","a51","a52","a53","a54","a55","a56","a57","a58","a59","a60","a61","a62","a63","a64","a65","a66","a67","a68","a69","a70","a71","a72","a73","a74","a75","a76","a77","a78","a79","a80","a81","a82","a83","a84","a85","a86","a87","a88","a89","a90","a91","a92","a93","a94","a95","a96","a97","a98","a99","a100","a101","a102","a103","a104","a105","a106","a107","a108","a109","a110","a111");
}
extern "C" void kernel_launch(void* const* d_in, const int* in_sizes, int n_in, void* d_out, int out_size, void* d_ws, size_t ws_size, hipStream_t stream) {
  (void)in_sizes; (void)n_in; (void)out_size; (void)ws_size;
  (void)hipFuncSetAttribute((const void*)k1_qattn, hipFuncAttributeMaxDynamicSharedMemorySize, 163840);
  char* ws = (char*)d_ws;
  k1_qattn<<<256, 256, 163840, stream>>>(d_in[0], d_in[1], d_in[2], d_in[3], d_in[4], ws, ws + (32u << 20));
  (void)hipFuncSetAttribute((const void*)k2_oproj, hipFuncAttributeMaxDynamicSharedMemorySize, 147712);
  k2_oproj<<<240, 256, 147712, stream>>>(ws, d_in[4], d_out, ws + (33u << 20));
}
```
